```python
import torch

def _softmax(x, axis=-1):
    return torch.softmax(x, dim=axis)
def _gelu(x):
    return torch.nn.functional.gelu(x, approximate="tanh")
def _silu(x):
    return torch.nn.functional.silu(x)
def _sigmoid(x):
    return torch.sigmoid(x)
def _layernorm(x, axis=-1, eps=1e-5):
    m = x.mean(dim=axis, keepdim=True)
    v = x.var(dim=axis, keepdim=True, unbiased=False)
    return (x - m) / torch.sqrt(v + eps)
def _scatter_add(tgt, idx, src, axis=0):
    return tgt.clone().index_add_(axis, idx, src)
def _segment_sum(src, seg, n):
    out = torch.zeros((n,) + src.shape[1:], dtype=src.dtype)
    return out.index_add_(0, seg, src)
def _pad(x, pw):
    flat = tuple(v for pair in reversed(pw) for v in pair)
    return torch.nn.functional.pad(x, flat)

kernel_name = 'standalone__mha_block_cached__near_ridge'

def setup_inputs():

    _w = (lambda a: (lambda t: t.to(torch.bfloat16) if t.is_floating_point() else t)(torch.as_tensor(a)))
    B, Sq, D = 8, 304, 512
    H, Sk, Dh = 32, 1024, 128
    return {
        "x": _w((lambda *s: torch.randn(*s, dtype=torch.bfloat16) * 0.8)(B, Sq, D)),
        "k_cache": _w((lambda *s: torch.randn(*s, dtype=torch.bfloat16) * 0.8)(B, H, Sk, Dh)),
        "v_cache": _w((lambda *s: torch.randn(*s, dtype=torch.bfloat16) * 0.05)(B, H, Sk, Dh)),
        "wq": _w((lambda *s: torch.randn(*s, dtype=torch.bfloat16) * 0.8)(D, H * Dh) * (D ** -0.5)),
        "wo": _w((lambda *s: torch.randn(*s, dtype=torch.bfloat16) * 0.05)(H * Dh, D) * ((H * Dh) ** -0.5)),
    }

def reference(x, k_cache, v_cache, wq, wo):
    B, Sq, D = x.shape
    H, Dh = 32, 128
    q = (lambda x, *d: x.permute(*d))((x @ wq).reshape(B, Sq, H, Dh), 0, 2, 1, 3)
    scores = torch.einsum("bhqd,bhkd->bhqk", q, k_cache) * (Dh ** -0.5)
    p = _softmax(scores, axis=-1)
    o = torch.einsum("bhqk,bhkd->bhqd", p, v_cache)
    return (lambda x, *d: x.permute(*d))(o, 0, 2, 1, 3).reshape(B, Sq, H * Dh) @ wo
```

```cpp
#include <hip/hip_runtime.h>
#include <hip/hip_bf16.h>
typedef __attribute__((ext_vector_type(8))) __bf16 bf16x8;
typedef __attribute__((ext_vector_type(4))) float f32x4;
typedef unsigned short u16;
constexpr int B=8,SQ=304,D=512,H=32,SK=1024,DH=128;
__device__ inline float bf2f(u16 v){ return __uint_as_float(((unsigned)v)<<16);}
__device__ inline u16 f2bf(float f){ __bf16 b=(__bf16)f; return __builtin_bit_cast(u16,b);}
__global__ void gemm_naive(const u16* A,const u16* W,u16* C,int M,int N,int K){
  int wave = (blockIdx.x*blockDim.x+threadIdx.x)>>6, lane=threadIdx.x&63;
  int nt=N/16; int tm=wave/nt, tn=wave%nt; if(tm*16>=M) return;
  f32x4 acc={0,0,0,0};
  int r=lane&15,g=lane>>4;
  for(int k=0;k<K;k+=32){
    bf16x8 a=*(const bf16x8*)(A+(size_t)(tm*16+r)*K+k+8*g);
    bf16x8 b; u16* bp=(u16*)&b;
    for(int j=0;j<8;j++) bp[j]=W[(size_t)(k+8*g+j)*N+tn*16+r];
    acc=__builtin_amdgcn_mfma_f32_16x16x32_bf16(a,b,acc,0,0,0);
  }
  for(int j=0;j<4;j++) C[(size_t)(tm*16+4*g+j)*N+tn*16+r]=f2bf(acc[j]);
}
__global__ void attn_naive(const u16* Q,const u16* Kc,const u16* Vc,u16* O){
  int wave=(blockIdx.x*blockDim.x+threadIdx.x)>>6, lane=threadIdx.x&63;
  int qb=wave%19, bh=wave/19; if(bh>=B*H) return; int b=bh/H,h=bh%H;
  int r=lane&15,g=lane>>4;
  bf16x8 q[4];
  for(int s=0;s<4;s++) q[s]=*(const bf16x8*)(Q+(size_t)(b*SQ+qb*16+r)*(H*DH)+h*DH+32*s+8*g);
  const u16* Kp=Kc+(size_t)bh*SK*DH; const u16* Vp=Vc+(size_t)bh*SK*DH;
  f32x4 o[8]; for(int i=0;i<8;i++) o[i]=f32x4{0,0,0,0};
  float m=-1e30f,l=0; const float c=0.08838834764831845f*1.4426950408889634f;
  for(int k0=0;k0<SK;k0+=32){
    f32x4 s[2];
    for(int t=0;t<2;t++){ s[t]=f32x4{0,0,0,0};
      int key=k0+8*(r>>2)+4*t+(r&3);
      for(int u=0;u<4;u++){ bf16x8 a=*(const bf16x8*)(Kp+(size_t)key*DH+32*u+8*g);
        s[t]=__builtin_amdgcn_mfma_f32_16x16x32_bf16(a,q[u],s[t],0,0,0);} }
    float mx=m; for(int t=0;t<2;t++) for(int j=0;j<4;j++) mx=fmaxf(mx,s[t][j]);
    mx=fmaxf(mx,__shfl_xor(mx,16)); mx=fmaxf(mx,__shfl_xor(mx,32));
    float al=exp2f((m-mx)*c); m=mx; l*=al; for(int i=0;i<8;i++) o[i]*=al;
    bf16x8 p; for(int t=0;t<2;t++) for(int j=0;j<4;j++){ float e=exp2f((s[t][j]-m)*c); l+=e; p[4*t+j]=(__bf16)e;}
    for(int i=0;i<8;i++){ bf16x8 a; u16* ap=(u16*)&a; for(int j=0;j<8;j++) ap[j]=Vp[(size_t)(k0+8*g+j)*DH+16*i+r];
      o[i]=__builtin_amdgcn_mfma_f32_16x16x32_bf16(a,p,o[i],0,0,0);}
  }
  l+=__shfl_xor(l,16); l+=__shfl_xor(l,32); float il=1.f/l;
  for(int i=0;i<8;i++) for(int j=0;j<4;j++) O[(size_t)(b*SQ+qb*16+r)*(H*DH)+h*DH+16*i+4*g+j]=f2bf(o[i][j]*il);
}
extern "C" void kernel_launch(void* const* d_in, const int* in_sizes, int n_in, void* d_out, int out_size, void* d_ws, size_t ws_size, hipStream_t stream){
  const u16* x=(const u16*)d_in[0]; const u16* kc=(const u16*)d_in[1]; const u16* vc=(const u16*)d_in[2];
  const u16* wq=(const u16*)d_in[3]; const u16* wo=(const u16*)d_in[4];
  u16* Q=(u16*)d_ws; u16* O=Q+(size_t)B*SQ*H*DH;
  int M=B*SQ;
  gemm_naive<<<(M/16)*(H*DH/16)/4,256,0,stream>>>(x,wq,Q,M,H*DH,D);
  attn_naive<<<B*H*19/4,256,0,stream>>>(Q,kc,vc,O);
  gemm_naive<<<(M/16)*(D/16)/4,256,0,stream>>>(O,wo,(u16*)d_out,M,D,H*DH);
}
```

```cpp
#include <hip/hip_runtime.h>

__global__ __launch_bounds__(256, 1) void mha_fused(const void* x, const void* kc, const void* vc, const void* wq, const void* wo, void* out, void* ws) {
  __shared__ __attribute__((aligned(16))) char lds[163840];
  const unsigned wave = __builtin_amdgcn_readfirstlane(threadIdx.x >> 6);
  asm volatile(
"s_load_dwordx8 s[4:11], %0, 0x0\n"
"s_load_dwordx4 s[12:15], %0, 0x20\n"
"s_load_dwordx2 s[16:17], %0, 0x30\n"
"s_getpc_b64 s[18:19]\n"
".Lmha_pc:\n"
"s_mov_b32 s48, %2\n"
"s_lshr_b32 s53, %1, 3\n"
"v_mbcnt_lo_u32_b32 v0, -1, 0\n"
"v_mbcnt_hi_u32_b32 v0, -1, v0\n"
"s_lshl2_add_u32 s52, s53, s48\n"
"s_mul_i32 s52, s52, 768\n"
"v_min_u32 v32, 5, v0\n"
"v_lshl_add_u32 v32, v32, 7, s52\n"
"v_min_u32 v32, .Lmha_done-.Lmha_pc, v32\n"
"global_load_dword v33, v32, s[18:19]\n"
"s_mul_i32 s52, s53, 3072\n"
"v_min_u32 v32, 23, v0\n"
"v_lshl_add_u32 v32, v32, 7, s52\n"
"v_min_u32 v32, .Lmha_done-.Lmha_pc, v32\n"
"s_and_b32 s52, %1, 7\n"
"s_and_b32 s49, s52, 1\n"
"s_and_b32 s54, s53, 3\n"
"s_lshl2_add_u32 s49, s49, s54\n"
"s_lshr_b32 s50, s52, 1\n"
"s_lshr_b32 s55, s53, 2\n"
"s_lshl3_add_u32 s50, s50, s55\n"
"s_lshl_b32 s57, s48, 17\n"
"s_mul_i32 s58, s48, 4112\n"
"v_bfe_u32 v1, v0, 2, 2\n"
"v_lshrrev_b32 v2, 4, v0\n"
"v_lshlrev_b32 v1, 13, v1\n"
"v_and_b32 v3, 3, v0\n"
"v_lshl_add_u32 v1, v2, 6, v1\n"
"v_lshl_add_u32 v22, v3, 4, v1\n"
"v_add_u32 v23, 31744, v22\n"
"v_add_u32 v24, 63480, v22\n"
"v_add_u32 v25, 95224, v22\n"
"s_add_u32 m0, s58, 16384\n"
"s_add_u32 s67, s57, 0\n"
"s_mul_i32 s56, s48, 81920\n"
"s_mul_i32 s60, s48, 10240\n"
"s_add_u32 s59, s60, 32832\n"
"s_add_u32 s60, s60, 108480\n"
"v_lshrrev_b32 v1, 3, v0\n"
"v_and_b32 v16, 7, v0\n"
"v_xor_b32 v16, v16, v2\n"
"v_lshlrev_b32 v16, 4, v16\n"
"v_lshl_add_u32 v16, v1, 10, v16\n"
"v_xor_b32 v17, 64, v16\n"
"v_add_u32 v17, 7168, v17\n"
"s_mul_i32 s52, s50, 10240\n"
"s_mul_i32 s53, s48, 5120\n"
"s_add_u32 s52, s52, s53\n"
"v_min_u32 v29, 39, v0\n"
"v_and_b32 v31, 7, v29\n"
"v_and_b32 v29, 56, v29\n"
"v_max_u32 v35, 6, v31\n"
"v_min_u32 v31, 5, v31\n"
"v_or_b32 v35, v35, v29\n"
"v_or_b32 v29, v29, v31\n"
"v_lshl_add_u32 v35, v35, 7, s52\n"
"v_lshl_add_u32 v29, v29, 7, s52\n"
"s_and_b32 s53, s48, 1\n"
"s_lshl1_add_u32 s53, s49, s53\n"
"s_lshl_b32 s53, s53, 15\n"
"v_lshlrev_b32 v30, 19, v1\n"
"v_bfe_u32 v31, v0, 1, 2\n"
"v_lshl_add_u32 v30, v31, 13, v30\n"
"v_and_b32 v31, 1, v0\n"
"v_lshl_add_u32 v30, v31, 7, v30\n"
"v_add_u32 v30, s53, v30\n"
"v_and_b32 v30, 0x7ffff, v30\n"
"v_max_u32 v31, 6, v1\n"
"v_lshl_add_u32 v31, v31, 19, v30\n"
"v_min_u32 v1, 5, v1\n"
"v_lshl_add_u32 v30, v1, 19, v30\n"
"s_lshl_b32 s52, s50, 8\n"
"s_sub_u32 s26, 4194304, s52\n"
"s_mov_b32 s27, 0x20000\n"
"s_mul_i32 s53, s49, 311296\n"
"s_mov_b32 s22, 311296\n"
"s_mov_b32 s23, 0x20000\n"
"s_waitcnt lgkmcnt(0)\n"
"s_add_u32 s24, s10, s52\n"
"s_addc_u32 s25, s11, 0\n"
"s_and_b32 s25, s25, 0xffff\n"
"buffer_load_dwordx4 v22, s[24:27], s67 offen lds\n"
"buffer_load_dwordx4 v23, s[24:27], s67 offen offset:1024 lds\n"
"buffer_load_dwordx4 v24, s[24:27], s67 offen offset:2056 lds\n"
"buffer_load_dwordx4 v25, s[24:27], s67 offen offset:3080 lds\n"
"s_add_u32 s20, s4, s53\n"
"s_addc_u32 s21, s5, 0\n"
"s_and_b32 s21, s21, 0xffff\n"
"s_cmp_eq_u32 s48, 3\n"
"s_cbranch_scc1 .Lmha_w3\n"
"s_add_u32 m0, s59, 0\n"
"s_add_u32 s66, s56, 0\n"
"buffer_load_dwordx4 v16, s[20:23], s66 offen lds\n"
"buffer_load_dwordx4 v17, s[20:23], s66 offen offset:1024 lds\n"
"s_add_u32 m0, s59, 2048\n"
"s_add_u32 s66, s56, 16384\n"
"buffer_load_dwordx4 v16, s[20:23], s66 offen lds\n"
"buffer_load_dwordx4 v17, s[20:23], s66 offen offset:1024 lds\n"
"s_cmp_eq_u32 s48, 2\n"
"s_cbranch_scc1 .Lmha_tw\n"
"buffer_load_dword v33, v29, s[20:23], 0 offen\n"
"s_branch .Lmha_tx\n"
".Lmha_tw:\n"
"buffer_load_dword v33, v30, s[24:27], 0 offen\n"
".Lmha_tx:\n"
"s_lshl_b32 s52, s49, 7\n"
"s_add_u32 s52, s52, 25296896\n"
"v_mov_b32 v34, s52\n"
"global_load_dwordx2 v[56:57], v34, s[16:17] sc1\n"
"s_add_u32 m0, s59, 4096\n"
"s_add_u32 s66, s56, 32768\n"
"buffer_load_dwordx4 v16, s[20:23], s66 offen lds\n"
"buffer_load_dwordx4 v17, s[20:23], s66 offen offset:1024 lds\n"
"s_add_u32 m0, s59, 6144\n"
"s_add_u32 s66, s56, 49152\n"
"buffer_load_dwordx4 v16, s[20:23], s66 offen lds\n"
"buffer_load_dwordx4 v17, s[20:23], s66 offen offset:1024 lds\n"
"s_add_u32 m0, s59, 8192\n"
"s_add_u32 s66, s56, 65536\n"
"buffer_load_dwordx4 v16, s[20:23], s66 offen lds\n"
"buffer_load_dwordx4 v17, s[20:23], s66 offen offset:1024 lds\n"
"v_and_b32 v1, 15, v0\n"
"v_lshrrev_b32 v36, 1, v1\n"
"v_xor_b32 v36, v36, v2\n"
"v_lshlrev_b32 v36, 4, v36\n"
"v_lshl_add_u32 v36, v1, 7, v36\n"
"v_xor_b32 v37, 64, v36\n"
"v_add_u32 v18, s59, v36\n"
"v_add_u32 v19, s59, v37\n"
"v_add_u32 v20, s60, v36\n"
"v_add_u32 v21, s60, v37\n"
"v_mul_u32_u24 v36, 2056, v2\n"
"v_lshl_add_u32 v36, v1, 4, v36\n"
"v_add_u32 v26, 16384, v36\n"
"v_add_u32 v27, 147392, v36\n"
"v_add_u32 v28, 81920, v36\n"
"v_and_b32 v1, 15, v0\n"
"v_lshrrev_b32 v2, 4, v0\n"
"v_and_b32 v3, 3, v0\n"
"v_xor_b32 v36, v1, v2\n"
"v_lshlrev_b32 v36, 4, v36\n"
"v_lshl_add_u32 v4, v1, 8, v36\n"
"v_xor_b32 v5, 64, v4\n"
"v_xor_b32 v6, 128, v4\n"
"v_xor_b32 v7, 192, v4\n"
"v_mov_b32 v10, 0x3f803f80\n"
"v_mov_b32 v11, 0x3f803f80\n"
"v_mov_b32 v12, 0x3f803f80\n"
"v_mov_b32 v13, 0x3f803f80\n"
"v_lshrrev_b32 v36, 5, v0\n"
"v_lshlrev_b32 v36, 11, v36\n"
"v_bfe_u32 v37, v0, 2, 3\n"
"v_lshl_add_u32 v36, v37, 6, v36\n"
"v_lshl_add_u32 v36, v3, 3, v36\n"
"v_and_b32 v38, 1, v2\n"
"v_lshl_add_u32 v36, v38, 5, v36\n"
"v_add_u32 v8, 65536, v36\n"
"v_xor_b32 v9, 32, v8\n"
"s_mov_b64 s[40:41], s[16:17]\n"
"s_lshl2_add_u32 s53, s49, s48\n"
"s_lshl_b32 s53, s53, 5\n"
"s_add_u32 s53, s53, s50\n"
"s_mul_i32 s53, s53, 20480\n"
"s_add_u32 s40, s40, s53\n"
"s_addc_u32 s41, s41, 0\n"
"s_and_b32 s41, s41, 0xffff\n"
"s_mov_b32 s42, 20480\n"
"s_mov_b32 s43, 0x20000\n"
"s_mov_b64 s[44:45], s[12:13]\n"
"s_and_b32 s53, s50, 7\n"
"s_lshl_b32 s53, s53, 7\n"
"s_andn2_b32 s52, 16, s50\n"
"s_lshl_b32 s52, s52, 17\n"
"s_add_u32 s53, s53, s52\n"
"s_add_u32 s44, s44, s53\n"
"s_addc_u32 s45, s45, 0\n"
"s_and_b32 s45, s45, 0xffff\n"
"s_mov_b32 s46, 0\n"
"s_mov_b32 s47, 0x20000\n"
"s_sub_u32 s46, 4194304, s53\n"
"s_lshr_b32 s69, s50, 3\n"
"s_add_u32 s69, s69, s49\n"
"s_and_b32 s69, s69, 1\n"
"s_lshr_b32 s52, s50, 4\n"
"s_lshl1_add_u32 s69, s69, s52\n"
"s_mov_b32 s64, 0x3e0293ee\n"
"s_mov_b32 s65, 0x3e0293ee\n"
"s_mov_b32 s68, 0\n"
"s_waitcnt vmcnt(12)\n"
"s_barrier\n"
"ds_read_b64_tr_b16 a[0:1], v26\n"
"ds_read_b64_tr_b16 a[2:3], v26 offset:1024\n"
"ds_read_b64_tr_b16 a[4:5], v26 offset:8224\n"
"ds_read_b64_tr_b16 a[6:7], v26 offset:9248\n"
"ds_read_b64_tr_b16 a[8:9], v26 offset:8\n"
"ds_read_b64_tr_b16 a[10:11], v26 offset:1032\n"
"ds_read_b64_tr_b16 a[12:13], v26 offset:8232\n"
"ds_read_b64_tr_b16 a[14:15], v26 offset:9256\n"
"ds_read_b64_tr_b16 a[16:17], v26 offset:256\n"
"ds_read_b64_tr_b16 a[18:19], v26 offset:1280\n"
"ds_read_b64_tr_b16 a[20:21], v26 offset:8480\n"
"ds_read_b64_tr_b16 a[22:23], v26 offset:9504\n"
"ds_read_b64_tr_b16 a[24:25], v26 offset:264\n"
"ds_read_b64_tr_b16 a[26:27], v26 offset:1288\n"
"ds_read_b64_tr_b16 a[28:29], v26 offset:8488\n"
"ds_read_b64_tr_b16 a[30:31], v26 offset:9512\n"
"ds_read_b64_tr_b16 a[32:33], v26 offset:512\n"
"ds_read_b64_tr_b16 a[34:35], v26 offset:1536\n"
"ds_read_b64_tr_b16 a[36:37], v26 offset:8736\n"
"ds_read_b64_tr_b16 a[38:39], v26 offset:9760\n"
"ds_read_b64_tr_b16 a[40:41], v26 offset:520\n"
"ds_read_b64_tr_b16 a[42:43], v26 offset:1544\n"
"ds_read_b64_tr_b16 a[44:45], v26 offset:8744\n"
"ds_read_b64_tr_b16 a[46:47], v26 offset:9768\n"
"ds_read_b64_tr_b16 a[48:49], v26 offset:768\n"
"ds_read_b64_tr_b16 a[50:51], v26 offset:1792\n"
"ds_read_b64_tr_b16 a[52:53], v26 offset:8992\n"
"ds_read_b64_tr_b16 a[54:55], v26 offset:10016\n"
"ds_read_b64_tr_b16 a[56:57], v26 offset:776\n"
"ds_read_b64_tr_b16 a[58:59], v26 offset:1800\n"
"ds_read_b64_tr_b16 a[60:61], v26 offset:9000\n"
"ds_read_b64_tr_b16 a[62:63], v26 offset:10024\n"
"s_waitcnt vmcnt(10)\n"
"ds_read_b128 a[160:163], v18\n"
"ds_read_b128 a[164:167], v19\n"
"s_waitcnt lgkmcnt(0)\n"
"v_mfma_f32_16x16x32_bf16 v[96:99], a[0:3], a[160:163], 0\n"
"s_add_u32 m0, s58, 147392\n"
"s_add_u32 s67, s57, 524288\n"
"v_mfma_f32_16x16x32_bf16 v[96:99], a[4:7], a[164:167], v[96:99]\n"
"buffer_load_dwordx4 v22, s[24:27], s67 offen lds\n"
"v_mfma_f32_16x16x32_bf16 v[100:103], a[8:11], a[160:163], 0\n"
"v_mfma_f32_16x16x32_bf16 v[100:103], a[12:15], a[164:167], v[100:103]\n"
"v_mfma_f32_16x16x32_bf16 v[104:107], a[16:19], a[160:163], 0\n"
"v_mfma_f32_16x16x32_bf16 v[104:107], a[20:23], a[164:167], v[104:107]\n"
"buffer_load_dwordx4 v23, s[24:27], s67 offen offset:1024 lds\n"
"v_mfma_f32_16x16x32_bf16 v[108:111], a[24:27], a[160:163], 0\n"
"v_mfma_f32_16x16x32_bf16 v[108:111], a[28:31], a[164:167], v[108:111]\n"
"v_mfma_f32_16x16x32_bf16 v[112:115], a[32:35], a[160:163], 0\n"
"s_waitcnt vmcnt(10)\n"
"v_mfma_f32_16x16x32_bf16 v[112:115], a[36:39], a[164:167], v[112:115]\n"
"ds_read_b128 a[168:171], v18 offset:2048\n"
"ds_read_b128 a[172:175], v19 offset:2048\n"
"v_mfma_f32_16x16x32_bf16 v[116:119], a[40:43], a[160:163], 0\n"
"buffer_load_dwordx4 v24, s[24:27], s67 offen offset:2056 lds\n"
"v_mfma_f32_16x16x32_bf16 v[116:119], a[44:47], a[164:167], v[116:119]\n"
"v_mfma_f32_16x16x32_bf16 v[120:123], a[48:51], a[160:163], 0\n"
"v_mfma_f32_16x16x32_bf16 v[120:123], a[52:55], a[164:167], v[120:123]\n"
"v_mfma_f32_16x16x32_bf16 v[124:127], a[56:59], a[160:163], 0\n"
"buffer_load_dwordx4 v25, s[24:27], s67 offen offset:3080 lds\n"
"v_mfma_f32_16x16x32_bf16 v[124:127], a[60:63], a[164:167], v[124:127]\n"
"s_waitcnt lgkmcnt(0)\n"
"v_mfma_f32_16x16x32_bf16 v[128:131], a[0:3], a[168:171], 0\n"
"v_mfma_f32_16x16x32_bf16 v[128:131], a[4:7], a[172:175], v[128:131]\n"
"s_add_u32 m0, s60, 0\n"
"s_add_u32 s66, s56, 128\n"
"v_mfma_f32_16x16x32_bf16 v[132:135], a[8:11], a[168:171], 0\n"
"buffer_load_dwordx4 v16, s[20:23], s66 offen lds\n"
"v_mfma_f32_16x16x32_bf16 v[132:135], a[12:15], a[172:175], v[132:135]\n"
"v_mfma_f32_16x16x32_bf16 v[136:139], a[16:19], a[168:171], 0\n"
"v_mfma_f32_16x16x32_bf16 v[136:139], a[20:23], a[172:175], v[136:139]\n"
"v_mfma_f32_16x16x32_bf16 v[140:143], a[24:27], a[168:171], 0\n"
"buffer_load_dwordx4 v17, s[20:23], s66 offen offset:1024 lds\n"
"v_mfma_f32_16x16x32_bf16 v[140:143], a[28:31], a[172:175], v[140:143]\n"
"v_mfma_f32_16x16x32_bf16 v[144:147], a[32:35], a[168:171], 0\n"
"s_waitcnt vmcnt(10)\n"
"v_mfma_f32_16x16x32_bf16 v[144:147], a[36:39], a[172:175], v[144:147]\n"
"ds_read_b128 a[176:179], v18 offset:4096\n"
"ds_read_b128 a[180:183], v19 offset:4096\n"
"v_mfma_f32_16x16x32_bf16 v[148:151], a[40:43], a[168:171], 0\n"
"s_add_u32 m0, s60, 2048\n"
"s_add_u32 s66, s56, 16512\n"
"v_mfma_f32_16x16x32_bf16 v[148:151], a[44:47], a[172:175], v[148:151]\n"
"buffer_load_dwordx4 v16, s[20:23], s66 offen lds\n"
"v_mfma_f32_16x16x32_bf16 v[152:155], a[48:51], a[168:171], 0\n"
"v_mfma_f32_16x16x32_bf16 v[152:155], a[52:55], a[172:175], v[152:155]\n"
"v_mfma_f32_16x16x32_bf16 v[156:159], a[56:59], a[168:171], 0\n"
"v_mfma_f32_16x16x32_bf16 v[156:159], a[60:63], a[172:175], v[156:159]\n"
"s_waitcnt lgkmcnt(0)\n"
"v_mfma_f32_16x16x32_bf16 v[160:163], a[0:3], a[176:179], 0\n"
"buffer_load_dwordx4 v17, s[20:23], s66 offen offset:1024 lds\n"
"v_mfma_f32_16x16x32_bf16 v[160:163], a[4:7], a[180:183], v[160:163]\n"
"v_mfma_f32_16x16x32_bf16 v[164:167], a[8:11], a[176:179], 0\n"
"v_mfma_f32_16x16x32_bf16 v[164:167], a[12:15], a[180:183], v[164:167]\n"
"s_add_u32 m0, s60, 4096\n"
"s_add_u32 s66, s56, 32896\n"
"v_mfma_f32_16x16x32_bf16 v[168:171], a[16:19], a[176:179], 0\n"
"buffer_load_dwordx4 v16, s[20:23], s66 offen lds\n"
"v_mfma_f32_16x16x32_bf16 v[168:171], a[20:23], a[180:183], v[168:171]\n"
"v_mfma_f32_16x16x32_bf16 v[172:175], a[24:27], a[176:179], 0\n"
"v_mfma_f32_16x16x32_bf16 v[172:175], a[28:31], a[180:183], v[172:175]\n"
"v_mfma_f32_16x16x32_bf16 v[176:179], a[32:35], a[176:179], 0\n"
"s_waitcnt vmcnt(11)\n"
"v_mfma_f32_16x16x32_bf16 v[176:179], a[36:39], a[180:183], v[176:179]\n"
"ds_read_b128 a[184:187], v18 offset:6144\n"
"ds_read_b128 a[188:191], v19 offset:6144\n"
"v_mfma_f32_16x16x32_bf16 v[180:183], a[40:43], a[176:179], 0\n"
"buffer_load_dwordx4 v17, s[20:23], s66 offen offset:1024 lds\n"
"v_mfma_f32_16x16x32_bf16 v[180:183], a[44:47], a[180:183], v[180:183]\n"
"v_mfma_f32_16x16x32_bf16 v[184:187], a[48:51], a[176:179], 0\n"
"v_mfma_f32_16x16x32_bf16 v[184:187], a[52:55], a[180:183], v[184:187]\n"
"s_add_u32 m0, s60, 6144\n"
"s_add_u32 s66, s56, 49280\n"
"v_mfma_f32_16x16x32_bf16 v[188:191], a[56:59], a[176:179], 0\n"
"buffer_load_dwordx4 v16, s[20:23], s66 offen lds\n"
"v_mfma_f32_16x16x32_bf16 v[188:191], a[60:63], a[180:183], v[188:191]\n"
"s_waitcnt vmcnt(7) lgkmcnt(0)\n"
"s_barrier\n"
"v_mfma_f32_16x16x32_bf16 v[192:195], a[0:3], a[184:187], 0\n"
"ds_read_b64_tr_b16 a[192:193], v27\n"
"ds_read_b64_tr_b16 a[194:195], v27 offset:1024\n"
"v_mfma_f32_16x16x32_bf16 v[192:195], a[4:7], a[188:191], v[192:195]\n"
"ds_read_b64_tr_b16 a[196:197], v27 offset:8224\n"
"v_mfma_f32_16x16x32_bf16 v[196:199], a[8:11], a[184:187], 0\n"
"ds_read_b64_tr_b16 a[198:199], v27 offset:9248\n"
"buffer_load_dwordx4 v17, s[20:23], s66 offen offset:1024 lds\n"
"v_mfma_f32_16x16x32_bf16 v[196:199], a[12:15], a[188:191], v[196:199]\n"
"ds_read_b64_tr_b16 a[200:201], v27 offset:8\n"
"v_mfma_f32_16x16x32_bf16 v[200:203], a[16:19], a[184:187], 0\n"
"ds_read_b64_tr_b16 a[202:203], v27 offset:1032\n"
"v_mfma_f32_16x16x32_bf16 v[200:203], a[20:23], a[188:191], v[200:203]\n"
"ds_read_b64_tr_b16 a[204:205], v27 offset:8232\n"
"ds_read_b64_tr_b16 a[206:207], v27 offset:9256\n"
"s_add_u32 m0, s60, 8192\n"
"s_add_u32 s66, s56, 65664\n"
"v_mfma_f32_16x16x32_bf16 v[204:207], a[24:27], a[184:187], 0\n"
"ds_read_b64_tr_b16 a[208:209], v27 offset:256\n"
"buffer_load_dwordx4 v16, s[20:23], s66 offen lds\n"
"v_mfma_f32_16x16x32_bf16 v[204:207], a[28:31], a[188:191], v[204:207]\n"
"ds_read_b64_tr_b16 a[210:211], v27 offset:1280\n"
"v_mfma_f32_16x16x32_bf16 v[208:211], a[32:35], a[184:187], 0\n"
"ds_read_b64_tr_b16 a[212:213], v27 offset:8480\n"
"v_mfma_f32_16x16x32_bf16 v[208:211], a[36:39], a[188:191], v[208:211]\n"
"ds_read_b64_tr_b16 a[214:215], v27 offset:9504\n"
"ds_read_b128 a[160:163], v18 offset:8192\n"
"ds_read_b128 a[164:167], v19 offset:8192\n"
"v_mfma_f32_16x16x32_bf16 v[212:215], a[40:43], a[184:187], 0\n"
"ds_read_b64_tr_b16 a[216:217], v27 offset:264\n"
"ds_read_b64_tr_b16 a[218:219], v27 offset:1288\n"
"buffer_load_dwordx4 v17, s[20:23], s66 offen offset:1024 lds\n"
"v_mfma_f32_16x16x32_bf16 v[212:215], a[44:47], a[188:191], v[212:215]\n"
"ds_read_b64_tr_b16 a[220:221], v27 offset:8488\n"
"v_mfma_f32_16x16x32_bf16 v[216:219], a[48:51], a[184:187], 0\n"
"ds_read_b64_tr_b16 a[222:223], v27 offset:9512\n"
"v_mfma_f32_16x16x32_bf16 v[216:219], a[52:55], a[188:191], v[216:219]\n"
"ds_read_b64_tr_b16 a[224:225], v27 offset:512\n"
"s_add_u32 m0, s58, 81920\n"
"s_add_u32 s67, s57, 1048576\n"
"v_mfma_f32_16x16x32_bf16 v[220:223], a[56:59], a[184:187], 0\n"
"ds_read_b64_tr_b16 a[226:227], v27 offset:1536\n"
"buffer_load_dwordx4 v22, s[24:27], s67 offen lds\n"
"v_mfma_f32_16x16x32_bf16 v[220:223], a[60:63], a[188:191], v[220:223]\n"
"s_waitcnt lgkmcnt(6)\n"
"v_mfma_f32_16x16x32_bf16 v[224:227], a[0:3], a[160:163], 0\n"
"ds_read_b64_tr_b16 a[228:229], v27 offset:8736\n"
"ds_read_b64_tr_b16 a[230:231], v27 offset:9760\n"
"v_mfma_f32_16x16x32_bf16 v[224:227], a[4:7], a[164:167], v[224:227]\n"
"ds_read_b64_tr_b16 a[232:233], v27 offset:520\n"
"v_mfma_f32_16x16x32_bf16 v[228:231], a[8:11], a[160:163], 0\n"
"ds_read_b64_tr_b16 a[234:235], v27 offset:1544\n"
"buffer_load_dwordx4 v23, s[24:27], s67 offen offset:1024 lds\n"
"v_mfma_f32_16x16x32_bf16 v[228:231], a[12:15], a[164:167], v[228:231]\n"
"ds_read_b64_tr_b16 a[236:237], v27 offset:8744\n"
"ds_read_b64_tr_b16 a[238:239], v27 offset:9768\n"
"v_mfma_f32_16x16x32_bf16 v[232:235], a[16:19], a[160:163], 0\n"
"ds_read_b64_tr_b16 a[240:241], v27 offset:768\n"
"v_mfma_f32_16x16x32_bf16 v[232:235], a[20:23], a[164:167], v[232:235]\n"
"ds_read_b64_tr_b16 a[242:243], v27 offset:1792\n"
"v_mfma_f32_16x16x32_bf16 v[236:239], a[24:27], a[160:163], 0\n"
"ds_read_b64_tr_b16 a[244:245], v27 offset:8992\n"
"buffer_load_dwordx4 v24, s[24:27], s67 offen offset:2056 lds\n"
"v_mfma_f32_16x16x32_bf16 v[236:239], a[28:31], a[164:167], v[236:239]\n"
"ds_read_b64_tr_b16 a[246:247], v27 offset:10016\n"
"ds_read_b64_tr_b16 a[248:249], v27 offset:776\n"
"v_mfma_f32_16x16x32_bf16 v[240:243], a[32:35], a[160:163], 0\n"
"ds_read_b64_tr_b16 a[250:251], v27 offset:1800\n"
"s_waitcnt vmcnt(11)\n"
"v_mfma_f32_16x16x32_bf16 v[240:243], a[36:39], a[164:167], v[240:243]\n"
"ds_read_b64_tr_b16 a[252:253], v27 offset:9000\n"
"ds_read_b128 a[168:171], v20\n"
"ds_read_b128 a[172:175], v21\n"
"v_mfma_f32_16x16x32_bf16 v[244:247], a[40:43], a[160:163], 0\n"
"ds_read_b64_tr_b16 a[254:255], v27 offset:10024\n"
"buffer_load_dwordx4 v25, s[24:27], s67 offen offset:3080 lds\n"
"v_mfma_f32_16x16x32_bf16 v[244:247], a[44:47], a[164:167], v[244:247]\n"
"v_mfma_f32_16x16x32_bf16 v[248:251], a[48:51], a[160:163], 0\n"
"v_mfma_f32_16x16x32_bf16 v[248:251], a[52:55], a[164:167], v[248:251]\n"
"s_add_u32 m0, s59, 0\n"
"s_add_u32 s66, s56, 256\n"
"v_mfma_f32_16x16x32_bf16 v[252:255], a[56:59], a[160:163], 0\n"
"buffer_load_dwordx4 v16, s[20:23], s66 offen lds\n"
"v_mfma_f32_16x16x32_bf16 v[252:255], a[60:63], a[164:167], v[252:255]\n"
"s_waitcnt lgkmcnt(0)\n"
"v_mfma_f32_16x16x32_bf16 v[96:99], a[192:195], a[168:171], v[96:99]\n"
"v_mfma_f32_16x16x32_bf16 v[96:99], a[196:199], a[172:175], v[96:99]\n"
"v_mfma_f32_16x16x32_bf16 v[100:103], a[200:203], a[168:171], v[100:103]\n"
"buffer_load_dwordx4 v17, s[20:23], s66 offen offset:1024 lds\n"
"v_mfma_f32_16x16x32_bf16 v[100:103], a[204:207], a[172:175], v[100:103]\n"
"v_mfma_f32_16x16x32_bf16 v[104:107], a[208:211], a[168:171], v[104:107]\n"
"v_mfma_f32_16x16x32_bf16 v[104:107], a[212:215], a[172:175], v[104:107]\n"
"s_add_u32 m0, s59, 2048\n"
"s_add_u32 s66, s56, 16640\n"
"v_mfma_f32_16x16x32_bf16 v[108:111], a[216:219], a[168:171], v[108:111]\n"
"buffer_load_dwordx4 v16, s[20:23], s66 offen lds\n"
"v_mfma_f32_16x16x32_bf16 v[108:111], a[220:223], a[172:175], v[108:111]\n"
"v_mfma_f32_16x16x32_bf16 v[112:115], a[224:227], a[168:171], v[112:115]\n"
"s_waitcnt vmcnt(13)\n"
"v_mfma_f32_16x16x32_bf16 v[112:115], a[228:231], a[172:175], v[112:115]\n"
"ds_read_b128 a[176:179], v20 offset:2048\n"
"ds_read_b128 a[180:183], v21 offset:2048\n"
"v_mfma_f32_16x16x32_bf16 v[116:119], a[232:235], a[168:171], v[116:119]\n"
"buffer_load_dwordx4 v17, s[20:23], s66 offen offset:1024 lds\n"
"v_mfma_f32_16x16x32_bf16 v[116:119], a[236:239], a[172:175], v[116:119]\n"
"v_mfma_f32_16x16x32_bf16 v[120:123], a[240:243], a[168:171], v[120:123]\n"
"v_mfma_f32_16x16x32_bf16 v[120:123], a[244:247], a[172:175], v[120:123]\n"
"s_add_u32 m0, s59, 4096\n"
"s_add_u32 s66, s56, 33024\n"
"v_mfma_f32_16x16x32_bf16 v[124:127], a[248:251], a[168:171], v[124:127]\n"
"buffer_load_dwordx4 v16, s[20:23], s66 offen lds\n"
"v_mfma_f32_16x16x32_bf16 v[124:127], a[252:255], a[172:175], v[124:127]\n"
"s_waitcnt lgkmcnt(0)\n"
"v_mfma_f32_16x16x32_bf16 v[128:131], a[192:195], a[176:179], v[128:131]\n"
"v_mfma_f32_16x16x32_bf16 v[128:131], a[196:199], a[180:183], v[128:131]\n"
"v_mfma_f32_16x16x32_bf16 v[132:135], a[200:203], a[176:179], v[132:135]\n"
"buffer_load_dwordx4 v17, s[20:23], s66 offen offset:1024 lds\n"
"v_mfma_f32_16x16x32_bf16 v[132:135], a[204:207], a[180:183], v[132:135]\n"
"v_mfma_f32_16x16x32_bf16 v[136:139], a[208:211], a[176:179], v[136:139]\n"
"v_mfma_f32_16x16x32_bf16 v[136:139], a[212:215], a[180:183], v[136:139]\n"
"s_add_u32 m0, s58, 16384\n"
"s_add_u32 s67, s57, 1572864\n"
"v_mfma_f32_16x16x32_bf16 v[140:143], a[216:219], a[176:179], v[140:143]\n"
"buffer_load_dwordx4 v22, s[24:27], s67 offen lds\n"
"v_mfma_f32_16x16x32_bf16 v[140:143], a[220:223], a[180:183], v[140:143]\n"
"v_mfma_f32_16x16x32_bf16 v[144:147], a[224:227], a[176:179], v[144:147]\n"
"s_waitcnt vmcnt(15)\n"
"v_mfma_f32_16x16x32_bf16 v[144:147], a[228:231], a[180:183], v[144:147]\n"
"ds_read_b128 a[184:187], v20 offset:4096\n"
"ds_read_b128 a[188:191], v21 offset:4096\n"
"v_mfma_f32_16x16x32_bf16 v[148:151], a[232:235], a[176:179], v[148:151]\n"
"buffer_load_dwordx4 v23, s[24:27], s67 offen offset:1024 lds\n"
"v_mfma_f32_16x16x32_bf16 v[148:151], a[236:239], a[180:183], v[148:151]\n"
"v_mfma_f32_16x16x32_bf16 v[152:155], a[240:243], a[176:179], v[152:155]\n"
"v_mfma_f32_16x16x32_bf16 v[152:155], a[244:247], a[180:183], v[152:155]\n"
"v_mfma_f32_16x16x32_bf16 v[156:159], a[248:251], a[176:179], v[156:159]\n"
"buffer_load_dwordx4 v24, s[24:27], s67 offen offset:2056 lds\n"
"v_mfma_f32_16x16x32_bf16 v[156:159], a[252:255], a[180:183], v[156:159]\n"
"s_waitcnt vmcnt(9) lgkmcnt(0)\n"
"s_barrier\n"
"v_mfma_f32_16x16x32_bf16 v[160:163], a[192:195], a[184:187], v[160:163]\n"
"ds_read_b64_tr_b16 a[64:65], v28\n"
"ds_read_b64_tr_b16 a[66:67], v28 offset:1024\n"
"v_mfma_f32_16x16x32_bf16 v[160:163], a[196:199], a[188:191], v[160:163]\n"
"ds_read_b64_tr_b16 a[68:69], v28 offset:8224\n"
"ds_read_b64_tr_b16 a[70:71], v28 offset:9248\n"
"v_mfma_f32_16x16x32_bf16 v[164:167], a[200:203], a[184:187], v[164:167]\n"
"buffer_load_dwordx4 v25, s[24:27], s67 offen offset:3080 lds\n"
"v_mfma_f32_16x16x32_bf16 v[164:167], a[204:207], a[188:191], v[164:167]\n"
"ds_read_b64_tr_b16 a[72:73], v28 offset:8\n"
"ds_read_b64_tr_b16 a[74:75], v28 offset:1032\n"
"v_mfma_f32_16x16x32_bf16 v[168:171], a[208:211], a[184:187], v[168:171]\n"
"ds_read_b64_tr_b16 a[76:77], v28 offset:8232\n"
"ds_read_b64_tr_b16 a[78:79], v28 offset:9256\n"
"v_mfma_f32_16x16x32_bf16 v[168:171], a[212:215], a[188:191], v[168:171]\n"
"s_add_u32 m0, s59, 6144\n"
"s_add_u32 s66, s56, 49408\n"
"v_mfma_f32_16x16x32_bf16 v[172:175], a[216:219], a[184:187], v[172:175]\n"
"buffer_load_dwordx4 v16, s[20:23], s66 offen lds\n"
"v_mfma_f32_16x16x32_bf16 v[172:175], a[220:223], a[188:191], v[172:175]\n"
"ds_read_b64_tr_b16 a[80:81], v28 offset:256\n"
"ds_read_b64_tr_b16 a[82:83], v28 offset:1280\n"
"v_mfma_f32_16x16x32_bf16 v[176:179], a[224:227], a[184:187], v[176:179]\n"
"v_mfma_f32_16x16x32_bf16 v[176:179], a[228:231], a[188:191], v[176:179]\n"
"ds_read_b128 a[160:163], v20 offset:6144\n"
"ds_read_b128 a[164:167], v21 offset:6144\n"
"v_mfma_f32_16x16x32_bf16 v[180:183], a[232:235], a[184:187], v[180:183]\n"
"buffer_load_dwordx4 v17, s[20:23], s66 offen offset:1024 lds\n"
"v_mfma_f32_16x16x32_bf16 v[180:183], a[236:239], a[188:191], v[180:183]\n"
"ds_read_b64_tr_b16 a[84:85], v28 offset:8480\n"
"ds_read_b64_tr_b16 a[86:87], v28 offset:9504\n"
"v_mfma_f32_16x16x32_bf16 v[184:187], a[240:243], a[184:187], v[184:187]\n"
"ds_read_b64_tr_b16 a[88:89], v28 offset:264\n"
"v_mfma_f32_16x16x32_bf16 v[184:187], a[244:247], a[188:191], v[184:187]\n"
"s_add_u32 m0, s59, 8192\n"
"s_add_u32 s66, s56, 65792\n"
"v_mfma_f32_16x16x32_bf16 v[188:191], a[248:251], a[184:187], v[188:191]\n"
"buffer_load_dwordx4 v16, s[20:23], s66 offen lds\n"
"v_mfma_f32_16x16x32_bf16 v[188:191], a[252:255], a[188:191], v[188:191]\n"
"s_waitcnt lgkmcnt(3)\n"
"v_mfma_f32_16x16x32_bf16 v[192:195], a[192:195], a[160:163], v[192:195]\n"
"ds_read_b64_tr_b16 a[90:91], v28 offset:1288\n"
"ds_read_b64_tr_b16 a[92:93], v28 offset:8488\n"
"v_mfma_f32_16x16x32_bf16 v[192:195], a[196:199], a[164:167], v[192:195]\n"
"ds_read_b64_tr_b16 a[94:95], v28 offset:9512\n"
"ds_read_b64_tr_b16 a[96:97], v28 offset:512\n"
"v_mfma_f32_16x16x32_bf16 v[196:199], a[200:203], a[160:163], v[196:199]\n"
"buffer_load_dwordx4 v17, s[20:23], s66 offen offset:1024 lds\n"
"v_mfma_f32_16x16x32_bf16 v[196:199], a[204:207], a[164:167], v[196:199]\n"
"ds_read_b64_tr_b16 a[98:99], v28 offset:1536\n"
"ds_read_b64_tr_b16 a[100:101], v28 offset:8736\n"
"v_mfma_f32_16x16x32_bf16 v[200:203], a[208:211], a[160:163], v[200:203]\n"
"ds_read_b64_tr_b16 a[102:103], v28 offset:9760\n"
"v_mfma_f32_16x16x32_bf16 v[200:203], a[212:215], a[164:167], v[200:203]\n"
"s_add_u32 m0, s60, 0\n"
"s_add_u32 s66, s56, 384\n"
"v_mfma_f32_16x16x32_bf16 v[204:207], a[216:219], a[160:163], v[204:207]\n"
"buffer_load_dwordx4 v16, s[20:23], s66 offen lds\n"
"v_mfma_f32_16x16x32_bf16 v[204:207], a[220:223], a[164:167], v[204:207]\n"
"ds_read_b64_tr_b16 a[104:105], v28 offset:520\n"
"ds_read_b64_tr_b16 a[106:107], v28 offset:1544\n"
"v_mfma_f32_16x16x32_bf16 v[208:211], a[224:227], a[160:163], v[208:211]\n"
"v_mfma_f32_16x16x32_bf16 v[208:211], a[228:231], a[164:167], v[208:211]\n"
"ds_read_b128 a[168:171], v20 offset:8192\n"
"ds_read_b128 a[172:175], v21 offset:8192\n"
"v_mfma_f32_16x16x32_bf16 v[212:215], a[232:235], a[160:163], v[212:215]\n"
"buffer_load_dwordx4 v17, s[20:23], s66 offen offset:1024 lds\n"
"v_mfma_f32_16x16x32_bf16 v[212:215], a[236:239], a[164:167], v[212:215]\n"
"ds_read_b64_tr_b16 a[108:109], v28 offset:8744\n"
"ds_read_b64_tr_b16 a[110:111], v28 offset:9768\n"
"v_mfma_f32_16x16x32_bf16 v[216:219], a[240:243], a[160:163], v[216:219]\n"
"ds_read_b64_tr_b16 a[112:113], v28 offset:768\n"
"v_mfma_f32_16x16x32_bf16 v[216:219], a[244:247], a[164:167], v[216:219]\n"
"s_add_u32 m0, s60, 2048\n"
"s_add_u32 s66, s56, 16768\n"
"v_mfma_f32_16x16x32_bf16 v[220:223], a[248:251], a[160:163], v[220:223]\n"
"buffer_load_dwordx4 v16, s[20:23], s66 offen lds\n"
"v_mfma_f32_16x16x32_bf16 v[220:223], a[252:255], a[164:167], v[220:223]\n"
"s_waitcnt lgkmcnt(3)\n"
"v_mfma_f32_16x16x32_bf16 v[224:227], a[192:195], a[168:171], v[224:227]\n"
"ds_read_b64_tr_b16 a[114:115], v28 offset:1792\n"
"ds_read_b64_tr_b16 a[116:117], v28 offset:8992\n"
"v_mfma_f32_16x16x32_bf16 v[224:227], a[196:199], a[172:175], v[224:227]\n"
"ds_read_b64_tr_b16 a[118:119], v28 offset:10016\n"
"v_mfma_f32_16x16x32_bf16 v[228:231], a[200:203], a[168:171], v[228:231]\n"
"buffer_load_dwordx4 v17, s[20:23], s66 offen offset:1024 lds\n"
"v_mfma_f32_16x16x32_bf16 v[228:231], a[204:207], a[172:175], v[228:231]\n"
"ds_read_b64_tr_b16 a[120:121], v28 offset:776\n"
"ds_read_b64_tr_b16 a[122:123], v28 offset:1800\n"
"v_mfma_f32_16x16x32_bf16 v[232:235], a[208:211], a[168:171], v[232:235]\n"
"ds_read_b64_tr_b16 a[124:125], v28 offset:9000\n"
"v_mfma_f32_16x16x32_bf16 v[232:235], a[212:215], a[172:175], v[232:235]\n"
"s_add_u32 m0, s58, 147392\n"
"s_add_u32 s67, s57, 2097152\n"
"v_mfma_f32_16x16x32_bf16 v[236:239], a[216:219], a[168:171], v[236:239]\n"
"buffer_load_dwordx4 v22, s[24:27], s67 offen lds\n"
"v_mfma_f32_16x16x32_bf16 v[236:239], a[220:223], a[172:175], v[236:239]\n"
"ds_read_b64_tr_b16 a[126:127], v28 offset:10024\n"
"v_mfma_f32_16x16x32_bf16 v[240:243], a[224:227], a[168:171], v[240:243]\n"
"s_waitcnt vmcnt(17)\n"
"v_mfma_f32_16x16x32_bf16 v[240:243], a[228:231], a[172:175], v[240:243]\n"
"ds_read_b128 a[176:179], v18\n"
"ds_read_b128 a[180:183], v19\n"
"v_mfma_f32_16x16x32_bf16 v[244:247], a[232:235], a[168:171], v[244:247]\n"
"buffer_load_dwordx4 v23, s[24:27], s67 offen offset:1024 lds\n"
"v_mfma_f32_16x16x32_bf16 v[244:247], a[236:239], a[172:175], v[244:247]\n"
"v_mfma_f32_16x16x32_bf16 v[248:251], a[240:243], a[168:171], v[248:251]\n"
"v_mfma_f32_16x16x32_bf16 v[248:251], a[244:247], a[172:175], v[248:251]\n"
"v_mfma_f32_16x16x32_bf16 v[252:255], a[248:251], a[168:171], v[252:255]\n"
"buffer_load_dwordx4 v24, s[24:27], s67 offen offset:2056 lds\n"
"v_mfma_f32_16x16x32_bf16 v[252:255], a[252:255], a[172:175], v[252:255]\n"
"s_waitcnt vmcnt(17) lgkmcnt(0)\n"
"v_mfma_f32_16x16x32_bf16 v[96:99], a[64:67], a[176:179], v[96:99]\n"
"ds_read_b128 a[184:187], v18 offset:2048\n"
"ds_read_b128 a[188:191], v19 offset:2048\n"
"v_mfma_f32_16x16x32_bf16 v[96:99], a[68:71], a[180:183], v[96:99]\n"
"v_mfma_f32_16x16x32_bf16 v[100:103], a[72:75], a[176:179], v[100:103]\n"
"buffer_load_dwordx4 v25, s[24:27], s67 offen offset:3080 lds\n"
"v_mfma_f32_16x16x32_bf16 v[100:103], a[76:79], a[180:183], v[100:103]\n"
"v_mfma_f32_16x16x32_bf16 v[104:107], a[80:83], a[176:179], v[104:107]\n"
"v_mfma_f32_16x16x32_bf16 v[104:107], a[84:87], a[180:183], v[104:107]\n"
"s_add_u32 m0, s60, 4096\n"
"s_add_u32 s66, s56, 33152\n"
"v_mfma_f32_16x16x32_bf16 v[108:111], a[88:91], a[176:179], v[108:111]\n"
"buffer_load_dwordx4 v16, s[20:23], s66 offen lds\n"
"v_mfma_f32_16x16x32_bf16 v[108:111], a[92:95], a[180:183], v[108:111]\n"
"v_mfma_f32_16x16x32_bf16 v[112:115], a[96:99], a[176:179], v[112:115]\n"
"v_mfma_f32_16x16x32_bf16 v[112:115], a[100:103], a[180:183], v[112:115]\n"
"v_mfma_f32_16x16x32_bf16 v[116:119], a[104:107], a[176:179], v[116:119]\n"
"buffer_load_dwordx4 v17, s[20:23], s66 offen offset:1024 lds\n"
"v_mfma_f32_16x16x32_bf16 v[116:119], a[108:111], a[180:183], v[116:119]\n"
"v_mfma_f32_16x16x32_bf16 v[120:123], a[112:115], a[176:179], v[120:123]\n"
"v_mfma_f32_16x16x32_bf16 v[120:123], a[116:119], a[180:183], v[120:123]\n"
"s_add_u32 m0, s60, 6144\n"
"s_add_u32 s66, s56, 49536\n"
"v_mfma_f32_16x16x32_bf16 v[124:127], a[120:123], a[176:179], v[124:127]\n"
"buffer_load_dwordx4 v16, s[20:23], s66 offen lds\n"
"v_mfma_f32_16x16x32_bf16 v[124:127], a[124:127], a[180:183], v[124:127]\n"
"s_waitcnt vmcnt(19) lgkmcnt(0)\n"
"v_mfma_f32_16x16x32_bf16 v[128:131], a[64:67], a[184:187], v[128:131]\n"
"ds_read_b128 a[160:163], v18 offset:4096\n"
"ds_read_b128 a[164:167], v19 offset:4096\n"
"v_mfma_f32_16x16x32_bf16 v[128:131], a[68:71], a[188:191], v[128:131]\n"
"v_mfma_f32_16x16x32_bf16 v[132:135], a[72:75], a[184:187], v[132:135]\n"
"buffer_load_dwordx4 v17, s[20:23], s66 offen offset:1024 lds\n"
"v_mfma_f32_16x16x32_bf16 v[132:135], a[76:79], a[188:191], v[132:135]\n"
"v_mfma_f32_16x16x32_bf16 v[136:139], a[80:83], a[184:187], v[136:139]\n"
"v_mfma_f32_16x16x32_bf16 v[136:139], a[84:87], a[188:191], v[136:139]\n"
"s_add_u32 m0, s60, 8192\n"
"s_add_u32 s66, s56, 65920\n"
"v_mfma_f32_16x16x32_bf16 v[140:143], a[88:91], a[184:187], v[140:143]\n"
"buffer_load_dwordx4 v16, s[20:23], s66 offen lds\n"
"v_mfma_f32_16x16x32_bf16 v[140:143], a[92:95], a[188:191], v[140:143]\n"
"v_mfma_f32_16x16x32_bf16 v[144:147], a[96:99], a[184:187], v[144:147]\n"
"v_mfma_f32_16x16x32_bf16 v[144:147], a[100:103], a[188:191], v[144:147]\n"
"v_mfma_f32_16x16x32_bf16 v[148:151], a[104:107], a[184:187], v[148:151]\n"
"buffer_load_dwordx4 v17, s[20:23], s66 offen offset:1024 lds\n"
"v_mfma_f32_16x16x32_bf16 v[148:151], a[108:111], a[188:191], v[148:151]\n"
"v_mfma_f32_16x16x32_bf16 v[152:155], a[112:115], a[184:187], v[152:155]\n"
"v_mfma_f32_16x16x32_bf16 v[152:155], a[116:119], a[188:191], v[152:155]\n"
"s_add_u32 m0, s59, 0\n"
"s_add_u32 s66, s56, 512\n"
"v_mfma_f32_16x16x32_bf16 v[156:159], a[120:123], a[184:187], v[156:159]\n"
"buffer_load_dwordx4 v16, s[20:23], s66 offen lds\n"
"v_mfma_f32_16x16x32_bf16 v[156:159], a[124:127], a[188:191], v[156:159]\n"
"s_waitcnt vmcnt(17) lgkmcnt(0)\n"
"s_barrier\n"
"v_mfma_f32_16x16x32_bf16 v[160:163], a[64:67], a[160:163], v[160:163]\n"
"ds_read_b128 a[168:171], v18 offset:6144\n"
"ds_read_b128 a[172:175], v19 offset:6144\n"
"v_mfma_f32_16x16x32_bf16 v[160:163], a[68:71], a[164:167], v[160:163]\n"
"ds_read_b64_tr_b16 a[0:1], v26\n"
"ds_read_b64_tr_b16 a[2:3], v26 offset:1024\n"
"v_mfma_f32_16x16x32_bf16 v[164:167], a[72:75], a[160:163], v[164:167]\n"
"buffer_load_dwordx4 v17, s[20:23], s66 offen offset:1024 lds\n"
"v_mfma_f32_16x16x32_bf16 v[164:167], a[76:79], a[164:167], v[164:167]\n"
"ds_read_b64_tr_b16 a[4:5], v26 offset:8224\n"
"ds_read_b64_tr_b16 a[6:7], v26 offset:9248\n"
"v_mfma_f32_16x16x32_bf16 v[168:171], a[80:83], a[160:163], v[168:171]\n"
"ds_read_b64_tr_b16 a[8:9], v26 offset:8\n"
"v_mfma_f32_16x16x32_bf16 v[168:171], a[84:87], a[164:167], v[168:171]\n"
"s_add_u32 m0, s59, 2048\n"
"s_add_u32 s66, s56, 16896\n"
"v_mfma_f32_16x16x32_bf16 v[172:175], a[88:91], a[160:163], v[172:175]\n"
"buffer_load_dwordx4 v16, s[20:23], s66 offen lds\n"
"v_mfma_f32_16x16x32_bf16 v[172:175], a[92:95], a[164:167], v[172:175]\n"
"ds_read_b64_tr_b16 a[10:11], v26 offset:1032\n"
"ds_read_b64_tr_b16 a[12:13], v26 offset:8232\n"
"v_mfma_f32_16x16x32_bf16 v[176:179], a[96:99], a[160:163], v[176:179]\n"
"ds_read_b64_tr_b16 a[14:15], v26 offset:9256\n"
"ds_read_b64_tr_b16 a[16:17], v26 offset:256\n"
"v_mfma_f32_16x16x32_bf16 v[176:179], a[100:103], a[164:167], v[176:179]\n"
"ds_read_b64_tr_b16 a[18:19], v26 offset:1280\n"
"v_mfma_f32_16x16x32_bf16 v[180:183], a[104:107], a[160:163], v[180:183]\n"
"buffer_load_dwordx4 v17, s[20:23], s66 offen offset:1024 lds\n"
"v_mfma_f32_16x16x32_bf16 v[180:183], a[108:111], a[164:167], v[180:183]\n"
"ds_read_b64_tr_b16 a[20:21], v26 offset:8480\n"
"ds_read_b64_tr_b16 a[22:23], v26 offset:9504\n"
"v_mfma_f32_16x16x32_bf16 v[184:187], a[112:115], a[160:163], v[184:187]\n"
"ds_read_b64_tr_b16 a[24:25], v26 offset:264\n"
"v_mfma_f32_16x16x32_bf16 v[184:187], a[116:119], a[164:167], v[184:187]\n"
"s_add_u32 m0, s58, 81920\n"
"s_add_u32 s67, s57, 2621440\n"
"v_mfma_f32_16x16x32_bf16 v[188:191], a[120:123], a[160:163], v[188:191]\n"
"buffer_load_dwordx4 v22, s[24:27], s67 offen lds\n"
"v_mfma_f32_16x16x32_bf16 v[188:191], a[124:127], a[164:167], v[188:191]\n"
"s_waitcnt vmcnt(19) lgkmcnt(13)\n"
"v_mfma_f32_16x16x32_bf16 v[192:195], a[64:67], a[168:171], v[192:195]\n"
"ds_read_b128 a[176:179], v18 offset:8192\n"
"ds_read_b128 a[180:183], v19 offset:8192\n"
"v_mfma_f32_16x16x32_bf16 v[192:195], a[68:71], a[172:175], v[192:195]\n"
"ds_read_b64_tr_b16 a[26:27], v26 offset:1288\n"
"ds_read_b64_tr_b16 a[28:29], v26 offset:8488\n"
"v_mfma_f32_16x16x32_bf16 v[196:199], a[72:75], a[168:171], v[196:199]\n"
"buffer_load_dwordx4 v23, s[24:27], s67 offen offset:1024 lds\n"
"v_mfma_f32_16x16x32_bf16 v[196:199], a[76:79], a[172:175], v[196:199]\n"
"ds_read_b64_tr_b16 a[30:31], v26 offset:9512\n"
"v_mfma_f32_16x16x32_bf16 v[200:203], a[80:83], a[168:171], v[200:203]\n"
"ds_read_b64_tr_b16 a[32:33], v26 offset:512\n"
"v_mfma_f32_16x16x32_bf16 v[200:203], a[84:87], a[172:175], v[200:203]\n"
"ds_read_b64_tr_b16 a[34:35], v26 offset:1536\n"
"ds_read_b64_tr_b16 a[36:37], v26 offset:8736\n"
"v_mfma_f32_16x16x32_bf16 v[204:207], a[88:91], a[168:171], v[204:207]\n"
"buffer_load_dwordx4 v24, s[24:27], s67 offen offset:2056 lds\n"
"v_mfma_f32_16x16x32_bf16 v[204:207], a[92:95], a[172:175], v[204:207]\n"
"ds_read_b64_tr_b16 a[38:39], v26 offset:9760\n"
"v_mfma_f32_16x16x32_bf16 v[208:211], a[96:99], a[168:171], v[208:211]\n"
"ds_read_b64_tr_b16 a[40:41], v26 offset:520\n"
"v_mfma_f32_16x16x32_bf16 v[208:211], a[100:103], a[172:175], v[208:211]\n"
"ds_read_b64_tr_b16 a[42:43], v26 offset:1544\n"
"ds_read_b64_tr_b16 a[44:45], v26 offset:8744\n"
"v_mfma_f32_16x16x32_bf16 v[212:215], a[104:107], a[168:171], v[212:215]\n"
"buffer_load_dwordx4 v25, s[24:27], s67 offen offset:3080 lds\n"
"v_mfma_f32_16x16x32_bf16 v[212:215], a[108:111], a[172:175], v[212:215]\n"
"ds_read_b64_tr_b16 a[46:47], v26 offset:9768\n"
"v_mfma_f32_16x16x32_bf16 v[216:219], a[112:115], a[168:171], v[216:219]\n"
"ds_read_b64_tr_b16 a[48:49], v26 offset:768\n"
"v_mfma_f32_16x16x32_bf16 v[216:219], a[116:119], a[172:175], v[216:219]\n"
"s_add_u32 m0, s59, 4096\n"
"s_add_u32 s66, s56, 33280\n"
"v_mfma_f32_16x16x32_bf16 v[220:223], a[120:123], a[168:171], v[220:223]\n"
"buffer_load_dwordx4 v16, s[20:23], s66 offen lds\n"
"v_mfma_f32_16x16x32_bf16 v[220:223], a[124:127], a[172:175], v[220:223]\n"
"s_waitcnt vmcnt(19) lgkmcnt(12)\n"
"v_mfma_f32_16x16x32_bf16 v[224:227], a[64:67], a[176:179], v[224:227]\n"
"ds_read_b128 a[184:187], v20\n"
"ds_read_b128 a[188:191], v21\n"
"v_mfma_f32_16x16x32_bf16 v[224:227], a[68:71], a[180:183], v[224:227]\n"
"ds_read_b128 a[160:163], v20 offset:2048\n"
"ds_read_b128 a[164:167], v21 offset:2048\n"
"v_mfma_f32_16x16x32_bf16 v[228:231], a[72:75], a[176:179], v[228:231]\n"
"buffer_load_dwordx4 v17, s[20:23], s66 offen offset:1024 lds\n"
"v_mfma_f32_16x16x32_bf16 v[228:231], a[76:79], a[180:183], v[228:231]\n"
"ds_read_b64_tr_b16 a[50:51], v26 offset:1792\n"
"ds_read_b64_tr_b16 a[52:53], v26 offset:8992\n"
"v_mfma_f32_16x16x32_bf16 v[232:235], a[80:83], a[176:179], v[232:235]\n"
"ds_read_b64_tr_b16 a[54:55], v26 offset:10016\n"
"v_mfma_f32_16x16x32_bf16 v[232:235], a[84:87], a[180:183], v[232:235]\n"
"s_add_u32 m0, s59, 6144\n"
"s_add_u32 s66, s56, 49664\n"
"v_mfma_f32_16x16x32_bf16 v[236:239], a[88:91], a[176:179], v[236:239]\n"
"buffer_load_dwordx4 v16, s[20:23], s66 offen lds\n"
"v_mfma_f32_16x16x32_bf16 v[236:239], a[92:95], a[180:183], v[236:239]\n"
"ds_read_b64_tr_b16 a[56:57], v26 offset:776\n"
"ds_read_b64_tr_b16 a[58:59], v26 offset:1800\n"
"v_mfma_f32_16x16x32_bf16 v[240:243], a[96:99], a[176:179], v[240:243]\n"
"ds_read_b64_tr_b16 a[60:61], v26 offset:9000\n"
"v_mfma_f32_16x16x32_bf16 v[240:243], a[100:103], a[180:183], v[240:243]\n"
"ds_read_b64_tr_b16 a[62:63], v26 offset:10024\n"
"v_mfma_f32_16x16x32_bf16 v[244:247], a[104:107], a[176:179], v[244:247]\n"
"buffer_load_dwordx4 v17, s[20:23], s66 offen offset:1024 lds\n"
"v_mfma_f32_16x16x32_bf16 v[244:247], a[108:111], a[180:183], v[244:247]\n"
"v_mfma_f32_16x16x32_bf16 v[248:251], a[112:115], a[176:179], v[248:251]\n"
"v_mfma_f32_16x16x32_bf16 v[248:251], a[116:119], a[180:183], v[248:251]\n"
"s_add_u32 m0, s59, 8192\n"
"s_add_u32 s66, s56, 66048\n"
"v_mfma_f32_16x16x32_bf16 v[252:255], a[120:123], a[176:179], v[252:255]\n"
"buffer_load_dwordx4 v16, s[20:23], s66 offen lds\n"
"v_mfma_f32_16x16x32_bf16 v[252:255], a[124:127], a[180:183], v[252:255]\n"
"s_waitcnt vmcnt(17) lgkmcnt(0)\n"
"v_mfma_f32_16x16x32_bf16 v[96:99], a[0:3], a[184:187], v[96:99]\n"
"ds_read_b128 a[168:171], v20 offset:4096\n"
"ds_read_b128 a[172:175], v21 offset:4096\n"
"v_mfma_f32_16x16x32_bf16 v[96:99], a[4:7], a[188:191], v[96:99]\n"
"v_mfma_f32_16x16x32_bf16 v[100:103], a[8:11], a[184:187], v[100:103]\n"
"buffer_load_dwordx4 v17, s[20:23], s66 offen offset:1024 lds\n"
"v_mfma_f32_16x16x32_bf16 v[100:103], a[12:15], a[188:191], v[100:103]\n"
"v_mfma_f32_16x16x32_bf16 v[104:107], a[16:19], a[184:187], v[104:107]\n"
"v_mfma_f32_16x16x32_bf16 v[104:107], a[20:23], a[188:191], v[104:107]\n"
"v_mfma_f32_16x16x32_bf16 v[108:111], a[24:27], a[184:187], v[108:111]\n"
"s_cmp_eq_u32 s48, 2\n"
"s_cbranch_scc1 .Lmha_twb\n"
"buffer_load_dword v33, v35, s[20:23], 0 offen\n"
"s_branch .Lmha_txb\n"
".Lmha_twb:\n"
"buffer_load_dword v33, v31, s[24:27], 0 offen\n"
".Lmha_txb:\n"
"s_nop 0\n"
"v_mfma_f32_16x16x32_bf16 v[108:111], a[28:31], a[188:191], v[108:111]\n"
"v_mfma_f32_16x16x32_bf16 v[112:115], a[32:35], a[184:187], v[112:115]\n"
"v_mfma_f32_16x16x32_bf16 v[112:115], a[36:39], a[188:191], v[112:115]\n"
"s_add_u32 m0, s60, 0\n"
"s_add_u32 s66, s56, 640\n"
"v_mfma_f32_16x16x32_bf16 v[116:119], a[40:43], a[184:187], v[116:119]\n"
"buffer_load_dwordx4 v16, s[20:23], s66 offen lds\n"
"v_mfma_f32_16x16x32_bf16 v[116:119], a[44:47], a[188:191], v[116:119]\n"
"v_mfma_f32_16x16x32_bf16 v[120:123], a[48:51], a[184:187], v[120:123]\n"
"v_mfma_f32_16x16x32_bf16 v[120:123], a[52:55], a[188:191], v[120:123]\n"
"v_mfma_f32_16x16x32_bf16 v[124:127], a[56:59], a[184:187], v[124:127]\n"
"buffer_load_dwordx4 v17, s[20:23], s66 offen offset:1024 lds\n"
"v_mfma_f32_16x16x32_bf16 v[124:127], a[60:63], a[188:191], v[124:127]\n"
"s_waitcnt vmcnt(19) lgkmcnt(0)\n"
"s_barrier\n"
"v_mfma_f32_16x16x32_bf16 v[128:131], a[0:3], a[160:163], v[128:131]\n"
"ds_read_b128 a[176:179], v20 offset:6144\n"
"ds_read_b128 a[180:183], v21 offset:6144\n"
"v_mfma_f32_16x16x32_bf16 v[128:131], a[4:7], a[164:167], v[128:131]\n"
"s_add_u32 m0, s58, 16384\n"
"s_add_u32 s67, s57, 3145728\n"
"v_mfma_f32_16x16x32_bf16 v[132:135], a[8:11], a[160:163], v[132:135]\n"
"buffer_load_dwordx4 v22, s[24:27], s67 offen lds\n"
"v_mfma_f32_16x16x32_bf16 v[132:135], a[12:15], a[164:167], v[132:135]\n"
"ds_read_b64_tr_b16 a[192:193], v27\n"
"ds_read_b64_tr_b16 a[194:195], v27 offset:1024\n"
"v_mfma_f32_16x16x32_bf16 v[136:139], a[16:19], a[160:163], v[136:139]\n"
"ds_read_b64_tr_b16 a[196:197], v27 offset:8224\n"
"v_mfma_f32_16x16x32_bf16 v[136:139], a[20:23], a[164:167], v[136:139]\n"
"ds_read_b64_tr_b16 a[198:199], v27 offset:9248\n"
"v_mfma_f32_16x16x32_bf16 v[140:143], a[24:27], a[160:163], v[140:143]\n"
"buffer_load_dwordx4 v23, s[24:27], s67 offen offset:1024 lds\n"
"v_mfma_f32_16x16x32_bf16 v[140:143], a[28:31], a[164:167], v[140:143]\n"
"ds_read_b64_tr_b16 a[200:201], v27 offset:8\n"
"v_mfma_f32_16x16x32_bf16 v[144:147], a[32:35], a[160:163], v[144:147]\n"
"ds_read_b64_tr_b16 a[202:203], v27 offset:1032\n"
"v_mfma_f32_16x16x32_bf16 v[144:147], a[36:39], a[164:167], v[144:147]\n"
"ds_read_b64_tr_b16 a[204:205], v27 offset:8232\n"
"v_mfma_f32_16x16x32_bf16 v[148:151], a[40:43], a[160:163], v[148:151]\n"
"buffer_load_dwordx4 v24, s[24:27], s67 offen offset:2056 lds\n"
"v_mfma_f32_16x16x32_bf16 v[148:151], a[44:47], a[164:167], v[148:151]\n"
"ds_read_b64_tr_b16 a[206:207], v27 offset:9256\n"
"v_mfma_f32_16x16x32_bf16 v[152:155], a[48:51], a[160:163], v[152:155]\n"
"ds_read_b64_tr_b16 a[208:209], v27 offset:256\n"
"v_mfma_f32_16x16x32_bf16 v[152:155], a[52:55], a[164:167], v[152:155]\n"
"ds_read_b64_tr_b16 a[210:211], v27 offset:1280\n"
"v_mfma_f32_16x16x32_bf16 v[156:159], a[56:59], a[160:163], v[156:159]\n"
"buffer_load_dwordx4 v25, s[24:27], s67 offen offset:3080 lds\n"
"v_mfma_f32_16x16x32_bf16 v[156:159], a[60:63], a[164:167], v[156:159]\n"
"s_waitcnt vmcnt(21) lgkmcnt(10)\n"
"v_mfma_f32_16x16x32_bf16 v[160:163], a[0:3], a[168:171], v[160:163]\n"
"ds_read_b128 a[184:187], v20 offset:8192\n"
"ds_read_b128 a[188:191], v21 offset:8192\n"
"v_mfma_f32_16x16x32_bf16 v[160:163], a[4:7], a[172:175], v[160:163]\n"
"s_add_u32 m0, s60, 2048\n"
"s_add_u32 s66, s56, 17024\n"
"v_mfma_f32_16x16x32_bf16 v[164:167], a[8:11], a[168:171], v[164:167]\n"
"buffer_load_dwordx4 v16, s[20:23], s66 offen lds\n"
"v_mfma_f32_16x16x32_bf16 v[164:167], a[12:15], a[172:175], v[164:167]\n"
"ds_read_b64_tr_b16 a[212:213], v27 offset:8480\n"
"ds_read_b64_tr_b16 a[214:215], v27 offset:9504\n"
"v_mfma_f32_16x16x32_bf16 v[168:171], a[16:19], a[168:171], v[168:171]\n"
"ds_read_b64_tr_b16 a[216:217], v27 offset:264\n"
"v_mfma_f32_16x16x32_bf16 v[168:171], a[20:23], a[172:175], v[168:171]\n"
"ds_read_b64_tr_b16 a[218:219], v27 offset:1288\n"
"v_mfma_f32_16x16x32_bf16 v[172:175], a[24:27], a[168:171], v[172:175]\n"
"buffer_load_dwordx4 v17, s[20:23], s66 offen offset:1024 lds\n"
"v_mfma_f32_16x16x32_bf16 v[172:175], a[28:31], a[172:175], v[172:175]\n"
"ds_read_b64_tr_b16 a[220:221], v27 offset:8488\n"
"v_mfma_f32_16x16x32_bf16 v[176:179], a[32:35], a[168:171], v[176:179]\n"
"ds_read_b64_tr_b16 a[222:223], v27 offset:9512\n"
"ds_read_b64_tr_b16 a[224:225], v27 offset:512\n"
"v_mfma_f32_16x16x32_bf16 v[176:179], a[36:39], a[172:175], v[176:179]\n"
"s_add_u32 m0, s60, 4096\n"
"s_add_u32 s66, s56, 33408\n"
"v_mfma_f32_16x16x32_bf16 v[180:183], a[40:43], a[168:171], v[180:183]\n"
"buffer_load_dwordx4 v16, s[20:23], s66 offen lds\n"
"v_mfma_f32_16x16x32_bf16 v[180:183], a[44:47], a[172:175], v[180:183]\n"
"ds_read_b64_tr_b16 a[226:227], v27 offset:1536\n"
"v_mfma_f32_16x16x32_bf16 v[184:187], a[48:51], a[168:171], v[184:187]\n"
"ds_read_b64_tr_b16 a[228:229], v27 offset:8736\n"
"v_mfma_f32_16x16x32_bf16 v[184:187], a[52:55], a[172:175], v[184:187]\n"
"ds_read_b64_tr_b16 a[230:231], v27 offset:9760\n"
"v_mfma_f32_16x16x32_bf16 v[188:191], a[56:59], a[168:171], v[188:191]\n"
"buffer_load_dwordx4 v17, s[20:23], s66 offen offset:1024 lds\n"
"v_mfma_f32_16x16x32_bf16 v[188:191], a[60:63], a[172:175], v[188:191]\n"
"s_waitcnt vmcnt(21) lgkmcnt(10)\n"
"v_mfma_f32_16x16x32_bf16 v[192:195], a[0:3], a[176:179], v[192:195]\n"
"ds_read_b128 a[160:163], v18\n"
"ds_read_b128 a[164:167], v19\n"
"v_mfma_f32_16x16x32_bf16 v[192:195], a[4:7], a[180:183], v[192:195]\n"
"ds_read_b128 a[168:171], v18 offset:2048\n"
"ds_read_b128 a[172:175], v19 offset:2048\n"
"v_mfma_f32_16x16x32_bf16 v[196:199], a[8:11], a[176:179], v[196:199]\n"
"s_add_u32 m0, s60, 6144\n"
"s_add_u32 s66, s56, 49792\n"
"v_mfma_f32_16x16x32_bf16 v[196:199], a[12:15], a[180:183], v[196:199]\n"
"buffer_load_dwordx4 v16, s[20:23], s66 offen lds\n"
"v_mfma_f32_16x16x32_bf16 v[200:203], a[16:19], a[176:179], v[200:203]\n"
"ds_read_b64_tr_b16 a[232:233], v27 offset:520\n"
"v_mfma_f32_16x16x32_bf16 v[200:203], a[20:23], a[180:183], v[200:203]\n"
"ds_read_b64_tr_b16 a[234:235], v27 offset:1544\n"
"v_mfma_f32_16x16x32_bf16 v[204:207], a[24:27], a[176:179], v[204:207]\n"
"ds_read_b64_tr_b16 a[236:237], v27 offset:8744\n"
"v_mfma_f32_16x16x32_bf16 v[204:207], a[28:31], a[180:183], v[204:207]\n"
"buffer_load_dwordx4 v17, s[20:23], s66 offen offset:1024 lds\n"
"v_mfma_f32_16x16x32_bf16 v[208:211], a[32:35], a[176:179], v[208:211]\n"
"ds_read_b64_tr_b16 a[238:239], v27 offset:9768\n"
"v_mfma_f32_16x16x32_bf16 v[208:211], a[36:39], a[180:183], v[208:211]\n"
"ds_read_b64_tr_b16 a[240:241], v27 offset:768\n"
"v_mfma_f32_16x16x32_bf16 v[212:215], a[40:43], a[176:179], v[212:215]\n"
"s_add_u32 m0, s60, 8192\n"
"s_add_u32 s66, s56, 66176\n"
"v_mfma_f32_16x16x32_bf16 v[212:215], a[44:47], a[180:183], v[212:215]\n"
"buffer_load_dwordx4 v16, s[20:23], s66 offen lds\n"
"v_mfma_f32_16x16x32_bf16 v[216:219], a[48:51], a[176:179], v[216:219]\n"
"ds_read_b64_tr_b16 a[242:243], v27 offset:1792\n"
"v_mfma_f32_16x16x32_bf16 v[216:219], a[52:55], a[180:183], v[216:219]\n"
"ds_read_b64_tr_b16 a[244:245], v27 offset:8992\n"
"v_mfma_f32_16x16x32_bf16 v[220:223], a[56:59], a[176:179], v[220:223]\n"
"ds_read_b64_tr_b16 a[246:247], v27 offset:10016\n"
"v_mfma_f32_16x16x32_bf16 v[220:223], a[60:63], a[180:183], v[220:223]\n"
"s_waitcnt vmcnt(18) lgkmcnt(8)\n"
"v_mfma_f32_16x16x32_bf16 v[224:227], a[0:3], a[184:187], v[224:227]\n"
"ds_read_b128 a[176:179], v18 offset:4096\n"
"ds_read_b128 a[180:183], v19 offset:4096\n"
"v_mfma_f32_16x16x32_bf16 v[224:227], a[4:7], a[188:191], v[224:227]\n"
"buffer_load_dwordx4 v17, s[20:23], s66 offen offset:1024 lds\n"
"v_mfma_f32_16x16x32_bf16 v[228:231], a[8:11], a[184:187], v[228:231]\n"
"ds_read_b64_tr_b16 a[248:249], v27 offset:776\n"
"v_mfma_f32_16x16x32_bf16 v[228:231], a[12:15], a[188:191], v[228:231]\n"
"v_mfma_f32_16x16x32_bf16 v[232:235], a[16:19], a[184:187], v[232:235]\n"
"ds_read_b64_tr_b16 a[250:251], v27 offset:1800\n"
"v_mfma_f32_16x16x32_bf16 v[232:235], a[20:23], a[188:191], v[232:235]\n"
"v_mfma_f32_16x16x32_bf16 v[236:239], a[24:27], a[184:187], v[236:239]\n"
"ds_read_b64_tr_b16 a[252:253], v27 offset:9000\n"
"v_mfma_f32_16x16x32_bf16 v[236:239], a[28:31], a[188:191], v[236:239]\n"
"v_mfma_f32_16x16x32_bf16 v[240:243], a[32:35], a[184:187], v[240:243]\n"
"ds_read_b64_tr_b16 a[254:255], v27 offset:10024\n"
"v_mfma_f32_16x16x32_bf16 v[240:243], a[36:39], a[188:191], v[240:243]\n"
"v_mfma_f32_16x16x32_bf16 v[244:247], a[40:43], a[184:187], v[244:247]\n"
"v_mfma_f32_16x16x32_bf16 v[244:247], a[44:47], a[188:191], v[244:247]\n"
"v_mfma_f32_16x16x32_bf16 v[248:251], a[48:51], a[184:187], v[248:251]\n"
"v_mfma_f32_16x16x32_bf16 v[248:251], a[52:55], a[188:191], v[248:251]\n"
"v_mfma_f32_16x16x32_bf16 v[252:255], a[56:59], a[184:187], v[252:255]\n"
"v_mfma_f32_16x16x32_bf16 v[252:255], a[60:63], a[188:191], v[252:255]\n"
"s_waitcnt vmcnt(17) lgkmcnt(0)\n"
"s_barrier\n"
"v_mfma_f32_16x16x32_bf16 v[96:99], a[192:195], a[160:163], v[96:99]\n"
"ds_read_b128 a[184:187], v18 offset:6144\n"
"ds_read_b128 a[188:191], v19 offset:6144\n"
"v_mfma_f32_16x16x32_bf16 v[96:99], a[196:199], a[164:167], v[96:99]\n"
"s_add_u32 m0, s58, 147392\n"
"s_add_u32 s67, s57, 3670016\n"
"v_mfma_f32_16x16x32_bf16 v[100:103], a[200:203], a[160:163], v[100:103]\n"
"buffer_load_dwordx4 v22, s[24:27], s67 offen lds\n"
"v_mfma_f32_16x16x32_bf16 v[100:103], a[204:207], a[164:167], v[100:103]\n"
"ds_read_b64_tr_b16 a[64:65], v28\n"
"ds_read_b64_tr_b16 a[66:67], v28 offset:1024\n"
"v_mfma_f32_16x16x32_bf16 v[104:107], a[208:211], a[160:163], v[104:107]\n"
"ds_read_b64_tr_b16 a[68:69], v28 offset:8224\n"
"v_mfma_f32_16x16x32_bf16 v[104:107], a[212:215], a[164:167], v[104:107]\n"
"ds_read_b64_tr_b16 a[70:71], v28 offset:9248\n"
"v_mfma_f32_16x16x32_bf16 v[108:111], a[216:219], a[160:163], v[108:111]\n"
"buffer_load_dwordx4 v23, s[24:27], s67 offen offset:1024 lds\n"
"v_mfma_f32_16x16x32_bf16 v[108:111], a[220:223], a[164:167], v[108:111]\n"
"ds_read_b64_tr_b16 a[72:73], v28 offset:8\n"
"v_mfma_f32_16x16x32_bf16 v[112:115], a[224:227], a[160:163], v[112:115]\n"
"ds_read_b64_tr_b16 a[74:75], v28 offset:1032\n"
"v_mfma_f32_16x16x32_bf16 v[112:115], a[228:231], a[164:167], v[112:115]\n"
"ds_read_b64_tr_b16 a[76:77], v28 offset:8232\n"
"v_mfma_f32_16x16x32_bf16 v[116:119], a[232:235], a[160:163], v[116:119]\n"
"buffer_load_dwordx4 v24, s[24:27], s67 offen offset:2056 lds\n"
"v_mfma_f32_16x16x32_bf16 v[116:119], a[236:239], a[164:167], v[116:119]\n"
"ds_read_b64_tr_b16 a[78:79], v28 offset:9256\n"
"v_mfma_f32_16x16x32_bf16 v[120:123], a[240:243], a[160:163], v[120:123]\n"
"ds_read_b64_tr_b16 a[80:81], v28 offset:256\n"
"v_mfma_f32_16x16x32_bf16 v[120:123], a[244:247], a[164:167], v[120:123]\n"
"ds_read_b64_tr_b16 a[82:83], v28 offset:1280\n"
"v_mfma_f32_16x16x32_bf16 v[124:127], a[248:251], a[160:163], v[124:127]\n"
"buffer_load_dwordx4 v25, s[24:27], s67 offen offset:3080 lds\n"
"v_mfma_f32_16x16x32_bf16 v[124:127], a[252:255], a[164:167], v[124:127]\n"
"s_waitcnt vmcnt(19) lgkmcnt(10)\n"
"v_mfma_f32_16x16x32_bf16 v[128:131], a[192:195], a[168:171], v[128:131]\n"
"ds_read_b128 a[160:163], v18 offset:8192\n"
"ds_read_b128 a[164:167], v19 offset:8192\n"
"v_mfma_f32_16x16x32_bf16 v[128:131], a[196:199], a[172:175], v[128:131]\n"
"s_add_u32 m0, s59, 0\n"
"s_add_u32 s66, s56, 768\n"
"v_mfma_f32_16x16x32_bf16 v[132:135], a[200:203], a[168:171], v[132:135]\n"
"buffer_load_dwordx4 v16, s[20:23], s66 offen lds\n"
"v_mfma_f32_16x16x32_bf16 v[132:135], a[204:207], a[172:175], v[132:135]\n"
"ds_read_b64_tr_b16 a[84:85], v28 offset:8480\n"
"ds_read_b64_tr_b16 a[86:87], v28 offset:9504\n"
"v_mfma_f32_16x16x32_bf16 v[136:139], a[208:211], a[168:171], v[136:139]\n"
"ds_read_b64_tr_b16 a[88:89], v28 offset:264\n"
"v_mfma_f32_16x16x32_bf16 v[136:139], a[212:215], a[172:175], v[136:139]\n"
"ds_read_b64_tr_b16 a[90:91], v28 offset:1288\n"
"v_mfma_f32_16x16x32_bf16 v[140:143], a[216:219], a[168:171], v[140:143]\n"
"buffer_load_dwordx4 v17, s[20:23], s66 offen offset:1024 lds\n"
"v_mfma_f32_16x16x32_bf16 v[140:143], a[220:223], a[172:175], v[140:143]\n"
"ds_read_b64_tr_b16 a[92:93], v28 offset:8488\n"
"v_mfma_f32_16x16x32_bf16 v[144:147], a[224:227], a[168:171], v[144:147]\n"
"ds_read_b64_tr_b16 a[94:95], v28 offset:9512\n"
"ds_read_b64_tr_b16 a[96:97], v28 offset:512\n"
"v_mfma_f32_16x16x32_bf16 v[144:147], a[228:231], a[172:175], v[144:147]\n"
"s_add_u32 m0, s59, 2048\n"
"s_add_u32 s66, s56, 17152\n"
"v_mfma_f32_16x16x32_bf16 v[148:151], a[232:235], a[168:171], v[148:151]\n"
"buffer_load_dwordx4 v16, s[20:23], s66 offen lds\n"
"v_mfma_f32_16x16x32_bf16 v[148:151], a[236:239], a[172:175], v[148:151]\n"
"ds_read_b64_tr_b16 a[98:99], v28 offset:1536\n"
"v_mfma_f32_16x16x32_bf16 v[152:155], a[240:243], a[168:171], v[152:155]\n"
"ds_read_b64_tr_b16 a[100:101], v28 offset:8736\n"
"v_mfma_f32_16x16x32_bf16 v[152:155], a[244:247], a[172:175], v[152:155]\n"
"ds_read_b64_tr_b16 a[102:103], v28 offset:9760\n"
"v_mfma_f32_16x16x32_bf16 v[156:159], a[248:251], a[168:171], v[156:159]\n"
"buffer_load_dwordx4 v17, s[20:23], s66 offen offset:1024 lds\n"
"v_mfma_f32_16x16x32_bf16 v[156:159], a[252:255], a[172:175], v[156:159]\n"
"s_waitcnt vmcnt(20) lgkmcnt(10)\n"
"v_mfma_f32_16x16x32_bf16 v[160:163], a[192:195], a[176:179], v[160:163]\n"
"ds_read_b128 a[168:171], v20\n"
"ds_read_b128 a[172:175], v21\n"
"v_mfma_f32_16x16x32_bf16 v[160:163], a[196:199], a[180:183], v[160:163]\n"
"s_add_u32 m0, s59, 4096\n"
"s_add_u32 s66, s56, 33536\n"
"v_mfma_f32_16x16x32_bf16 v[164:167], a[200:203], a[176:179], v[164:167]\n"
"buffer_load_dwordx4 v16, s[20:23], s66 offen lds\n"
"v_mfma_f32_16x16x32_bf16 v[164:167], a[204:207], a[180:183], v[164:167]\n"
"ds_read_b64_tr_b16 a[104:105], v28 offset:520\n"
"v_mfma_f32_16x16x32_bf16 v[168:171], a[208:211], a[176:179], v[168:171]\n"
"ds_read_b64_tr_b16 a[106:107], v28 offset:1544\n"
"v_mfma_f32_16x16x32_bf16 v[168:171], a[212:215], a[180:183], v[168:171]\n"
"ds_read_b64_tr_b16 a[108:109], v28 offset:8744\n"
"v_mfma_f32_16x16x32_bf16 v[172:175], a[216:219], a[176:179], v[172:175]\n"
"buffer_load_dwordx4 v17, s[20:23], s66 offen offset:1024 lds\n"
"v_mfma_f32_16x16x32_bf16 v[172:175], a[220:223], a[180:183], v[172:175]\n"
"ds_read_b64_tr_b16 a[110:111], v28 offset:9768\n"
"v_mfma_f32_16x16x32_bf16 v[176:179], a[224:227], a[176:179], v[176:179]\n"
"ds_read_b64_tr_b16 a[112:113], v28 offset:768\n"
"v_mfma_f32_16x16x32_bf16 v[176:179], a[228:231], a[180:183], v[176:179]\n"
"s_add_u32 m0, s59, 6144\n"
"s_add_u32 s66, s56, 49920\n"
"v_mfma_f32_16x16x32_bf16 v[180:183], a[232:235], a[176:179], v[180:183]\n"
"buffer_load_dwordx4 v16, s[20:23], s66 offen lds\n"
"v_mfma_f32_16x16x32_bf16 v[180:183], a[236:239], a[180:183], v[180:183]\n"
"ds_read_b64_tr_b16 a[114:115], v28 offset:1792\n"
"v_mfma_f32_16x16x32_bf16 v[184:187], a[240:243], a[176:179], v[184:187]\n"
"ds_read_b64_tr_b16 a[116:117], v28 offset:8992\n"
"v_mfma_f32_16x16x32_bf16 v[184:187], a[244:247], a[180:183], v[184:187]\n"
"ds_read_b64_tr_b16 a[118:119], v28 offset:10016\n"
"v_mfma_f32_16x16x32_bf16 v[188:191], a[248:251], a[176:179], v[188:191]\n"
"buffer_load_dwordx4 v17, s[20:23], s66 offen offset:1024 lds\n"
"v_mfma_f32_16x16x32_bf16 v[188:191], a[252:255], a[180:183], v[188:191]\n"
"s_waitcnt vmcnt(18) lgkmcnt(8)\n"
"v_mfma_f32_16x16x32_bf16 v[192:195], a[192:195], a[184:187], v[192:195]\n"
"ds_read_b128 a[176:179], v20 offset:2048\n"
"ds_read_b128 a[180:183], v21 offset:2048\n"
"v_mfma_f32_16x16x32_bf16 v[192:195], a[196:199], a[188:191], v[192:195]\n"
"s_add_u32 m0, s59, 8192\n"
"s_add_u32 s66, s56, 66304\n"
"v_mfma_f32_16x16x32_bf16 v[196:199], a[200:203], a[184:187], v[196:199]\n"
"buffer_load_dwordx4 v16, s[20:23], s66 offen lds\n"
"v_mfma_f32_16x16x32_bf16 v[196:199], a[204:207], a[188:191], v[196:199]\n"
"ds_read_b64_tr_b16 a[120:121], v28 offset:776\n"
"v_mfma_f32_16x16x32_bf16 v[200:203], a[208:211], a[184:187], v[200:203]\n"
"ds_read_b64_tr_b16 a[122:123], v28 offset:1800\n"
"v_mfma_f32_16x16x32_bf16 v[200:203], a[212:215], a[188:191], v[200:203]\n"
"ds_read_b64_tr_b16 a[124:125], v28 offset:9000\n"
"v_mfma_f32_16x16x32_bf16 v[204:207], a[216:219], a[184:187], v[204:207]\n"
"buffer_load_dwordx4 v17, s[20:23], s66 offen offset:1024 lds\n"
"v_mfma_f32_16x16x32_bf16 v[204:207], a[220:223], a[188:191], v[204:207]\n"
"ds_read_b64_tr_b16 a[126:127], v28 offset:10024\n"
"v_mfma_f32_16x16x32_bf16 v[208:211], a[224:227], a[184:187], v[208:211]\n"
"v_mfma_f32_16x16x32_bf16 v[208:211], a[228:231], a[188:191], v[208:211]\n"
"s_add_u32 m0, s60, 0\n"
"s_add_u32 s66, s56, 896\n"
"v_mfma_f32_16x16x32_bf16 v[212:215], a[232:235], a[184:187], v[212:215]\n"
"buffer_load_dwordx4 v16, s[20:23], s66 offen lds\n"
"v_mfma_f32_16x16x32_bf16 v[212:215], a[236:239], a[188:191], v[212:215]\n"
"v_mfma_f32_16x16x32_bf16 v[216:219], a[240:243], a[184:187], v[216:219]\n"
"v_mfma_f32_16x16x32_bf16 v[216:219], a[244:247], a[188:191], v[216:219]\n"
"v_mfma_f32_16x16x32_bf16 v[220:223], a[248:251], a[184:187], v[220:223]\n"
"buffer_load_dwordx4 v17, s[20:23], s66 offen offset:1024 lds\n"
"v_mfma_f32_16x16x32_bf16 v[220:223], a[252:255], a[188:191], v[220:223]\n"
"s_waitcnt vmcnt(20) lgkmcnt(0)\n"
"s_barrier\n"
"v_mfma_f32_16x16x32_bf16 v[224:227], a[192:195], a[160:163], v[224:227]\n"
"ds_read_b128 a[184:187], v20 offset:4096\n"
"ds_read_b128 a[188:191], v21 offset:4096\n"
"v_mfma_f32_16x16x32_bf16 v[224:227], a[196:199], a[164:167], v[224:227]\n"
"s_add_u32 m0, s60, 2048\n"
"s_add_u32 s66, s56, 17280\n"
"v_mfma_f32_16x16x32_bf16 v[228:231], a[200:203], a[160:163], v[228:231]\n"
"buffer_load_dwordx4 v16, s[20:23], s66 offen lds\n"
"v_mfma_f32_16x16x32_bf16 v[228:231], a[204:207], a[164:167], v[228:231]\n"
"ds_read_b64_tr_b16 a[0:1], v26\n"
"v_mfma_f32_16x16x32_bf16 v[232:235], a[208:211], a[160:163], v[232:235]\n"
"ds_read_b64_tr_b16 a[2:3], v26 offset:1024\n"
"v_mfma_f32_16x16x32_bf16 v[232:235], a[212:215], a[164:167], v[232:235]\n"
"ds_read_b64_tr_b16 a[4:5], v26 offset:8224\n"
"v_mfma_f32_16x16x32_bf16 v[236:239], a[216:219], a[160:163], v[236:239]\n"
"buffer_load_dwordx4 v17, s[20:23], s66 offen offset:1024 lds\n"
"v_mfma_f32_16x16x32_bf16 v[236:239], a[220:223], a[164:167], v[236:239]\n"
"ds_read_b64_tr_b16 a[6:7], v26 offset:9248\n"
"v_mfma_f32_16x16x32_bf16 v[240:243], a[224:227], a[160:163], v[240:243]\n"
"ds_read_b64_tr_b16 a[8:9], v26 offset:8\n"
"v_mfma_f32_16x16x32_bf16 v[240:243], a[228:231], a[164:167], v[240:243]\n"
"ds_read_b64_tr_b16 a[10:11], v26 offset:1032\n"
"v_mfma_f32_16x16x32_bf16 v[244:247], a[232:235], a[160:163], v[244:247]\n"
"ds_read_b64_tr_b16 a[12:13], v26 offset:8232\n"
"v_mfma_f32_16x16x32_bf16 v[244:247], a[236:239], a[164:167], v[244:247]\n"
"ds_read_b64_tr_b16 a[14:15], v26 offset:9256\n"
"v_mfma_f32_16x16x32_bf16 v[248:251], a[240:243], a[160:163], v[248:251]\n"
"ds_read_b64_tr_b16 a[16:17], v26 offset:256\n"
"v_mfma_f32_16x16x32_bf16 v[248:251], a[244:247], a[164:167], v[248:251]\n"
"ds_read_b64_tr_b16 a[18:19], v26 offset:1280\n"
"v_mfma_f32_16x16x32_bf16 v[252:255], a[248:251], a[160:163], v[252:255]\n"
"v_mfma_f32_16x16x32_bf16 v[252:255], a[252:255], a[164:167], v[252:255]\n"
"s_waitcnt vmcnt(20) lgkmcnt(10)\n"
"v_mfma_f32_16x16x32_bf16 v[96:99], a[64:67], a[168:171], v[96:99]\n"
"ds_read_b128 a[160:163], v20 offset:6144\n"
"ds_read_b128 a[164:167], v21 offset:6144\n"
"v_mfma_f32_16x16x32_bf16 v[96:99], a[68:71], a[172:175], v[96:99]\n"
"s_add_u32 m0, s60, 4096\n"
"s_add_u32 s66, s56, 33664\n"
"v_mfma_f32_16x16x32_bf16 v[100:103], a[72:75], a[168:171], v[100:103]\n"
"buffer_load_dwordx4 v16, s[20:23], s66 offen lds\n"
"v_mfma_f32_16x16x32_bf16 v[100:103], a[76:79], a[172:175], v[100:103]\n"
"ds_read_b64_tr_b16 a[20:21], v26 offset:8480\n"
"v_mfma_f32_16x16x32_bf16 v[104:107], a[80:83], a[168:171], v[104:107]\n"
"ds_read_b64_tr_b16 a[22:23], v26 offset:9504\n"
"v_mfma_f32_16x16x32_bf16 v[104:107], a[84:87], a[172:175], v[104:107]\n"
"ds_read_b64_tr_b16 a[24:25], v26 offset:264\n"
"v_mfma_f32_16x16x32_bf16 v[108:111], a[88:91], a[168:171], v[108:111]\n"
"buffer_load_dwordx4 v17, s[20:23], s66 offen offset:1024 lds\n"
"v_mfma_f32_16x16x32_bf16 v[108:111], a[92:95], a[172:175], v[108:111]\n"
"ds_read_b64_tr_b16 a[26:27], v26 offset:1288\n"
"v_mfma_f32_16x16x32_bf16 v[112:115], a[96:99], a[168:171], v[112:115]\n"
"ds_read_b64_tr_b16 a[28:29], v26 offset:8488\n"
"v_mfma_f32_16x16x32_bf16 v[112:115], a[100:103], a[172:175], v[112:115]\n"
"ds_read_b64_tr_b16 a[30:31], v26 offset:9512\n"
"v_mfma_f32_16x16x32_bf16 v[116:119], a[104:107], a[168:171], v[116:119]\n"
"ds_read_b64_tr_b16 a[32:33], v26 offset:512\n"
"v_mfma_f32_16x16x32_bf16 v[116:119], a[108:111], a[172:175], v[116:119]\n"
"ds_read_b64_tr_b16 a[34:35], v26 offset:1536\n"
"v_mfma_f32_16x16x32_bf16 v[120:123], a[112:115], a[168:171], v[120:123]\n"
"ds_read_b64_tr_b16 a[36:37], v26 offset:8736\n"
"v_mfma_f32_16x16x32_bf16 v[120:123], a[116:119], a[172:175], v[120:123]\n"
"ds_read_b64_tr_b16 a[38:39], v26 offset:9760\n"
"v_mfma_f32_16x16x32_bf16 v[124:127], a[120:123], a[168:171], v[124:127]\n"
"v_mfma_f32_16x16x32_bf16 v[124:127], a[124:127], a[172:175], v[124:127]\n"
"s_waitcnt vmcnt(20) lgkmcnt(10)\n"
"v_mfma_f32_16x16x32_bf16 v[128:131], a[64:67], a[176:179], v[128:131]\n"
"ds_read_b128 a[168:171], v20 offset:8192\n"
"ds_read_b128 a[172:175], v21 offset:8192\n"
"v_mfma_f32_16x16x32_bf16 v[128:131], a[68:71], a[180:183], v[128:131]\n"
"s_add_u32 m0, s60, 6144\n"
"s_add_u32 s66, s56, 50048\n"
"v_mfma_f32_16x16x32_bf16 v[132:135], a[72:75], a[176:179], v[132:135]\n"
"buffer_load_dwordx4 v16, s[20:23], s66 offen lds\n"
"v_mfma_f32_16x16x32_bf16 v[132:135], a[76:79], a[180:183], v[132:135]\n"
"ds_read_b64_tr_b16 a[40:41], v26 offset:520\n"
"v_mfma_f32_16x16x32_bf16 v[136:139], a[80:83], a[176:179], v[136:139]\n"
"ds_read_b64_tr_b16 a[42:43], v26 offset:1544\n"
"v_mfma_f32_16x16x32_bf16 v[136:139], a[84:87], a[180:183], v[136:139]\n"
"ds_read_b64_tr_b16 a[44:45], v26 offset:8744\n"
"v_mfma_f32_16x16x32_bf16 v[140:143], a[88:91], a[176:179], v[140:143]\n"
"buffer_load_dwordx4 v17, s[20:23], s66 offen offset:1024 lds\n"
"v_mfma_f32_16x16x32_bf16 v[140:143], a[92:95], a[180:183], v[140:143]\n"
"v_mfma_f32_16x16x32_bf16 v[144:147], a[96:99], a[176:179], v[144:147]\n"
"ds_read_b64_tr_b16 a[46:47], v26 offset:9768\n"
"v_mfma_f32_16x16x32_bf16 v[144:147], a[100:103], a[180:183], v[144:147]\n"
"ds_read_b64_tr_b16 a[48:49], v26 offset:768\n"
"v_mfma_f32_16x16x32_bf16 v[148:151], a[104:107], a[176:179], v[148:151]\n"
"ds_read_b64_tr_b16 a[50:51], v26 offset:1792\n"
"v_mfma_f32_16x16x32_bf16 v[148:151], a[108:111], a[180:183], v[148:151]\n"
"v_mfma_f32_16x16x32_bf16 v[152:155], a[112:115], a[176:179], v[152:155]\n"
"ds_read_b64_tr_b16 a[52:53], v26 offset:8992\n"
"v_mfma_f32_16x16x32_bf16 v[152:155], a[116:119], a[180:183], v[152:155]\n"
"ds_read_b64_tr_b16 a[54:55], v26 offset:10016\n"
"v_mfma_f32_16x16x32_bf16 v[156:159], a[120:123], a[176:179], v[156:159]\n"
"v_mfma_f32_16x16x32_bf16 v[156:159], a[124:127], a[180:183], v[156:159]\n"
"s_waitcnt vmcnt(16) lgkmcnt(8)\n"
"v_mfma_f32_16x16x32_bf16 v[160:163], a[64:67], a[184:187], v[160:163]\n"
"ds_read_b128 a[176:179], v18\n"
"ds_read_b128 a[180:183], v19\n"
"v_mfma_f32_16x16x32_bf16 v[160:163], a[68:71], a[188:191], v[160:163]\n"
"s_add_u32 m0, s60, 8192\n"
"s_add_u32 s66, s56, 66432\n"
"v_mfma_f32_16x16x32_bf16 v[164:167], a[72:75], a[184:187], v[164:167]\n"
"buffer_load_dwordx4 v16, s[20:23], s66 offen lds\n"
"v_mfma_f32_16x16x32_bf16 v[164:167], a[76:79], a[188:191], v[164:167]\n"
"ds_read_b64_tr_b16 a[56:57], v26 offset:776\n"
"v_mfma_f32_16x16x32_bf16 v[168:171], a[80:83], a[184:187], v[168:171]\n"
"ds_read_b64_tr_b16 a[58:59], v26 offset:1800\n"
"v_mfma_f32_16x16x32_bf16 v[168:171], a[84:87], a[188:191], v[168:171]\n"
"v_mfma_f32_16x16x32_bf16 v[172:175], a[88:91], a[184:187], v[172:175]\n"
"buffer_load_dwordx4 v17, s[20:23], s66 offen offset:1024 lds\n"
"v_mfma_f32_16x16x32_bf16 v[172:175], a[92:95], a[188:191], v[172:175]\n"
"ds_read_b64_tr_b16 a[60:61], v26 offset:9000\n"
"v_mfma_f32_16x16x32_bf16 v[176:179], a[96:99], a[184:187], v[176:179]\n"
"v_mfma_f32_16x16x32_bf16 v[176:179], a[100:103], a[188:191], v[176:179]\n"
"ds_read_b64_tr_b16 a[62:63], v26 offset:10024\n"
"v_mfma_f32_16x16x32_bf16 v[180:183], a[104:107], a[184:187], v[180:183]\n"
"v_mfma_f32_16x16x32_bf16 v[180:183], a[108:111], a[188:191], v[180:183]\n"
"v_mfma_f32_16x16x32_bf16 v[184:187], a[112:115], a[184:187], v[184:187]\n"
"v_mfma_f32_16x16x32_bf16 v[184:187], a[116:119], a[188:191], v[184:187]\n"
"v_mfma_f32_16x16x32_bf16 v[188:191], a[120:123], a[184:187], v[188:191]\n"
"v_mfma_f32_16x16x32_bf16 v[188:191], a[124:127], a[188:191], v[188:191]\n"
"s_waitcnt vmcnt(8) lgkmcnt(0)\n"
"s_barrier\n"
"v_mfma_f32_16x16x32_bf16 v[192:195], a[64:67], a[160:163], v[192:195]\n"
"ds_read_b128 a[184:187], v20\n"
"ds_read_b128 a[188:191], v21\n"
"v_mfma_f32_16x16x32_bf16 v[192:195], a[68:71], a[164:167], v[192:195]\n"
"ds_read_b64_tr_b16 a[192:193], v27\n"
"v_mfma_f32_16x16x32_bf16 v[196:199], a[72:75], a[160:163], v[196:199]\n"
"ds_read_b64_tr_b16 a[194:195], v27 offset:1024\n"
"v_mfma_f32_16x16x32_bf16 v[196:199], a[76:79], a[164:167], v[196:199]\n"
"ds_read_b64_tr_b16 a[196:197], v27 offset:8224\n"
"v_mfma_f32_16x16x32_bf16 v[200:203], a[80:83], a[160:163], v[200:203]\n"
"ds_read_b64_tr_b16 a[198:199], v27 offset:9248\n"
"v_mfma_f32_16x16x32_bf16 v[200:203], a[84:87], a[164:167], v[200:203]\n"
"ds_read_b64_tr_b16 a[200:201], v27 offset:8\n"
"v_mfma_f32_16x16x32_bf16 v[204:207], a[88:91], a[160:163], v[204:207]\n"
"ds_read_b64_tr_b16 a[202:203], v27 offset:1032\n"
"v_mfma_f32_16x16x32_bf16 v[204:207], a[92:95], a[164:167], v[204:207]\n"
"ds_read_b64_tr_b16 a[204:205], v27 offset:8232\n"
"v_mfma_f32_16x16x32_bf16 v[208:211], a[96:99], a[160:163], v[208:211]\n"
"ds_read_b64_tr_b16 a[206:207], v27 offset:9256\n"
"v_mfma_f32_16x16x32_bf16 v[208:211], a[100:103], a[164:167], v[208:211]\n"
"ds_read_b64_tr_b16 a[208:209], v27 offset:256\n"
"v_mfma_f32_16x16x32_bf16 v[212:215], a[104:107], a[160:163], v[212:215]\n"
"ds_read_b64_tr_b16 a[210:211], v27 offset:1280\n"
"v_mfma_f32_16x16x32_bf16 v[212:215], a[108:111], a[164:167], v[212:215]\n"
"ds_read_b64_tr_b16 a[212:213], v27 offset:8480\n"
"v_mfma_f32_16x16x32_bf16 v[216:219], a[112:115], a[160:163], v[216:219]\n"
"ds_read_b64_tr_b16 a[214:215], v27 offset:9504\n"
"v_mfma_f32_16x16x32_bf16 v[216:219], a[116:119], a[164:167], v[216:219]\n"
"ds_read_b64_tr_b16 a[216:217], v27 offset:264\n"
"v_mfma_f32_16x16x32_bf16 v[220:223], a[120:123], a[160:163], v[220:223]\n"
"v_mfma_f32_16x16x32_bf16 v[220:223], a[124:127], a[164:167], v[220:223]\n"
"s_waitcnt lgkmcnt(13)\n"
"v_mfma_f32_16x16x32_bf16 v[224:227], a[64:67], a[168:171], v[224:227]\n"
"ds_read_b128 a[160:163], v18 offset:2048\n"
"ds_read_b128 a[164:167], v19 offset:2048\n"
"v_mfma_f32_16x16x32_bf16 v[224:227], a[68:71], a[172:175], v[224:227]\n"
"ds_read_b64_tr_b16 a[218:219], v27 offset:1288\n"
"v_mfma_f32_16x16x32_bf16 v[228:231], a[72:75], a[168:171], v[228:231]\n"
"ds_read_b64_tr_b16 a[220:221], v27 offset:8488\n"
"v_mfma_f32_16x16x32_bf16 v[228:231], a[76:79], a[172:175], v[228:231]\n"
"ds_read_b64_tr_b16 a[222:223], v27 offset:9512\n"
"v_mfma_f32_16x16x32_bf16 v[232:235], a[80:83], a[168:171], v[232:235]\n"
"ds_read_b64_tr_b16 a[224:225], v27 offset:512\n"
"v_mfma_f32_16x16x32_bf16 v[232:235], a[84:87], a[172:175], v[232:235]\n"
"ds_read_b64_tr_b16 a[226:227], v27 offset:1536\n"
"v_mfma_f32_16x16x32_bf16 v[236:239], a[88:91], a[168:171], v[236:239]\n"
"ds_read_b64_tr_b16 a[228:229], v27 offset:8736\n"
"v_mfma_f32_16x16x32_bf16 v[236:239], a[92:95], a[172:175], v[236:239]\n"
"v_mfma_f32_16x16x32_bf16 v[240:243], a[96:99], a[168:171], v[240:243]\n"
"ds_read_b64_tr_b16 a[230:231], v27 offset:9760\n"
"v_mfma_f32_16x16x32_bf16 v[240:243], a[100:103], a[172:175], v[240:243]\n"
"ds_read_b64_tr_b16 a[232:233], v27 offset:520\n"
"v_mfma_f32_16x16x32_bf16 v[244:247], a[104:107], a[168:171], v[244:247]\n"
"ds_read_b64_tr_b16 a[234:235], v27 offset:1544\n"
"v_mfma_f32_16x16x32_bf16 v[244:247], a[108:111], a[172:175], v[244:247]\n"
"ds_read_b64_tr_b16 a[236:237], v27 offset:8744\n"
"v_mfma_f32_16x16x32_bf16 v[248:251], a[112:115], a[168:171], v[248:251]\n"
"ds_read_b64_tr_b16 a[238:239], v27 offset:9768\n"
"v_mfma_f32_16x16x32_bf16 v[248:251], a[116:119], a[172:175], v[248:251]\n"
"ds_read_b64_tr_b16 a[240:241], v27 offset:768\n"
"v_mfma_f32_16x16x32_bf16 v[252:255], a[120:123], a[168:171], v[252:255]\n"
"v_mfma_f32_16x16x32_bf16 v[252:255], a[124:127], a[172:175], v[252:255]\n"
"s_waitcnt vmcnt(6) lgkmcnt(12)\n"
"v_mfma_f32_16x16x32_bf16 v[96:99], a[0:3], a[176:179], v[96:99]\n"
"ds_read_b128 a[168:171], v20 offset:2048\n"
"ds_read_b128 a[172:175], v21 offset:2048\n"
"v_mfma_f32_16x16x32_bf16 v[96:99], a[4:7], a[180:183], v[96:99]\n"
"ds_read_b64_tr_b16 a[242:243], v27 offset:1792\n"
"v_mfma_f32_16x16x32_bf16 v[100:103], a[8:11], a[176:179], v[100:103]\n"
"ds_read_b64_tr_b16 a[244:245], v27 offset:8992\n"
"v_mfma_f32_16x16x32_bf16 v[100:103], a[12:15], a[180:183], v[100:103]\n"
"ds_read_b64_tr_b16 a[246:247], v27 offset:10016\n"
"v_mfma_f32_16x16x32_bf16 v[104:107], a[16:19], a[176:179], v[104:107]\n"
"v_mfma_f32_16x16x32_bf16 v[104:107], a[20:23], a[180:183], v[104:107]\n"
"ds_read_b64_tr_b16 a[248:249], v27 offset:776\n"
"v_mfma_f32_16x16x32_bf16 v[108:111], a[24:27], a[176:179], v[108:111]\n"
"ds_read_b64_tr_b16 a[250:251], v27 offset:1800\n"
"v_mfma_f32_16x16x32_bf16 v[108:111], a[28:31], a[180:183], v[108:111]\n"
"v_mfma_f32_16x16x32_bf16 v[112:115], a[32:35], a[176:179], v[112:115]\n"
"ds_read_b64_tr_b16 a[252:253], v27 offset:9000\n"
"v_mfma_f32_16x16x32_bf16 v[112:115], a[36:39], a[180:183], v[112:115]\n"
"ds_read_b64_tr_b16 a[254:255], v27 offset:10024\n"
"v_mfma_f32_16x16x32_bf16 v[116:119], a[40:43], a[176:179], v[116:119]\n"
"v_mfma_f32_16x16x32_bf16 v[116:119], a[44:47], a[180:183], v[116:119]\n"
"v_mfma_f32_16x16x32_bf16 v[120:123], a[48:51], a[176:179], v[120:123]\n"
"v_mfma_f32_16x16x32_bf16 v[120:123], a[52:55], a[180:183], v[120:123]\n"
"v_mfma_f32_16x16x32_bf16 v[124:127], a[56:59], a[176:179], v[124:127]\n"
"v_mfma_f32_16x16x32_bf16 v[124:127], a[60:63], a[180:183], v[124:127]\n"
"s_waitcnt lgkmcnt(0)\n"
"v_mfma_f32_16x16x32_bf16 v[96:99], a[192:195], a[184:187], v[96:99]\n"
"ds_read_b128 a[176:179], v18 offset:4096\n"
"ds_read_b128 a[180:183], v19 offset:4096\n"
"v_mfma_f32_16x16x32_bf16 v[96:99], a[196:199], a[188:191], v[96:99]\n"
"v_readfirstlane_b32 s34, v56\n"
"v_readfirstlane_b32 s35, v57\n"
"v_mfma_f32_16x16x32_bf16 v[100:103], a[200:203], a[184:187], v[100:103]\n"
"v_mov_b32 v29, 0\n"
"v_mov_b32 v30, 0\n"
"v_mfma_f32_16x16x32_bf16 v[100:103], a[204:207], a[188:191], v[100:103]\n"
"v_mov_b32 v31, 0\n"
"v_mov_b32 v34, 0\n"
"v_mfma_f32_16x16x32_bf16 v[104:107], a[208:211], a[184:187], v[104:107]\n"
"v_mov_b32 v35, 0\n"
"s_mul_i32 s70, s34, 0x9e3779b1\n"
"v_mfma_f32_16x16x32_bf16 v[104:107], a[212:215], a[188:191], v[104:107]\n"
"s_add_u32 s70, s70, 0x7f4a7c15\n"
"s_mul_i32 s71, s35, 0x85ebca6b\n"
"v_mfma_f32_16x16x32_bf16 v[108:111], a[216:219], a[184:187], v[108:111]\n"
"s_xor_b32 s71, s71, s70\n"
"s_add_u32 s71, s71, 0x165667b1\n"
"v_mfma_f32_16x16x32_bf16 v[108:111], a[220:223], a[188:191], v[108:111]\n"
"v_lshrrev_b32 v2, 5, v0\n"
"v_lshlrev_b32 v16, 10, v2\n"
"v_mfma_f32_16x16x32_bf16 v[112:115], a[224:227], a[184:187], v[112:115]\n"
"v_bfe_u32 v2, v0, 2, 3\n"
"v_lshl_add_u32 v16, v2, 6, v16\n"
"v_mfma_f32_16x16x32_bf16 v[112:115], a[228:231], a[188:191], v[112:115]\n"
"v_and_b32 v2, 3, v0\n"
"v_lshl_add_u32 v16, v2, 3, v16\n"
"v_mfma_f32_16x16x32_bf16 v[116:119], a[232:235], a[184:187], v[116:119]\n"
"v_bfe_u32 v2, v0, 4, 1\n"
"v_lshl_add_u32 v16, v2, 5, v16\n"
"v_mfma_f32_16x16x32_bf16 v[116:119], a[236:239], a[188:191], v[116:119]\n"
"s_lshl_b32 s58, s48, 12\n"
"v_add_u32 v16, 131072, v16\n"
"v_mfma_f32_16x16x32_bf16 v[120:123], a[240:243], a[184:187], v[120:123]\n"
"v_add_u32 v16, s58, v16\n"
"v_xor_b32 v17, 32, v16\n"
"v_mfma_f32_16x16x32_bf16 v[120:123], a[244:247], a[188:191], v[120:123]\n"
"v_mfma_f32_16x16x32_bf16 v[124:127], a[248:251], a[184:187], v[124:127]\n"
"v_mfma_f32_16x16x32_bf16 v[124:127], a[252:255], a[188:191], v[124:127]\n"
"s_waitcnt vmcnt(4) lgkmcnt(0)\n"
"v_mfma_f32_16x16x32_bf16 v[128:131], a[0:3], a[160:163], v[128:131]\n"
"ds_read_b128 a[184:187], v20 offset:4096\n"
"ds_read_b128 a[188:191], v21 offset:4096\n"
"v_mfma_f32_16x16x32_bf16 v[128:131], a[4:7], a[164:167], v[128:131]\n"
"v_mul_f32 v96, s64, v96\n"
"v_mul_f32 v97, s64, v97\n"
"v_mfma_f32_16x16x32_bf16 v[132:135], a[8:11], a[160:163], v[132:135]\n"
"v_mul_f32 v98, s64, v98\n"
"v_mul_f32 v99, s64, v99\n"
"v_mfma_f32_16x16x32_bf16 v[132:135], a[12:15], a[164:167], v[132:135]\n"
"v_mul_f32 v100, s64, v100\n"
"v_mul_f32 v101, s64, v101\n"
"v_mfma_f32_16x16x32_bf16 v[136:139], a[16:19], a[160:163], v[136:139]\n"
"v_mul_f32 v102, s64, v102\n"
"v_mul_f32 v103, s64, v103\n"
"v_mfma_f32_16x16x32_bf16 v[136:139], a[20:23], a[164:167], v[136:139]\n"
"v_cvt_pk_bf16_f32 v80, v96, v97\n"
"v_cvt_pk_bf16_f32 v81, v98, v99\n"
"v_mfma_f32_16x16x32_bf16 v[140:143], a[24:27], a[160:163], v[140:143]\n"
"v_mul_f32 v104, s64, v104\n"
"v_mul_f32 v105, s64, v105\n"
"v_mfma_f32_16x16x32_bf16 v[140:143], a[28:31], a[164:167], v[140:143]\n"
"v_mul_f32 v106, s64, v106\n"
"v_mul_f32 v107, s64, v107\n"
"v_mfma_f32_16x16x32_bf16 v[144:147], a[32:35], a[160:163], v[144:147]\n"
"v_cvt_pk_bf16_f32 v82, v100, v101\n"
"v_cvt_pk_bf16_f32 v83, v102, v103\n"
"v_mfma_f32_16x16x32_bf16 v[144:147], a[36:39], a[164:167], v[144:147]\n"
"v_mul_f32 v108, s64, v108\n"
"v_mul_f32 v109, s64, v109\n"
"v_mfma_f32_16x16x32_bf16 v[148:151], a[40:43], a[160:163], v[148:151]\n"
"v_mul_f32 v110, s64, v110\n"
"v_mul_f32 v111, s64, v111\n"
"v_mfma_f32_16x16x32_bf16 v[148:151], a[44:47], a[164:167], v[148:151]\n"
"v_cvt_pk_bf16_f32 v84, v104, v105\n"
"v_cvt_pk_bf16_f32 v85, v106, v107\n"
"v_mfma_f32_16x16x32_bf16 v[152:155], a[48:51], a[160:163], v[152:155]\n"
"v_mul_f32 v112, s64, v112\n"
"v_mul_f32 v113, s64, v113\n"
"v_mfma_f32_16x16x32_bf16 v[152:155], a[52:55], a[164:167], v[152:155]\n"
"v_mul_f32 v114, s64, v114\n"
"v_mul_f32 v115, s64, v115\n"
"v_mfma_f32_16x16x32_bf16 v[156:159], a[56:59], a[160:163], v[156:159]\n"
"v_cvt_pk_bf16_f32 v86, v108, v109\n"
"v_cvt_pk_bf16_f32 v87, v110, v111\n"
"v_mfma_f32_16x16x32_bf16 v[156:159], a[60:63], a[164:167], v[156:159]\n"
"s_waitcnt lgkmcnt(0)\n"
"v_mul_f32 v116, s64, v116\n"
"v_mfma_f32_16x16x32_bf16 v[128:131], a[192:195], a[168:171], v[128:131]\n"
"ds_read_b128 a[160:163], v18 offset:6144\n"
"ds_read_b128 a[164:167], v19 offset:6144\n"
"v_mfma_f32_16x16x32_bf16 v[128:131], a[196:199], a[172:175], v[128:131]\n"
"v_mul_f32 v117, s64, v117\n"
"v_mul_f32 v118, s64, v118\n"
"v_mfma_f32_16x16x32_bf16 v[132:135], a[200:203], a[168:171], v[132:135]\n"
"v_mul_f32 v119, s64, v119\n"
"v_cvt_pk_bf16_f32 v88, v112, v113\n"
"v_mfma_f32_16x16x32_bf16 v[132:135], a[204:207], a[172:175], v[132:135]\n"
"v_cvt_pk_bf16_f32 v89, v114, v115\n"
"v_mul_f32 v120, s64, v120\n"
"v_mfma_f32_16x16x32_bf16 v[136:139], a[208:211], a[168:171], v[136:139]\n"
"v_mul_f32 v121, s64, v121\n"
"v_mul_f32 v122, s64, v122\n"
"v_mfma_f32_16x16x32_bf16 v[136:139], a[212:215], a[172:175], v[136:139]\n"
"v_mul_f32 v123, s64, v123\n"
"v_cvt_pk_bf16_f32 v90, v116, v117\n"
"v_mfma_f32_16x16x32_bf16 v[140:143], a[216:219], a[168:171], v[140:143]\n"
"v_cvt_pk_bf16_f32 v91, v118, v119\n"
"v_mul_f32 v124, s64, v124\n"
"v_mfma_f32_16x16x32_bf16 v[140:143], a[220:223], a[172:175], v[140:143]\n"
"v_mul_f32 v125, s64, v125\n"
"v_mul_f32 v126, s64, v126\n"
"v_mfma_f32_16x16x32_bf16 v[144:147], a[224:227], a[168:171], v[144:147]\n"
"v_mul_f32 v127, s64, v127\n"
"v_cvt_pk_bf16_f32 v92, v120, v121\n"
"v_mfma_f32_16x16x32_bf16 v[144:147], a[228:231], a[172:175], v[144:147]\n"
"v_cvt_pk_bf16_f32 v93, v122, v123\n"
"v_cvt_pk_bf16_f32 v94, v124, v125\n"
"v_mfma_f32_16x16x32_bf16 v[148:151], a[232:235], a[168:171], v[148:151]\n"
"v_cvt_pk_bf16_f32 v95, v126, v127\n"
"v_mfma_f32_16x16x32_bf16 v[148:151], a[236:239], a[172:175], v[148:151]\n"
"v_mfma_f32_16x16x32_bf16 v[152:155], a[240:243], a[168:171], v[152:155]\n"
"v_mfma_f32_16x16x32_bf16 v[152:155], a[244:247], a[172:175], v[152:155]\n"
"v_mfma_f32_16x16x32_bf16 v[156:159], a[248:251], a[168:171], v[156:159]\n"
"v_mfma_f32_16x16x32_bf16 v[156:159], a[252:255], a[172:175], v[156:159]\n"
"s_waitcnt vmcnt(2) lgkmcnt(0)\n"
"v_mfma_f32_16x16x32_bf16 v[160:163], a[0:3], a[176:179], v[160:163]\n"
"ds_read_b128 a[168:171], v20 offset:6144\n"
"ds_read_b128 a[172:175], v21 offset:6144\n"
"v_mfma_f32_16x16x32_bf16 v[160:163], a[4:7], a[180:183], v[160:163]\n"
"v_mul_f32 v128, s64, v128\n"
"v_mul_f32 v129, s64, v129\n"
"v_mfma_f32_16x16x32_bf16 v[164:167], a[8:11], a[176:179], v[164:167]\n"
"v_mul_f32 v130, s64, v130\n"
"v_mul_f32 v131, s64, v131\n"
"v_mfma_f32_16x16x32_bf16 v[164:167], a[12:15], a[180:183], v[164:167]\n"
"v_mul_f32 v132, s64, v132\n"
"v_mul_f32 v133, s64, v133\n"
"v_mfma_f32_16x16x32_bf16 v[168:171], a[16:19], a[176:179], v[168:171]\n"
"v_mul_f32 v134, s64, v134\n"
"v_mul_f32 v135, s64, v135\n"
"v_mfma_f32_16x16x32_bf16 v[168:171], a[20:23], a[180:183], v[168:171]\n"
"v_cvt_pk_bf16_f32 v96, v128, v129\n"
"v_cvt_pk_bf16_f32 v97, v130, v131\n"
"v_mfma_f32_16x16x32_bf16 v[172:175], a[24:27], a[176:179], v[172:175]\n"
"v_mul_f32 v136, s64, v136\n"
"v_mul_f32 v137, s64, v137\n"
"v_mfma_f32_16x16x32_bf16 v[172:175], a[28:31], a[180:183], v[172:175]\n"
"v_mul_f32 v138, s64, v138\n"
"v_mul_f32 v139, s64, v139\n"
"v_mfma_f32_16x16x32_bf16 v[176:179], a[32:35], a[176:179], v[176:179]\n"
"v_cvt_pk_bf16_f32 v98, v132, v133\n"
"v_cvt_pk_bf16_f32 v99, v134, v135\n"
"v_mfma_f32_16x16x32_bf16 v[176:179], a[36:39], a[180:183], v[176:179]\n"
"v_mul_f32 v140, s64, v140\n"
"v_mul_f32 v141, s64, v141\n"
"v_mfma_f32_16x16x32_bf16 v[180:183], a[40:43], a[176:179], v[180:183]\n"
"v_mul_f32 v142, s64, v142\n"
"v_mul_f32 v143, s64, v143\n"
"v_mfma_f32_16x16x32_bf16 v[180:183], a[44:47], a[180:183], v[180:183]\n"
"v_cvt_pk_bf16_f32 v100, v136, v137\n"
"v_cvt_pk_bf16_f32 v101, v138, v139\n"
"v_mfma_f32_16x16x32_bf16 v[184:187], a[48:51], a[176:179], v[184:187]\n"
"v_mul_f32 v144, s64, v144\n"
"v_mul_f32 v145, s64, v145\n"
"v_mfma_f32_16x16x32_bf16 v[184:187], a[52:55], a[180:183], v[184:187]\n"
"v_mul_f32 v146, s64, v146\n"
"v_mul_f32 v147, s64, v147\n"
"v_mfma_f32_16x16x32_bf16 v[188:191], a[56:59], a[176:179], v[188:191]\n"
"v_cvt_pk_bf16_f32 v102, v140, v141\n"
"v_cvt_pk_bf16_f32 v103, v142, v143\n"
"v_mfma_f32_16x16x32_bf16 v[188:191], a[60:63], a[180:183], v[188:191]\n"
"s_waitcnt lgkmcnt(0)\n"
"v_mul_f32 v148, s64, v148\n"
"v_mfma_f32_16x16x32_bf16 v[160:163], a[192:195], a[184:187], v[160:163]\n"
"ds_read_b128 a[176:179], v18 offset:8192\n"
"ds_read_b128 a[180:183], v19 offset:8192\n"
"v_mfma_f32_16x16x32_bf16 v[160:163], a[196:199], a[188:191], v[160:163]\n"
"v_mul_f32 v149, s64, v149\n"
"v_mul_f32 v150, s64, v150\n"
"v_mfma_f32_16x16x32_bf16 v[164:167], a[200:203], a[184:187], v[164:167]\n"
"v_mul_f32 v151, s64, v151\n"
"v_cvt_pk_bf16_f32 v104, v144, v145\n"
"v_mfma_f32_16x16x32_bf16 v[164:167], a[204:207], a[188:191], v[164:167]\n"
"v_cvt_pk_bf16_f32 v105, v146, v147\n"
"v_mul_f32 v152, s64, v152\n"
"v_mfma_f32_16x16x32_bf16 v[168:171], a[208:211], a[184:187], v[168:171]\n"
"v_mul_f32 v153, s64, v153\n"
"v_mul_f32 v154, s64, v154\n"
"v_mfma_f32_16x16x32_bf16 v[168:171], a[212:215], a[188:191], v[168:171]\n"
"v_mul_f32 v155, s64, v155\n"
"v_cvt_pk_bf16_f32 v106, v148, v149\n"
"v_mfma_f32_16x16x32_bf16 v[172:175], a[216:219], a[184:187], v[172:175]\n"
"v_cvt_pk_bf16_f32 v107, v150, v151\n"
"v_mul_f32 v156, s64, v156\n"
"v_mfma_f32_16x16x32_bf16 v[172:175], a[220:223], a[188:191], v[172:175]\n"
"v_mul_f32 v157, s64, v157\n"
"v_mul_f32 v158, s64, v158\n"
"v_mfma_f32_16x16x32_bf16 v[176:179], a[224:227], a[184:187], v[176:179]\n"
"v_mul_f32 v159, s64, v159\n"
"v_cvt_pk_bf16_f32 v108, v152, v153\n"
"v_mfma_f32_16x16x32_bf16 v[176:179], a[228:231], a[188:191], v[176:179]\n"
"v_cvt_pk_bf16_f32 v109, v154, v155\n"
"v_cvt_pk_bf16_f32 v110, v156, v157\n"
"v_mfma_f32_16x16x32_bf16 v[180:183], a[232:235], a[184:187], v[180:183]\n"
"v_cvt_pk_bf16_f32 v111, v158, v159\n"
"v_mfma_f32_16x16x32_bf16 v[180:183], a[236:239], a[188:191], v[180:183]\n"
"v_mfma_f32_16x16x32_bf16 v[184:187], a[240:243], a[184:187], v[184:187]\n"
"v_mfma_f32_16x16x32_bf16 v[184:187], a[244:247], a[188:191], v[184:187]\n"
"v_mfma_f32_16x16x32_bf16 v[188:191], a[248:251], a[184:187], v[188:191]\n"
"v_mfma_f32_16x16x32_bf16 v[188:191], a[252:255], a[188:191], v[188:191]\n"
"s_waitcnt vmcnt(0) lgkmcnt(0)\n"
"s_barrier\n"
"v_mfma_f32_16x16x32_bf16 v[192:195], a[0:3], a[160:163], v[192:195]\n"
"ds_read_b128 a[184:187], v20 offset:8192\n"
"ds_read_b128 a[188:191], v21 offset:8192\n"
"v_mfma_f32_16x16x32_bf16 v[192:195], a[4:7], a[164:167], v[192:195]\n"
"v_mul_f32 v160, s64, v160\n"
"v_mul_f32 v161, s64, v161\n"
"v_mfma_f32_16x16x32_bf16 v[196:199], a[8:11], a[160:163], v[196:199]\n"
"ds_read_b128 a[64:67], v4\n"
"ds_read_b128 a[68:71], v5\n"
"v_mfma_f32_16x16x32_bf16 v[196:199], a[12:15], a[164:167], v[196:199]\n"
"ds_read_b128 a[72:75], v6\n"
"ds_read_b128 a[76:79], v7\n"
"v_mfma_f32_16x16x32_bf16 v[200:203], a[16:19], a[160:163], v[200:203]\n"
"ds_read_b128 a[80:83], v4 offset:4096\n"
"ds_read_b128 a[84:87], v5 offset:4096\n"
"v_mfma_f32_16x16x32_bf16 v[200:203], a[20:23], a[164:167], v[200:203]\n"
"ds_read_b128 a[88:91], v6 offset:4096\n"
"ds_read_b128 a[92:95], v7 offset:4096\n"
"v_mfma_f32_16x16x32_bf16 v[204:207], a[24:27], a[160:163], v[204:207]\n"
"v_mul_f32 v162, s64, v162\n"
"v_mul_f32 v163, s64, v163\n"
"v_mfma_f32_16x16x32_bf16 v[204:207], a[28:31], a[164:167], v[204:207]\n"
"v_mul_f32 v164, s64, v164\n"
"v_mul_f32 v165, s64, v165\n"
"v_mfma_f32_16x16x32_bf16 v[208:211], a[32:35], a[160:163], v[208:211]\n"
"v_mul_f32 v166, s64, v166\n"
"v_mul_f32 v167, s64, v167\n"
"v_mfma_f32_16x16x32_bf16 v[208:211], a[36:39], a[164:167], v[208:211]\n"
"v_cvt_pk_bf16_f32 v112, v160, v161\n"
"v_cvt_pk_bf16_f32 v113, v162, v163\n"
"v_mfma_f32_16x16x32_bf16 v[212:215], a[40:43], a[160:163], v[212:215]\n"
"v_mul_f32 v168, s64, v168\n"
"v_mul_f32 v169, s64, v169\n"
"v_mfma_f32_16x16x32_bf16 v[212:215], a[44:47], a[164:167], v[212:215]\n"
"v_mul_f32 v170, s64, v170\n"
"v_mul_f32 v171, s64, v171\n"
"v_mfma_f32_16x16x32_bf16 v[216:219], a[48:51], a[160:163], v[216:219]\n"
"v_cvt_pk_bf16_f32 v114, v164, v165\n"
"v_cvt_pk_bf16_f32 v115, v166, v167\n"
"v_mfma_f32_16x16x32_bf16 v[216:219], a[52:55], a[164:167], v[216:219]\n"
"v_mul_f32 v172, s64, v172\n"
"v_mul_f32 v173, s64, v173\n"
"v_mfma_f32_16x16x32_bf16 v[220:223], a[56:59], a[160:163], v[220:223]\n"
"v_mul_f32 v174, s64, v174\n"
"v_mul_f32 v175, s64, v175\n"
"v_mfma_f32_16x16x32_bf16 v[220:223], a[60:63], a[164:167], v[220:223]\n"
"s_waitcnt lgkmcnt(0)\n"
"v_cvt_pk_bf16_f32 v116, v168, v169\n"
"v_mfma_f32_16x16x32_bf16 v[192:195], a[192:195], a[168:171], v[192:195]\n"
"v_cvt_pk_bf16_f32 v117, v170, v171\n"
"v_mul_f32 v176, s64, v176\n"
"v_mfma_f32_16x16x32_bf16 v[36:39], a[64:67], v[80:83], 0\n"
"v_mul_f32 v177, s64, v177\n"
"v_mul_f32 v178, s64, v178\n"
"v_mfma_f32_16x16x32_bf16 v[36:39], a[68:71], v[84:87], v[36:39]\n"
"v_mul_f32 v179, s64, v179\n"
"v_cvt_pk_bf16_f32 v118, v172, v173\n"
"v_mfma_f32_16x16x32_bf16 v[36:39], a[72:75], v[88:91], v[36:39]\n"
"v_cvt_pk_bf16_f32 v119, v174, v175\n"
"v_mul_f32 v180, s64, v180\n"
"v_mfma_f32_16x16x32_bf16 v[36:39], a[76:79], v[92:95], v[36:39]\n"
"v_mul_f32 v181, s64, v181\n"
"v_mul_f32 v182, s64, v182\n"
"v_mfma_f32_16x16x32_bf16 v[192:195], a[196:199], a[172:175], v[192:195]\n"
"v_mul_f32 v183, s64, v183\n"
"v_cvt_pk_bf16_f32 v120, v176, v177\n"
"v_mfma_f32_16x16x32_bf16 v[40:43], a[64:67], v[96:99], 0\n"
"v_cvt_pk_bf16_f32 v121, v178, v179\n"
"v_mul_f32 v184, s64, v184\n"
"v_mfma_f32_16x16x32_bf16 v[40:43], a[68:71], v[100:103], v[40:43]\n"
"v_mul_f32 v185, s64, v185\n"
"v_mul_f32 v186, s64, v186\n"
"v_mfma_f32_16x16x32_bf16 v[40:43], a[72:75], v[104:107], v[40:43]\n"
"v_mul_f32 v187, s64, v187\n"
"v_cvt_pk_bf16_f32 v122, v180, v181\n"
"v_mfma_f32_16x16x32_bf16 v[40:43], a[76:79], v[108:111], v[40:43]\n"
"v_cvt_pk_bf16_f32 v123, v182, v183\n"
"v_mul_f32 v188, s64, v188\n"
"v_mfma_f32_16x16x32_bf16 v[196:199], a[200:203], a[168:171], v[196:199]\n"
"v_mul_f32 v189, s64, v189\n"
"v_max3_f32 v56, v36, v37, v38\n"
"v_mfma_f32_16x16x32_bf16 v[196:199], a[204:207], a[172:175], v[196:199]\n"
"v_mul_f32 v190, s64, v190\n"
"v_mul_f32 v191, s64, v191\n"
"v_mfma_f32_16x16x32_bf16 v[200:203], a[208:211], a[168:171], v[200:203]\n"
"v_cvt_pk_bf16_f32 v124, v184, v185\n"
"v_max3_f32 v57, v40, v41, v42\n"
"v_mfma_f32_16x16x32_bf16 v[200:203], a[212:215], a[172:175], v[200:203]\n"
"v_cvt_pk_bf16_f32 v125, v186, v187\n"
"v_max_f32 v56, v56, v39\n"
"v_mfma_f32_16x16x32_bf16 v[204:207], a[216:219], a[168:171], v[204:207]\n"
"v_cvt_pk_bf16_f32 v126, v188, v189\n"
"v_max_f32 v57, v57, v43\n"
"v_mfma_f32_16x16x32_bf16 v[204:207], a[220:223], a[172:175], v[204:207]\n"
"v_cvt_pk_bf16_f32 v127, v190, v191\n"
"v_mov_b32 v72, v56\n"
"v_mfma_f32_16x16x32_bf16 v[208:211], a[224:227], a[168:171], v[208:211]\n"
"v_mov_b32 v73, v57\n"
"v_permlane32_swap_b32 v56, v72\n"
"v_mfma_f32_16x16x32_bf16 v[44:47], a[64:67], v[112:115], 0\n"
"v_permlane32_swap_b32 v57, v73\n"
"v_max_f32 v56, v56, v72\n"
"v_mfma_f32_16x16x32_bf16 v[44:47], a[68:71], v[116:119], v[44:47]\n"
"v_max_f32 v57, v57, v73\n"
"v_mov_b32 v72, v56\n"
"v_mfma_f32_16x16x32_bf16 v[44:47], a[72:75], v[120:123], v[44:47]\n"
"v_mov_b32 v73, v57\n"
"v_permlane16_swap_b32 v56, v72\n"
"v_mfma_f32_16x16x32_bf16 v[44:47], a[76:79], v[124:127], v[44:47]\n"
"v_permlane16_swap_b32 v57, v73\n"
"v_max_f32 v56, v56, v72\n"
"v_mfma_f32_16x16x32_bf16 v[208:211], a[228:231], a[172:175], v[208:211]\n"
"v_max_f32 v57, v57, v73\n"
"v_sub_f32 v36, 0, v56\n"
"v_mfma_f32_16x16x32_bf16 v[212:215], a[232:235], a[168:171], v[212:215]\n"
"v_sub_f32 v37, 0, v56\n"
"v_sub_f32 v38, 0, v56\n"
"v_mfma_f32_16x16x32_bf16 v[212:215], a[236:239], a[172:175], v[212:215]\n"
"v_sub_f32 v39, 0, v56\n"
"v_sub_f32 v40, 0, v57\n"
"v_mfma_f32_16x16x32_bf16 v[216:219], a[240:243], a[168:171], v[216:219]\n"
"v_sub_f32 v41, 0, v57\n"
"v_sub_f32 v42, 0, v57\n"
"v_mfma_f32_16x16x32_bf16 v[216:219], a[244:247], a[172:175], v[216:219]\n"
"v_sub_f32 v43, 0, v57\n"
"v_max3_f32 v58, v44, v45, v46\n"
"v_mfma_f32_16x16x32_bf16 v[220:223], a[248:251], a[168:171], v[220:223]\n"
"v_max_f32 v58, v58, v47\n"
"v_mov_b32 v74, v58\n"
"v_mfma_f32_16x16x32_bf16 v[220:223], a[252:255], a[172:175], v[220:223]\n"
"v_mfma_f32_16x16x32_bf16 v[224:227], a[0:3], a[176:179], v[224:227]\n"
"v_mul_f32 v192, s64, v192\n"
"v_permlane32_swap_b32 v58, v74\n"
"v_mfma_f32_16x16x32_bf16 v[224:227], a[4:7], a[180:183], v[224:227]\n"
"v_mul_f32 v193, s64, v193\n"
"v_max_f32 v58, v58, v74\n"
"v_mfma_f32_16x16x32_bf16 v[228:231], a[8:11], a[176:179], v[228:231]\n"
"v_mul_f32 v194, s64, v194\n"
"v_mov_b32 v74, v58\n"
"v_mfma_f32_16x16x32_bf16 v[228:231], a[12:15], a[180:183], v[228:231]\n"
"v_mul_f32 v195, s64, v195\n"
"v_permlane16_swap_b32 v58, v74\n"
"v_mfma_f32_16x16x32_bf16 v[232:235], a[16:19], a[176:179], v[232:235]\n"
"v_mul_f32 v196, s64, v196\n"
"v_max_f32 v58, v58, v74\n"
"v_mfma_f32_16x16x32_bf16 v[232:235], a[20:23], a[180:183], v[232:235]\n"
"v_mul_f32 v197, s64, v197\n"
"v_sub_f32 v44, 0, v58\n"
"v_mfma_f32_16x16x32_bf16 v[236:239], a[24:27], a[176:179], v[236:239]\n"
"v_mul_f32 v198, s64, v198\n"
"v_sub_f32 v45, 0, v58\n"
"v_mfma_f32_16x16x32_bf16 v[236:239], a[28:31], a[180:183], v[236:239]\n"
"v_mul_f32 v199, s64, v199\n"
"v_sub_f32 v46, 0, v58\n"
"v_mfma_f32_16x16x32_bf16 v[240:243], a[32:35], a[176:179], v[240:243]\n"
"v_cvt_pk_bf16_f32 v128, v192, v193\n"
"v_sub_f32 v47, 0, v58\n"
"v_mfma_f32_16x16x32_bf16 v[240:243], a[36:39], a[180:183], v[240:243]\n"
"v_cvt_pk_bf16_f32 v129, v194, v195\n"
"v_mul_f32 v200, s64, v200\n"
"v_mfma_f32_16x16x32_bf16 v[244:247], a[40:43], a[176:179], v[244:247]\n"
"v_mul_f32 v201, s64, v201\n"
"v_mul_f32 v202, s64, v202\n"
"v_mfma_f32_16x16x32_bf16 v[244:247], a[44:47], a[180:183], v[244:247]\n"
"v_mul_f32 v203, s64, v203\n"
"v_cvt_pk_bf16_f32 v130, v196, v197\n"
"v_mfma_f32_16x16x32_bf16 v[248:251], a[48:51], a[176:179], v[248:251]\n"
"v_cvt_pk_bf16_f32 v131, v198, v199\n"
"v_mul_f32 v204, s64, v204\n"
"v_mfma_f32_16x16x32_bf16 v[248:251], a[52:55], a[180:183], v[248:251]\n"
"v_mul_f32 v205, s64, v205\n"
"v_mul_f32 v206, s64, v206\n"
"v_mfma_f32_16x16x32_bf16 v[252:255], a[56:59], a[176:179], v[252:255]\n"
"v_mul_f32 v207, s64, v207\n"
"v_cvt_pk_bf16_f32 v132, v200, v201\n"
"v_mfma_f32_16x16x32_bf16 v[252:255], a[60:63], a[180:183], v[252:255]\n"
"v_cvt_pk_bf16_f32 v133, v202, v203\n"
"v_mul_f32 v208, s64, v208\n"
"v_mfma_f32_16x16x32_bf16 v[224:227], a[192:195], a[184:187], v[224:227]\n"
"v_mul_f32 v209, s64, v209\n"
"v_mul_f32 v210, s64, v210\n"
"v_mfma_f32_16x16x32_bf16 v[224:227], a[196:199], a[188:191], v[224:227]\n"
"v_mul_f32 v211, s64, v211\n"
"v_cvt_pk_bf16_f32 v134, v204, v205\n"
"v_mfma_f32_16x16x32_bf16 v[228:231], a[200:203], a[184:187], v[228:231]\n"
"v_cvt_pk_bf16_f32 v135, v206, v207\n"
"v_mul_f32 v212, s64, v212\n"
"v_mfma_f32_16x16x32_bf16 v[228:231], a[204:207], a[188:191], v[228:231]\n"
"v_mul_f32 v213, s64, v213\n"
"v_mul_f32 v214, s64, v214\n"
"v_mfma_f32_16x16x32_bf16 v[232:235], a[208:211], a[184:187], v[232:235]\n"
"v_mul_f32 v215, s64, v215\n"
"v_cvt_pk_bf16_f32 v136, v208, v209\n"
"v_mfma_f32_16x16x32_bf16 v[232:235], a[212:215], a[188:191], v[232:235]\n"
"v_cvt_pk_bf16_f32 v137, v210, v211\n"
"v_mul_f32 v216, s64, v216\n"
"v_mfma_f32_16x16x32_bf16 v[236:239], a[216:219], a[184:187], v[236:239]\n"
"v_mul_f32 v217, s64, v217\n"
"v_mul_f32 v218, s64, v218\n"
"v_mfma_f32_16x16x32_bf16 v[236:239], a[220:223], a[188:191], v[236:239]\n"
"v_mul_f32 v219, s64, v219\n"
"v_cvt_pk_bf16_f32 v138, v212, v213\n"
"v_mfma_f32_16x16x32_bf16 v[240:243], a[224:227], a[184:187], v[240:243]\n"
"v_cvt_pk_bf16_f32 v139, v214, v215\n"
"v_mul_f32 v220, s64, v220\n"
"v_mfma_f32_16x16x32_bf16 v[240:243], a[228:231], a[188:191], v[240:243]\n"
"v_mul_f32 v221, s64, v221\n"
"v_mul_f32 v222, s64, v222\n"
"v_mfma_f32_16x16x32_bf16 v[244:247], a[232:235], a[184:187], v[244:247]\n"
"v_mul_f32 v223, s64, v223\n"
"v_cvt_pk_bf16_f32 v140, v216, v217\n"
"v_mfma_f32_16x16x32_bf16 v[244:247], a[236:239], a[188:191], v[244:247]\n"
"v_cvt_pk_bf16_f32 v141, v218, v219\n"
"v_cvt_pk_bf16_f32 v142, v220, v221\n"
"v_mfma_f32_16x16x32_bf16 v[248:251], a[240:243], a[184:187], v[248:251]\n"
"v_cvt_pk_bf16_f32 v143, v222, v223\n"
"v_mfma_f32_16x16x32_bf16 v[248:251], a[244:247], a[188:191], v[248:251]\n"
"v_mfma_f32_16x16x32_bf16 v[48:51], a[64:67], v[128:131], 0\n"
"v_mfma_f32_16x16x32_bf16 v[48:51], a[68:71], v[132:135], v[48:51]\n"
"v_mfma_f32_16x16x32_bf16 v[48:51], a[72:75], v[136:139], v[48:51]\n"
"v_mfma_f32_16x16x32_bf16 v[48:51], a[76:79], v[140:143], v[48:51]\n"
"v_mfma_f32_16x16x32_bf16 v[252:255], a[248:251], a[184:187], v[252:255]\n"
"v_mfma_f32_16x16x32_bf16 v[252:255], a[252:255], a[188:191], v[252:255]\n"
"v_pk_mul_f32 v[224:225], v[224:225], s[64:65]\n"
"v_pk_mul_f32 v[226:227], v[226:227], s[64:65]\n"
"v_pk_mul_f32 v[228:229], v[228:229], s[64:65]\n"
"v_pk_mul_f32 v[230:231], v[230:231], s[64:65]\n"
"v_pk_mul_f32 v[232:233], v[232:233], s[64:65]\n"
"v_pk_mul_f32 v[234:235], v[234:235], s[64:65]\n"
"v_pk_mul_f32 v[236:237], v[236:237], s[64:65]\n"
"v_pk_mul_f32 v[238:239], v[238:239], s[64:65]\n"
"v_pk_mul_f32 v[240:241], v[240:241], s[64:65]\n"
"v_pk_mul_f32 v[242:243], v[242:243], s[64:65]\n"
"v_pk_mul_f32 v[244:245], v[244:245], s[64:65]\n"
"v_pk_mul_f32 v[246:247], v[246:247], s[64:65]\n"
"v_pk_mul_f32 v[248:249], v[248:249], s[64:65]\n"
"v_pk_mul_f32 v[250:251], v[250:251], s[64:65]\n"
"v_pk_mul_f32 v[252:253], v[252:253], s[64:65]\n"
"v_pk_mul_f32 v[254:255], v[254:255], s[64:65]\n"
"v_mfma_f32_16x16x32_bf16 v[64:67], a[64:67], v[80:83], v[36:39]\n"
"v_cvt_pk_bf16_f32 v144, v224, v225\n"
"v_cvt_pk_bf16_f32 v145, v226, v227\n"
"v_mfma_f32_16x16x32_bf16 v[64:67], a[68:71], v[84:87], v[64:67]\n"
"v_cvt_pk_bf16_f32 v146, v228, v229\n"
"v_cvt_pk_bf16_f32 v147, v230, v231\n"
"v_mfma_f32_16x16x32_bf16 v[64:67], a[72:75], v[88:91], v[64:67]\n"
"v_cvt_pk_bf16_f32 v148, v232, v233\n"
"v_cvt_pk_bf16_f32 v149, v234, v235\n"
"v_mfma_f32_16x16x32_bf16 v[64:67], a[76:79], v[92:95], v[64:67]\n"
"v_cvt_pk_bf16_f32 v150, v236, v237\n"
"v_cvt_pk_bf16_f32 v151, v238, v239\n"
"v_mfma_f32_16x16x32_bf16 v[68:71], a[80:83], v[80:83], v[36:39]\n"
"v_cvt_pk_bf16_f32 v152, v240, v241\n"
"v_cvt_pk_bf16_f32 v153, v242, v243\n"
"v_mfma_f32_16x16x32_bf16 v[68:71], a[84:87], v[84:87], v[68:71]\n"
"v_cvt_pk_bf16_f32 v154, v244, v245\n"
"v_cvt_pk_bf16_f32 v155, v246, v247\n"
"v_mfma_f32_16x16x32_bf16 v[68:71], a[88:91], v[88:91], v[68:71]\n"
"v_cvt_pk_bf16_f32 v156, v248, v249\n"
"v_cvt_pk_bf16_f32 v157, v250, v251\n"
"v_mfma_f32_16x16x32_bf16 v[68:71], a[92:95], v[92:95], v[68:71]\n"
"v_cvt_pk_bf16_f32 v158, v252, v253\n"
"v_cvt_pk_bf16_f32 v159, v254, v255\n"
"s_barrier\n"
"v_mfma_f32_16x16x32_bf16 v[52:55], a[64:67], v[144:147], 0\n"
"ds_read_b64_tr_b16 a[128:129], v8\n"
"ds_read_b64_tr_b16 a[130:131], v8 offset:4096\n"
"v_mfma_f32_16x16x32_bf16 v[52:55], a[68:71], v[148:151], v[52:55]\n"
"ds_read_b64_tr_b16 a[132:133], v9\n"
"ds_read_b64_tr_b16 a[134:135], v9 offset:4096\n"
"v_mfma_f32_16x16x32_bf16 v[52:55], a[72:75], v[152:155], v[52:55]\n"
"ds_read_b64_tr_b16 a[136:137], v8 offset:512\n"
"ds_read_b64_tr_b16 a[138:139], v8 offset:4608\n"
"v_mfma_f32_16x16x32_bf16 v[52:55], a[76:79], v[156:159], v[52:55]\n"
"ds_read_b64_tr_b16 a[140:141], v9 offset:512\n"
"ds_read_b64_tr_b16 a[142:143], v9 offset:4608\n"
"ds_read_b64_tr_b16 a[144:145], v8 offset:1024\n"
"ds_read_b64_tr_b16 a[146:147], v8 offset:5120\n"
"ds_read_b64_tr_b16 a[148:149], v9 offset:1024\n"
"ds_read_b64_tr_b16 a[150:151], v9 offset:5120\n"
"ds_read_b64_tr_b16 a[152:153], v8 offset:1536\n"
"ds_read_b64_tr_b16 a[154:155], v8 offset:5632\n"
"ds_read_b64_tr_b16 a[156:157], v9 offset:1536\n"
"ds_read_b64_tr_b16 a[158:159], v9 offset:5632\n"
"ds_read_b128 a[96:99], v4 offset:8192\n"
"ds_read_b128 a[100:103], v5 offset:8192\n"
"ds_read_b128 a[104:107], v6 offset:8192\n"
"ds_read_b128 a[108:111], v7 offset:8192\n"
"ds_read_b128 a[112:115], v4 offset:12288\n"
"ds_read_b128 a[116:119], v5 offset:12288\n"
"ds_read_b128 a[120:123], v6 offset:12288\n"
"ds_read_b128 a[124:127], v7 offset:12288\n"
"v_max3_f32 v60, v52, v53, v54\n"
"v_max3_f32 v59, v48, v49, v50\n"
"v_max_f32 v60, v60, v55\n"
"v_max_f32 v59, v59, v51\n"
"v_mov_b32 v76, v60\n"
"v_mov_b32 v75, v59\n"
"s_nop 0\n"
"v_permlane32_swap_b32 v60, v76\n"
"v_permlane32_swap_b32 v59, v75\n"
"v_max_f32 v60, v60, v76\n"
"v_max_f32 v59, v59, v75\n"
"v_mov_b32 v76, v60\n"
"v_mov_b32 v75, v59\n"
"s_nop 0\n"
"v_permlane16_swap_b32 v60, v76\n"
"v_permlane16_swap_b32 v59, v75\n"
"v_max_f32 v60, v60, v76\n"
"v_max_f32 v59, v59, v75\n"
"v_sub_f32 v52, 0, v60\n"
"v_sub_f32 v48, 0, v59\n"
"v_sub_f32 v53, 0, v60\n"
"v_sub_f32 v49, 0, v59\n"
"v_sub_f32 v54, 0, v60\n"
"v_sub_f32 v50, 0, v59\n"
"v_sub_f32 v55, 0, v60\n"
"v_sub_f32 v51, 0, v59\n"
".Lmha_att5:\n"
"v_mfma_f32_16x16x32_bf16 v[72:75], a[64:67], v[96:99], v[40:43]\n"
"v_exp_f32 v64, v64\n"
"v_mfma_f32_16x16x32_bf16 v[72:75], a[68:71], v[100:103], v[72:75]\n"
"v_mfma_f32_16x16x32_bf16 v[72:75], a[72:75], v[104:107], v[72:75]\n"
"v_exp_f32 v65, v65\n"
"v_mfma_f32_16x16x32_bf16 v[72:75], a[76:79], v[108:111], v[72:75]\n"
"v_add_f32 v29, v29, v64\n"
"v_exp_f32 v66, v66\n"
"v_cvt_pk_bf16_f32 v56, v64, v65\n"
"v_add_f32 v29, v29, v65\n"
"v_exp_f32 v67, v67\n"
"v_add_f32 v29, v29, v66\n"
"v_mfma_f32_16x16x32_bf16 v[76:79], a[80:83], v[96:99], v[40:43]\n"
"v_exp_f32 v68, v68\n"
"v_mfma_f32_16x16x32_bf16 v[76:79], a[84:87], v[100:103], v[76:79]\n"
"v_cvt_pk_bf16_f32 v57, v66, v67\n"
"v_add_f32 v29, v29, v67\n"
"v_mfma_f32_16x16x32_bf16 v[76:79], a[88:91], v[104:107], v[76:79]\n"
"v_exp_f32 v69, v69\n"
"v_mfma_f32_16x16x32_bf16 v[76:79], a[92:95], v[108:111], v[76:79]\n"
"v_add_f32 v29, v29, v68\n"
"v_exp_f32 v70, v70\n"
"v_cvt_pk_bf16_f32 v58, v68, v69\n"
"v_add_f32 v29, v29, v69\n"
"v_exp_f32 v71, v71\n"
"v_add_f32 v29, v29, v70\n"
"v_mfma_f32_16x16x32_bf16 v[64:67], a[64:67], v[112:115], v[44:47]\n"
"v_exp_f32 v72, v72\n"
"v_mfma_f32_16x16x32_bf16 v[64:67], a[68:71], v[116:119], v[64:67]\n"
"v_cvt_pk_bf16_f32 v59, v70, v71\n"
"v_add_f32 v29, v29, v71\n"
"v_mfma_f32_16x16x32_bf16 v[64:67], a[72:75], v[120:123], v[64:67]\n"
"v_exp_f32 v73, v73\n"
"v_mfma_f32_16x16x32_bf16 v[64:67], a[76:79], v[124:127], v[64:67]\n"
"s_waitcnt lgkmcnt(0)\n"
"s_barrier\n"
"v_mfma_f32_16x16x32_bf16 v[160:163], a[128:131], v[56:59], 0\n"
"v_exp_f32 v74, v74\n"
"v_mfma_f32_16x16x32_bf16 v[164:167], a[132:135], v[56:59], 0\n"
"ds_read_b64_tr_b16 a[160:161], v8 offset:8192\n"
"v_cvt_pk_bf16_f32 v60, v72, v73\n"
"v_mfma_f32_16x16x32_bf16 v[168:171], a[136:139], v[56:59], 0\n"
"v_exp_f32 v75, v75\n"
"v_mfma_f32_16x16x32_bf16 v[172:175], a[140:143], v[56:59], 0\n"
"ds_read_b64_tr_b16 a[162:163], v8 offset:12288\n"
"ds_read_b64_tr_b16 a[164:165], v9 offset:8192\n"
"v_mfma_f32_16x16x32_bf16 v[68:71], a[80:83], v[112:115], v[44:47]\n"
"v_exp_f32 v76, v76\n"
"v_mfma_f32_16x16x32_bf16 v[68:71], a[84:87], v[116:119], v[68:71]\n"
"ds_read_b64_tr_b16 a[166:167], v9 offset:12288\n"
"v_cvt_pk_bf16_f32 v61, v74, v75\n"
"v_mfma_f32_16x16x32_bf16 v[68:71], a[88:91], v[120:123], v[68:71]\n"
"v_exp_f32 v77, v77\n"
"v_mfma_f32_16x16x32_bf16 v[68:71], a[92:95], v[124:127], v[68:71]\n"
"ds_read_b64_tr_b16 a[168:169], v8 offset:8704\n"
"ds_read_b64_tr_b16 a[170:171], v8 offset:12800\n"
"v_mfma_f32_16x16x32_bf16 v[176:179], a[144:147], v[56:59], 0\n"
"v_exp_f32 v78, v78\n"
"v_mfma_f32_16x16x32_bf16 v[180:183], a[148:151], v[56:59], 0\n"
"ds_read_b64_tr_b16 a[172:173], v9 offset:8704\n"
"v_cvt_pk_bf16_f32 v62, v76, v77\n"
"v_mfma_f32_16x16x32_bf16 v[184:187], a[152:155], v[56:59], 0\n"
"v_exp_f32 v79, v79\n"
"v_mfma_f32_16x16x32_bf16 v[188:191], a[156:159], v[56:59], 0\n"
"ds_read_b64_tr_b16 a[174:175], v9 offset:12800\n"
"ds_read_b64_tr_b16 a[176:177], v8 offset:9216\n"
"v_mfma_f32_16x16x32_bf16 v[72:75], a[64:67], v[128:131], v[48:51]\n"
"v_exp_f32 v64, v64\n"
"v_mfma_f32_16x16x32_bf16 v[72:75], a[68:71], v[132:135], v[72:75]\n"
"ds_read_b64_tr_b16 a[178:179], v8 offset:13312\n"
"v_cvt_pk_bf16_f32 v63, v78, v79\n"
"v_mfma_f32_16x16x32_bf16 v[72:75], a[72:75], v[136:139], v[72:75]\n"
"v_exp_f32 v65, v65\n"
"v_mfma_f32_16x16x32_bf16 v[72:75], a[76:79], v[140:143], v[72:75]\n"
"ds_read_b64_tr_b16 a[180:181], v9 offset:9216\n"
"v_add_f32 v31, v31, v64\n"
"v_mfma_f32_16x16x32_bf16 v[192:195], a[128:131], v[60:63], 0\n"
"v_exp_f32 v66, v66\n"
"v_mfma_f32_16x16x32_bf16 v[196:199], a[132:135], v[60:63], 0\n"
"v_cvt_pk_bf16_f32 v56, v64, v65\n"
"v_add_f32 v31, v31, v65\n"
"v_mfma_f32_16x16x32_bf16 v[200:203], a[136:139], v[60:63], 0\n"
"v_exp_f32 v67, v67\n"
"v_mfma_f32_16x16x32_bf16 v[204:207], a[140:143], v[60:63], 0\n"
"ds_read_b64_tr_b16 a[182:183], v9 offset:13312\n"
"v_add_f32 v31, v31, v66\n"
"v_mfma_f32_16x16x32_bf16 v[18:21], v[10:13], v[60:63], 0\n"
"ds_read_b64_tr_b16 a[184:185], v8 offset:9728\n"
"ds_read_b64_tr_b16 a[186:187], v8 offset:13824\n"
"v_mfma_f32_16x16x32_bf16 v[76:79], a[80:83], v[128:131], v[48:51]\n"
"v_exp_f32 v68, v68\n"
"v_mfma_f32_16x16x32_bf16 v[76:79], a[84:87], v[132:135], v[76:79]\n"
"v_cvt_pk_bf16_f32 v57, v66, v67\n"
"v_add_f32 v31, v31, v67\n"
"v_mfma_f32_16x16x32_bf16 v[76:79], a[88:91], v[136:139], v[76:79]\n"
"v_exp_f32 v69, v69\n"
"v_mfma_f32_16x16x32_bf16 v[76:79], a[92:95], v[140:143], v[76:79]\n"
"ds_read_b64_tr_b16 a[188:189], v9 offset:9728\n"
"v_add_f32 v31, v31, v68\n"
"v_mfma_f32_16x16x32_bf16 v[208:211], a[144:147], v[60:63], 0\n"
"v_exp_f32 v70, v70\n"
"v_mfma_f32_16x16x32_bf16 v[212:215], a[148:151], v[60:63], 0\n"
"v_cvt_pk_bf16_f32 v58, v68, v69\n"
"v_add_f32 v31, v31, v69\n"
"v_mfma_f32_16x16x32_bf16 v[216:219], a[152:155], v[60:63], 0\n"
"v_exp_f32 v71, v71\n"
"v_mfma_f32_16x16x32_bf16 v[220:223], a[156:159], v[60:63], 0\n"
"ds_read_b64_tr_b16 a[190:191], v9 offset:13824\n"
"v_add_f32 v31, v31, v70\n"
"v_mfma_f32_16x16x32_bf16 v[64:67], a[64:67], v[144:147], v[52:55]\n"
"v_exp_f32 v72, v72\n"
"v_mfma_f32_16x16x32_bf16 v[64:67], a[68:71], v[148:151], v[64:67]\n"
"v_cvt_pk_bf16_f32 v59, v70, v71\n"
"v_add_f32 v31, v31, v71\n"
"v_mfma_f32_16x16x32_bf16 v[64:67], a[72:75], v[152:155], v[64:67]\n"
"v_exp_f32 v73, v73\n"
"v_mfma_f32_16x16x32_bf16 v[64:67], a[76:79], v[156:159], v[64:67]\n"
"ds_read_b128 a[64:67], v4 offset:16384\n"
"v_add_f32 v34, v34, v72\n"
"v_mfma_f32_16x16x32_bf16 v[224:227], a[128:131], v[56:59], 0\n"
"v_exp_f32 v74, v74\n"
"v_mfma_f32_16x16x32_bf16 v[228:231], a[132:135], v[56:59], 0\n"
"v_cvt_pk_bf16_f32 v60, v72, v73\n"
"v_add_f32 v34, v34, v73\n"
"v_mfma_f32_16x16x32_bf16 v[232:235], a[136:139], v[56:59], 0\n"
"v_exp_f32 v75, v75\n"
"v_mfma_f32_16x16x32_bf16 v[236:239], a[140:143], v[56:59], 0\n"
"ds_read_b128 a[68:71], v5 offset:16384\n"
"v_add_f32 v34, v34, v74\n"
"v_mfma_f32_16x16x32_bf16 v[68:71], a[80:83], v[144:147], v[52:55]\n"
"v_exp_f32 v76, v76\n"
"v_mfma_f32_16x16x32_bf16 v[68:71], a[84:87], v[148:151], v[68:71]\n"
"v_cvt_pk_bf16_f32 v61, v74, v75\n"
"v_add_f32 v34, v34, v75\n"
"v_mfma_f32_16x16x32_bf16 v[68:71], a[88:91], v[152:155], v[68:71]\n"
"v_exp_f32 v77, v77\n"
"v_mfma_f32_16x16x32_bf16 v[68:71], a[92:95], v[156:159], v[68:71]\n"
"ds_read_b128 a[72:75], v6 offset:16384\n"
"v_add_f32 v34, v34, v76\n"
"v_mfma_f32_16x16x32_bf16 v[240:243], a[144:147], v[56:59], 0\n"
"v_exp_f32 v78, v78\n"
"v_mfma_f32_16x16x32_bf16 v[244:247], a[148:151], v[56:59], 0\n"
"v_cvt_pk_bf16_f32 v62, v76, v77\n"
"v_add_f32 v34, v34, v77\n"
"v_mfma_f32_16x16x32_bf16 v[248:251], a[152:155], v[56:59], 0\n"
"v_exp_f32 v79, v79\n"
"v_mfma_f32_16x16x32_bf16 v[252:255], a[156:159], v[56:59], 0\n"
"ds_read_b128 a[76:79], v7 offset:16384\n"
"v_add_f32 v34, v34, v78\n"
"v_mfma_f32_16x16x32_bf16 v[72:75], a[96:99], v[80:83], v[36:39]\n"
"v_exp_f32 v64, v64\n"
"v_mfma_f32_16x16x32_bf16 v[72:75], a[100:103], v[84:87], v[72:75]\n"
"v_cvt_pk_bf16_f32 v63, v78, v79\n"
"v_add_f32 v34, v34, v79\n"
"v_mfma_f32_16x16x32_bf16 v[72:75], a[104:107], v[88:91], v[72:75]\n"
"v_exp_f32 v65, v65\n"
"v_mfma_f32_16x16x32_bf16 v[72:75], a[108:111], v[92:95], v[72:75]\n"
"ds_read_b128 a[80:83], v4 offset:20480\n"
"v_add_f32 v35, v35, v64\n"
"v_mfma_f32_16x16x32_bf16 a[192:195], a[128:131], v[60:63], 0\n"
"v_exp_f32 v66, v66\n"
"v_mfma_f32_16x16x32_bf16 a[196:199], a[132:135], v[60:63], 0\n"
"v_cvt_pk_bf16_f32 v56, v64, v65\n"
"v_add_f32 v35, v35, v65\n"
"v_mfma_f32_16x16x32_bf16 a[200:203], a[136:139], v[60:63], 0\n"
"v_exp_f32 v67, v67\n"
"v_mfma_f32_16x16x32_bf16 a[204:207], a[140:143], v[60:63], 0\n"
"ds_read_b128 a[84:87], v5 offset:20480\n"
"v_add_f32 v35, v35, v66\n"
"v_mfma_f32_16x16x32_bf16 v[76:79], a[112:115], v[80:83], v[36:39]\n"
"v_exp_f32 v68, v68\n"
"v_mfma_f32_16x16x32_bf16 v[76:79], a[116:119], v[84:87], v[76:79]\n"
"v_cvt_pk_bf16_f32 v57, v66, v67\n"
"v_add_f32 v35, v35, v67\n"
"v_mfma_f32_16x16x32_bf16 v[76:79], a[120:123], v[88:91], v[76:79]\n"
"v_exp_f32 v69, v69\n"
"v_mfma_f32_16x16x32_bf16 v[76:79], a[124:127], v[92:95], v[76:79]\n"
"ds_read_b128 a[88:91], v6 offset:20480\n"
"v_add_f32 v35, v35, v68\n"
"v_mfma_f32_16x16x32_bf16 a[208:211], a[144:147], v[60:63], 0\n"
"v_exp_f32 v70, v70\n"
"v_mfma_f32_16x16x32_bf16 a[212:215], a[148:151], v[60:63], 0\n"
"v_cvt_pk_bf16_f32 v58, v68, v69\n"
"v_add_f32 v35, v35, v69\n"
"v_mfma_f32_16x16x32_bf16 a[216:219], a[152:155], v[60:63], 0\n"
"v_exp_f32 v71, v71\n"
"v_mfma_f32_16x16x32_bf16 a[220:223], a[156:159], v[60:63], 0\n"
"ds_read_b128 a[92:95], v7 offset:20480\n"
"v_add_f32 v35, v35, v70\n"
"v_mfma_f32_16x16x32_bf16 v[64:67], a[96:99], v[96:99], v[40:43]\n"
"v_exp_f32 v72, v72\n"
"v_mfma_f32_16x16x32_bf16 v[64:67], a[100:103], v[100:103], v[64:67]\n"
"v_cvt_pk_bf16_f32 v59, v70, v71\n"
"v_add_f32 v35, v35, v71\n"
"v_mfma_f32_16x16x32_bf16 v[64:67], a[104:107], v[104:107], v[64:67]\n"
"v_exp_f32 v73, v73\n"
"v_mfma_f32_16x16x32_bf16 v[64:67], a[108:111], v[108:111], v[64:67]\n"
"v_add_f32 v29, v29, v72\n"
"v_mfma_f32_16x16x32_bf16 a[224:227], a[128:131], v[56:59], 0\n"
"v_exp_f32 v74, v74\n"
"v_mfma_f32_16x16x32_bf16 a[228:231], a[132:135], v[56:59], 0\n"
"v_cvt_pk_bf16_f32 v60, v72, v73\n"
"v_add_f32 v29, v29, v73\n"
"v_mfma_f32_16x16x32_bf16 a[232:235], a[136:139], v[56:59], 0\n"
"v_exp_f32 v75, v75\n"
"v_mfma_f32_16x16x32_bf16 a[236:239], a[140:143], v[56:59], 0\n"
"v_add_f32 v29, v29, v74\n"
"v_mfma_f32_16x16x32_bf16 v[68:71], a[112:115], v[96:99], v[40:43]\n"
"v_exp_f32 v76, v76\n"
"v_mfma_f32_16x16x32_bf16 v[68:71], a[116:119], v[100:103], v[68:71]\n"
"v_cvt_pk_bf16_f32 v61, v74, v75\n"
"v_add_f32 v29, v29, v75\n"
"v_mfma_f32_16x16x32_bf16 v[68:71], a[120:123], v[104:107], v[68:71]\n"
"v_exp_f32 v77, v77\n"
"v_mfma_f32_16x16x32_bf16 v[68:71], a[124:127], v[108:111], v[68:71]\n"
"v_add_f32 v29, v29, v76\n"
"v_mfma_f32_16x16x32_bf16 a[240:243], a[144:147], v[56:59], 0\n"
"v_exp_f32 v78, v78\n"
"v_mfma_f32_16x16x32_bf16 a[244:247], a[148:151], v[56:59], 0\n"
"v_cvt_pk_bf16_f32 v62, v76, v77\n"
"v_add_f32 v29, v29, v77\n"
"v_mfma_f32_16x16x32_bf16 a[248:251], a[152:155], v[56:59], 0\n"
"v_exp_f32 v79, v79\n"
"v_mfma_f32_16x16x32_bf16 a[252:255], a[156:159], v[56:59], 0\n"
"v_add_f32 v29, v29, v78\n"
"v_mfma_f32_16x16x32_bf16 v[72:75], a[96:99], v[112:115], v[44:47]\n"
"v_exp_f32 v64, v64\n"
"v_mfma_f32_16x16x32_bf16 v[72:75], a[100:103], v[116:119], v[72:75]\n"
"v_cvt_pk_bf16_f32 v63, v78, v79\n"
"v_add_f32 v29, v29, v79\n"
"v_mfma_f32_16x16x32_bf16 v[72:75], a[104:107], v[120:123], v[72:75]\n"
"v_exp_f32 v65, v65\n"
"v_mfma_f32_16x16x32_bf16 v[72:75], a[108:111], v[124:127], v[72:75]\n"
"s_waitcnt lgkmcnt(0)\n"
"s_barrier\n"
"v_mfma_f32_16x16x32_bf16 v[160:163], a[160:163], v[60:63], v[160:163]\n"
"v_exp_f32 v66, v66\n"
"v_mfma_f32_16x16x32_bf16 v[164:167], a[164:167], v[60:63], v[164:167]\n"
"ds_read_b64_tr_b16 a[128:129], v8 offset:16384\n"
"v_cvt_pk_bf16_f32 v56, v64, v65\n"
"v_mfma_f32_16x16x32_bf16 v[168:171], a[168:171], v[60:63], v[168:171]\n"
"v_exp_f32 v67, v67\n"
"v_mfma_f32_16x16x32_bf16 v[172:175], a[172:175], v[60:63], v[172:175]\n"
"ds_read_b64_tr_b16 a[130:131], v8 offset:20480\n"
"ds_read_b64_tr_b16 a[132:133], v9 offset:16384\n"
"v_mfma_f32_16x16x32_bf16 v[76:79], a[112:115], v[112:115], v[44:47]\n"
"v_exp_f32 v68, v68\n"
"v_mfma_f32_16x16x32_bf16 v[76:79], a[116:119], v[116:119], v[76:79]\n"
"ds_read_b64_tr_b16 a[134:135], v9 offset:20480\n"
"v_cvt_pk_bf16_f32 v57, v66, v67\n"
"v_mfma_f32_16x16x32_bf16 v[76:79], a[120:123], v[120:123], v[76:79]\n"
"v_exp_f32 v69, v69\n"
"v_mfma_f32_16x16x32_bf16 v[76:79], a[124:127], v[124:127], v[76:79]\n"
"ds_read_b64_tr_b16 a[136:137], v8 offset:16896\n"
"ds_read_b64_tr_b16 a[138:139], v8 offset:20992\n"
"v_mfma_f32_16x16x32_bf16 v[176:179], a[176:179], v[60:63], v[176:179]\n"
"v_exp_f32 v70, v70\n"
"v_mfma_f32_16x16x32_bf16 v[180:183], a[180:183], v[60:63], v[180:183]\n"
"ds_read_b64_tr_b16 a[140:141], v9 offset:16896\n"
"v_cvt_pk_bf16_f32 v58, v68, v69\n"
"v_mfma_f32_16x16x32_bf16 v[184:187], a[184:187], v[60:63], v[184:187]\n"
"v_exp_f32 v71, v71\n"
"v_mfma_f32_16x16x32_bf16 v[188:191], a[188:191], v[60:63], v[188:191]\n"
"ds_read_b64_tr_b16 a[142:143], v9 offset:20992\n"
"ds_read_b64_tr_b16 a[144:145], v8 offset:17408\n"
"s_mov_b32 s63, 3\n"
"s_branch .Lmha_ent5\n"
".Lmha_back5:\n"
"s_cmp_eq_u32 s63, 2\n"
"s_cbranch_scc0 .Lmha_blk5\n"
"ds_read_b64_tr_b16 a[0:1], v16\n"
"ds_read_b64_tr_b16 a[2:3], v16 offset:2048\n"
"ds_read_b64_tr_b16 a[4:5], v17\n"
"ds_read_b64_tr_b16 a[6:7], v17 offset:2048\n"
"ds_read_b64_tr_b16 a[8:9], v16 offset:512\n"
"ds_read_b64_tr_b16 a[10:11], v16 offset:2560\n"
"ds_read_b64_tr_b16 a[12:13], v17 offset:512\n"
"ds_read_b64_tr_b16 a[14:15], v17 offset:2560\n"
"ds_read_b64_tr_b16 a[16:17], v16 offset:16384\n"
"ds_read_b64_tr_b16 a[18:19], v16 offset:18432\n"
"ds_read_b64_tr_b16 a[20:21], v17 offset:16384\n"
"ds_read_b64_tr_b16 a[22:23], v17 offset:18432\n"
"ds_read_b64_tr_b16 a[24:25], v16 offset:16896\n"
"ds_read_b64_tr_b16 a[26:27], v16 offset:18944\n"
"ds_read_b64_tr_b16 a[28:29], v17 offset:16896\n"
"ds_read_b64_tr_b16 a[30:31], v17 offset:18944\n"
"s_branch .Lmha_top5\n"
".Lmha_blk5:\n"
"s_cmp_eq_u32 s63, 1\n"
"s_cbranch_scc0 .Lmha_top5\n"
"ds_read_b64_tr_b16 a[32:33], v16\n"
"ds_read_b64_tr_b16 a[34:35], v16 offset:2048\n"
"ds_read_b64_tr_b16 a[36:37], v17\n"
"ds_read_b64_tr_b16 a[38:39], v17 offset:2048\n"
"ds_read_b64_tr_b16 a[40:41], v16 offset:512\n"
"ds_read_b64_tr_b16 a[42:43], v16 offset:2560\n"
"ds_read_b64_tr_b16 a[44:45], v17 offset:512\n"
"ds_read_b64_tr_b16 a[46:47], v17 offset:2560\n"
"ds_read_b64_tr_b16 a[48:49], v16 offset:16384\n"
"ds_read_b64_tr_b16 a[50:51], v16 offset:18432\n"
"ds_read_b64_tr_b16 a[52:53], v17 offset:16384\n"
"ds_read_b64_tr_b16 a[54:55], v17 offset:18432\n"
"ds_read_b64_tr_b16 a[56:57], v16 offset:16896\n"
"ds_read_b64_tr_b16 a[58:59], v16 offset:18944\n"
"ds_read_b64_tr_b16 a[60:61], v17 offset:16896\n"
"ds_read_b64_tr_b16 a[62:63], v17 offset:18944\n"
".Lmha_top5:\n"
"v_mfma_f32_16x16x32_bf16 v[64:67], a[96:99], v[128:131], v[48:51]\n"
"v_exp_f32 v72, v72\n"
"v_mfma_f32_16x16x32_bf16 v[64:67], a[100:103], v[132:135], v[64:67]\n"
"ds_read_b64_tr_b16 a[146:147], v8 offset:5120\n"
"v_cvt_pk_bf16_f32 v59, v70, v71\n"
"v_mfma_f32_16x16x32_bf16 v[64:67], a[104:107], v[136:139], v[64:67]\n"
"v_exp_f32 v73, v73\n"
"v_mfma_f32_16x16x32_bf16 v[64:67], a[108:111], v[140:143], v[64:67]\n"
"ds_read_b64_tr_b16 a[148:149], v9 offset:1024\n"
"v_add_f32 v31, v31, v72\n"
"v_mfma_f32_16x16x32_bf16 v[192:195], a[160:163], v[56:59], v[192:195]\n"
"v_exp_f32 v74, v74\n"
"v_mfma_f32_16x16x32_bf16 v[196:199], a[164:167], v[56:59], v[196:199]\n"
"v_cvt_pk_bf16_f32 v60, v72, v73\n"
"v_add_f32 v31, v31, v73\n"
"v_mfma_f32_16x16x32_bf16 v[200:203], a[168:171], v[56:59], v[200:203]\n"
"v_exp_f32 v75, v75\n"
"v_mfma_f32_16x16x32_bf16 v[204:207], a[172:175], v[56:59], v[204:207]\n"
"ds_read_b64_tr_b16 a[150:151], v9 offset:5120\n"
"v_add_f32 v31, v31, v74\n"
"v_mfma_f32_16x16x32_bf16 v[18:21], v[10:13], v[56:59], v[18:21]\n"
"ds_read_b64_tr_b16 a[152:153], v8 offset:1536\n"
"ds_read_b64_tr_b16 a[154:155], v8 offset:5632\n"
"v_mfma_f32_16x16x32_bf16 v[68:71], a[112:115], v[128:131], v[48:51]\n"
"v_exp_f32 v76, v76\n"
"v_mfma_f32_16x16x32_bf16 v[68:71], a[116:119], v[132:135], v[68:71]\n"
"v_cvt_pk_bf16_f32 v61, v74, v75\n"
"v_add_f32 v31, v31, v75\n"
"v_mfma_f32_16x16x32_bf16 v[68:71], a[120:123], v[136:139], v[68:71]\n"
"v_exp_f32 v77, v77\n"
"v_mfma_f32_16x16x32_bf16 v[68:71], a[124:127], v[140:143], v[68:71]\n"
"ds_read_b64_tr_b16 a[156:157], v9 offset:1536\n"
"v_add_f32 v31, v31, v76\n"
"v_mfma_f32_16x16x32_bf16 v[208:211], a[176:179], v[56:59], v[208:211]\n"
"v_exp_f32 v78, v78\n"
"v_mfma_f32_16x16x32_bf16 v[212:215], a[180:183], v[56:59], v[212:215]\n"
"v_cvt_pk_bf16_f32 v62, v76, v77\n"
"v_add_f32 v31, v31, v77\n"
"v_mfma_f32_16x16x32_bf16 v[216:219], a[184:187], v[56:59], v[216:219]\n"
"v_exp_f32 v79, v79\n"
"v_mfma_f32_16x16x32_bf16 v[220:223], a[188:191], v[56:59], v[220:223]\n"
"ds_read_b64_tr_b16 a[158:159], v9 offset:5632\n"
"v_add_f32 v31, v31, v78\n"
"v_mfma_f32_16x16x32_bf16 v[72:75], a[96:99], v[144:147], v[52:55]\n"
"v_exp_f32 v64, v64\n"
"v_mfma_f32_16x16x32_bf16 v[72:75], a[100:103], v[148:151], v[72:75]\n"
"v_cvt_pk_bf16_f32 v63, v78, v79\n"
"v_add_f32 v31, v31, v79\n"
"v_mfma_f32_16x16x32_bf16 v[72:75], a[104:107], v[152:155], v[72:75]\n"
"v_exp_f32 v65, v65\n"
"v_mfma_f32_16x16x32_bf16 v[72:75], a[108:111], v[156:159], v[72:75]\n"
"ds_read_b128 a[96:99], v4 offset:8192\n"
"v_add_f32 v34, v34, v64\n"
"v_mfma_f32_16x16x32_bf16 v[224:227], a[160:163], v[60:63], v[224:227]\n"
"v_exp_f32 v66, v66\n"
"v_mfma_f32_16x16x32_bf16 v[228:231], a[164:167], v[60:63], v[228:231]\n"
"v_cvt_pk_bf16_f32 v56, v64, v65\n"
"v_add_f32 v34, v34, v65\n"
"v_mfma_f32_16x16x32_bf16 v[232:235], a[168:171], v[60:63], v[232:235]\n"
"v_exp_f32 v67, v67\n"
"v_mfma_f32_16x16x32_bf16 v[236:239], a[172:175], v[60:63], v[236:239]\n"
"ds_read_b128 a[100:103], v5 offset:8192\n"
"v_add_f32 v34, v34, v66\n"
"v_mfma_f32_16x16x32_bf16 v[76:79], a[112:115], v[144:147], v[52:55]\n"
"v_exp_f32 v68, v68\n"
"v_mfma_f32_16x16x32_bf16 v[76:79], a[116:119], v[148:151], v[76:79]\n"
"v_cvt_pk_bf16_f32 v57, v66, v67\n"
"v_add_f32 v34, v34, v67\n"
"v_mfma_f32_16x16x32_bf16 v[76:79], a[120:123], v[152:155], v[76:79]\n"
"v_exp_f32 v69, v69\n"
"v_mfma_f32_16x16x32_bf16 v[76:79], a[124:127], v[156:159], v[76:79]\n"
"ds_read_b128 a[104:107], v6 offset:8192\n"
"v_add_f32 v34, v34, v68\n"
"v_mfma_f32_16x16x32_bf16 v[240:243], a[176:179], v[60:63], v[240:243]\n"
"v_exp_f32 v70, v70\n"
"v_mfma_f32_16x16x32_bf16 v[244:247], a[180:183], v[60:63], v[244:247]\n"
"v_cvt_pk_bf16_f32 v58, v68, v69\n"
"v_add_f32 v34, v34, v69\n"
"v_mfma_f32_16x16x32_bf16 v[248:251], a[184:187], v[60:63], v[248:251]\n"
"v_exp_f32 v71, v71\n"
"v_mfma_f32_16x16x32_bf16 v[252:255], a[188:191], v[60:63], v[252:255]\n"
"ds_read_b128 a[108:111], v7 offset:8192\n"
"v_add_f32 v34, v34, v70\n"
"v_mfma_f32_16x16x32_bf16 v[64:67], a[64:67], v[80:83], v[36:39]\n"
"v_exp_f32 v72, v72\n"
"v_mfma_f32_16x16x32_bf16 v[64:67], a[68:71], v[84:87], v[64:67]\n"
"v_cvt_pk_bf16_f32 v59, v70, v71\n"
"v_add_f32 v34, v34, v71\n"
"v_mfma_f32_16x16x32_bf16 v[64:67], a[72:75], v[88:91], v[64:67]\n"
"v_exp_f32 v73, v73\n"
"v_mfma_f32_16x16x32_bf16 v[64:67], a[76:79], v[92:95], v[64:67]\n"
"ds_read_b128 a[112:115], v4 offset:12288\n"
"v_add_f32 v35, v35, v72\n"
"v_mfma_f32_16x16x32_bf16 a[192:195], a[160:163], v[56:59], a[192:195]\n"
"v_exp_f32 v74, v74\n"
"v_mfma_f32_16x16x32_bf16 a[196:199], a[164:167], v[56:59], a[196:199]\n"
"v_cvt_pk_bf16_f32 v60, v72, v73\n"
"v_add_f32 v35, v35, v73\n"
"v_mfma_f32_16x16x32_bf16 a[200:203], a[168:171], v[56:59], a[200:203]\n"
"v_exp_f32 v75, v75\n"
"v_mfma_f32_16x16x32_bf16 a[204:207], a[172:175], v[56:59], a[204:207]\n"
"ds_read_b128 a[116:119], v5 offset:12288\n"
"v_add_f32 v35, v35, v74\n"
"v_mfma_f32_16x16x32_bf16 v[68:71], a[80:83], v[80:83], v[36:39]\n"
"v_exp_f32 v76, v76\n"
"v_mfma_f32_16x16x32_bf16 v[68:71], a[84:87], v[84:87], v[68:71]\n"
"v_cvt_pk_bf16_f32 v61, v74, v75\n"
"v_add_f32 v35, v35, v75\n"
"v_mfma_f32_16x16x32_bf16 v[68:71], a[88:91], v[88:91], v[68:71]\n"
"v_exp_f32 v77, v77\n"
"v_mfma_f32_16x16x32_bf16 v[68:71], a[92:95], v[92:95], v[68:71]\n"
"ds_read_b128 a[120:123], v6 offset:12288\n"
"v_add_f32 v35, v35, v76\n"
"v_mfma_f32_16x16x32_bf16 a[208:211], a[176:179], v[56:59], a[208:211]\n"
"v_exp_f32 v78, v78\n"
"v_mfma_f32_16x16x32_bf16 a[212:215], a[180:183], v[56:59], a[212:215]\n"
"v_cvt_pk_bf16_f32 v62, v76, v77\n"
"v_add_f32 v35, v35, v77\n"
"v_mfma_f32_16x16x32_bf16 a[216:219], a[184:187], v[56:59], a[216:219]\n"
"v_exp_f32 v79, v79\n"
"v_mfma_f32_16x16x32_bf16 a[220:223], a[188:191], v[56:59], a[220:223]\n"
"ds_read_b128 a[124:127], v7 offset:12288\n"
"v_add_f32 v35, v35, v78\n"
"v_mfma_f32_16x16x32_bf16 v[72:75], a[64:67], v[96:99], v[40:43]\n"
"v_exp_f32 v64, v64\n"
"v_mfma_f32_16x16x32_bf16 v[72:75], a[68:71], v[100:103], v[72:75]\n"
"v_cvt_pk_bf16_f32 v63, v78, v79\n"
"v_add_f32 v35, v35, v79\n"
"v_mfma_f32_16x16x32_bf16 v[72:75], a[72:75], v[104:107], v[72:75]\n"
"v_exp_f32 v65, v65\n"
"v_mfma_f32_16x16x32_bf16 v[72:75], a[76:79], v[108:111], v[72:75]\n"
"v_add_f32 v29, v29, v64\n"
"v_mfma_f32_16x16x32_bf16 a[224:227], a[160:163], v[60:63], a[224:227]\n"
"v_exp_f32 v66, v66\n"
"v_mfma_f32_16x16x32_bf16 a[228:231], a[164:167], v[60:63], a[228:231]\n"
"v_cvt_pk_bf16_f32 v56, v64, v65\n"
"v_add_f32 v29, v29, v65\n"
"v_mfma_f32_16x16x32_bf16 a[232:235], a[168:171], v[60:63], a[232:235]\n"
"v_exp_f32 v67, v67\n"
"v_mfma_f32_16x16x32_bf16 a[236:239], a[172:175], v[60:63], a[236:239]\n"
"v_add_f32 v29, v29, v66\n"
"v_mfma_f32_16x16x32_bf16 v[76:79], a[80:83], v[96:99], v[40:43]\n"
"v_exp_f32 v68, v68\n"
"v_mfma_f32_16x16x32_bf16 v[76:79], a[84:87], v[100:103], v[76:79]\n"
"v_cvt_pk_bf16_f32 v57, v66, v67\n"
"v_add_f32 v29, v29, v67\n"
"v_mfma_f32_16x16x32_bf16 v[76:79], a[88:91], v[104:107], v[76:79]\n"
"v_exp_f32 v69, v69\n"
"v_mfma_f32_16x16x32_bf16 v[76:79], a[92:95], v[108:111], v[76:79]\n"
"v_add_f32 v29, v29, v68\n"
"v_mfma_f32_16x16x32_bf16 a[240:243], a[176:179], v[60:63], a[240:243]\n"
"v_exp_f32 v70, v70\n"
"v_mfma_f32_16x16x32_bf16 a[244:247], a[180:183], v[60:63], a[244:247]\n"
"v_cvt_pk_bf16_f32 v58, v68, v69\n"
"v_add_f32 v29, v29, v69\n"
"v_mfma_f32_16x16x32_bf16 a[248:251], a[184:187], v[60:63], a[248:251]\n"
"v_exp_f32 v71, v71\n"
"v_mfma_f32_16x16x32_bf16 a[252:255], a[188:191], v[60:63], a[252:255]\n"
"v_add_f32 v29, v29, v70\n"
"v_mfma_f32_16x16x32_bf16 v[64:67], a[64:67], v[112:115], v[44:47]\n"
"v_exp_f32 v72, v72\n"
"v_mfma_f32_16x16x32_bf16 v[64:67], a[68:71], v[116:119], v[64:67]\n"
"v_cvt_pk_bf16_f32 v59, v70, v71\n"
"v_add_f32 v29, v29, v71\n"
"v_mfma_f32_16x16x32_bf16 v[64:67], a[72:75], v[120:123], v[64:67]\n"
"v_exp_f32 v73, v73\n"
"v_mfma_f32_16x16x32_bf16 v[64:67], a[76:79], v[124:127], v[64:67]\n"
"s_waitcnt lgkmcnt(0)\n"
"s_barrier\n"
"v_mfma_f32_16x16x32_bf16 v[160:163], a[128:131], v[56:59], v[160:163]\n"
"v_exp_f32 v74, v74\n"
"v_mfma_f32_16x16x32_bf16 v[164:167], a[132:135], v[56:59], v[164:167]\n"
"ds_read_b64_tr_b16 a[160:161], v8 offset:8192\n"
"v_cvt_pk_bf16_f32 v60, v72, v73\n"
"v_mfma_f32_16x16x32_bf16 v[168:171], a[136:139], v[56:59], v[168:171]\n"
"v_exp_f32 v75, v75\n"
"v_mfma_f32_16x16x32_bf16 v[172:175], a[140:143], v[56:59], v[172:175]\n"
"ds_read_b64_tr_b16 a[162:163], v8 offset:12288\n"
"ds_read_b64_tr_b16 a[164:165], v9 offset:8192\n"
"v_mfma_f32_16x16x32_bf16 v[68:71], a[80:83], v[112:115], v[44:47]\n"
"v_exp_f32 v76, v76\n"
"v_mfma_f32_16x16x32_bf16 v[68:71], a[84:87], v[116:119], v[68:71]\n"
"ds_read_b64_tr_b16 a[166:167], v9 offset:12288\n"
"v_cvt_pk_bf16_f32 v61, v74, v75\n"
"v_mfma_f32_16x16x32_bf16 v[68:71], a[88:91], v[120:123], v[68:71]\n"
"v_exp_f32 v77, v77\n"
"v_mfma_f32_16x16x32_bf16 v[68:71], a[92:95], v[124:127], v[68:71]\n"
"ds_read_b64_tr_b16 a[168:169], v8 offset:8704\n"
"ds_read_b64_tr_b16 a[170:171], v8 offset:12800\n"
"v_mfma_f32_16x16x32_bf16 v[176:179], a[144:147], v[56:59], v[176:179]\n"
"v_exp_f32 v78, v78\n"
"v_mfma_f32_16x16x32_bf16 v[180:183], a[148:151], v[56:59], v[180:183]\n"
"ds_read_b64_tr_b16 a[172:173], v9 offset:8704\n"
"v_cvt_pk_bf16_f32 v62, v76, v77\n"
"v_mfma_f32_16x16x32_bf16 v[184:187], a[152:155], v[56:59], v[184:187]\n"
"v_exp_f32 v79, v79\n"
"v_mfma_f32_16x16x32_bf16 v[188:191], a[156:159], v[56:59], v[188:191]\n"
"ds_read_b64_tr_b16 a[174:175], v9 offset:12800\n"
"ds_read_b64_tr_b16 a[176:177], v8 offset:9216\n"
"v_mfma_f32_16x16x32_bf16 v[72:75], a[64:67], v[128:131], v[48:51]\n"
"v_exp_f32 v64, v64\n"
"v_mfma_f32_16x16x32_bf16 v[72:75], a[68:71], v[132:135], v[72:75]\n"
"ds_read_b64_tr_b16 a[178:179], v8 offset:13312\n"
"v_cvt_pk_bf16_f32 v63, v78, v79\n"
"v_mfma_f32_16x16x32_bf16 v[72:75], a[72:75], v[136:139], v[72:75]\n"
"v_exp_f32 v65, v65\n"
"v_mfma_f32_16x16x32_bf16 v[72:75], a[76:79], v[140:143], v[72:75]\n"
"ds_read_b64_tr_b16 a[180:181], v9 offset:9216\n"
"v_add_f32 v31, v31, v64\n"
"v_mfma_f32_16x16x32_bf16 v[192:195], a[128:131], v[60:63], v[192:195]\n"
"v_exp_f32 v66, v66\n"
"v_mfma_f32_16x16x32_bf16 v[196:199], a[132:135], v[60:63], v[196:199]\n"
"v_cvt_pk_bf16_f32 v56, v64, v65\n"
"v_add_f32 v31, v31, v65\n"
"v_mfma_f32_16x16x32_bf16 v[200:203], a[136:139], v[60:63], v[200:203]\n"
"v_exp_f32 v67, v67\n"
"v_mfma_f32_16x16x32_bf16 v[204:207], a[140:143], v[60:63], v[204:207]\n"
"ds_read_b64_tr_b16 a[182:183], v9 offset:13312\n"
"v_add_f32 v31, v31, v66\n"
"v_mfma_f32_16x16x32_bf16 v[18:21], v[10:13], v[60:63], v[18:21]\n"
"ds_read_b64_tr_b16 a[184:185], v8 offset:9728\n"
"ds_read_b64_tr_b16 a[186:187], v8 offset:13824\n"
"v_mfma_f32_16x16x32_bf16 v[76:79], a[80:83], v[128:131], v[48:51]\n"
"v_exp_f32 v68, v68\n"
"v_mfma_f32_16x16x32_bf16 v[76:79], a[84:87], v[132:135], v[76:79]\n"
"v_cvt_pk_bf16_f32 v57, v66, v67\n"
"v_add_f32 v31, v31, v67\n"
"v_mfma_f32_16x16x32_bf16 v[76:79], a[88:91], v[136:139], v[76:79]\n"
"v_exp_f32 v69, v69\n"
"v_mfma_f32_16x16x32_bf16 v[76:79], a[92:95], v[140:143], v[76:79]\n"
"ds_read_b64_tr_b16 a[188:189], v9 offset:9728\n"
"v_add_f32 v31, v31, v68\n"
"v_mfma_f32_16x16x32_bf16 v[208:211], a[144:147], v[60:63], v[208:211]\n"
"v_exp_f32 v70, v70\n"
"v_mfma_f32_16x16x32_bf16 v[212:215], a[148:151], v[60:63], v[212:215]\n"
"v_cvt_pk_bf16_f32 v58, v68, v69\n"
"v_add_f32 v31, v31, v69\n"
"v_mfma_f32_16x16x32_bf16 v[216:219], a[152:155], v[60:63], v[216:219]\n"
"v_exp_f32 v71, v71\n"
"v_mfma_f32_16x16x32_bf16 v[220:223], a[156:159], v[60:63], v[220:223]\n"
"ds_read_b64_tr_b16 a[190:191], v9 offset:13824\n"
"v_add_f32 v31, v31, v70\n"
"v_mfma_f32_16x16x32_bf16 v[64:67], a[64:67], v[144:147], v[52:55]\n"
"v_exp_f32 v72, v72\n"
"v_mfma_f32_16x16x32_bf16 v[64:67], a[68:71], v[148:151], v[64:67]\n"
"v_cvt_pk_bf16_f32 v59, v70, v71\n"
"v_add_f32 v31, v31, v71\n"
"v_mfma_f32_16x16x32_bf16 v[64:67], a[72:75], v[152:155], v[64:67]\n"
"v_exp_f32 v73, v73\n"
"v_mfma_f32_16x16x32_bf16 v[64:67], a[76:79], v[156:159], v[64:67]\n"
"ds_read_b128 a[64:67], v4 offset:16384\n"
"v_add_f32 v34, v34, v72\n"
"v_mfma_f32_16x16x32_bf16 v[224:227], a[128:131], v[56:59], v[224:227]\n"
"v_exp_f32 v74, v74\n"
"v_mfma_f32_16x16x32_bf16 v[228:231], a[132:135], v[56:59], v[228:231]\n"
"v_cvt_pk_bf16_f32 v60, v72, v73\n"
"v_add_f32 v34, v34, v73\n"
"v_mfma_f32_16x16x32_bf16 v[232:235], a[136:139], v[56:59], v[232:235]\n"
"v_exp_f32 v75, v75\n"
"v_mfma_f32_16x16x32_bf16 v[236:239], a[140:143], v[56:59], v[236:239]\n"
"ds_read_b128 a[68:71], v5 offset:16384\n"
"v_add_f32 v34, v34, v74\n"
"v_mfma_f32_16x16x32_bf16 v[68:71], a[80:83], v[144:147], v[52:55]\n"
"v_exp_f32 v76, v76\n"
"v_mfma_f32_16x16x32_bf16 v[68:71], a[84:87], v[148:151], v[68:71]\n"
"v_cvt_pk_bf16_f32 v61, v74, v75\n"
"v_add_f32 v34, v34, v75\n"
"v_mfma_f32_16x16x32_bf16 v[68:71], a[88:91], v[152:155], v[68:71]\n"
"v_exp_f32 v77, v77\n"
"v_mfma_f32_16x16x32_bf16 v[68:71], a[92:95], v[156:159], v[68:71]\n"
"ds_read_b128 a[72:75], v6 offset:16384\n"
"v_add_f32 v34, v34, v76\n"
"v_mfma_f32_16x16x32_bf16 v[240:243], a[144:147], v[56:59], v[240:243]\n"
"v_exp_f32 v78, v78\n"
"v_mfma_f32_16x16x32_bf16 v[244:247], a[148:151], v[56:59], v[244:247]\n"
"v_cvt_pk_bf16_f32 v62, v76, v77\n"
"v_add_f32 v34, v34, v77\n"
"v_mfma_f32_16x16x32_bf16 v[248:251], a[152:155], v[56:59], v[248:251]\n"
"v_exp_f32 v79, v79\n"
"v_mfma_f32_16x16x32_bf16 v[252:255], a[156:159], v[56:59], v[252:255]\n"
"ds_read_b128 a[76:79], v7 offset:16384\n"
"v_add_f32 v34, v34, v78\n"
"v_mfma_f32_16x16x32_bf16 v[72:75], a[96:99], v[80:83], v[36:39]\n"
"v_exp_f32 v64, v64\n"
"v_mfma_f32_16x16x32_bf16 v[72:75], a[100:103], v[84:87], v[72:75]\n"
"v_cvt_pk_bf16_f32 v63, v78, v79\n"
"v_add_f32 v34, v34, v79\n"
"v_mfma_f32_16x16x32_bf16 v[72:75], a[104:107], v[88:91], v[72:75]\n"
"v_exp_f32 v65, v65\n"
"v_mfma_f32_16x16x32_bf16 v[72:75], a[108:111], v[92:95], v[72:75]\n"
"ds_read_b128 a[80:83], v4 offset:20480\n"
"v_add_f32 v35, v35, v64\n"
"v_mfma_f32_16x16x32_bf16 a[192:195], a[128:131], v[60:63], a[192:195]\n"
"v_exp_f32 v66, v66\n"
"v_mfma_f32_16x16x32_bf16 a[196:199], a[132:135], v[60:63], a[196:199]\n"
"v_cvt_pk_bf16_f32 v56, v64, v65\n"
"v_add_f32 v35, v35, v65\n"
"v_mfma_f32_16x16x32_bf16 a[200:203], a[136:139], v[60:63], a[200:203]\n"
"v_exp_f32 v67, v67\n"
"v_mfma_f32_16x16x32_bf16 a[204:207], a[140:143], v[60:63], a[204:207]\n"
"ds_read_b128 a[84:87], v5 offset:20480\n"
"v_add_f32 v35, v35, v66\n"
"v_mfma_f32_16x16x32_bf16 v[76:79], a[112:115], v[80:83], v[36:39]\n"
"v_exp_f32 v68, v68\n"
"v_mfma_f32_16x16x32_bf16 v[76:79], a[116:119], v[84:87], v[76:79]\n"
"v_cvt_pk_bf16_f32 v57, v66, v67\n"
"v_add_f32 v35, v35, v67\n"
"v_mfma_f32_16x16x32_bf16 v[76:79], a[120:123], v[88:91], v[76:79]\n"
"v_exp_f32 v69, v69\n"
"v_mfma_f32_16x16x32_bf16 v[76:79], a[124:127], v[92:95], v[76:79]\n"
"ds_read_b128 a[88:91], v6 offset:20480\n"
"v_add_f32 v35, v35, v68\n"
"v_mfma_f32_16x16x32_bf16 a[208:211], a[144:147], v[60:63], a[208:211]\n"
"v_exp_f32 v70, v70\n"
"v_mfma_f32_16x16x32_bf16 a[212:215], a[148:151], v[60:63], a[212:215]\n"
"v_cvt_pk_bf16_f32 v58, v68, v69\n"
"v_add_f32 v35, v35, v69\n"
"v_mfma_f32_16x16x32_bf16 a[216:219], a[152:155], v[60:63], a[216:219]\n"
"v_exp_f32 v71, v71\n"
"v_mfma_f32_16x16x32_bf16 a[220:223], a[156:159], v[60:63], a[220:223]\n"
"ds_read_b128 a[92:95], v7 offset:20480\n"
"v_add_f32 v35, v35, v70\n"
"v_mfma_f32_16x16x32_bf16 v[64:67], a[96:99], v[96:99], v[40:43]\n"
"v_exp_f32 v72, v72\n"
"v_mfma_f32_16x16x32_bf16 v[64:67], a[100:103], v[100:103], v[64:67]\n"
"v_cvt_pk_bf16_f32 v59, v70, v71\n"
"v_add_f32 v35, v35, v71\n"
"v_mfma_f32_16x16x32_bf16 v[64:67], a[104:107], v[104:107], v[64:67]\n"
"v_exp_f32 v73, v73\n"
"v_mfma_f32_16x16x32_bf16 v[64:67], a[108:111], v[108:111], v[64:67]\n"
"v_add_f32 v29, v29, v72\n"
"v_mfma_f32_16x16x32_bf16 a[224:227], a[128:131], v[56:59], a[224:227]\n"
"v_exp_f32 v74, v74\n"
"v_mfma_f32_16x16x32_bf16 a[228:231], a[132:135], v[56:59], a[228:231]\n"
"v_cvt_pk_bf16_f32 v60, v72, v73\n"
"v_add_f32 v29, v29, v73\n"
"v_mfma_f32_16x16x32_bf16 a[232:235], a[136:139], v[56:59], a[232:235]\n"
"v_exp_f32 v75, v75\n"
"v_mfma_f32_16x16x32_bf16 a[236:239], a[140:143], v[56:59], a[236:239]\n"
"v_add_f32 v29, v29, v74\n"
"v_mfma_f32_16x16x32_bf16 v[68:71], a[112:115], v[96:99], v[40:43]\n"
"v_exp_f32 v76, v76\n"
"v_mfma_f32_16x16x32_bf16 v[68:71], a[116:119], v[100:103], v[68:71]\n"
"v_cvt_pk_bf16_f32 v61, v74, v75\n"
"v_add_f32 v29, v29, v75\n"
"v_mfma_f32_16x16x32_bf16 v[68:71], a[120:123], v[104:107], v[68:71]\n"
"v_exp_f32 v77, v77\n"
"v_mfma_f32_16x16x32_bf16 v[68:71], a[124:127], v[108:111], v[68:71]\n"
"v_add_f32 v29, v29, v76\n"
"v_mfma_f32_16x16x32_bf16 a[240:243], a[144:147], v[56:59], a[240:243]\n"
"v_exp_f32 v78, v78\n"
"v_mfma_f32_16x16x32_bf16 a[244:247], a[148:151], v[56:59], a[244:247]\n"
"v_cvt_pk_bf16_f32 v62, v76, v77\n"
"v_add_f32 v29, v29, v77\n"
"v_mfma_f32_16x16x32_bf16 a[248:251], a[152:155], v[56:59], a[248:251]\n"
"v_exp_f32 v79, v79\n"
"v_mfma_f32_16x16x32_bf16 a[252:255], a[156:159], v[56:59], a[252:255]\n"
"v_add_f32 v29, v29, v78\n"
"v_mfma_f32_16x16x32_bf16 v[72:75], a[96:99], v[112:115], v[44:47]\n"
"v_exp_f32 v64, v64\n"
"v_mfma_f32_16x16x32_bf16 v[72:75], a[100:103], v[116:119], v[72:75]\n"
"v_cvt_pk_bf16_f32 v63, v78, v79\n"
"v_add_f32 v29, v29, v79\n"
"v_mfma_f32_16x16x32_bf16 v[72:75], a[104:107], v[120:123], v[72:75]\n"
"v_exp_f32 v65, v65\n"
"v_mfma_f32_16x16x32_bf16 v[72:75], a[108:111], v[124:127], v[72:75]\n"
"s_waitcnt lgkmcnt(0)\n"
"s_barrier\n"
"v_mfma_f32_16x16x32_bf16 v[160:163], a[160:163], v[60:63], v[160:163]\n"
"v_exp_f32 v66, v66\n"
"v_mfma_f32_16x16x32_bf16 v[164:167], a[164:167], v[60:63], v[164:167]\n"
"ds_read_b64_tr_b16 a[128:129], v8 offset:16384\n"
"v_cvt_pk_bf16_f32 v56, v64, v65\n"
"v_mfma_f32_16x16x32_bf16 v[168:171], a[168:171], v[60:63], v[168:171]\n"
"v_exp_f32 v67, v67\n"
"v_mfma_f32_16x16x32_bf16 v[172:175], a[172:175], v[60:63], v[172:175]\n"
"ds_read_b64_tr_b16 a[130:131], v8 offset:20480\n"
"ds_read_b64_tr_b16 a[132:133], v9 offset:16384\n"
"v_mfma_f32_16x16x32_bf16 v[76:79], a[112:115], v[112:115], v[44:47]\n"
"v_exp_f32 v68, v68\n"
"v_mfma_f32_16x16x32_bf16 v[76:79], a[116:119], v[116:119], v[76:79]\n"
"ds_read_b64_tr_b16 a[134:135], v9 offset:20480\n"
"v_cvt_pk_bf16_f32 v57, v66, v67\n"
"v_mfma_f32_16x16x32_bf16 v[76:79], a[120:123], v[120:123], v[76:79]\n"
"v_exp_f32 v69, v69\n"
"v_mfma_f32_16x16x32_bf16 v[76:79], a[124:127], v[124:127], v[76:79]\n"
"ds_read_b64_tr_b16 a[136:137], v8 offset:16896\n"
"ds_read_b64_tr_b16 a[138:139], v8 offset:20992\n"
"v_mfma_f32_16x16x32_bf16 v[176:179], a[176:179], v[60:63], v[176:179]\n"
"v_exp_f32 v70, v70\n"
"v_mfma_f32_16x16x32_bf16 v[180:183], a[180:183], v[60:63], v[180:183]\n"
"ds_read_b64_tr_b16 a[140:141], v9 offset:16896\n"
"v_cvt_pk_bf16_f32 v58, v68, v69\n"
"v_mfma_f32_16x16x32_bf16 v[184:187], a[184:187], v[60:63], v[184:187]\n"
"v_exp_f32 v71, v71\n"
"v_mfma_f32_16x16x32_bf16 v[188:191], a[188:191], v[60:63], v[188:191]\n"
"ds_read_b64_tr_b16 a[142:143], v9 offset:20992\n"
"ds_read_b64_tr_b16 a[144:145], v8 offset:17408\n"
".Lmha_ent5:\n"
"v_mfma_f32_16x16x32_bf16 v[64:67], a[96:99], v[128:131], v[48:51]\n"
"v_exp_f32 v72, v72\n"
"v_mfma_f32_16x16x32_bf16 v[64:67], a[100:103], v[132:135], v[64:67]\n"
"ds_read_b64_tr_b16 a[146:147], v8 offset:21504\n"
"v_cvt_pk_bf16_f32 v59, v70, v71\n"
"v_mfma_f32_16x16x32_bf16 v[64:67], a[104:107], v[136:139], v[64:67]\n"
"v_exp_f32 v73, v73\n"
"v_mfma_f32_16x16x32_bf16 v[64:67], a[108:111], v[140:143], v[64:67]\n"
"ds_read_b64_tr_b16 a[148:149], v9 offset:17408\n"
"v_add_f32 v31, v31, v72\n"
"v_mfma_f32_16x16x32_bf16 v[192:195], a[160:163], v[56:59], v[192:195]\n"
"v_exp_f32 v74, v74\n"
"v_mfma_f32_16x16x32_bf16 v[196:199], a[164:167], v[56:59], v[196:199]\n"
"v_cvt_pk_bf16_f32 v60, v72, v73\n"
"v_add_f32 v31, v31, v73\n"
"v_mfma_f32_16x16x32_bf16 v[200:203], a[168:171], v[56:59], v[200:203]\n"
"v_exp_f32 v75, v75\n"
"v_mfma_f32_16x16x32_bf16 v[204:207], a[172:175], v[56:59], v[204:207]\n"
"ds_read_b64_tr_b16 a[150:151], v9 offset:21504\n"
"v_add_f32 v31, v31, v74\n"
"v_mfma_f32_16x16x32_bf16 v[18:21], v[10:13], v[56:59], v[18:21]\n"
"ds_read_b64_tr_b16 a[152:153], v8 offset:17920\n"
"ds_read_b64_tr_b16 a[154:155], v8 offset:22016\n"
"v_mfma_f32_16x16x32_bf16 v[68:71], a[112:115], v[128:131], v[48:51]\n"
"v_exp_f32 v76, v76\n"
"v_mfma_f32_16x16x32_bf16 v[68:71], a[116:119], v[132:135], v[68:71]\n"
"v_cvt_pk_bf16_f32 v61, v74, v75\n"
"v_add_f32 v31, v31, v75\n"
"v_mfma_f32_16x16x32_bf16 v[68:71], a[120:123], v[136:139], v[68:71]\n"
"v_exp_f32 v77, v77\n"
"v_mfma_f32_16x16x32_bf16 v[68:71], a[124:127], v[140:143], v[68:71]\n"
"ds_read_b64_tr_b16 a[156:157], v9 offset:17920\n"
"v_add_f32 v31, v31, v76\n"
"v_mfma_f32_16x16x32_bf16 v[208:211], a[176:179], v[56:59], v[208:211]\n"
"v_exp_f32 v78, v78\n"
"v_mfma_f32_16x16x32_bf16 v[212:215], a[180:183], v[56:59], v[212:215]\n"
"v_cvt_pk_bf16_f32 v62, v76, v77\n"
"v_add_f32 v31, v31, v77\n"
"v_mfma_f32_16x16x32_bf16 v[216:219], a[184:187], v[56:59], v[216:219]\n"
"v_exp_f32 v79, v79\n"
"v_mfma_f32_16x16x32_bf16 v[220:223], a[188:191], v[56:59], v[220:223]\n"
"ds_read_b64_tr_b16 a[158:159], v9 offset:22016\n"
"v_add_f32 v31, v31, v78\n"
"v_mfma_f32_16x16x32_bf16 v[72:75], a[96:99], v[144:147], v[52:55]\n"
"v_exp_f32 v64, v64\n"
"v_mfma_f32_16x16x32_bf16 v[72:75], a[100:103], v[148:151], v[72:75]\n"
"v_cvt_pk_bf16_f32 v63, v78, v79\n"
"v_add_f32 v31, v31, v79\n"
"v_mfma_f32_16x16x32_bf16 v[72:75], a[104:107], v[152:155], v[72:75]\n"
"v_exp_f32 v65, v65\n"
"v_mfma_f32_16x16x32_bf16 v[72:75], a[108:111], v[156:159], v[72:75]\n"
"ds_read_b128 a[96:99], v4 offset:24576\n"
"v_add_f32 v34, v34, v64\n"
"v_mfma_f32_16x16x32_bf16 v[224:227], a[160:163], v[60:63], v[224:227]\n"
"v_exp_f32 v66, v66\n"
"v_mfma_f32_16x16x32_bf16 v[228:231], a[164:167], v[60:63], v[228:231]\n"
"v_cvt_pk_bf16_f32 v56, v64, v65\n"
"v_add_f32 v34, v34, v65\n"
"v_mfma_f32_16x16x32_bf16 v[232:235], a[168:171], v[60:63], v[232:235]\n"
"v_exp_f32 v67, v67\n"
"v_mfma_f32_16x16x32_bf16 v[236:239], a[172:175], v[60:63], v[236:239]\n"
"ds_read_b128 a[100:103], v5 offset:24576\n"
"v_add_f32 v34, v34, v66\n"
"v_mfma_f32_16x16x32_bf16 v[76:79], a[112:115], v[144:147], v[52:55]\n"
"v_exp_f32 v68, v68\n"
"v_mfma_f32_16x16x32_bf16 v[76:79], a[116:119], v[148:151], v[76:79]\n"
"v_cvt_pk_bf16_f32 v57, v66, v67\n"
"v_add_f32 v34, v34, v67\n"
"v_mfma_f32_16x16x32_bf16 v[76:79], a[120:123], v[152:155], v[76:79]\n"
"v_exp_f32 v69, v69\n"
"v_mfma_f32_16x16x32_bf16 v[76:79], a[124:127], v[156:159], v[76:79]\n"
"ds_read_b128 a[104:107], v6 offset:24576\n"
"v_add_f32 v34, v34, v68\n"
"v_mfma_f32_16x16x32_bf16 v[240:243], a[176:179], v[60:63], v[240:243]\n"
"v_exp_f32 v70, v70\n"
"v_mfma_f32_16x16x32_bf16 v[244:247], a[180:183], v[60:63], v[244:247]\n"
"v_cvt_pk_bf16_f32 v58, v68, v69\n"
"v_add_f32 v34, v34, v69\n"
"v_mfma_f32_16x16x32_bf16 v[248:251], a[184:187], v[60:63], v[248:251]\n"
"v_exp_f32 v71, v71\n"
"v_mfma_f32_16x16x32_bf16 v[252:255], a[188:191], v[60:63], v[252:255]\n"
"ds_read_b128 a[108:111], v7 offset:24576\n"
"v_add_f32 v34, v34, v70\n"
"v_mfma_f32_16x16x32_bf16 v[64:67], a[64:67], v[80:83], v[36:39]\n"
"v_exp_f32 v72, v72\n"
"v_mfma_f32_16x16x32_bf16 v[64:67], a[68:71], v[84:87], v[64:67]\n"
"v_cvt_pk_bf16_f32 v59, v70, v71\n"
"v_add_f32 v34, v34, v71\n"
"v_mfma_f32_16x16x32_bf16 v[64:67], a[72:75], v[88:91], v[64:67]\n"
"v_exp_f32 v73, v73\n"
"v_mfma_f32_16x16x32_bf16 v[64:67], a[76:79], v[92:95], v[64:67]\n"
"ds_read_b128 a[112:115], v4 offset:28672\n"
"v_add_f32 v35, v35, v72\n"
"v_mfma_f32_16x16x32_bf16 a[192:195], a[160:163], v[56:59], a[192:195]\n"
"v_exp_f32 v74, v74\n"
"v_mfma_f32_16x16x32_bf16 a[196:199], a[164:167], v[56:59], a[196:199]\n"
"v_cvt_pk_bf16_f32 v60, v72, v73\n"
"v_add_f32 v35, v35, v73\n"
"v_mfma_f32_16x16x32_bf16 a[200:203], a[168:171], v[56:59], a[200:203]\n"
"v_exp_f32 v75, v75\n"
"v_mfma_f32_16x16x32_bf16 a[204:207], a[172:175], v[56:59], a[204:207]\n"
"ds_read_b128 a[116:119], v5 offset:28672\n"
"v_add_f32 v35, v35, v74\n"
"v_mfma_f32_16x16x32_bf16 v[68:71], a[80:83], v[80:83], v[36:39]\n"
"v_exp_f32 v76, v76\n"
"v_mfma_f32_16x16x32_bf16 v[68:71], a[84:87], v[84:87], v[68:71]\n"
"v_cvt_pk_bf16_f32 v61, v74, v75\n"
"v_add_f32 v35, v35, v75\n"
"v_mfma_f32_16x16x32_bf16 v[68:71], a[88:91], v[88:91], v[68:71]\n"
"v_exp_f32 v77, v77\n"
"v_mfma_f32_16x16x32_bf16 v[68:71], a[92:95], v[92:95], v[68:71]\n"
"ds_read_b128 a[120:123], v6 offset:28672\n"
"v_add_f32 v35, v35, v76\n"
"v_mfma_f32_16x16x32_bf16 a[208:211], a[176:179], v[56:59], a[208:211]\n"
"v_exp_f32 v78, v78\n"
"v_mfma_f32_16x16x32_bf16 a[212:215], a[180:183], v[56:59], a[212:215]\n"
"v_cvt_pk_bf16_f32 v62, v76, v77\n"
"v_add_f32 v35, v35, v77\n"
"v_mfma_f32_16x16x32_bf16 a[216:219], a[184:187], v[56:59], a[216:219]\n"
"v_exp_f32 v79, v79\n"
"v_mfma_f32_16x16x32_bf16 a[220:223], a[188:191], v[56:59], a[220:223]\n"
"ds_read_b128 a[124:127], v7 offset:28672\n"
"v_add_f32 v35, v35, v78\n"
"v_mfma_f32_16x16x32_bf16 v[72:75], a[64:67], v[96:99], v[40:43]\n"
"v_exp_f32 v64, v64\n"
"v_mfma_f32_16x16x32_bf16 v[72:75], a[68:71], v[100:103], v[72:75]\n"
"v_cvt_pk_bf16_f32 v63, v78, v79\n"
"v_add_f32 v35, v35, v79\n"
"v_mfma_f32_16x16x32_bf16 v[72:75], a[72:75], v[104:107], v[72:75]\n"
"v_exp_f32 v65, v65\n"
"v_mfma_f32_16x16x32_bf16 v[72:75], a[76:79], v[108:111], v[72:75]\n"
"v_add_f32 v29, v29, v64\n"
"v_mfma_f32_16x16x32_bf16 a[224:227], a[160:163], v[60:63], a[224:227]\n"
"v_exp_f32 v66, v66\n"
"v_mfma_f32_16x16x32_bf16 a[228:231], a[164:167], v[60:63], a[228:231]\n"
"v_cvt_pk_bf16_f32 v56, v64, v65\n"
"v_add_f32 v29, v29, v65\n"
"v_mfma_f32_16x16x32_bf16 a[232:235], a[168:171], v[60:63], a[232:235]\n"
"v_exp_f32 v67, v67\n"
"v_mfma_f32_16x16x32_bf16 a[236:239], a[172:175], v[60:63], a[236:239]\n"
"v_add_f32 v29, v29, v66\n"
"v_mfma_f32_16x16x32_bf16 v[76:79], a[80:83], v[96:99], v[40:43]\n"
"v_exp_f32 v68, v68\n"
"v_mfma_f32_16x16x32_bf16 v[76:79], a[84:87], v[100:103], v[76:79]\n"
"v_cvt_pk_bf16_f32 v57, v66, v67\n"
"v_add_f32 v29, v29, v67\n"
"v_mfma_f32_16x16x32_bf16 v[76:79], a[88:91], v[104:107], v[76:79]\n"
"v_exp_f32 v69, v69\n"
"v_mfma_f32_16x16x32_bf16 v[76:79], a[92:95], v[108:111], v[76:79]\n"
"v_add_f32 v29, v29, v68\n"
"v_mfma_f32_16x16x32_bf16 a[240:243], a[176:179], v[60:63], a[240:243]\n"
"v_exp_f32 v70, v70\n"
"v_mfma_f32_16x16x32_bf16 a[244:247], a[180:183], v[60:63], a[244:247]\n"
"v_cvt_pk_bf16_f32 v58, v68, v69\n"
"v_add_f32 v29, v29, v69\n"
"v_mfma_f32_16x16x32_bf16 a[248:251], a[184:187], v[60:63], a[248:251]\n"
"v_exp_f32 v71, v71\n"
"v_mfma_f32_16x16x32_bf16 a[252:255], a[188:191], v[60:63], a[252:255]\n"
"v_add_f32 v29, v29, v70\n"
"v_mfma_f32_16x16x32_bf16 v[64:67], a[64:67], v[112:115], v[44:47]\n"
"v_exp_f32 v72, v72\n"
"v_mfma_f32_16x16x32_bf16 v[64:67], a[68:71], v[116:119], v[64:67]\n"
"v_cvt_pk_bf16_f32 v59, v70, v71\n"
"v_add_f32 v29, v29, v71\n"
"v_mfma_f32_16x16x32_bf16 v[64:67], a[72:75], v[120:123], v[64:67]\n"
"v_exp_f32 v73, v73\n"
"v_mfma_f32_16x16x32_bf16 v[64:67], a[76:79], v[124:127], v[64:67]\n"
"s_waitcnt lgkmcnt(0)\n"
"s_barrier\n"
"v_mfma_f32_16x16x32_bf16 v[160:163], a[128:131], v[56:59], v[160:163]\n"
"v_exp_f32 v74, v74\n"
"v_mfma_f32_16x16x32_bf16 v[164:167], a[132:135], v[56:59], v[164:167]\n"
"ds_read_b64_tr_b16 a[160:161], v8 offset:24576\n"
"v_cvt_pk_bf16_f32 v60, v72, v73\n"
"v_mfma_f32_16x16x32_bf16 v[168:171], a[136:139], v[56:59], v[168:171]\n"
"v_exp_f32 v75, v75\n"
"v_mfma_f32_16x16x32_bf16 v[172:175], a[140:143], v[56:59], v[172:175]\n"
"ds_read_b64_tr_b16 a[162:163], v8 offset:28672\n"
"ds_read_b64_tr_b16 a[164:165], v9 offset:24576\n"
"v_mfma_f32_16x16x32_bf16 v[68:71], a[80:83], v[112:115], v[44:47]\n"
"v_exp_f32 v76, v76\n"
"v_mfma_f32_16x16x32_bf16 v[68:71], a[84:87], v[116:119], v[68:71]\n"
"ds_read_b64_tr_b16 a[166:167], v9 offset:28672\n"
"v_cvt_pk_bf16_f32 v61, v74, v75\n"
"v_mfma_f32_16x16x32_bf16 v[68:71], a[88:91], v[120:123], v[68:71]\n"
"v_exp_f32 v77, v77\n"
"v_mfma_f32_16x16x32_bf16 v[68:71], a[92:95], v[124:127], v[68:71]\n"
"ds_read_b64_tr_b16 a[168:169], v8 offset:25088\n"
"ds_read_b64_tr_b16 a[170:171], v8 offset:29184\n"
"v_mfma_f32_16x16x32_bf16 v[176:179], a[144:147], v[56:59], v[176:179]\n"
"v_exp_f32 v78, v78\n"
"v_mfma_f32_16x16x32_bf16 v[180:183], a[148:151], v[56:59], v[180:183]\n"
"ds_read_b64_tr_b16 a[172:173], v9 offset:25088\n"
"v_cvt_pk_bf16_f32 v62, v76, v77\n"
"v_mfma_f32_16x16x32_bf16 v[184:187], a[152:155], v[56:59], v[184:187]\n"
"v_exp_f32 v79, v79\n"
"v_mfma_f32_16x16x32_bf16 v[188:191], a[156:159], v[56:59], v[188:191]\n"
"ds_read_b64_tr_b16 a[174:175], v9 offset:29184\n"
"ds_read_b64_tr_b16 a[176:177], v8 offset:25600\n"
"v_mfma_f32_16x16x32_bf16 v[72:75], a[64:67], v[128:131], v[48:51]\n"
"v_exp_f32 v64, v64\n"
"v_mfma_f32_16x16x32_bf16 v[72:75], a[68:71], v[132:135], v[72:75]\n"
"ds_read_b64_tr_b16 a[178:179], v8 offset:29696\n"
"v_cvt_pk_bf16_f32 v63, v78, v79\n"
"v_mfma_f32_16x16x32_bf16 v[72:75], a[72:75], v[136:139], v[72:75]\n"
"v_exp_f32 v65, v65\n"
"v_mfma_f32_16x16x32_bf16 v[72:75], a[76:79], v[140:143], v[72:75]\n"
"ds_read_b64_tr_b16 a[180:181], v9 offset:25600\n"
"v_add_f32 v31, v31, v64\n"
"v_mfma_f32_16x16x32_bf16 v[192:195], a[128:131], v[60:63], v[192:195]\n"
"v_exp_f32 v66, v66\n"
"v_mfma_f32_16x16x32_bf16 v[196:199], a[132:135], v[60:63], v[196:199]\n"
"v_cvt_pk_bf16_f32 v56, v64, v65\n"
"v_add_f32 v31, v31, v65\n"
"v_mfma_f32_16x16x32_bf16 v[200:203], a[136:139], v[60:63], v[200:203]\n"
"v_exp_f32 v67, v67\n"
"v_mfma_f32_16x16x32_bf16 v[204:207], a[140:143], v[60:63], v[204:207]\n"
"ds_read_b64_tr_b16 a[182:183], v9 offset:29696\n"
"v_add_f32 v31, v31, v66\n"
"v_mfma_f32_16x16x32_bf16 v[18:21], v[10:13], v[60:63], v[18:21]\n"
"ds_read_b64_tr_b16 a[184:185], v8 offset:26112\n"
"ds_read_b64_tr_b16 a[186:187], v8 offset:30208\n"
"v_mfma_f32_16x16x32_bf16 v[76:79], a[80:83], v[128:131], v[48:51]\n"
"v_exp_f32 v68, v68\n"
"v_mfma_f32_16x16x32_bf16 v[76:79], a[84:87], v[132:135], v[76:79]\n"
"v_cvt_pk_bf16_f32 v57, v66, v67\n"
"v_add_f32 v31, v31, v67\n"
"v_mfma_f32_16x16x32_bf16 v[76:79], a[88:91], v[136:139], v[76:79]\n"
"v_exp_f32 v69, v69\n"
"v_mfma_f32_16x16x32_bf16 v[76:79], a[92:95], v[140:143], v[76:79]\n"
"ds_read_b64_tr_b16 a[188:189], v9 offset:26112\n"
"v_add_f32 v31, v31, v68\n"
"v_mfma_f32_16x16x32_bf16 v[208:211], a[144:147], v[60:63], v[208:211]\n"
"v_exp_f32 v70, v70\n"
"v_mfma_f32_16x16x32_bf16 v[212:215], a[148:151], v[60:63], v[212:215]\n"
"v_cvt_pk_bf16_f32 v58, v68, v69\n"
"v_add_f32 v31, v31, v69\n"
"v_mfma_f32_16x16x32_bf16 v[216:219], a[152:155], v[60:63], v[216:219]\n"
"v_exp_f32 v71, v71\n"
"v_mfma_f32_16x16x32_bf16 v[220:223], a[156:159], v[60:63], v[220:223]\n"
"ds_read_b64_tr_b16 a[190:191], v9 offset:30208\n"
"v_add_f32 v31, v31, v70\n"
"v_mfma_f32_16x16x32_bf16 v[64:67], a[64:67], v[144:147], v[52:55]\n"
"v_exp_f32 v72, v72\n"
"v_mfma_f32_16x16x32_bf16 v[64:67], a[68:71], v[148:151], v[64:67]\n"
"v_cvt_pk_bf16_f32 v59, v70, v71\n"
"v_add_f32 v31, v31, v71\n"
"v_mfma_f32_16x16x32_bf16 v[64:67], a[72:75], v[152:155], v[64:67]\n"
"v_exp_f32 v73, v73\n"
"v_mfma_f32_16x16x32_bf16 v[64:67], a[76:79], v[156:159], v[64:67]\n"
"ds_read_b128 a[64:67], v4 offset:32768\n"
"v_add_f32 v34, v34, v72\n"
"v_mfma_f32_16x16x32_bf16 v[224:227], a[128:131], v[56:59], v[224:227]\n"
"v_exp_f32 v74, v74\n"
"v_mfma_f32_16x16x32_bf16 v[228:231], a[132:135], v[56:59], v[228:231]\n"
"v_cvt_pk_bf16_f32 v60, v72, v73\n"
"v_add_f32 v34, v34, v73\n"
"v_mfma_f32_16x16x32_bf16 v[232:235], a[136:139], v[56:59], v[232:235]\n"
"v_exp_f32 v75, v75\n"
"v_mfma_f32_16x16x32_bf16 v[236:239], a[140:143], v[56:59], v[236:239]\n"
"ds_read_b128 a[68:71], v5 offset:32768\n"
"v_add_f32 v34, v34, v74\n"
"v_mfma_f32_16x16x32_bf16 v[68:71], a[80:83], v[144:147], v[52:55]\n"
"v_exp_f32 v76, v76\n"
"v_mfma_f32_16x16x32_bf16 v[68:71], a[84:87], v[148:151], v[68:71]\n"
"v_cvt_pk_bf16_f32 v61, v74, v75\n"
"v_add_f32 v34, v34, v75\n"
"v_mfma_f32_16x16x32_bf16 v[68:71], a[88:91], v[152:155], v[68:71]\n"
"v_exp_f32 v77, v77\n"
"v_mfma_f32_16x16x32_bf16 v[68:71], a[92:95], v[156:159], v[68:71]\n"
"ds_read_b128 a[72:75], v6 offset:32768\n"
"v_add_f32 v34, v34, v76\n"
"v_mfma_f32_16x16x32_bf16 v[240:243], a[144:147], v[56:59], v[240:243]\n"
"v_exp_f32 v78, v78\n"
"v_mfma_f32_16x16x32_bf16 v[244:247], a[148:151], v[56:59], v[244:247]\n"
"v_cvt_pk_bf16_f32 v62, v76, v77\n"
"v_add_f32 v34, v34, v77\n"
"v_mfma_f32_16x16x32_bf16 v[248:251], a[152:155], v[56:59], v[248:251]\n"
"v_exp_f32 v79, v79\n"
"v_mfma_f32_16x16x32_bf16 v[252:255], a[156:159], v[56:59], v[252:255]\n"
"ds_read_b128 a[76:79], v7 offset:32768\n"
"v_add_f32 v34, v34, v78\n"
"v_mfma_f32_16x16x32_bf16 v[72:75], a[96:99], v[80:83], v[36:39]\n"
"v_exp_f32 v64, v64\n"
"v_mfma_f32_16x16x32_bf16 v[72:75], a[100:103], v[84:87], v[72:75]\n"
"v_cvt_pk_bf16_f32 v63, v78, v79\n"
"v_add_f32 v34, v34, v79\n"
"v_mfma_f32_16x16x32_bf16 v[72:75], a[104:107], v[88:91], v[72:75]\n"
"v_exp_f32 v65, v65\n"
"v_mfma_f32_16x16x32_bf16 v[72:75], a[108:111], v[92:95], v[72:75]\n"
"ds_read_b128 a[80:83], v4 offset:36864\n"
"v_add_f32 v35, v35, v64\n"
"v_mfma_f32_16x16x32_bf16 a[192:195], a[128:131], v[60:63], a[192:195]\n"
"v_exp_f32 v66, v66\n"
"v_mfma_f32_16x16x32_bf16 a[196:199], a[132:135], v[60:63], a[196:199]\n"
"v_cvt_pk_bf16_f32 v56, v64, v65\n"
"v_add_f32 v35, v35, v65\n"
"v_mfma_f32_16x16x32_bf16 a[200:203], a[136:139], v[60:63], a[200:203]\n"
"v_exp_f32 v67, v67\n"
"v_mfma_f32_16x16x32_bf16 a[204:207], a[140:143], v[60:63], a[204:207]\n"
"ds_read_b128 a[84:87], v5 offset:36864\n"
"v_add_f32 v35, v35, v66\n"
"v_mfma_f32_16x16x32_bf16 v[76:79], a[112:115], v[80:83], v[36:39]\n"
"v_exp_f32 v68, v68\n"
"v_mfma_f32_16x16x32_bf16 v[76:79], a[116:119], v[84:87], v[76:79]\n"
"v_cvt_pk_bf16_f32 v57, v66, v67\n"
"v_add_f32 v35, v35, v67\n"
"v_mfma_f32_16x16x32_bf16 v[76:79], a[120:123], v[88:91], v[76:79]\n"
"v_exp_f32 v69, v69\n"
"v_mfma_f32_16x16x32_bf16 v[76:79], a[124:127], v[92:95], v[76:79]\n"
"ds_read_b128 a[88:91], v6 offset:36864\n"
"v_add_f32 v35, v35, v68\n"
"v_mfma_f32_16x16x32_bf16 a[208:211], a[144:147], v[60:63], a[208:211]\n"
"v_exp_f32 v70, v70\n"
"v_mfma_f32_16x16x32_bf16 a[212:215], a[148:151], v[60:63], a[212:215]\n"
"v_cvt_pk_bf16_f32 v58, v68, v69\n"
"v_add_f32 v35, v35, v69\n"
"v_mfma_f32_16x16x32_bf16 a[216:219], a[152:155], v[60:63], a[216:219]\n"
"v_exp_f32 v71, v71\n"
"v_mfma_f32_16x16x32_bf16 a[220:223], a[156:159], v[60:63], a[220:223]\n"
"ds_read_b128 a[92:95], v7 offset:36864\n"
"v_add_f32 v35, v35, v70\n"
"v_mfma_f32_16x16x32_bf16 v[64:67], a[96:99], v[96:99], v[40:43]\n"
"v_exp_f32 v72, v72\n"
"v_mfma_f32_16x16x32_bf16 v[64:67], a[100:103], v[100:103], v[64:67]\n"
"v_cvt_pk_bf16_f32 v59, v70, v71\n"
"v_add_f32 v35, v35, v71\n"
"v_mfma_f32_16x16x32_bf16 v[64:67], a[104:107], v[104:107], v[64:67]\n"
"v_exp_f32 v73, v73\n"
"v_mfma_f32_16x16x32_bf16 v[64:67], a[108:111], v[108:111], v[64:67]\n"
"v_add_f32 v29, v29, v72\n"
"v_mfma_f32_16x16x32_bf16 a[224:227], a[128:131], v[56:59], a[224:227]\n"
"v_exp_f32 v74, v74\n"
"v_mfma_f32_16x16x32_bf16 a[228:231], a[132:135], v[56:59], a[228:231]\n"
"v_cvt_pk_bf16_f32 v60, v72, v73\n"
"v_add_f32 v29, v29, v73\n"
"v_mfma_f32_16x16x32_bf16 a[232:235], a[136:139], v[56:59], a[232:235]\n"
"v_exp_f32 v75, v75\n"
"v_mfma_f32_16x16x32_bf16 a[236:239], a[140:143], v[56:59], a[236:239]\n"
"v_add_f32 v29, v29, v74\n"
"v_mfma_f32_16x16x32_bf16 v[68:71], a[112:115], v[96:99], v[40:43]\n"
"v_exp_f32 v76, v76\n"
"v_mfma_f32_16x16x32_bf16 v[68:71], a[116:119], v[100:103], v[68:71]\n"
"v_cvt_pk_bf16_f32 v61, v74, v75\n"
"v_add_f32 v29, v29, v75\n"
"v_mfma_f32_16x16x32_bf16 v[68:71], a[120:123], v[104:107], v[68:71]\n"
"v_exp_f32 v77, v77\n"
"v_mfma_f32_16x16x32_bf16 v[68:71], a[124:127], v[108:111], v[68:71]\n"
"v_add_f32 v29, v29, v76\n"
"v_mfma_f32_16x16x32_bf16 a[240:243], a[144:147], v[56:59], a[240:243]\n"
"v_exp_f32 v78, v78\n"
"v_mfma_f32_16x16x32_bf16 a[244:247], a[148:151], v[56:59], a[244:247]\n"
"v_cvt_pk_bf16_f32 v62, v76, v77\n"
"v_add_f32 v29, v29, v77\n"
"v_mfma_f32_16x16x32_bf16 a[248:251], a[152:155], v[56:59], a[248:251]\n"
"v_exp_f32 v79, v79\n"
"v_mfma_f32_16x16x32_bf16 a[252:255], a[156:159], v[56:59], a[252:255]\n"
"v_add_f32 v29, v29, v78\n"
"v_mfma_f32_16x16x32_bf16 v[72:75], a[96:99], v[112:115], v[44:47]\n"
"v_exp_f32 v64, v64\n"
"v_mfma_f32_16x16x32_bf16 v[72:75], a[100:103], v[116:119], v[72:75]\n"
"v_cvt_pk_bf16_f32 v63, v78, v79\n"
"v_add_f32 v29, v29, v79\n"
"v_mfma_f32_16x16x32_bf16 v[72:75], a[104:107], v[120:123], v[72:75]\n"
"v_exp_f32 v65, v65\n"
"v_mfma_f32_16x16x32_bf16 v[72:75], a[108:111], v[124:127], v[72:75]\n"
"s_waitcnt lgkmcnt(0)\n"
"s_barrier\n"
"v_mfma_f32_16x16x32_bf16 v[160:163], a[160:163], v[60:63], v[160:163]\n"
"v_exp_f32 v66, v66\n"
"v_mfma_f32_16x16x32_bf16 v[164:167], a[164:167], v[60:63], v[164:167]\n"
"ds_read_b64_tr_b16 a[128:129], v8 offset:32768\n"
"v_cvt_pk_bf16_f32 v56, v64, v65\n"
"v_mfma_f32_16x16x32_bf16 v[168:171], a[168:171], v[60:63], v[168:171]\n"
"v_exp_f32 v67, v67\n"
"v_mfma_f32_16x16x32_bf16 v[172:175], a[172:175], v[60:63], v[172:175]\n"
"ds_read_b64_tr_b16 a[130:131], v8 offset:36864\n"
"ds_read_b64_tr_b16 a[132:133], v9 offset:32768\n"
"v_mfma_f32_16x16x32_bf16 v[76:79], a[112:115], v[112:115], v[44:47]\n"
"v_exp_f32 v68, v68\n"
"v_mfma_f32_16x16x32_bf16 v[76:79], a[116:119], v[116:119], v[76:79]\n"
"ds_read_b64_tr_b16 a[134:135], v9 offset:36864\n"
"v_cvt_pk_bf16_f32 v57, v66, v67\n"
"v_mfma_f32_16x16x32_bf16 v[76:79], a[120:123], v[120:123], v[76:79]\n"
"v_exp_f32 v69, v69\n"
"v_mfma_f32_16x16x32_bf16 v[76:79], a[124:127], v[124:127], v[76:79]\n"
"ds_read_b64_tr_b16 a[136:137], v8 offset:33280\n"
"ds_read_b64_tr_b16 a[138:139], v8 offset:37376\n"
"v_mfma_f32_16x16x32_bf16 v[176:179], a[176:179], v[60:63], v[176:179]\n"
"v_exp_f32 v70, v70\n"
"v_mfma_f32_16x16x32_bf16 v[180:183], a[180:183], v[60:63], v[180:183]\n"
"ds_read_b64_tr_b16 a[140:141], v9 offset:33280\n"
"v_cvt_pk_bf16_f32 v58, v68, v69\n"
"v_mfma_f32_16x16x32_bf16 v[184:187], a[184:187], v[60:63], v[184:187]\n"
"v_exp_f32 v71, v71\n"
"v_mfma_f32_16x16x32_bf16 v[188:191], a[188:191], v[60:63], v[188:191]\n"
"ds_read_b64_tr_b16 a[142:143], v9 offset:37376\n"
"ds_read_b64_tr_b16 a[144:145], v8 offset:33792\n"
"v_mfma_f32_16x16x32_bf16 v[64:67], a[96:99], v[128:131], v[48:51]\n"
"v_exp_f32 v72, v72\n"
"v_mfma_f32_16x16x32_bf16 v[64:67], a[100:103], v[132:135], v[64:67]\n"
"ds_read_b64_tr_b16 a[146:147], v8 offset:37888\n"
"v_cvt_pk_bf16_f32 v59, v70, v71\n"
"v_mfma_f32_16x16x32_bf16 v[64:67], a[104:107], v[136:139], v[64:67]\n"
"v_exp_f32 v73, v73\n"
"v_mfma_f32_16x16x32_bf16 v[64:67], a[108:111], v[140:143], v[64:67]\n"
"ds_read_b64_tr_b16 a[148:149], v9 offset:33792\n"
"v_add_f32 v31, v31, v72\n"
"v_mfma_f32_16x16x32_bf16 v[192:195], a[160:163], v[56:59], v[192:195]\n"
"v_exp_f32 v74, v74\n"
"v_mfma_f32_16x16x32_bf16 v[196:199], a[164:167], v[56:59], v[196:199]\n"
"v_cvt_pk_bf16_f32 v60, v72, v73\n"
"v_add_f32 v31, v31, v73\n"
"v_mfma_f32_16x16x32_bf16 v[200:203], a[168:171], v[56:59], v[200:203]\n"
"v_exp_f32 v75, v75\n"
"v_mfma_f32_16x16x32_bf16 v[204:207], a[172:175], v[56:59], v[204:207]\n"
"ds_read_b64_tr_b16 a[150:151], v9 offset:37888\n"
"v_add_f32 v31, v31, v74\n"
"v_mfma_f32_16x16x32_bf16 v[18:21], v[10:13], v[56:59], v[18:21]\n"
"ds_read_b64_tr_b16 a[152:153], v8 offset:34304\n"
"ds_read_b64_tr_b16 a[154:155], v8 offset:38400\n"
"v_mfma_f32_16x16x32_bf16 v[68:71], a[112:115], v[128:131], v[48:51]\n"
"v_exp_f32 v76, v76\n"
"v_mfma_f32_16x16x32_bf16 v[68:71], a[116:119], v[132:135], v[68:71]\n"
"v_cvt_pk_bf16_f32 v61, v74, v75\n"
"v_add_f32 v31, v31, v75\n"
"v_mfma_f32_16x16x32_bf16 v[68:71], a[120:123], v[136:139], v[68:71]\n"
"v_exp_f32 v77, v77\n"
"v_mfma_f32_16x16x32_bf16 v[68:71], a[124:127], v[140:143], v[68:71]\n"
"ds_read_b64_tr_b16 a[156:157], v9 offset:34304\n"
"v_add_f32 v31, v31, v76\n"
"v_mfma_f32_16x16x32_bf16 v[208:211], a[176:179], v[56:59], v[208:211]\n"
"v_exp_f32 v78, v78\n"
"v_mfma_f32_16x16x32_bf16 v[212:215], a[180:183], v[56:59], v[212:215]\n"
"v_cvt_pk_bf16_f32 v62, v76, v77\n"
"v_add_f32 v31, v31, v77\n"
"v_mfma_f32_16x16x32_bf16 v[216:219], a[184:187], v[56:59], v[216:219]\n"
"v_exp_f32 v79, v79\n"
"v_mfma_f32_16x16x32_bf16 v[220:223], a[188:191], v[56:59], v[220:223]\n"
"ds_read_b64_tr_b16 a[158:159], v9 offset:38400\n"
"v_add_f32 v31, v31, v78\n"
"v_mfma_f32_16x16x32_bf16 v[72:75], a[96:99], v[144:147], v[52:55]\n"
"v_exp_f32 v64, v64\n"
"v_mfma_f32_16x16x32_bf16 v[72:75], a[100:103], v[148:151], v[72:75]\n"
"v_cvt_pk_bf16_f32 v63, v78, v79\n"
"v_add_f32 v31, v31, v79\n"
"v_mfma_f32_16x16x32_bf16 v[72:75], a[104:107], v[152:155], v[72:75]\n"
"v_exp_f32 v65, v65\n"
"v_mfma_f32_16x16x32_bf16 v[72:75], a[108:111], v[156:159], v[72:75]\n"
"ds_read_b128 a[96:99], v4 offset:40960\n"
"v_add_f32 v34, v34, v64\n"
"v_mfma_f32_16x16x32_bf16 v[224:227], a[160:163], v[60:63], v[224:227]\n"
"v_exp_f32 v66, v66\n"
"v_mfma_f32_16x16x32_bf16 v[228:231], a[164:167], v[60:63], v[228:231]\n"
"v_cvt_pk_bf16_f32 v56, v64, v65\n"
"v_add_f32 v34, v34, v65\n"
"v_mfma_f32_16x16x32_bf16 v[232:235], a[168:171], v[60:63], v[232:235]\n"
"v_exp_f32 v67, v67\n"
"v_mfma_f32_16x16x32_bf16 v[236:239], a[172:175], v[60:63], v[236:239]\n"
"ds_read_b128 a[100:103], v5 offset:40960\n"
"v_add_f32 v34, v34, v66\n"
"v_mfma_f32_16x16x32_bf16 v[76:79], a[112:115], v[144:147], v[52:55]\n"
"v_exp_f32 v68, v68\n"
"v_mfma_f32_16x16x32_bf16 v[76:79], a[116:119], v[148:151], v[76:79]\n"
"v_cvt_pk_bf16_f32 v57, v66, v67\n"
"v_add_f32 v34, v34, v67\n"
"v_mfma_f32_16x16x32_bf16 v[76:79], a[120:123], v[152:155], v[76:79]\n"
"v_exp_f32 v69, v69\n"
"v_mfma_f32_16x16x32_bf16 v[76:79], a[124:127], v[156:159], v[76:79]\n"
"ds_read_b128 a[104:107], v6 offset:40960\n"
"v_add_f32 v34, v34, v68\n"
"v_mfma_f32_16x16x32_bf16 v[240:243], a[176:179], v[60:63], v[240:243]\n"
"v_exp_f32 v70, v70\n"
"v_mfma_f32_16x16x32_bf16 v[244:247], a[180:183], v[60:63], v[244:247]\n"
"v_cvt_pk_bf16_f32 v58, v68, v69\n"
"v_add_f32 v34, v34, v69\n"
"v_mfma_f32_16x16x32_bf16 v[248:251], a[184:187], v[60:63], v[248:251]\n"
"v_exp_f32 v71, v71\n"
"v_mfma_f32_16x16x32_bf16 v[252:255], a[188:191], v[60:63], v[252:255]\n"
"ds_read_b128 a[108:111], v7 offset:40960\n"
"v_add_f32 v34, v34, v70\n"
"v_mfma_f32_16x16x32_bf16 v[64:67], a[64:67], v[80:83], v[36:39]\n"
"v_exp_f32 v72, v72\n"
"v_mfma_f32_16x16x32_bf16 v[64:67], a[68:71], v[84:87], v[64:67]\n"
"v_cvt_pk_bf16_f32 v59, v70, v71\n"
"v_add_f32 v34, v34, v71\n"
"v_mfma_f32_16x16x32_bf16 v[64:67], a[72:75], v[88:91], v[64:67]\n"
"v_exp_f32 v73, v73\n"
"v_mfma_f32_16x16x32_bf16 v[64:67], a[76:79], v[92:95], v[64:67]\n"
"ds_read_b128 a[112:115], v4 offset:45056\n"
"v_add_f32 v35, v35, v72\n"
"v_mfma_f32_16x16x32_bf16 a[192:195], a[160:163], v[56:59], a[192:195]\n"
"v_exp_f32 v74, v74\n"
"v_mfma_f32_16x16x32_bf16 a[196:199], a[164:167], v[56:59], a[196:199]\n"
"v_cvt_pk_bf16_f32 v60, v72, v73\n"
"v_add_f32 v35, v35, v73\n"
"v_mfma_f32_16x16x32_bf16 a[200:203], a[168:171], v[56:59], a[200:203]\n"
"v_exp_f32 v75, v75\n"
"v_mfma_f32_16x16x32_bf16 a[204:207], a[172:175], v[56:59], a[204:207]\n"
"ds_read_b128 a[116:119], v5 offset:45056\n"
"v_add_f32 v35, v35, v74\n"
"v_mfma_f32_16x16x32_bf16 v[68:71], a[80:83], v[80:83], v[36:39]\n"
"v_exp_f32 v76, v76\n"
"v_mfma_f32_16x16x32_bf16 v[68:71], a[84:87], v[84:87], v[68:71]\n"
"v_cvt_pk_bf16_f32 v61, v74, v75\n"
"v_add_f32 v35, v35, v75\n"
"v_mfma_f32_16x16x32_bf16 v[68:71], a[88:91], v[88:91], v[68:71]\n"
"v_exp_f32 v77, v77\n"
"v_mfma_f32_16x16x32_bf16 v[68:71], a[92:95], v[92:95], v[68:71]\n"
"ds_read_b128 a[120:123], v6 offset:45056\n"
"v_add_f32 v35, v35, v76\n"
"v_mfma_f32_16x16x32_bf16 a[208:211], a[176:179], v[56:59], a[208:211]\n"
"v_exp_f32 v78, v78\n"
"v_mfma_f32_16x16x32_bf16 a[212:215], a[180:183], v[56:59], a[212:215]\n"
"v_cvt_pk_bf16_f32 v62, v76, v77\n"
"v_add_f32 v35, v35, v77\n"
"v_mfma_f32_16x16x32_bf16 a[216:219], a[184:187], v[56:59], a[216:219]\n"
"v_exp_f32 v79, v79\n"
"v_mfma_f32_16x16x32_bf16 a[220:223], a[188:191], v[56:59], a[220:223]\n"
"ds_read_b128 a[124:127], v7 offset:45056\n"
"v_add_f32 v35, v35, v78\n"
"v_mfma_f32_16x16x32_bf16 v[72:75], a[64:67], v[96:99], v[40:43]\n"
"v_exp_f32 v64, v64\n"
"v_mfma_f32_16x16x32_bf16 v[72:75], a[68:71], v[100:103], v[72:75]\n"
"v_cvt_pk_bf16_f32 v63, v78, v79\n"
"v_add_f32 v35, v35, v79\n"
"v_mfma_f32_16x16x32_bf16 v[72:75], a[72:75], v[104:107], v[72:75]\n"
"v_exp_f32 v65, v65\n"
"v_mfma_f32_16x16x32_bf16 v[72:75], a[76:79], v[108:111], v[72:75]\n"
"v_add_f32 v29, v29, v64\n"
"v_mfma_f32_16x16x32_bf16 a[224:227], a[160:163], v[60:63], a[224:227]\n"
"v_exp_f32 v66, v66\n"
"v_mfma_f32_16x16x32_bf16 a[228:231], a[164:167], v[60:63], a[228:231]\n"
"v_cvt_pk_bf16_f32 v56, v64, v65\n"
"v_add_f32 v29, v29, v65\n"
"v_mfma_f32_16x16x32_bf16 a[232:235], a[168:171], v[60:63], a[232:235]\n"
"v_exp_f32 v67, v67\n"
"v_mfma_f32_16x16x32_bf16 a[236:239], a[172:175], v[60:63], a[236:239]\n"
"v_add_f32 v29, v29, v66\n"
"v_mfma_f32_16x16x32_bf16 v[76:79], a[80:83], v[96:99], v[40:43]\n"
"v_exp_f32 v68, v68\n"
"v_mfma_f32_16x16x32_bf16 v[76:79], a[84:87], v[100:103], v[76:79]\n"
"v_cvt_pk_bf16_f32 v57, v66, v67\n"
"v_add_f32 v29, v29, v67\n"
"v_mfma_f32_16x16x32_bf16 v[76:79], a[88:91], v[104:107], v[76:79]\n"
"v_exp_f32 v69, v69\n"
"v_mfma_f32_16x16x32_bf16 v[76:79], a[92:95], v[108:111], v[76:79]\n"
"v_add_f32 v29, v29, v68\n"
"v_mfma_f32_16x16x32_bf16 a[240:243], a[176:179], v[60:63], a[240:243]\n"
"v_exp_f32 v70, v70\n"
"v_mfma_f32_16x16x32_bf16 a[244:247], a[180:183], v[60:63], a[244:247]\n"
"v_cvt_pk_bf16_f32 v58, v68, v69\n"
"v_add_f32 v29, v29, v69\n"
"v_mfma_f32_16x16x32_bf16 a[248:251], a[184:187], v[60:63], a[248:251]\n"
"v_exp_f32 v71, v71\n"
"v_mfma_f32_16x16x32_bf16 a[252:255], a[188:191], v[60:63], a[252:255]\n"
"v_add_f32 v29, v29, v70\n"
"v_mfma_f32_16x16x32_bf16 v[64:67], a[64:67], v[112:115], v[44:47]\n"
"v_exp_f32 v72, v72\n"
"v_mfma_f32_16x16x32_bf16 v[64:67], a[68:71], v[116:119], v[64:67]\n"
"v_cvt_pk_bf16_f32 v59, v70, v71\n"
"v_add_f32 v29, v29, v71\n"
"v_mfma_f32_16x16x32_bf16 v[64:67], a[72:75], v[120:123], v[64:67]\n"
"v_exp_f32 v73, v73\n"
"v_mfma_f32_16x16x32_bf16 v[64:67], a[76:79], v[124:127], v[64:67]\n"
"s_waitcnt lgkmcnt(0)\n"
"s_barrier\n"
"v_mfma_f32_16x16x32_bf16 v[160:163], a[128:131], v[56:59], v[160:163]\n"
"v_exp_f32 v74, v74\n"
"v_mfma_f32_16x16x32_bf16 v[164:167], a[132:135], v[56:59], v[164:167]\n"
"ds_read_b64_tr_b16 a[160:161], v8 offset:40960\n"
"v_cvt_pk_bf16_f32 v60, v72, v73\n"
"v_mfma_f32_16x16x32_bf16 v[168:171], a[136:139], v[56:59], v[168:171]\n"
"v_exp_f32 v75, v75\n"
"v_mfma_f32_16x16x32_bf16 v[172:175], a[140:143], v[56:59], v[172:175]\n"
"ds_read_b64_tr_b16 a[162:163], v8 offset:45056\n"
"ds_read_b64_tr_b16 a[164:165], v9 offset:40960\n"
"v_mfma_f32_16x16x32_bf16 v[68:71], a[80:83], v[112:115], v[44:47]\n"
"v_exp_f32 v76, v76\n"
"v_mfma_f32_16x16x32_bf16 v[68:71], a[84:87], v[116:119], v[68:71]\n"
"ds_read_b64_tr_b16 a[166:167], v9 offset:45056\n"
"v_cvt_pk_bf16_f32 v61, v74, v75\n"
"v_mfma_f32_16x16x32_bf16 v[68:71], a[88:91], v[120:123], v[68:71]\n"
"v_exp_f32 v77, v77\n"
"v_mfma_f32_16x16x32_bf16 v[68:71], a[92:95], v[124:127], v[68:71]\n"
"ds_read_b64_tr_b16 a[168:169], v8 offset:41472\n"
"ds_read_b64_tr_b16 a[170:171], v8 offset:45568\n"
"v_mfma_f32_16x16x32_bf16 v[176:179], a[144:147], v[56:59], v[176:179]\n"
"v_exp_f32 v78, v78\n"
"v_mfma_f32_16x16x32_bf16 v[180:183], a[148:151], v[56:59], v[180:183]\n"
"ds_read_b64_tr_b16 a[172:173], v9 offset:41472\n"
"v_cvt_pk_bf16_f32 v62, v76, v77\n"
"v_mfma_f32_16x16x32_bf16 v[184:187], a[152:155], v[56:59], v[184:187]\n"
"v_exp_f32 v79, v79\n"
"v_mfma_f32_16x16x32_bf16 v[188:191], a[156:159], v[56:59], v[188:191]\n"
"ds_read_b64_tr_b16 a[174:175], v9 offset:45568\n"
"ds_read_b64_tr_b16 a[176:177], v8 offset:41984\n"
"v_mfma_f32_16x16x32_bf16 v[72:75], a[64:67], v[128:131], v[48:51]\n"
"v_exp_f32 v64, v64\n"
"v_mfma_f32_16x16x32_bf16 v[72:75], a[68:71], v[132:135], v[72:75]\n"
"ds_read_b64_tr_b16 a[178:179], v8 offset:46080\n"
"v_cvt_pk_bf16_f32 v63, v78, v79\n"
"v_mfma_f32_16x16x32_bf16 v[72:75], a[72:75], v[136:139], v[72:75]\n"
"v_exp_f32 v65, v65\n"
"v_mfma_f32_16x16x32_bf16 v[72:75], a[76:79], v[140:143], v[72:75]\n"
"ds_read_b64_tr_b16 a[180:181], v9 offset:41984\n"
"v_add_f32 v31, v31, v64\n"
"v_mfma_f32_16x16x32_bf16 v[192:195], a[128:131], v[60:63], v[192:195]\n"
"v_exp_f32 v66, v66\n"
"v_mfma_f32_16x16x32_bf16 v[196:199], a[132:135], v[60:63], v[196:199]\n"
"v_cvt_pk_bf16_f32 v56, v64, v65\n"
"v_add_f32 v31, v31, v65\n"
"v_mfma_f32_16x16x32_bf16 v[200:203], a[136:139], v[60:63], v[200:203]\n"
"v_exp_f32 v67, v67\n"
"v_mfma_f32_16x16x32_bf16 v[204:207], a[140:143], v[60:63], v[204:207]\n"
"ds_read_b64_tr_b16 a[182:183], v9 offset:46080\n"
"v_add_f32 v31, v31, v66\n"
"v_mfma_f32_16x16x32_bf16 v[18:21], v[10:13], v[60:63], v[18:21]\n"
"ds_read_b64_tr_b16 a[184:185], v8 offset:42496\n"
"ds_read_b64_tr_b16 a[186:187], v8 offset:46592\n"
"v_mfma_f32_16x16x32_bf16 v[76:79], a[80:83], v[128:131], v[48:51]\n"
"v_exp_f32 v68, v68\n"
"v_mfma_f32_16x16x32_bf16 v[76:79], a[84:87], v[132:135], v[76:79]\n"
"v_cvt_pk_bf16_f32 v57, v66, v67\n"
"v_add_f32 v31, v31, v67\n"
"v_mfma_f32_16x16x32_bf16 v[76:79], a[88:91], v[136:139], v[76:79]\n"
"v_exp_f32 v69, v69\n"
"v_mfma_f32_16x16x32_bf16 v[76:79], a[92:95], v[140:143], v[76:79]\n"
"ds_read_b64_tr_b16 a[188:189], v9 offset:42496\n"
"v_add_f32 v31, v31, v68\n"
"v_mfma_f32_16x16x32_bf16 v[208:211], a[144:147], v[60:63], v[208:211]\n"
"v_exp_f32 v70, v70\n"
"v_mfma_f32_16x16x32_bf16 v[212:215], a[148:151], v[60:63], v[212:215]\n"
"v_cvt_pk_bf16_f32 v58, v68, v69\n"
"v_add_f32 v31, v31, v69\n"
"v_mfma_f32_16x16x32_bf16 v[216:219], a[152:155], v[60:63], v[216:219]\n"
"v_exp_f32 v71, v71\n"
"v_mfma_f32_16x16x32_bf16 v[220:223], a[156:159], v[60:63], v[220:223]\n"
"ds_read_b64_tr_b16 a[190:191], v9 offset:46592\n"
"v_add_f32 v31, v31, v70\n"
"v_mfma_f32_16x16x32_bf16 v[64:67], a[64:67], v[144:147], v[52:55]\n"
"v_exp_f32 v72, v72\n"
"v_mfma_f32_16x16x32_bf16 v[64:67], a[68:71], v[148:151], v[64:67]\n"
"v_cvt_pk_bf16_f32 v59, v70, v71\n"
"v_add_f32 v31, v31, v71\n"
"v_mfma_f32_16x16x32_bf16 v[64:67], a[72:75], v[152:155], v[64:67]\n"
"v_exp_f32 v73, v73\n"
"v_mfma_f32_16x16x32_bf16 v[64:67], a[76:79], v[156:159], v[64:67]\n"
"ds_read_b128 a[64:67], v4 offset:49152\n"
"v_add_f32 v34, v34, v72\n"
"v_mfma_f32_16x16x32_bf16 v[224:227], a[128:131], v[56:59], v[224:227]\n"
"v_exp_f32 v74, v74\n"
"v_mfma_f32_16x16x32_bf16 v[228:231], a[132:135], v[56:59], v[228:231]\n"
"v_cvt_pk_bf16_f32 v60, v72, v73\n"
"v_add_f32 v34, v34, v73\n"
"v_mfma_f32_16x16x32_bf16 v[232:235], a[136:139], v[56:59], v[232:235]\n"
"v_exp_f32 v75, v75\n"
"v_mfma_f32_16x16x32_bf16 v[236:239], a[140:143], v[56:59], v[236:239]\n"
"ds_read_b128 a[68:71], v5 offset:49152\n"
"v_add_f32 v34, v34, v74\n"
"v_mfma_f32_16x16x32_bf16 v[68:71], a[80:83], v[144:147], v[52:55]\n"
"v_exp_f32 v76, v76\n"
"v_mfma_f32_16x16x32_bf16 v[68:71], a[84:87], v[148:151], v[68:71]\n"
"v_cvt_pk_bf16_f32 v61, v74, v75\n"
"v_add_f32 v34, v34, v75\n"
"v_mfma_f32_16x16x32_bf16 v[68:71], a[88:91], v[152:155], v[68:71]\n"
"v_exp_f32 v77, v77\n"
"v_mfma_f32_16x16x32_bf16 v[68:71], a[92:95], v[156:159], v[68:71]\n"
"ds_read_b128 a[72:75], v6 offset:49152\n"
"v_add_f32 v34, v34, v76\n"
"v_mfma_f32_16x16x32_bf16 v[240:243], a[144:147], v[56:59], v[240:243]\n"
"v_exp_f32 v78, v78\n"
"v_mfma_f32_16x16x32_bf16 v[244:247], a[148:151], v[56:59], v[244:247]\n"
"v_cvt_pk_bf16_f32 v62, v76, v77\n"
"v_add_f32 v34, v34, v77\n"
"v_mfma_f32_16x16x32_bf16 v[248:251], a[152:155], v[56:59], v[248:251]\n"
"v_exp_f32 v79, v79\n"
"v_mfma_f32_16x16x32_bf16 v[252:255], a[156:159], v[56:59], v[252:255]\n"
"ds_read_b128 a[76:79], v7 offset:49152\n"
"v_add_f32 v34, v34, v78\n"
"v_mfma_f32_16x16x32_bf16 v[72:75], a[96:99], v[80:83], v[36:39]\n"
"v_exp_f32 v64, v64\n"
"v_mfma_f32_16x16x32_bf16 v[72:75], a[100:103], v[84:87], v[72:75]\n"
"v_cvt_pk_bf16_f32 v63, v78, v79\n"
"v_add_f32 v34, v34, v79\n"
"v_mfma_f32_16x16x32_bf16 v[72:75], a[104:107], v[88:91], v[72:75]\n"
"v_exp_f32 v65, v65\n"
"v_mfma_f32_16x16x32_bf16 v[72:75], a[108:111], v[92:95], v[72:75]\n"
"ds_read_b128 a[80:83], v4 offset:53248\n"
"v_add_f32 v35, v35, v64\n"
"v_mfma_f32_16x16x32_bf16 a[192:195], a[128:131], v[60:63], a[192:195]\n"
"v_exp_f32 v66, v66\n"
"v_mfma_f32_16x16x32_bf16 a[196:199], a[132:135], v[60:63], a[196:199]\n"
"v_cvt_pk_bf16_f32 v56, v64, v65\n"
"v_add_f32 v35, v35, v65\n"
"v_mfma_f32_16x16x32_bf16 a[200:203], a[136:139], v[60:63], a[200:203]\n"
"v_exp_f32 v67, v67\n"
"v_mfma_f32_16x16x32_bf16 a[204:207], a[140:143], v[60:63], a[204:207]\n"
"ds_read_b128 a[84:87], v5 offset:53248\n"
"v_add_f32 v35, v35, v66\n"
"v_mfma_f32_16x16x32_bf16 v[76:79], a[112:115], v[80:83], v[36:39]\n"
"v_exp_f32 v68, v68\n"
"v_mfma_f32_16x16x32_bf16 v[76:79], a[116:119], v[84:87], v[76:79]\n"
"v_cvt_pk_bf16_f32 v57, v66, v67\n"
"v_add_f32 v35, v35, v67\n"
"v_mfma_f32_16x16x32_bf16 v[76:79], a[120:123], v[88:91], v[76:79]\n"
"v_exp_f32 v69, v69\n"
"v_mfma_f32_16x16x32_bf16 v[76:79], a[124:127], v[92:95], v[76:79]\n"
"ds_read_b128 a[88:91], v6 offset:53248\n"
"v_add_f32 v35, v35, v68\n"
"v_mfma_f32_16x16x32_bf16 a[208:211], a[144:147], v[60:63], a[208:211]\n"
"v_exp_f32 v70, v70\n"
"v_mfma_f32_16x16x32_bf16 a[212:215], a[148:151], v[60:63], a[212:215]\n"
"v_cvt_pk_bf16_f32 v58, v68, v69\n"
"v_add_f32 v35, v35, v69\n"
"v_mfma_f32_16x16x32_bf16 a[216:219], a[152:155], v[60:63], a[216:219]\n"
"v_exp_f32 v71, v71\n"
"v_mfma_f32_16x16x32_bf16 a[220:223], a[156:159], v[60:63], a[220:223]\n"
"ds_read_b128 a[92:95], v7 offset:53248\n"
"v_add_f32 v35, v35, v70\n"
"v_mfma_f32_16x16x32_bf16 v[64:67], a[96:99], v[96:99], v[40:43]\n"
"v_exp_f32 v72, v72\n"
"v_mfma_f32_16x16x32_bf16 v[64:67], a[100:103], v[100:103], v[64:67]\n"
"v_cvt_pk_bf16_f32 v59, v70, v71\n"
"v_add_f32 v35, v35, v71\n"
"v_mfma_f32_16x16x32_bf16 v[64:67], a[104:107], v[104:107], v[64:67]\n"
"v_exp_f32 v73, v73\n"
"v_mfma_f32_16x16x32_bf16 v[64:67], a[108:111], v[108:111], v[64:67]\n"
"v_add_f32 v29, v29, v72\n"
"v_mfma_f32_16x16x32_bf16 a[224:227], a[128:131], v[56:59], a[224:227]\n"
"v_exp_f32 v74, v74\n"
"v_mfma_f32_16x16x32_bf16 a[228:231], a[132:135], v[56:59], a[228:231]\n"
"v_cvt_pk_bf16_f32 v60, v72, v73\n"
"v_add_f32 v29, v29, v73\n"
"v_mfma_f32_16x16x32_bf16 a[232:235], a[136:139], v[56:59], a[232:235]\n"
"v_exp_f32 v75, v75\n"
"v_mfma_f32_16x16x32_bf16 a[236:239], a[140:143], v[56:59], a[236:239]\n"
"v_add_f32 v29, v29, v74\n"
"v_mfma_f32_16x16x32_bf16 v[68:71], a[112:115], v[96:99], v[40:43]\n"
"v_exp_f32 v76, v76\n"
"v_mfma_f32_16x16x32_bf16 v[68:71], a[116:119], v[100:103], v[68:71]\n"
"v_cvt_pk_bf16_f32 v61, v74, v75\n"
"v_add_f32 v29, v29, v75\n"
"v_mfma_f32_16x16x32_bf16 v[68:71], a[120:123], v[104:107], v[68:71]\n"
"v_exp_f32 v77, v77\n"
"v_mfma_f32_16x16x32_bf16 v[68:71], a[124:127], v[108:111], v[68:71]\n"
"v_add_f32 v29, v29, v76\n"
"v_mfma_f32_16x16x32_bf16 a[240:243], a[144:147], v[56:59], a[240:243]\n"
"v_exp_f32 v78, v78\n"
"v_mfma_f32_16x16x32_bf16 a[244:247], a[148:151], v[56:59], a[244:247]\n"
"v_cvt_pk_bf16_f32 v62, v76, v77\n"
"v_add_f32 v29, v29, v77\n"
"v_mfma_f32_16x16x32_bf16 a[248:251], a[152:155], v[56:59], a[248:251]\n"
"v_exp_f32 v79, v79\n"
"v_mfma_f32_16x16x32_bf16 a[252:255], a[156:159], v[56:59], a[252:255]\n"
"v_add_f32 v29, v29, v78\n"
"v_mfma_f32_16x16x32_bf16 v[72:75], a[96:99], v[112:115], v[44:47]\n"
"v_exp_f32 v64, v64\n"
"v_mfma_f32_16x16x32_bf16 v[72:75], a[100:103], v[116:119], v[72:75]\n"
"v_cvt_pk_bf16_f32 v63, v78, v79\n"
"v_add_f32 v29, v29, v79\n"
"v_mfma_f32_16x16x32_bf16 v[72:75], a[104:107], v[120:123], v[72:75]\n"
"v_exp_f32 v65, v65\n"
"v_mfma_f32_16x16x32_bf16 v[72:75], a[108:111], v[124:127], v[72:75]\n"
"s_waitcnt lgkmcnt(0)\n"
"s_barrier\n"
"v_mfma_f32_16x16x32_bf16 v[160:163], a[160:163], v[60:63], v[160:163]\n"
"v_exp_f32 v66, v66\n"
"v_mfma_f32_16x16x32_bf16 v[164:167], a[164:167], v[60:63], v[164:167]\n"
"ds_read_b64_tr_b16 a[128:129], v8 offset:49152\n"
"v_cvt_pk_bf16_f32 v56, v64, v65\n"
"v_mfma_f32_16x16x32_bf16 v[168:171], a[168:171], v[60:63], v[168:171]\n"
"v_exp_f32 v67, v67\n"
"v_mfma_f32_16x16x32_bf16 v[172:175], a[172:175], v[60:63], v[172:175]\n"
"ds_read_b64_tr_b16 a[130:131], v8 offset:53248\n"
"ds_read_b64_tr_b16 a[132:133], v9 offset:49152\n"
"v_mfma_f32_16x16x32_bf16 v[76:79], a[112:115], v[112:115], v[44:47]\n"
"v_exp_f32 v68, v68\n"
"v_mfma_f32_16x16x32_bf16 v[76:79], a[116:119], v[116:119], v[76:79]\n"
"ds_read_b64_tr_b16 a[134:135], v9 offset:53248\n"
"v_cvt_pk_bf16_f32 v57, v66, v67\n"
"v_mfma_f32_16x16x32_bf16 v[76:79], a[120:123], v[120:123], v[76:79]\n"
"v_exp_f32 v69, v69\n"
"v_mfma_f32_16x16x32_bf16 v[76:79], a[124:127], v[124:127], v[76:79]\n"
"ds_read_b64_tr_b16 a[136:137], v8 offset:49664\n"
"ds_read_b64_tr_b16 a[138:139], v8 offset:53760\n"
"v_mfma_f32_16x16x32_bf16 v[176:179], a[176:179], v[60:63], v[176:179]\n"
"v_exp_f32 v70, v70\n"
"v_mfma_f32_16x16x32_bf16 v[180:183], a[180:183], v[60:63], v[180:183]\n"
"ds_read_b64_tr_b16 a[140:141], v9 offset:49664\n"
"v_cvt_pk_bf16_f32 v58, v68, v69\n"
"v_mfma_f32_16x16x32_bf16 v[184:187], a[184:187], v[60:63], v[184:187]\n"
"v_exp_f32 v71, v71\n"
"v_mfma_f32_16x16x32_bf16 v[188:191], a[188:191], v[60:63], v[188:191]\n"
"ds_read_b64_tr_b16 a[142:143], v9 offset:53760\n"
"ds_read_b64_tr_b16 a[144:145], v8 offset:50176\n"
"v_mfma_f32_16x16x32_bf16 v[64:67], a[96:99], v[128:131], v[48:51]\n"
"v_exp_f32 v72, v72\n"
"v_mfma_f32_16x16x32_bf16 v[64:67], a[100:103], v[132:135], v[64:67]\n"
"ds_read_b64_tr_b16 a[146:147], v8 offset:54272\n"
"v_cvt_pk_bf16_f32 v59, v70, v71\n"
"v_mfma_f32_16x16x32_bf16 v[64:67], a[104:107], v[136:139], v[64:67]\n"
"v_exp_f32 v73, v73\n"
"v_mfma_f32_16x16x32_bf16 v[64:67], a[108:111], v[140:143], v[64:67]\n"
"ds_read_b64_tr_b16 a[148:149], v9 offset:50176\n"
"v_add_f32 v31, v31, v72\n"
"v_mfma_f32_16x16x32_bf16 v[192:195], a[160:163], v[56:59], v[192:195]\n"
"v_exp_f32 v74, v74\n"
"v_mfma_f32_16x16x32_bf16 v[196:199], a[164:167], v[56:59], v[196:199]\n"
"v_cvt_pk_bf16_f32 v60, v72, v73\n"
"v_add_f32 v31, v31, v73\n"
"v_mfma_f32_16x16x32_bf16 v[200:203], a[168:171], v[56:59], v[200:203]\n"
"v_exp_f32 v75, v75\n"
"v_mfma_f32_16x16x32_bf16 v[204:207], a[172:175], v[56:59], v[204:207]\n"
"ds_read_b64_tr_b16 a[150:151], v9 offset:54272\n"
"v_add_f32 v31, v31, v74\n"
"v_mfma_f32_16x16x32_bf16 v[18:21], v[10:13], v[56:59], v[18:21]\n"
"ds_read_b64_tr_b16 a[152:153], v8 offset:50688\n"
"ds_read_b64_tr_b16 a[154:155], v8 offset:54784\n"
"v_mfma_f32_16x16x32_bf16 v[68:71], a[112:115], v[128:131], v[48:51]\n"
"v_exp_f32 v76, v76\n"
"v_mfma_f32_16x16x32_bf16 v[68:71], a[116:119], v[132:135], v[68:71]\n"
"v_cvt_pk_bf16_f32 v61, v74, v75\n"
"v_add_f32 v31, v31, v75\n"
"v_mfma_f32_16x16x32_bf16 v[68:71], a[120:123], v[136:139], v[68:71]\n"
"v_exp_f32 v77, v77\n"
"v_mfma_f32_16x16x32_bf16 v[68:71], a[124:127], v[140:143], v[68:71]\n"
"ds_read_b64_tr_b16 a[156:157], v9 offset:50688\n"
"v_add_f32 v31, v31, v76\n"
"v_mfma_f32_16x16x32_bf16 v[208:211], a[176:179], v[56:59], v[208:211]\n"
"v_exp_f32 v78, v78\n"
"v_mfma_f32_16x16x32_bf16 v[212:215], a[180:183], v[56:59], v[212:215]\n"
"v_cvt_pk_bf16_f32 v62, v76, v77\n"
"v_add_f32 v31, v31, v77\n"
"v_mfma_f32_16x16x32_bf16 v[216:219], a[184:187], v[56:59], v[216:219]\n"
"v_exp_f32 v79, v79\n"
"v_mfma_f32_16x16x32_bf16 v[220:223], a[188:191], v[56:59], v[220:223]\n"
"ds_read_b64_tr_b16 a[158:159], v9 offset:54784\n"
"v_add_f32 v31, v31, v78\n"
"v_mfma_f32_16x16x32_bf16 v[72:75], a[96:99], v[144:147], v[52:55]\n"
"v_exp_f32 v64, v64\n"
"v_mfma_f32_16x16x32_bf16 v[72:75], a[100:103], v[148:151], v[72:75]\n"
"v_cvt_pk_bf16_f32 v63, v78, v79\n"
"v_add_f32 v31, v31, v79\n"
"v_mfma_f32_16x16x32_bf16 v[72:75], a[104:107], v[152:155], v[72:75]\n"
"v_exp_f32 v65, v65\n"
"v_mfma_f32_16x16x32_bf16 v[72:75], a[108:111], v[156:159], v[72:75]\n"
"ds_read_b128 a[96:99], v4 offset:57344\n"
"v_add_f32 v34, v34, v64\n"
"v_mfma_f32_16x16x32_bf16 v[224:227], a[160:163], v[60:63], v[224:227]\n"
"v_exp_f32 v66, v66\n"
"v_mfma_f32_16x16x32_bf16 v[228:231], a[164:167], v[60:63], v[228:231]\n"
"v_cvt_pk_bf16_f32 v56, v64, v65\n"
"v_add_f32 v34, v34, v65\n"
"v_mfma_f32_16x16x32_bf16 v[232:235], a[168:171], v[60:63], v[232:235]\n"
"v_exp_f32 v67, v67\n"
"v_mfma_f32_16x16x32_bf16 v[236:239], a[172:175], v[60:63], v[236:239]\n"
"ds_read_b128 a[100:103], v5 offset:57344\n"
"v_add_f32 v34, v34, v66\n"
"v_mfma_f32_16x16x32_bf16 v[76:79], a[112:115], v[144:147], v[52:55]\n"
"v_exp_f32 v68, v68\n"
"v_mfma_f32_16x16x32_bf16 v[76:79], a[116:119], v[148:151], v[76:79]\n"
"v_cvt_pk_bf16_f32 v57, v66, v67\n"
"v_add_f32 v34, v34, v67\n"
"v_mfma_f32_16x16x32_bf16 v[76:79], a[120:123], v[152:155], v[76:79]\n"
"v_exp_f32 v69, v69\n"
"v_mfma_f32_16x16x32_bf16 v[76:79], a[124:127], v[156:159], v[76:79]\n"
"ds_read_b128 a[104:107], v6 offset:57344\n"
"v_add_f32 v34, v34, v68\n"
"v_mfma_f32_16x16x32_bf16 v[240:243], a[176:179], v[60:63], v[240:243]\n"
"v_exp_f32 v70, v70\n"
"v_mfma_f32_16x16x32_bf16 v[244:247], a[180:183], v[60:63], v[244:247]\n"
"v_cvt_pk_bf16_f32 v58, v68, v69\n"
"v_add_f32 v34, v34, v69\n"
"v_mfma_f32_16x16x32_bf16 v[248:251], a[184:187], v[60:63], v[248:251]\n"
"v_exp_f32 v71, v71\n"
"v_mfma_f32_16x16x32_bf16 v[252:255], a[188:191], v[60:63], v[252:255]\n"
"ds_read_b128 a[108:111], v7 offset:57344\n"
"v_add_f32 v34, v34, v70\n"
"v_mfma_f32_16x16x32_bf16 v[64:67], a[64:67], v[80:83], v[36:39]\n"
"v_exp_f32 v72, v72\n"
"v_mfma_f32_16x16x32_bf16 v[64:67], a[68:71], v[84:87], v[64:67]\n"
"v_cvt_pk_bf16_f32 v59, v70, v71\n"
"v_add_f32 v34, v34, v71\n"
"v_mfma_f32_16x16x32_bf16 v[64:67], a[72:75], v[88:91], v[64:67]\n"
"v_exp_f32 v73, v73\n"
"v_mfma_f32_16x16x32_bf16 v[64:67], a[76:79], v[92:95], v[64:67]\n"
"ds_read_b128 a[112:115], v4 offset:61440\n"
"v_add_f32 v35, v35, v72\n"
"v_mfma_f32_16x16x32_bf16 a[192:195], a[160:163], v[56:59], a[192:195]\n"
"v_exp_f32 v74, v74\n"
"v_mfma_f32_16x16x32_bf16 a[196:199], a[164:167], v[56:59], a[196:199]\n"
"v_cvt_pk_bf16_f32 v60, v72, v73\n"
"v_add_f32 v35, v35, v73\n"
"v_mfma_f32_16x16x32_bf16 a[200:203], a[168:171], v[56:59], a[200:203]\n"
"v_exp_f32 v75, v75\n"
"v_mfma_f32_16x16x32_bf16 a[204:207], a[172:175], v[56:59], a[204:207]\n"
"ds_read_b128 a[116:119], v5 offset:61440\n"
"v_add_f32 v35, v35, v74\n"
"v_mfma_f32_16x16x32_bf16 v[68:71], a[80:83], v[80:83], v[36:39]\n"
"v_exp_f32 v76, v76\n"
"v_mfma_f32_16x16x32_bf16 v[68:71], a[84:87], v[84:87], v[68:71]\n"
"v_cvt_pk_bf16_f32 v61, v74, v75\n"
"v_add_f32 v35, v35, v75\n"
"v_mfma_f32_16x16x32_bf16 v[68:71], a[88:91], v[88:91], v[68:71]\n"
"v_exp_f32 v77, v77\n"
"v_mfma_f32_16x16x32_bf16 v[68:71], a[92:95], v[92:95], v[68:71]\n"
"ds_read_b128 a[120:123], v6 offset:61440\n"
"v_add_f32 v35, v35, v76\n"
"v_mfma_f32_16x16x32_bf16 a[208:211], a[176:179], v[56:59], a[208:211]\n"
"v_exp_f32 v78, v78\n"
"v_mfma_f32_16x16x32_bf16 a[212:215], a[180:183], v[56:59], a[212:215]\n"
"v_cvt_pk_bf16_f32 v62, v76, v77\n"
"v_add_f32 v35, v35, v77\n"
"v_mfma_f32_16x16x32_bf16 a[216:219], a[184:187], v[56:59], a[216:219]\n"
"v_exp_f32 v79, v79\n"
"v_mfma_f32_16x16x32_bf16 a[220:223], a[188:191], v[56:59], a[220:223]\n"
"ds_read_b128 a[124:127], v7 offset:61440\n"
"v_add_f32 v35, v35, v78\n"
"s_cmp_eq_u32 s63, 0\n"
"s_cbranch_scc1 .Lmha_exit5\n"
"v_mfma_f32_16x16x32_bf16 v[72:75], a[64:67], v[96:99], v[40:43]\n"
"v_exp_f32 v64, v64\n"
"v_mfma_f32_16x16x32_bf16 v[72:75], a[68:71], v[100:103], v[72:75]\n"
"v_cvt_pk_bf16_f32 v63, v78, v79\n"
"v_add_f32 v35, v35, v79\n"
"v_mfma_f32_16x16x32_bf16 v[72:75], a[72:75], v[104:107], v[72:75]\n"
"v_exp_f32 v65, v65\n"
"v_mfma_f32_16x16x32_bf16 v[72:75], a[76:79], v[108:111], v[72:75]\n"
"v_add_f32 v29, v29, v64\n"
"v_mfma_f32_16x16x32_bf16 a[224:227], a[160:163], v[60:63], a[224:227]\n"
"v_exp_f32 v66, v66\n"
"v_mfma_f32_16x16x32_bf16 a[228:231], a[164:167], v[60:63], a[228:231]\n"
"v_cvt_pk_bf16_f32 v56, v64, v65\n"
"v_add_f32 v29, v29, v65\n"
"v_mfma_f32_16x16x32_bf16 a[232:235], a[168:171], v[60:63], a[232:235]\n"
"v_exp_f32 v67, v67\n"
"v_mfma_f32_16x16x32_bf16 a[236:239], a[172:175], v[60:63], a[236:239]\n"
"v_add_f32 v29, v29, v66\n"
"v_mfma_f32_16x16x32_bf16 v[76:79], a[80:83], v[96:99], v[40:43]\n"
"v_exp_f32 v68, v68\n"
"v_mfma_f32_16x16x32_bf16 v[76:79], a[84:87], v[100:103], v[76:79]\n"
"v_cvt_pk_bf16_f32 v57, v66, v67\n"
"v_add_f32 v29, v29, v67\n"
"v_mfma_f32_16x16x32_bf16 v[76:79], a[88:91], v[104:107], v[76:79]\n"
"v_exp_f32 v69, v69\n"
"v_mfma_f32_16x16x32_bf16 v[76:79], a[92:95], v[108:111], v[76:79]\n"
"v_add_f32 v29, v29, v68\n"
"v_mfma_f32_16x16x32_bf16 a[240:243], a[176:179], v[60:63], a[240:243]\n"
"v_exp_f32 v70, v70\n"
"v_mfma_f32_16x16x32_bf16 a[244:247], a[180:183], v[60:63], a[244:247]\n"
"v_cvt_pk_bf16_f32 v58, v68, v69\n"
"v_add_f32 v29, v29, v69\n"
"v_mfma_f32_16x16x32_bf16 a[248:251], a[184:187], v[60:63], a[248:251]\n"
"v_exp_f32 v71, v71\n"
"v_mfma_f32_16x16x32_bf16 a[252:255], a[188:191], v[60:63], a[252:255]\n"
"v_add_f32 v29, v29, v70\n"
"v_mfma_f32_16x16x32_bf16 v[64:67], a[64:67], v[112:115], v[44:47]\n"
"v_exp_f32 v72, v72\n"
"v_mfma_f32_16x16x32_bf16 v[64:67], a[68:71], v[116:119], v[64:67]\n"
"v_cvt_pk_bf16_f32 v59, v70, v71\n"
"v_add_f32 v29, v29, v71\n"
"v_mfma_f32_16x16x32_bf16 v[64:67], a[72:75], v[120:123], v[64:67]\n"
"v_exp_f32 v73, v73\n"
"v_mfma_f32_16x16x32_bf16 v[64:67], a[76:79], v[124:127], v[64:67]\n"
"s_waitcnt lgkmcnt(0)\n"
"s_barrier\n"
"v_mfma_f32_16x16x32_bf16 v[160:163], a[128:131], v[56:59], v[160:163]\n"
"v_exp_f32 v74, v74\n"
"v_mfma_f32_16x16x32_bf16 v[164:167], a[132:135], v[56:59], v[164:167]\n"
"ds_read_b64_tr_b16 a[160:161], v8 offset:57344\n"
"v_cvt_pk_bf16_f32 v60, v72, v73\n"
"v_mfma_f32_16x16x32_bf16 v[168:171], a[136:139], v[56:59], v[168:171]\n"
"v_exp_f32 v75, v75\n"
"v_mfma_f32_16x16x32_bf16 v[172:175], a[140:143], v[56:59], v[172:175]\n"
"ds_read_b64_tr_b16 a[162:163], v8 offset:61440\n"
"ds_read_b64_tr_b16 a[164:165], v9 offset:57344\n"
"v_mfma_f32_16x16x32_bf16 v[68:71], a[80:83], v[112:115], v[44:47]\n"
"v_exp_f32 v76, v76\n"
"v_mfma_f32_16x16x32_bf16 v[68:71], a[84:87], v[116:119], v[68:71]\n"
"ds_read_b64_tr_b16 a[166:167], v9 offset:61440\n"
"v_cvt_pk_bf16_f32 v61, v74, v75\n"
"v_mfma_f32_16x16x32_bf16 v[68:71], a[88:91], v[120:123], v[68:71]\n"
"v_exp_f32 v77, v77\n"
"v_mfma_f32_16x16x32_bf16 v[68:71], a[92:95], v[124:127], v[68:71]\n"
"ds_read_b64_tr_b16 a[168:169], v8 offset:57856\n"
"ds_read_b64_tr_b16 a[170:171], v8 offset:61952\n"
"v_mfma_f32_16x16x32_bf16 v[176:179], a[144:147], v[56:59], v[176:179]\n"
"v_exp_f32 v78, v78\n"
"v_mfma_f32_16x16x32_bf16 v[180:183], a[148:151], v[56:59], v[180:183]\n"
"ds_read_b64_tr_b16 a[172:173], v9 offset:57856\n"
"v_cvt_pk_bf16_f32 v62, v76, v77\n"
"v_mfma_f32_16x16x32_bf16 v[184:187], a[152:155], v[56:59], v[184:187]\n"
"v_exp_f32 v79, v79\n"
"v_mfma_f32_16x16x32_bf16 v[188:191], a[156:159], v[56:59], v[188:191]\n"
"ds_read_b64_tr_b16 a[174:175], v9 offset:61952\n"
"ds_read_b64_tr_b16 a[176:177], v8 offset:58368\n"
"v_mfma_f32_16x16x32_bf16 v[72:75], a[64:67], v[128:131], v[48:51]\n"
"v_exp_f32 v64, v64\n"
"v_mfma_f32_16x16x32_bf16 v[72:75], a[68:71], v[132:135], v[72:75]\n"
"ds_read_b64_tr_b16 a[178:179], v8 offset:62464\n"
"v_cvt_pk_bf16_f32 v63, v78, v79\n"
"v_mfma_f32_16x16x32_bf16 v[72:75], a[72:75], v[136:139], v[72:75]\n"
"v_exp_f32 v65, v65\n"
"v_mfma_f32_16x16x32_bf16 v[72:75], a[76:79], v[140:143], v[72:75]\n"
"ds_read_b64_tr_b16 a[180:181], v9 offset:58368\n"
"v_add_f32 v31, v31, v64\n"
"v_mfma_f32_16x16x32_bf16 v[192:195], a[128:131], v[60:63], v[192:195]\n"
"v_exp_f32 v66, v66\n"
"v_mfma_f32_16x16x32_bf16 v[196:199], a[132:135], v[60:63], v[196:199]\n"
"v_cvt_pk_bf16_f32 v56, v64, v65\n"
"v_add_f32 v31, v31, v65\n"
"v_mfma_f32_16x16x32_bf16 v[200:203], a[136:139], v[60:63], v[200:203]\n"
"v_exp_f32 v67, v67\n"
"v_mfma_f32_16x16x32_bf16 v[204:207], a[140:143], v[60:63], v[204:207]\n"
"ds_read_b64_tr_b16 a[182:183], v9 offset:62464\n"
"v_add_f32 v31, v31, v66\n"
"v_mfma_f32_16x16x32_bf16 v[18:21], v[10:13], v[60:63], v[18:21]\n"
"ds_read_b64_tr_b16 a[184:185], v8 offset:58880\n"
"ds_read_b64_tr_b16 a[186:187], v8 offset:62976\n"
"v_mfma_f32_16x16x32_bf16 v[76:79], a[80:83], v[128:131], v[48:51]\n"
"v_exp_f32 v68, v68\n"
"v_mfma_f32_16x16x32_bf16 v[76:79], a[84:87], v[132:135], v[76:79]\n"
"v_cvt_pk_bf16_f32 v57, v66, v67\n"
"v_add_f32 v31, v31, v67\n"
"v_mfma_f32_16x16x32_bf16 v[76:79], a[88:91], v[136:139], v[76:79]\n"
"v_exp_f32 v69, v69\n"
"v_mfma_f32_16x16x32_bf16 v[76:79], a[92:95], v[140:143], v[76:79]\n"
"ds_read_b64_tr_b16 a[188:189], v9 offset:58880\n"
"v_add_f32 v31, v31, v68\n"
"v_mfma_f32_16x16x32_bf16 v[208:211], a[144:147], v[60:63], v[208:211]\n"
"v_exp_f32 v70, v70\n"
"v_mfma_f32_16x16x32_bf16 v[212:215], a[148:151], v[60:63], v[212:215]\n"
"v_cvt_pk_bf16_f32 v58, v68, v69\n"
"v_add_f32 v31, v31, v69\n"
"v_mfma_f32_16x16x32_bf16 v[216:219], a[152:155], v[60:63], v[216:219]\n"
"v_exp_f32 v71, v71\n"
"v_mfma_f32_16x16x32_bf16 v[220:223], a[156:159], v[60:63], v[220:223]\n"
"ds_read_b64_tr_b16 a[190:191], v9 offset:62976\n"
"v_add_f32 v31, v31, v70\n"
"v_mfma_f32_16x16x32_bf16 v[64:67], a[64:67], v[144:147], v[52:55]\n"
"v_exp_f32 v72, v72\n"
"v_mfma_f32_16x16x32_bf16 v[64:67], a[68:71], v[148:151], v[64:67]\n"
"v_cvt_pk_bf16_f32 v59, v70, v71\n"
"v_add_f32 v31, v31, v71\n"
"v_mfma_f32_16x16x32_bf16 v[64:67], a[72:75], v[152:155], v[64:67]\n"
"v_exp_f32 v73, v73\n"
"v_mfma_f32_16x16x32_bf16 v[64:67], a[76:79], v[156:159], v[64:67]\n"
"ds_read_b128 a[64:67], v4\n"
"v_add_f32 v34, v34, v72\n"
"v_mfma_f32_16x16x32_bf16 v[224:227], a[128:131], v[56:59], v[224:227]\n"
"v_exp_f32 v74, v74\n"
"v_mfma_f32_16x16x32_bf16 v[228:231], a[132:135], v[56:59], v[228:231]\n"
"v_cvt_pk_bf16_f32 v60, v72, v73\n"
"v_add_f32 v34, v34, v73\n"
"v_mfma_f32_16x16x32_bf16 v[232:235], a[136:139], v[56:59], v[232:235]\n"
"v_exp_f32 v75, v75\n"
"v_mfma_f32_16x16x32_bf16 v[236:239], a[140:143], v[56:59], v[236:239]\n"
"ds_read_b128 a[68:71], v5\n"
"v_add_f32 v34, v34, v74\n"
"v_mfma_f32_16x16x32_bf16 v[68:71], a[80:83], v[144:147], v[52:55]\n"
"v_exp_f32 v76, v76\n"
"v_mfma_f32_16x16x32_bf16 v[68:71], a[84:87], v[148:151], v[68:71]\n"
"v_cvt_pk_bf16_f32 v61, v74, v75\n"
"v_add_f32 v34, v34, v75\n"
"v_mfma_f32_16x16x32_bf16 v[68:71], a[88:91], v[152:155], v[68:71]\n"
"v_exp_f32 v77, v77\n"
"v_mfma_f32_16x16x32_bf16 v[68:71], a[92:95], v[156:159], v[68:71]\n"
"ds_read_b128 a[72:75], v6\n"
"v_add_f32 v34, v34, v76\n"
"v_mfma_f32_16x16x32_bf16 v[240:243], a[144:147], v[56:59], v[240:243]\n"
"v_exp_f32 v78, v78\n"
"v_mfma_f32_16x16x32_bf16 v[244:247], a[148:151], v[56:59], v[244:247]\n"
"v_cvt_pk_bf16_f32 v62, v76, v77\n"
"v_add_f32 v34, v34, v77\n"
"v_mfma_f32_16x16x32_bf16 v[248:251], a[152:155], v[56:59], v[248:251]\n"
"v_exp_f32 v79, v79\n"
"v_mfma_f32_16x16x32_bf16 v[252:255], a[156:159], v[56:59], v[252:255]\n"
"ds_read_b128 a[76:79], v7\n"
"v_add_f32 v34, v34, v78\n"
"v_mfma_f32_16x16x32_bf16 v[72:75], a[96:99], v[80:83], v[36:39]\n"
"v_exp_f32 v64, v64\n"
"v_mfma_f32_16x16x32_bf16 v[72:75], a[100:103], v[84:87], v[72:75]\n"
"v_cvt_pk_bf16_f32 v63, v78, v79\n"
"v_add_f32 v34, v34, v79\n"
"v_mfma_f32_16x16x32_bf16 v[72:75], a[104:107], v[88:91], v[72:75]\n"
"v_exp_f32 v65, v65\n"
"v_mfma_f32_16x16x32_bf16 v[72:75], a[108:111], v[92:95], v[72:75]\n"
"ds_read_b128 a[80:83], v4 offset:4096\n"
"v_add_f32 v35, v35, v64\n"
"v_mfma_f32_16x16x32_bf16 a[192:195], a[128:131], v[60:63], a[192:195]\n"
"v_exp_f32 v66, v66\n"
"v_mfma_f32_16x16x32_bf16 a[196:199], a[132:135], v[60:63], a[196:199]\n"
"v_cvt_pk_bf16_f32 v56, v64, v65\n"
"v_add_f32 v35, v35, v65\n"
"v_mfma_f32_16x16x32_bf16 a[200:203], a[136:139], v[60:63], a[200:203]\n"
"v_exp_f32 v67, v67\n"
"v_mfma_f32_16x16x32_bf16 a[204:207], a[140:143], v[60:63], a[204:207]\n"
"ds_read_b128 a[84:87], v5 offset:4096\n"
"v_add_f32 v35, v35, v66\n"
"v_mfma_f32_16x16x32_bf16 v[76:79], a[112:115], v[80:83], v[36:39]\n"
"v_exp_f32 v68, v68\n"
"v_mfma_f32_16x16x32_bf16 v[76:79], a[116:119], v[84:87], v[76:79]\n"
"v_cvt_pk_bf16_f32 v57, v66, v67\n"
"v_add_f32 v35, v35, v67\n"
"v_mfma_f32_16x16x32_bf16 v[76:79], a[120:123], v[88:91], v[76:79]\n"
"v_exp_f32 v69, v69\n"
"v_mfma_f32_16x16x32_bf16 v[76:79], a[124:127], v[92:95], v[76:79]\n"
"ds_read_b128 a[88:91], v6 offset:4096\n"
"v_add_f32 v35, v35, v68\n"
"v_mfma_f32_16x16x32_bf16 a[208:211], a[144:147], v[60:63], a[208:211]\n"
"v_exp_f32 v70, v70\n"
"v_mfma_f32_16x16x32_bf16 a[212:215], a[148:151], v[60:63], a[212:215]\n"
"v_cvt_pk_bf16_f32 v58, v68, v69\n"
"v_add_f32 v35, v35, v69\n"
"v_mfma_f32_16x16x32_bf16 a[216:219], a[152:155], v[60:63], a[216:219]\n"
"v_exp_f32 v71, v71\n"
"v_mfma_f32_16x16x32_bf16 a[220:223], a[156:159], v[60:63], a[220:223]\n"
"ds_read_b128 a[92:95], v7 offset:4096\n"
"v_add_f32 v35, v35, v70\n"
"v_mfma_f32_16x16x32_bf16 v[64:67], a[96:99], v[96:99], v[40:43]\n"
"v_exp_f32 v72, v72\n"
"v_mfma_f32_16x16x32_bf16 v[64:67], a[100:103], v[100:103], v[64:67]\n"
"v_cvt_pk_bf16_f32 v59, v70, v71\n"
"v_add_f32 v35, v35, v71\n"
"v_mfma_f32_16x16x32_bf16 v[64:67], a[104:107], v[104:107], v[64:67]\n"
"v_exp_f32 v73, v73\n"
"v_mfma_f32_16x16x32_bf16 v[64:67], a[108:111], v[108:111], v[64:67]\n"
"v_add_f32 v29, v29, v72\n"
"v_mfma_f32_16x16x32_bf16 a[224:227], a[128:131], v[56:59], a[224:227]\n"
"v_exp_f32 v74, v74\n"
"v_mfma_f32_16x16x32_bf16 a[228:231], a[132:135], v[56:59], a[228:231]\n"
"v_cvt_pk_bf16_f32 v60, v72, v73\n"
"v_add_f32 v29, v29, v73\n"
"v_mfma_f32_16x16x32_bf16 a[232:235], a[136:139], v[56:59], a[232:235]\n"
"v_exp_f32 v75, v75\n"
"v_mfma_f32_16x16x32_bf16 a[236:239], a[140:143], v[56:59], a[236:239]\n"
"v_add_f32 v29, v29, v74\n"
"v_mfma_f32_16x16x32_bf16 v[68:71], a[112:115], v[96:99], v[40:43]\n"
"v_exp_f32 v76, v76\n"
"v_mfma_f32_16x16x32_bf16 v[68:71], a[116:119], v[100:103], v[68:71]\n"
"v_cvt_pk_bf16_f32 v61, v74, v75\n"
"v_add_f32 v29, v29, v75\n"
"v_mfma_f32_16x16x32_bf16 v[68:71], a[120:123], v[104:107], v[68:71]\n"
"v_exp_f32 v77, v77\n"
"v_mfma_f32_16x16x32_bf16 v[68:71], a[124:127], v[108:111], v[68:71]\n"
"v_add_f32 v29, v29, v76\n"
"v_mfma_f32_16x16x32_bf16 a[240:243], a[144:147], v[56:59], a[240:243]\n"
"v_exp_f32 v78, v78\n"
"v_mfma_f32_16x16x32_bf16 a[244:247], a[148:151], v[56:59], a[244:247]\n"
"v_cvt_pk_bf16_f32 v62, v76, v77\n"
"v_add_f32 v29, v29, v77\n"
"v_mfma_f32_16x16x32_bf16 a[248:251], a[152:155], v[56:59], a[248:251]\n"
"v_exp_f32 v79, v79\n"
"v_mfma_f32_16x16x32_bf16 a[252:255], a[156:159], v[56:59], a[252:255]\n"
"v_add_f32 v29, v29, v78\n"
"v_mfma_f32_16x16x32_bf16 v[72:75], a[96:99], v[112:115], v[44:47]\n"
"v_exp_f32 v64, v64\n"
"v_mfma_f32_16x16x32_bf16 v[72:75], a[100:103], v[116:119], v[72:75]\n"
"v_cvt_pk_bf16_f32 v63, v78, v79\n"
"v_add_f32 v29, v29, v79\n"
"v_mfma_f32_16x16x32_bf16 v[72:75], a[104:107], v[120:123], v[72:75]\n"
"v_exp_f32 v65, v65\n"
"v_mfma_f32_16x16x32_bf16 v[72:75], a[108:111], v[124:127], v[72:75]\n"
"s_waitcnt lgkmcnt(0)\n"
"s_barrier\n"
"v_mfma_f32_16x16x32_bf16 v[160:163], a[160:163], v[60:63], v[160:163]\n"
"v_exp_f32 v66, v66\n"
"v_mfma_f32_16x16x32_bf16 v[164:167], a[164:167], v[60:63], v[164:167]\n"
"ds_read_b64_tr_b16 a[128:129], v8\n"
"v_cvt_pk_bf16_f32 v56, v64, v65\n"
"v_mfma_f32_16x16x32_bf16 v[168:171], a[168:171], v[60:63], v[168:171]\n"
"v_exp_f32 v67, v67\n"
"v_mfma_f32_16x16x32_bf16 v[172:175], a[172:175], v[60:63], v[172:175]\n"
"ds_read_b64_tr_b16 a[130:131], v8 offset:4096\n"
"ds_read_b64_tr_b16 a[132:133], v9\n"
"v_mfma_f32_16x16x32_bf16 v[76:79], a[112:115], v[112:115], v[44:47]\n"
"v_exp_f32 v68, v68\n"
"v_mfma_f32_16x16x32_bf16 v[76:79], a[116:119], v[116:119], v[76:79]\n"
"ds_read_b64_tr_b16 a[134:135], v9 offset:4096\n"
"v_cvt_pk_bf16_f32 v57, v66, v67\n"
"v_mfma_f32_16x16x32_bf16 v[76:79], a[120:123], v[120:123], v[76:79]\n"
"v_exp_f32 v69, v69\n"
"v_mfma_f32_16x16x32_bf16 v[76:79], a[124:127], v[124:127], v[76:79]\n"
"ds_read_b64_tr_b16 a[136:137], v8 offset:512\n"
"ds_read_b64_tr_b16 a[138:139], v8 offset:4608\n"
"v_mfma_f32_16x16x32_bf16 v[176:179], a[176:179], v[60:63], v[176:179]\n"
"v_exp_f32 v70, v70\n"
"v_mfma_f32_16x16x32_bf16 v[180:183], a[180:183], v[60:63], v[180:183]\n"
"ds_read_b64_tr_b16 a[140:141], v9 offset:512\n"
"v_cvt_pk_bf16_f32 v58, v68, v69\n"
"v_mfma_f32_16x16x32_bf16 v[184:187], a[184:187], v[60:63], v[184:187]\n"
"v_exp_f32 v71, v71\n"
"v_mfma_f32_16x16x32_bf16 v[188:191], a[188:191], v[60:63], v[188:191]\n"
"ds_read_b64_tr_b16 a[142:143], v9 offset:4608\n"
"ds_read_b64_tr_b16 a[144:145], v8 offset:1024\n"
"s_sub_u32 s63, s63, 1\n"
"s_branch .Lmha_back5\n"
".Lmha_exit5:\n"
"s_waitcnt lgkmcnt(4)\n"
"v_mfma_f32_16x16x32_bf16 v[72:75], a[96:99], v[80:83], v[36:39]\n"
"v_exp_f32 v64, v64\n"
"v_mfma_f32_16x16x32_bf16 v[72:75], a[100:103], v[84:87], v[72:75]\n"
"v_cvt_pk_bf16_f32 v63, v78, v79\n"
"v_add_f32 v35, v35, v79\n"
"v_mfma_f32_16x16x32_bf16 v[72:75], a[104:107], v[88:91], v[72:75]\n"
"v_exp_f32 v65, v65\n"
"v_mfma_f32_16x16x32_bf16 v[72:75], a[108:111], v[92:95], v[72:75]\n"
"v_add_f32 v29, v29, v64\n"
"v_mfma_f32_16x16x32_bf16 a[224:227], a[160:163], v[60:63], a[224:227]\n"
"v_exp_f32 v66, v66\n"
"v_mfma_f32_16x16x32_bf16 a[228:231], a[164:167], v[60:63], a[228:231]\n"
"v_cvt_pk_bf16_f32 v56, v64, v65\n"
"v_add_f32 v29, v29, v65\n"
"v_mfma_f32_16x16x32_bf16 a[232:235], a[168:171], v[60:63], a[232:235]\n"
"v_exp_f32 v67, v67\n"
"v_mfma_f32_16x16x32_bf16 a[236:239], a[172:175], v[60:63], a[236:239]\n"
"s_waitcnt lgkmcnt(0)\n"
"s_barrier\n"
"v_add_f32 v29, v29, v66\n"
"v_mfma_f32_16x16x32_bf16 v[76:79], a[112:115], v[80:83], v[36:39]\n"
"v_exp_f32 v68, v68\n"
"v_mfma_f32_16x16x32_bf16 v[76:79], a[116:119], v[84:87], v[76:79]\n"
"v_cvt_pk_bf16_f32 v57, v66, v67\n"
"v_add_f32 v29, v29, v67\n"
"v_mfma_f32_16x16x32_bf16 v[76:79], a[120:123], v[88:91], v[76:79]\n"
"v_exp_f32 v69, v69\n"
"v_mfma_f32_16x16x32_bf16 v[76:79], a[124:127], v[92:95], v[76:79]\n"
"ds_read_b64_tr_b16 a[160:161], v8 offset:57344\n"
"ds_read_b64_tr_b16 a[162:163], v8 offset:61440\n"
"v_add_f32 v29, v29, v68\n"
"v_mfma_f32_16x16x32_bf16 a[240:243], a[176:179], v[60:63], a[240:243]\n"
"v_exp_f32 v70, v70\n"
"v_mfma_f32_16x16x32_bf16 a[244:247], a[180:183], v[60:63], a[244:247]\n"
"v_cvt_pk_bf16_f32 v58, v68, v69\n"
"v_add_f32 v29, v29, v69\n"
"v_mfma_f32_16x16x32_bf16 a[248:251], a[184:187], v[60:63], a[248:251]\n"
"v_exp_f32 v71, v71\n"
"v_mfma_f32_16x16x32_bf16 a[252:255], a[188:191], v[60:63], a[252:255]\n"
"ds_read_b64_tr_b16 a[164:165], v9 offset:57344\n"
"ds_read_b64_tr_b16 a[166:167], v9 offset:61440\n"
"v_add_f32 v29, v29, v70\n"
"v_mfma_f32_16x16x32_bf16 v[64:67], a[64:67], v[96:99], v[40:43]\n"
"v_exp_f32 v72, v72\n"
"v_mfma_f32_16x16x32_bf16 v[64:67], a[68:71], v[100:103], v[64:67]\n"
"v_cvt_pk_bf16_f32 v59, v70, v71\n"
"v_add_f32 v29, v29, v71\n"
"v_mfma_f32_16x16x32_bf16 v[64:67], a[72:75], v[104:107], v[64:67]\n"
"v_exp_f32 v73, v73\n"
"v_mfma_f32_16x16x32_bf16 v[64:67], a[76:79], v[108:111], v[64:67]\n"
"ds_read_b64_tr_b16 a[168:169], v8 offset:57856\n"
"ds_read_b64_tr_b16 a[170:171], v8 offset:61952\n"
"v_add_f32 v29, v29, v72\n"
"v_mfma_f32_16x16x32_bf16 v[160:163], a[128:131], v[56:59], v[160:163]\n"
"v_exp_f32 v74, v74\n"
"v_mfma_f32_16x16x32_bf16 v[164:167], a[132:135], v[56:59], v[164:167]\n"
"v_cvt_pk_bf16_f32 v60, v72, v73\n"
"v_add_f32 v29, v29, v73\n"
"v_mfma_f32_16x16x32_bf16 v[168:171], a[136:139], v[56:59], v[168:171]\n"
"v_exp_f32 v75, v75\n"
"v_mfma_f32_16x16x32_bf16 v[172:175], a[140:143], v[56:59], v[172:175]\n"
"ds_read_b64_tr_b16 a[172:173], v9 offset:57856\n"
"ds_read_b64_tr_b16 a[174:175], v9 offset:61952\n"
"v_add_f32 v29, v29, v74\n"
"v_mfma_f32_16x16x32_bf16 v[68:71], a[80:83], v[96:99], v[40:43]\n"
"v_exp_f32 v76, v76\n"
"v_mfma_f32_16x16x32_bf16 v[68:71], a[84:87], v[100:103], v[68:71]\n"
"v_cvt_pk_bf16_f32 v61, v74, v75\n"
"v_add_f32 v29, v29, v75\n"
"v_mfma_f32_16x16x32_bf16 v[68:71], a[88:91], v[104:107], v[68:71]\n"
"v_exp_f32 v77, v77\n"
"v_mfma_f32_16x16x32_bf16 v[68:71], a[92:95], v[108:111], v[68:71]\n"
"ds_read_b64_tr_b16 a[176:177], v8 offset:58368\n"
"ds_read_b64_tr_b16 a[178:179], v8 offset:62464\n"
"v_add_f32 v29, v29, v76\n"
"v_mfma_f32_16x16x32_bf16 v[176:179], a[144:147], v[56:59], v[176:179]\n"
"v_exp_f32 v78, v78\n"
"v_mfma_f32_16x16x32_bf16 v[180:183], a[148:151], v[56:59], v[180:183]\n"
"v_cvt_pk_bf16_f32 v62, v76, v77\n"
"v_add_f32 v29, v29, v77\n"
"v_mfma_f32_16x16x32_bf16 v[184:187], a[152:155], v[56:59], v[184:187]\n"
"v_exp_f32 v79, v79\n"
"v_mfma_f32_16x16x32_bf16 v[188:191], a[156:159], v[56:59], v[188:191]\n"
"ds_read_b64_tr_b16 a[180:181], v9 offset:58368\n"
"ds_read_b64_tr_b16 a[182:183], v9 offset:62464\n"
"v_add_f32 v29, v29, v78\n"
"v_mfma_f32_16x16x32_bf16 v[72:75], a[96:99], v[96:99], v[40:43]\n"
"v_exp_f32 v64, v64\n"
"v_mfma_f32_16x16x32_bf16 v[72:75], a[100:103], v[100:103], v[72:75]\n"
"v_cvt_pk_bf16_f32 v63, v78, v79\n"
"v_add_f32 v29, v29, v79\n"
"v_mfma_f32_16x16x32_bf16 v[72:75], a[104:107], v[104:107], v[72:75]\n"
"v_exp_f32 v65, v65\n"
"v_mfma_f32_16x16x32_bf16 v[72:75], a[108:111], v[108:111], v[72:75]\n"
"ds_read_b64_tr_b16 a[184:185], v8 offset:58880\n"
"s_waitcnt lgkmcnt(5)\n"
"v_mfma_f32_16x16x32_bf16 v[160:163], a[160:163], v[60:63], v[160:163]\n"
"v_exp_f32 v66, v66\n"
"v_mfma_f32_16x16x32_bf16 v[164:167], a[164:167], v[60:63], v[164:167]\n"
"ds_read_b64_tr_b16 a[186:187], v8 offset:62976\n"
"v_cvt_pk_bf16_f32 v56, v64, v65\n"
"v_mfma_f32_16x16x32_bf16 v[168:171], a[168:171], v[60:63], v[168:171]\n"
"v_exp_f32 v67, v67\n"
"v_mfma_f32_16x16x32_bf16 v[172:175], a[172:175], v[60:63], v[172:175]\n"
"ds_read_b64_tr_b16 a[188:189], v9 offset:58880\n"
"ds_read_b64_tr_b16 a[190:191], v9 offset:62976\n"
"v_mfma_f32_16x16x32_bf16 v[76:79], a[112:115], v[96:99], v[40:43]\n"
"v_exp_f32 v68, v68\n"
"v_mfma_f32_16x16x32_bf16 v[76:79], a[116:119], v[100:103], v[76:79]\n"
"v_cvt_pk_bf16_f32 v57, v66, v67\n"
"v_mfma_f32_16x16x32_bf16 v[76:79], a[120:123], v[104:107], v[76:79]\n"
"v_exp_f32 v69, v69\n"
"v_mfma_f32_16x16x32_bf16 v[76:79], a[124:127], v[108:111], v[76:79]\n"
"s_waitcnt lgkmcnt(0)\n"
"v_mfma_f32_16x16x32_bf16 v[176:179], a[176:179], v[60:63], v[176:179]\n"
"v_exp_f32 v70, v70\n"
"v_mfma_f32_16x16x32_bf16 v[180:183], a[180:183], v[60:63], v[180:183]\n"
"v_cvt_pk_bf16_f32 v58, v68, v69\n"
"v_mfma_f32_16x16x32_bf16 v[184:187], a[184:187], v[60:63], v[184:187]\n"
"v_exp_f32 v71, v71\n"
"v_mfma_f32_16x16x32_bf16 v[188:191], a[188:191], v[60:63], v[188:191]\n"
"v_lshlrev_b32 v1, 4, v0\n"
"s_mov_b64 s[64:65], 0\n"
"v_mov_b32 v27, v29\n"
"s_nop 0\n"
"s_nop 0\n"
"v_permlane32_swap_b32 v29, v27\n"
"v_add_f32 v29, v29, v27\n"
"v_mov_b32 v27, v29\n"
"s_nop 0\n"
"s_nop 0\n"
"v_permlane16_swap_b32 v29, v27\n"
"v_add_f32 v29, v29, v27\n"
"v_cmp_nge_f32 vcc, 0x53800000, v29\n"
"v_rcp_f32 v28, v29\n"
"s_or_b64 s[64:65], s[64:65], vcc\n"
"v_pk_mul_f32 v[160:161], v[160:161], v[28:29] op_sel_hi:[1,0]\n"
"v_pk_mul_f32 v[162:163], v[162:163], v[28:29] op_sel_hi:[1,0]\n"
"v_pk_mul_f32 v[164:165], v[164:165], v[28:29] op_sel_hi:[1,0]\n"
"v_pk_mul_f32 v[166:167], v[166:167], v[28:29] op_sel_hi:[1,0]\n"
"v_cvt_pk_bf16_f32 v160, v160, v161\n"
"v_cvt_pk_bf16_f32 v161, v162, v163\n"
"v_cvt_pk_bf16_f32 v162, v164, v165\n"
"v_cvt_pk_bf16_f32 v163, v166, v167\n"
"buffer_store_dwordx4 v[160:163], v1, s[40:43], 0 offen sc1\n"
"v_pk_mul_f32 v[168:169], v[168:169], v[28:29] op_sel_hi:[1,0]\n"
"v_pk_mul_f32 v[170:171], v[170:171], v[28:29] op_sel_hi:[1,0]\n"
"v_pk_mul_f32 v[172:173], v[172:173], v[28:29] op_sel_hi:[1,0]\n"
"v_pk_mul_f32 v[174:175], v[174:175], v[28:29] op_sel_hi:[1,0]\n"
"v_cvt_pk_bf16_f32 v168, v168, v169\n"
"v_cvt_pk_bf16_f32 v169, v170, v171\n"
"v_cvt_pk_bf16_f32 v170, v172, v173\n"
"v_cvt_pk_bf16_f32 v171, v174, v175\n"
"buffer_store_dwordx4 v[168:171], v1, s[40:43], 0 offen offset:1024 sc1\n"
"v_pk_mul_f32 v[176:177], v[176:177], v[28:29] op_sel_hi:[1,0]\n"
"v_pk_mul_f32 v[178:179], v[178:179], v[28:29] op_sel_hi:[1,0]\n"
"v_pk_mul_f32 v[180:181], v[180:181], v[28:29] op_sel_hi:[1,0]\n"
"v_pk_mul_f32 v[182:183], v[182:183], v[28:29] op_sel_hi:[1,0]\n"
"v_cvt_pk_bf16_f32 v176, v176, v177\n"
"v_cvt_pk_bf16_f32 v177, v178, v179\n"
"v_cvt_pk_bf16_f32 v178, v180, v181\n"
"v_cvt_pk_bf16_f32 v179, v182, v183\n"
"buffer_store_dwordx4 v[176:179], v1, s[40:43], 0 offen offset:2048 sc1\n"
"v_pk_mul_f32 v[184:185], v[184:185], v[28:29] op_sel_hi:[1,0]\n"
"v_pk_mul_f32 v[186:187], v[186:187], v[28:29] op_sel_hi:[1,0]\n"
"v_pk_mul_f32 v[188:189], v[188:189], v[28:29] op_sel_hi:[1,0]\n"
"v_pk_mul_f32 v[190:191], v[190:191], v[28:29] op_sel_hi:[1,0]\n"
"v_cvt_pk_bf16_f32 v184, v184, v185\n"
"v_cvt_pk_bf16_f32 v185, v186, v187\n"
"v_cvt_pk_bf16_f32 v186, v188, v189\n"
"v_cvt_pk_bf16_f32 v187, v190, v191\n"
"buffer_store_dwordx4 v[184:187], v1, s[40:43], 0 offen offset:3072 sc1\n"
"v_mfma_f32_16x16x32_bf16 v[64:67], a[64:67], v[112:115], v[44:47]\n"
"v_exp_f32 v72, v72\n"
"v_mfma_f32_16x16x32_bf16 v[64:67], a[68:71], v[116:119], v[64:67]\n"
"v_cvt_pk_bf16_f32 v59, v70, v71\n"
"v_mfma_f32_16x16x32_bf16 v[64:67], a[72:75], v[120:123], v[64:67]\n"
"v_exp_f32 v73, v73\n"
"v_mfma_f32_16x16x32_bf16 v[64:67], a[76:79], v[124:127], v[64:67]\n"
"v_mfma_f32_16x16x32_bf16 v[192:195], a[128:131], v[56:59], v[192:195]\n"
"v_exp_f32 v74, v74\n"
"v_mfma_f32_16x16x32_bf16 v[196:199], a[132:135], v[56:59], v[196:199]\n"
"v_cvt_pk_bf16_f32 v60, v72, v73\n"
"v_mfma_f32_16x16x32_bf16 v[200:203], a[136:139], v[56:59], v[200:203]\n"
"v_exp_f32 v75, v75\n"
"v_mfma_f32_16x16x32_bf16 v[204:207], a[140:143], v[56:59], v[204:207]\n"
"v_mfma_f32_16x16x32_bf16 v[18:21], v[10:13], v[56:59], v[18:21]\n"
"v_mfma_f32_16x16x32_bf16 v[68:71], a[80:83], v[112:115], v[44:47]\n"
"v_exp_f32 v76, v76\n"
"v_mfma_f32_16x16x32_bf16 v[68:71], a[84:87], v[116:119], v[68:71]\n"
"v_cvt_pk_bf16_f32 v61, v74, v75\n"
"v_mfma_f32_16x16x32_bf16 v[68:71], a[88:91], v[120:123], v[68:71]\n"
"v_exp_f32 v77, v77\n"
"v_mfma_f32_16x16x32_bf16 v[68:71], a[92:95], v[124:127], v[68:71]\n"
"v_mfma_f32_16x16x32_bf16 v[208:211], a[144:147], v[56:59], v[208:211]\n"
"v_exp_f32 v78, v78\n"
"v_mfma_f32_16x16x32_bf16 v[212:215], a[148:151], v[56:59], v[212:215]\n"
"v_cvt_pk_bf16_f32 v62, v76, v77\n"
"v_mfma_f32_16x16x32_bf16 v[216:219], a[152:155], v[56:59], v[216:219]\n"
"v_exp_f32 v79, v79\n"
"v_mfma_f32_16x16x32_bf16 v[220:223], a[156:159], v[56:59], v[220:223]\n"
"v_mfma_f32_16x16x32_bf16 v[72:75], a[96:99], v[112:115], v[44:47]\n"
"v_exp_f32 v64, v64\n"
"v_mfma_f32_16x16x32_bf16 v[72:75], a[100:103], v[116:119], v[72:75]\n"
"v_cvt_pk_bf16_f32 v63, v78, v79\n"
"v_mfma_f32_16x16x32_bf16 v[72:75], a[104:107], v[120:123], v[72:75]\n"
"v_exp_f32 v65, v65\n"
"v_mfma_f32_16x16x32_bf16 v[72:75], a[108:111], v[124:127], v[72:75]\n"
"v_add_f32 v31, v31, v64\n"
"v_mfma_f32_16x16x32_bf16 v[192:195], a[160:163], v[60:63], v[192:195]\n"
"v_exp_f32 v66, v66\n"
"v_mfma_f32_16x16x32_bf16 v[196:199], a[164:167], v[60:63], v[196:199]\n"
"v_cvt_pk_bf16_f32 v56, v64, v65\n"
"v_add_f32 v31, v31, v65\n"
"v_mfma_f32_16x16x32_bf16 v[200:203], a[168:171], v[60:63], v[200:203]\n"
"v_exp_f32 v67, v67\n"
"v_mfma_f32_16x16x32_bf16 v[204:207], a[172:175], v[60:63], v[204:207]\n"
"v_add_f32 v31, v31, v66\n"
"v_mfma_f32_16x16x32_bf16 v[18:21], v[10:13], v[60:63], v[18:21]\n"
"v_mfma_f32_16x16x32_bf16 v[76:79], a[112:115], v[112:115], v[44:47]\n"
"v_exp_f32 v68, v68\n"
"v_mfma_f32_16x16x32_bf16 v[76:79], a[116:119], v[116:119], v[76:79]\n"
"v_cvt_pk_bf16_f32 v57, v66, v67\n"
"v_add_f32 v31, v31, v67\n"
"v_mfma_f32_16x16x32_bf16 v[76:79], a[120:123], v[120:123], v[76:79]\n"
"v_exp_f32 v69, v69\n"
"v_mfma_f32_16x16x32_bf16 v[76:79], a[124:127], v[124:127], v[76:79]\n"
"v_add_f32 v31, v31, v68\n"
"v_mfma_f32_16x16x32_bf16 v[208:211], a[176:179], v[60:63], v[208:211]\n"
"v_exp_f32 v70, v70\n"
"v_mfma_f32_16x16x32_bf16 v[212:215], a[180:183], v[60:63], v[212:215]\n"
"v_cvt_pk_bf16_f32 v58, v68, v69\n"
"v_add_f32 v31, v31, v69\n"
"v_mfma_f32_16x16x32_bf16 v[216:219], a[184:187], v[60:63], v[216:219]\n"
"v_exp_f32 v71, v71\n"
"v_mfma_f32_16x16x32_bf16 v[220:223], a[188:191], v[60:63], v[220:223]\n"
"v_add_f32 v31, v31, v70\n"
"v_mov_b32 v30, v18\n"
"v_cmp_nge_f32 vcc, 0x53800000, v30\n"
"v_rcp_f32 v28, v30\n"
"s_or_b64 s[64:65], s[64:65], vcc\n"
"s_mov_b32 s55, 4096\n"
"v_pk_mul_f32 v[192:193], v[192:193], v[28:29] op_sel_hi:[1,0]\n"
"v_pk_mul_f32 v[194:195], v[194:195], v[28:29] op_sel_hi:[1,0]\n"
"v_pk_mul_f32 v[196:197], v[196:197], v[28:29] op_sel_hi:[1,0]\n"
"v_pk_mul_f32 v[198:199], v[198:199], v[28:29] op_sel_hi:[1,0]\n"
"v_cvt_pk_bf16_f32 v192, v192, v193\n"
"v_cvt_pk_bf16_f32 v193, v194, v195\n"
"v_cvt_pk_bf16_f32 v194, v196, v197\n"
"v_cvt_pk_bf16_f32 v195, v198, v199\n"
"buffer_store_dwordx4 v[192:195], v1, s[40:43], s55 offen sc1\n"
"v_pk_mul_f32 v[200:201], v[200:201], v[28:29] op_sel_hi:[1,0]\n"
"v_pk_mul_f32 v[202:203], v[202:203], v[28:29] op_sel_hi:[1,0]\n"
"v_pk_mul_f32 v[204:205], v[204:205], v[28:29] op_sel_hi:[1,0]\n"
"v_pk_mul_f32 v[206:207], v[206:207], v[28:29] op_sel_hi:[1,0]\n"
"v_cvt_pk_bf16_f32 v200, v200, v201\n"
"v_cvt_pk_bf16_f32 v201, v202, v203\n"
"v_cvt_pk_bf16_f32 v202, v204, v205\n"
"v_cvt_pk_bf16_f32 v203, v206, v207\n"
"buffer_store_dwordx4 v[200:203], v1, s[40:43], s55 offen offset:1024 sc1\n"
"v_pk_mul_f32 v[208:209], v[208:209], v[28:29] op_sel_hi:[1,0]\n"
"v_pk_mul_f32 v[210:211], v[210:211], v[28:29] op_sel_hi:[1,0]\n"
"v_pk_mul_f32 v[212:213], v[212:213], v[28:29] op_sel_hi:[1,0]\n"
"v_pk_mul_f32 v[214:215], v[214:215], v[28:29] op_sel_hi:[1,0]\n"
"v_cvt_pk_bf16_f32 v208, v208, v209\n"
"v_cvt_pk_bf16_f32 v209, v210, v211\n"
"v_cvt_pk_bf16_f32 v210, v212, v213\n"
"v_cvt_pk_bf16_f32 v211, v214, v215\n"
"buffer_store_dwordx4 v[208:211], v1, s[40:43], s55 offen offset:2048 sc1\n"
"v_pk_mul_f32 v[216:217], v[216:217], v[28:29] op_sel_hi:[1,0]\n"
"v_pk_mul_f32 v[218:219], v[218:219], v[28:29] op_sel_hi:[1,0]\n"
"v_pk_mul_f32 v[220:221], v[220:221], v[28:29] op_sel_hi:[1,0]\n"
"v_pk_mul_f32 v[222:223], v[222:223], v[28:29] op_sel_hi:[1,0]\n"
"v_cvt_pk_bf16_f32 v216, v216, v217\n"
"v_cvt_pk_bf16_f32 v217, v218, v219\n"
"v_cvt_pk_bf16_f32 v218, v220, v221\n"
"v_cvt_pk_bf16_f32 v219, v222, v223\n"
"buffer_store_dwordx4 v[216:219], v1, s[40:43], s55 offen offset:3072 sc1\n"
"v_mfma_f32_16x16x32_bf16 v[64:67], a[64:67], v[128:131], v[48:51]\n"
"v_exp_f32 v72, v72\n"
"v_mfma_f32_16x16x32_bf16 v[64:67], a[68:71], v[132:135], v[64:67]\n"
"v_cvt_pk_bf16_f32 v59, v70, v71\n"
"v_add_f32 v31, v31, v71\n"
"v_mfma_f32_16x16x32_bf16 v[64:67], a[72:75], v[136:139], v[64:67]\n"
"v_exp_f32 v73, v73\n"
"v_mfma_f32_16x16x32_bf16 v[64:67], a[76:79], v[140:143], v[64:67]\n"
"v_add_f32 v31, v31, v72\n"
"v_mfma_f32_16x16x32_bf16 v[224:227], a[128:131], v[56:59], v[224:227]\n"
"v_exp_f32 v74, v74\n"
"v_mfma_f32_16x16x32_bf16 v[228:231], a[132:135], v[56:59], v[228:231]\n"
"v_cvt_pk_bf16_f32 v60, v72, v73\n"
"v_add_f32 v31, v31, v73\n"
"v_mfma_f32_16x16x32_bf16 v[232:235], a[136:139], v[56:59], v[232:235]\n"
"v_exp_f32 v75, v75\n"
"v_mfma_f32_16x16x32_bf16 v[236:239], a[140:143], v[56:59], v[236:239]\n"
"v_add_f32 v31, v31, v74\n"
"v_mfma_f32_16x16x32_bf16 v[68:71], a[80:83], v[128:131], v[48:51]\n"
"v_exp_f32 v76, v76\n"
"v_mfma_f32_16x16x32_bf16 v[68:71], a[84:87], v[132:135], v[68:71]\n"
"v_cvt_pk_bf16_f32 v61, v74, v75\n"
"v_add_f32 v31, v31, v75\n"
"v_mfma_f32_16x16x32_bf16 v[68:71], a[88:91], v[136:139], v[68:71]\n"
"v_exp_f32 v77, v77\n"
"v_mfma_f32_16x16x32_bf16 v[68:71], a[92:95], v[140:143], v[68:71]\n"
"v_add_f32 v31, v31, v76\n"
"v_mfma_f32_16x16x32_bf16 v[240:243], a[144:147], v[56:59], v[240:243]\n"
"v_exp_f32 v78, v78\n"
"v_mfma_f32_16x16x32_bf16 v[244:247], a[148:151], v[56:59], v[244:247]\n"
"v_cvt_pk_bf16_f32 v62, v76, v77\n"
"v_add_f32 v31, v31, v77\n"
"v_mfma_f32_16x16x32_bf16 v[248:251], a[152:155], v[56:59], v[248:251]\n"
"v_exp_f32 v79, v79\n"
"v_mfma_f32_16x16x32_bf16 v[252:255], a[156:159], v[56:59], v[252:255]\n"
"v_add_f32 v31, v31, v78\n"
"v_mfma_f32_16x16x32_bf16 v[72:75], a[96:99], v[128:131], v[48:51]\n"
"v_exp_f32 v64, v64\n"
"v_mfma_f32_16x16x32_bf16 v[72:75], a[100:103], v[132:135], v[72:75]\n"
"v_cvt_pk_bf16_f32 v63, v78, v79\n"
"v_add_f32 v31, v31, v79\n"
"v_mfma_f32_16x16x32_bf16 v[72:75], a[104:107], v[136:139], v[72:75]\n"
"v_exp_f32 v65, v65\n"
"v_mfma_f32_16x16x32_bf16 v[72:75], a[108:111], v[140:143], v[72:75]\n"
"v_add_f32 v34, v34, v64\n"
"v_mfma_f32_16x16x32_bf16 v[224:227], a[160:163], v[60:63], v[224:227]\n"
"v_exp_f32 v66, v66\n"
"v_mfma_f32_16x16x32_bf16 v[228:231], a[164:167], v[60:63], v[228:231]\n"
"v_cvt_pk_bf16_f32 v56, v64, v65\n"
"v_add_f32 v34, v34, v65\n"
"v_mfma_f32_16x16x32_bf16 v[232:235], a[168:171], v[60:63], v[232:235]\n"
"v_exp_f32 v67, v67\n"
"v_mfma_f32_16x16x32_bf16 v[236:239], a[172:175], v[60:63], v[236:239]\n"
"v_add_f32 v34, v34, v66\n"
"v_mfma_f32_16x16x32_bf16 v[76:79], a[112:115], v[128:131], v[48:51]\n"
"v_exp_f32 v68, v68\n"
"v_mfma_f32_16x16x32_bf16 v[76:79], a[116:119], v[132:135], v[76:79]\n"
"v_cvt_pk_bf16_f32 v57, v66, v67\n"
"v_add_f32 v34, v34, v67\n"
"v_mfma_f32_16x16x32_bf16 v[76:79], a[120:123], v[136:139], v[76:79]\n"
"v_exp_f32 v69, v69\n"
"v_mfma_f32_16x16x32_bf16 v[76:79], a[124:127], v[140:143], v[76:79]\n"
"v_add_f32 v34, v34, v68\n"
"v_mfma_f32_16x16x32_bf16 v[240:243], a[176:179], v[60:63], v[240:243]\n"
"v_exp_f32 v70, v70\n"
"v_mfma_f32_16x16x32_bf16 v[244:247], a[180:183], v[60:63], v[244:247]\n"
"v_cvt_pk_bf16_f32 v58, v68, v69\n"
"v_add_f32 v34, v34, v69\n"
"v_mfma_f32_16x16x32_bf16 v[248:251], a[184:187], v[60:63], v[248:251]\n"
"v_exp_f32 v71, v71\n"
"v_mfma_f32_16x16x32_bf16 v[252:255], a[188:191], v[60:63], v[252:255]\n"
"v_add_f32 v34, v34, v70\n"
"v_mov_b32 v27, v31\n"
"s_nop 0\n"
"s_nop 0\n"
"v_permlane32_swap_b32 v31, v27\n"
"v_add_f32 v31, v31, v27\n"
"v_mov_b32 v27, v31\n"
"s_nop 0\n"
"s_nop 0\n"
"v_permlane16_swap_b32 v31, v27\n"
"v_add_f32 v31, v31, v27\n"
"v_cmp_nge_f32 vcc, 0x53800000, v31\n"
"v_rcp_f32 v28, v31\n"
"s_or_b64 s[64:65], s[64:65], vcc\n"
"s_mov_b32 s55, 8192\n"
"v_pk_mul_f32 v[224:225], v[224:225], v[28:29] op_sel_hi:[1,0]\n"
"v_pk_mul_f32 v[226:227], v[226:227], v[28:29] op_sel_hi:[1,0]\n"
"v_pk_mul_f32 v[228:229], v[228:229], v[28:29] op_sel_hi:[1,0]\n"
"v_pk_mul_f32 v[230:231], v[230:231], v[28:29] op_sel_hi:[1,0]\n"
"v_cvt_pk_bf16_f32 v224, v224, v225\n"
"v_cvt_pk_bf16_f32 v225, v226, v227\n"
"v_cvt_pk_bf16_f32 v226, v228, v229\n"
"v_cvt_pk_bf16_f32 v227, v230, v231\n"
"buffer_store_dwordx4 v[224:227], v1, s[40:43], s55 offen sc1\n"
"v_pk_mul_f32 v[232:233], v[232:233], v[28:29] op_sel_hi:[1,0]\n"
"v_pk_mul_f32 v[234:235], v[234:235], v[28:29] op_sel_hi:[1,0]\n"
"v_pk_mul_f32 v[236:237], v[236:237], v[28:29] op_sel_hi:[1,0]\n"
"v_pk_mul_f32 v[238:239], v[238:239], v[28:29] op_sel_hi:[1,0]\n"
"v_cvt_pk_bf16_f32 v232, v232, v233\n"
"v_cvt_pk_bf16_f32 v233, v234, v235\n"
"v_cvt_pk_bf16_f32 v234, v236, v237\n"
"v_cvt_pk_bf16_f32 v235, v238, v239\n"
"buffer_store_dwordx4 v[232:235], v1, s[40:43], s55 offen offset:1024 sc1\n"
"v_pk_mul_f32 v[240:241], v[240:241], v[28:29] op_sel_hi:[1,0]\n"
"v_pk_mul_f32 v[242:243], v[242:243], v[28:29] op_sel_hi:[1,0]\n"
"v_pk_mul_f32 v[244:245], v[244:245], v[28:29] op_sel_hi:[1,0]\n"
"v_pk_mul_f32 v[246:247], v[246:247], v[28:29] op_sel_hi:[1,0]\n"
"v_cvt_pk_bf16_f32 v240, v240, v241\n"
"v_cvt_pk_bf16_f32 v241, v242, v243\n"
"v_cvt_pk_bf16_f32 v242, v244, v245\n"
"v_cvt_pk_bf16_f32 v243, v246, v247\n"
"buffer_store_dwordx4 v[240:243], v1, s[40:43], s55 offen offset:2048 sc1\n"
"v_pk_mul_f32 v[248:249], v[248:249], v[28:29] op_sel_hi:[1,0]\n"
"v_pk_mul_f32 v[250:251], v[250:251], v[28:29] op_sel_hi:[1,0]\n"
"v_pk_mul_f32 v[252:253], v[252:253], v[28:29] op_sel_hi:[1,0]\n"
"v_pk_mul_f32 v[254:255], v[254:255], v[28:29] op_sel_hi:[1,0]\n"
"v_cvt_pk_bf16_f32 v248, v248, v249\n"
"v_cvt_pk_bf16_f32 v249, v250, v251\n"
"v_cvt_pk_bf16_f32 v250, v252, v253\n"
"v_cvt_pk_bf16_f32 v251, v254, v255\n"
"buffer_store_dwordx4 v[248:251], v1, s[40:43], s55 offen offset:3072 sc1\n"
"v_subrev_u32 v4, 131072, v16\n"
"v_subrev_u32 v5, 131072, v17\n"
"v_subrev_u32 v6, 65536, v16\n"
"v_subrev_u32 v7, 65536, v17\n"
"v_subrev_u32 v8, 0, v16\n"
"v_subrev_u32 v9, 0, v17\n"
"v_bfe_u32 v2, v0, 2, 3\n"
"v_lshlrev_b32 v10, 10, v2\n"
"v_lshrrev_b32 v2, 5, v0\n"
"v_lshl_add_u32 v10, v2, 6, v10\n"
"v_bfe_u32 v2, v0, 4, 1\n"
"v_lshlrev_b32 v2, 1, v2\n"
"v_and_b32 v3, 3, v0\n"
"v_xor_b32 v2, v2, v3\n"
"v_lshl_add_u32 v10, v2, 4, v10\n"
"v_add_u32 v11, 7168, v10\n"
"v_add_u32 v12, 14336, v10\n"
"v_add_u32 v13, 21504, v10\n"
"s_mov_b64 s[60:61], s[16:17]\n"
"s_lshl_b32 s52, s49, 14\n"
"s_add_u32 s52, s52, 25165824\n"
"s_add_u32 s60, s60, s52\n"
"s_addc_u32 s61, s61, 0\n"
"s_lshl_b32 s52, s48, 5\n"
"s_add_u32 s52, s52, s50\n"
"s_lshl_b32 s52, s52, 7\n"
"v_mov_b32 v21, s52\n"
"v_mov_b32 v18, s70\n"
"v_mov_b32 v19, s71\n"
"s_andn2_b32 s63, 16, s50\n"
"s_lshl2_add_u32 s63, s48, s63\n"
"s_xor_b32 s52, s69, 1\n"
"s_lshl_b32 s52, s52, 5\n"
"s_add_u32 s52, s52, s63\n"
"v_and_b32 v15, 3, v0\n"
"v_add_u32 v15, s52, v15\n"
"v_lshlrev_b32 v15, 7, v15\n"
"v_mfma_f32_16x16x32_bf16 v[64:67], a[64:67], v[144:147], v[52:55]\n"
"v_exp_f32 v72, v72\n"
"v_mfma_f32_16x16x32_bf16 v[64:67], a[68:71], v[148:151], v[64:67]\n"
"v_cvt_pk_bf16_f32 v59, v70, v71\n"
"v_add_f32 v34, v34, v71\n"
"v_mfma_f32_16x16x32_bf16 v[64:67], a[72:75], v[152:155], v[64:67]\n"
"v_exp_f32 v73, v73\n"
"v_mfma_f32_16x16x32_bf16 v[64:67], a[76:79], v[156:159], v[64:67]\n"
"v_add_f32 v34, v34, v72\n"
"v_mfma_f32_16x16x32_bf16 a[192:195], a[128:131], v[56:59], a[192:195]\n"
"v_exp_f32 v74, v74\n"
"v_mfma_f32_16x16x32_bf16 a[196:199], a[132:135], v[56:59], a[196:199]\n"
"v_cvt_pk_bf16_f32 v60, v72, v73\n"
"v_add_f32 v34, v34, v73\n"
"v_mfma_f32_16x16x32_bf16 a[200:203], a[136:139], v[56:59], a[200:203]\n"
"v_exp_f32 v75, v75\n"
"v_mfma_f32_16x16x32_bf16 a[204:207], a[140:143], v[56:59], a[204:207]\n"
"v_add_f32 v34, v34, v74\n"
"v_mfma_f32_16x16x32_bf16 v[68:71], a[80:83], v[144:147], v[52:55]\n"
"v_exp_f32 v76, v76\n"
"v_mfma_f32_16x16x32_bf16 v[68:71], a[84:87], v[148:151], v[68:71]\n"
"v_cvt_pk_bf16_f32 v61, v74, v75\n"
"v_add_f32 v34, v34, v75\n"
"v_mfma_f32_16x16x32_bf16 v[68:71], a[88:91], v[152:155], v[68:71]\n"
"v_exp_f32 v77, v77\n"
"v_mfma_f32_16x16x32_bf16 v[68:71], a[92:95], v[156:159], v[68:71]\n"
"v_add_f32 v34, v34, v76\n"
"v_mfma_f32_16x16x32_bf16 a[208:211], a[144:147], v[56:59], a[208:211]\n"
"v_exp_f32 v78, v78\n"
"v_mfma_f32_16x16x32_bf16 a[212:215], a[148:151], v[56:59], a[212:215]\n"
"v_cvt_pk_bf16_f32 v62, v76, v77\n"
"v_add_f32 v34, v34, v77\n"
"v_mfma_f32_16x16x32_bf16 a[216:219], a[152:155], v[56:59], a[216:219]\n"
"v_exp_f32 v79, v79\n"
"v_mfma_f32_16x16x32_bf16 a[220:223], a[156:159], v[56:59], a[220:223]\n"
"v_add_f32 v34, v34, v78\n"
"v_mfma_f32_16x16x32_bf16 v[72:75], a[96:99], v[144:147], v[52:55]\n"
"v_exp_f32 v64, v64\n"
"v_mfma_f32_16x16x32_bf16 v[72:75], a[100:103], v[148:151], v[72:75]\n"
"v_cvt_pk_bf16_f32 v63, v78, v79\n"
"v_add_f32 v34, v34, v79\n"
"v_mfma_f32_16x16x32_bf16 v[72:75], a[104:107], v[152:155], v[72:75]\n"
"v_exp_f32 v65, v65\n"
"v_mfma_f32_16x16x32_bf16 v[72:75], a[108:111], v[156:159], v[72:75]\n"
"v_add_f32 v35, v35, v64\n"
"v_mfma_f32_16x16x32_bf16 a[192:195], a[160:163], v[60:63], a[192:195]\n"
"v_exp_f32 v66, v66\n"
"v_mfma_f32_16x16x32_bf16 a[196:199], a[164:167], v[60:63], a[196:199]\n"
"v_cvt_pk_bf16_f32 v56, v64, v65\n"
"v_add_f32 v35, v35, v65\n"
"v_mfma_f32_16x16x32_bf16 a[200:203], a[168:171], v[60:63], a[200:203]\n"
"v_exp_f32 v67, v67\n"
"v_mfma_f32_16x16x32_bf16 a[204:207], a[172:175], v[60:63], a[204:207]\n"
"v_add_f32 v35, v35, v66\n"
"v_mfma_f32_16x16x32_bf16 v[76:79], a[112:115], v[144:147], v[52:55]\n"
"v_exp_f32 v68, v68\n"
"v_mfma_f32_16x16x32_bf16 v[76:79], a[116:119], v[148:151], v[76:79]\n"
"v_cvt_pk_bf16_f32 v57, v66, v67\n"
"v_add_f32 v35, v35, v67\n"
"v_mfma_f32_16x16x32_bf16 v[76:79], a[120:123], v[152:155], v[76:79]\n"
"v_exp_f32 v69, v69\n"
"v_mfma_f32_16x16x32_bf16 v[76:79], a[124:127], v[156:159], v[76:79]\n"
"v_add_f32 v35, v35, v68\n"
"v_mfma_f32_16x16x32_bf16 a[208:211], a[176:179], v[60:63], a[208:211]\n"
"v_exp_f32 v70, v70\n"
"v_mfma_f32_16x16x32_bf16 a[212:215], a[180:183], v[60:63], a[212:215]\n"
"v_cvt_pk_bf16_f32 v58, v68, v69\n"
"v_add_f32 v35, v35, v69\n"
"v_mfma_f32_16x16x32_bf16 a[216:219], a[184:187], v[60:63], a[216:219]\n"
"v_exp_f32 v71, v71\n"
"v_mfma_f32_16x16x32_bf16 a[220:223], a[188:191], v[60:63], a[220:223]\n"
"v_add_f32 v35, v35, v70\n"
"v_mov_b32 v27, v34\n"
"s_nop 0\n"
"s_nop 0\n"
"v_permlane32_swap_b32 v34, v27\n"
"v_add_f32 v34, v34, v27\n"
"v_mov_b32 v27, v34\n"
"s_nop 0\n"
"s_nop 0\n"
"v_permlane16_swap_b32 v34, v27\n"
"v_add_f32 v34, v34, v27\n"
"v_cmp_nge_f32 vcc, 0x53800000, v34\n"
"v_rcp_f32 v28, v34\n"
"s_or_b64 s[64:65], s[64:65], vcc\n"
"s_mov_b32 s55, 12288\n"
"v_accvgpr_read_b32 v160, a192\n"
"v_accvgpr_read_b32 v161, a193\n"
"v_accvgpr_read_b32 v162, a194\n"
"v_accvgpr_read_b32 v163, a195\n"
"v_accvgpr_read_b32 v164, a196\n"
"v_accvgpr_read_b32 v165, a197\n"
"v_accvgpr_read_b32 v166, a198\n"
"v_accvgpr_read_b32 v167, a199\n"
"v_pk_mul_f32 v[160:161], v[160:161], v[28:29] op_sel_hi:[1,0]\n"
"v_pk_mul_f32 v[162:163], v[162:163], v[28:29] op_sel_hi:[1,0]\n"
"v_pk_mul_f32 v[164:165], v[164:165], v[28:29] op_sel_hi:[1,0]\n"
"v_pk_mul_f32 v[166:167], v[166:167], v[28:29] op_sel_hi:[1,0]\n"
"v_cvt_pk_bf16_f32 v160, v160, v161\n"
"v_cvt_pk_bf16_f32 v161, v162, v163\n"
"v_cvt_pk_bf16_f32 v162, v164, v165\n"
"v_cvt_pk_bf16_f32 v163, v166, v167\n"
"buffer_store_dwordx4 v[160:163], v1, s[40:43], s55 offen sc1\n"
"v_accvgpr_read_b32 v168, a200\n"
"v_accvgpr_read_b32 v169, a201\n"
"v_accvgpr_read_b32 v170, a202\n"
"v_accvgpr_read_b32 v171, a203\n"
"v_accvgpr_read_b32 v172, a204\n"
"v_accvgpr_read_b32 v173, a205\n"
"v_accvgpr_read_b32 v174, a206\n"
"v_accvgpr_read_b32 v175, a207\n"
"v_pk_mul_f32 v[168:169], v[168:169], v[28:29] op_sel_hi:[1,0]\n"
"v_pk_mul_f32 v[170:171], v[170:171], v[28:29] op_sel_hi:[1,0]\n"
"v_pk_mul_f32 v[172:173], v[172:173], v[28:29] op_sel_hi:[1,0]\n"
"v_pk_mul_f32 v[174:175], v[174:175], v[28:29] op_sel_hi:[1,0]\n"
"v_cvt_pk_bf16_f32 v168, v168, v169\n"
"v_cvt_pk_bf16_f32 v169, v170, v171\n"
"v_cvt_pk_bf16_f32 v170, v172, v173\n"
"v_cvt_pk_bf16_f32 v171, v174, v175\n"
"buffer_store_dwordx4 v[168:171], v1, s[40:43], s55 offen offset:1024 sc1\n"
"v_accvgpr_read_b32 v176, a208\n"
"v_accvgpr_read_b32 v177, a209\n"
"v_accvgpr_read_b32 v178, a210\n"
"v_accvgpr_read_b32 v179, a211\n"
"v_accvgpr_read_b32 v180, a212\n"
"v_accvgpr_read_b32 v181, a213\n"
"v_accvgpr_read_b32 v182, a214\n"
"v_accvgpr_read_b32 v183, a215\n"
"v_pk_mul_f32 v[176:177], v[176:177], v[28:29] op_sel_hi:[1,0]\n"
"v_pk_mul_f32 v[178:179], v[178:179], v[28:29] op_sel_hi:[1,0]\n"
"v_pk_mul_f32 v[180:181], v[180:181], v[28:29] op_sel_hi:[1,0]\n"
"v_pk_mul_f32 v[182:183], v[182:183], v[28:29] op_sel_hi:[1,0]\n"
"v_cvt_pk_bf16_f32 v176, v176, v177\n"
"v_cvt_pk_bf16_f32 v177, v178, v179\n"
"v_cvt_pk_bf16_f32 v178, v180, v181\n"
"v_cvt_pk_bf16_f32 v179, v182, v183\n"
"buffer_store_dwordx4 v[176:179], v1, s[40:43], s55 offen offset:2048 sc1\n"
"v_accvgpr_read_b32 v184, a216\n"
"v_accvgpr_read_b32 v185, a217\n"
"v_accvgpr_read_b32 v186, a218\n"
"v_accvgpr_read_b32 v187, a219\n"
"v_accvgpr_read_b32 v188, a220\n"
"v_accvgpr_read_b32 v189, a221\n"
"v_accvgpr_read_b32 v190, a222\n"
"v_accvgpr_read_b32 v191, a223\n"
"v_pk_mul_f32 v[184:185], v[184:185], v[28:29] op_sel_hi:[1,0]\n"
"v_pk_mul_f32 v[186:187], v[186:187], v[28:29] op_sel_hi:[1,0]\n"
"v_pk_mul_f32 v[188:189], v[188:189], v[28:29] op_sel_hi:[1,0]\n"
"v_pk_mul_f32 v[190:191], v[190:191], v[28:29] op_sel_hi:[1,0]\n"
"v_cvt_pk_bf16_f32 v184, v184, v185\n"
"v_cvt_pk_bf16_f32 v185, v186, v187\n"
"v_cvt_pk_bf16_f32 v186, v188, v189\n"
"v_cvt_pk_bf16_f32 v187, v190, v191\n"
"buffer_store_dwordx4 v[184:187], v1, s[40:43], s55 offen offset:3072 sc1\n"
"s_and_b32 s52, s69, 2\n"
"s_lshl2_add_u32 s52, s49, s52\n"
"s_lshl_b32 s52, s52, 5\n"
"s_add_u32 s52, s52, s63\n"
"s_mul_i32 s52, s52, 20480\n"
"s_mov_b64 s[24:25], s[16:17]\n"
"s_add_u32 s24, s24, s52\n"
"s_addc_u32 s25, s25, 0\n"
"s_and_b32 s25, s25, 0xffff\n"
"s_mov_b32 s26, 737280\n"
"s_mov_b32 s27, 0x20000\n"
"s_and_b32 s52, s69, 1\n"
"s_mul_i32 s57, s52, 655360\n"
"s_sub_u32 s56, 655360, s57\n"
"s_lshl_b32 s67, s48, 19\n"
"v_exp_f32 v72, v72\n"
"v_cvt_pk_bf16_f32 v59, v70, v71\n"
"v_add_f32 v35, v35, v71\n"
"v_exp_f32 v73, v73\n"
"v_add_f32 v35, v35, v72\n"
"v_mfma_f32_16x16x32_bf16 a[224:227], a[128:131], v[56:59], a[224:227]\n"
"v_exp_f32 v74, v74\n"
"v_mfma_f32_16x16x32_bf16 a[228:231], a[132:135], v[56:59], a[228:231]\n"
"v_cvt_pk_bf16_f32 v60, v72, v73\n"
"v_add_f32 v35, v35, v73\n"
"v_mfma_f32_16x16x32_bf16 a[232:235], a[136:139], v[56:59], a[232:235]\n"
"v_exp_f32 v75, v75\n"
"v_mfma_f32_16x16x32_bf16 a[236:239], a[140:143], v[56:59], a[236:239]\n"
"v_add_f32 v35, v35, v74\n"
"v_exp_f32 v76, v76\n"
"v_cvt_pk_bf16_f32 v61, v74, v75\n"
"v_add_f32 v35, v35, v75\n"
"v_exp_f32 v77, v77\n"
"v_add_f32 v35, v35, v76\n"
"v_mfma_f32_16x16x32_bf16 a[240:243], a[144:147], v[56:59], a[240:243]\n"
"v_exp_f32 v78, v78\n"
"v_mfma_f32_16x16x32_bf16 a[244:247], a[148:151], v[56:59], a[244:247]\n"
"v_cvt_pk_bf16_f32 v62, v76, v77\n"
"v_add_f32 v35, v35, v77\n"
"v_mfma_f32_16x16x32_bf16 a[248:251], a[152:155], v[56:59], a[248:251]\n"
"v_exp_f32 v79, v79\n"
"v_mfma_f32_16x16x32_bf16 a[252:255], a[156:159], v[56:59], a[252:255]\n"
"v_add_f32 v35, v35, v78\n"
"v_cvt_pk_bf16_f32 v63, v78, v79\n"
"v_add_f32 v35, v35, v79\n"
"s_nop 0\n"
"v_mfma_f32_16x16x32_bf16 a[224:227], a[160:163], v[60:63], a[224:227]\n"
"v_mfma_f32_16x16x32_bf16 a[228:231], a[164:167], v[60:63], a[228:231]\n"
"v_mfma_f32_16x16x32_bf16 a[232:235], a[168:171], v[60:63], a[232:235]\n"
"v_mfma_f32_16x16x32_bf16 a[236:239], a[172:175], v[60:63], a[236:239]\n"
"v_mfma_f32_16x16x32_bf16 a[240:243], a[176:179], v[60:63], a[240:243]\n"
"v_mfma_f32_16x16x32_bf16 a[244:247], a[180:183], v[60:63], a[244:247]\n"
"v_mfma_f32_16x16x32_bf16 a[248:251], a[184:187], v[60:63], a[248:251]\n"
"v_mfma_f32_16x16x32_bf16 a[252:255], a[188:191], v[60:63], a[252:255]\n"
"v_mov_b32 v27, v35\n"
"s_nop 0\n"
"s_nop 0\n"
"v_permlane32_swap_b32 v35, v27\n"
"v_add_f32 v35, v35, v27\n"
"v_mov_b32 v27, v35\n"
"s_nop 0\n"
"s_nop 0\n"
"v_permlane16_swap_b32 v35, v27\n"
"v_add_f32 v35, v35, v27\n"
"v_cmp_nge_f32 vcc, 0x53800000, v35\n"
"v_rcp_f32 v28, v35\n"
"s_or_b64 s[64:65], s[64:65], vcc\n"
"s_or_b32 s64, s64, s65\n"
"s_lshl_b32 s52, s48, 2\n"
"s_add_u32 s52, s52, 114688\n"
"v_mov_b32 v2, s52\n"
"v_mov_b32 v3, s64\n"
"ds_write_b32 v2, v3\n"
"v_mov_b32 v48, 114688\n"
"s_waitcnt lgkmcnt(0)\n"
"s_barrier\n"
"ds_read_b128 v[48:51], v48\n"
"v_accvgpr_read_b32 v192, a224\n"
"v_accvgpr_read_b32 v193, a225\n"
"v_accvgpr_read_b32 v194, a226\n"
"v_accvgpr_read_b32 v195, a227\n"
"v_accvgpr_read_b32 v196, a228\n"
"v_accvgpr_read_b32 v197, a229\n"
"v_accvgpr_read_b32 v198, a230\n"
"v_accvgpr_read_b32 v199, a231\n"
"v_pk_mul_f32 v[192:193], v[192:193], v[28:29] op_sel_hi:[1,0]\n"
"v_pk_mul_f32 v[194:195], v[194:195], v[28:29] op_sel_hi:[1,0]\n"
"v_pk_mul_f32 v[196:197], v[196:197], v[28:29] op_sel_hi:[1,0]\n"
"v_pk_mul_f32 v[198:199], v[198:199], v[28:29] op_sel_hi:[1,0]\n"
"v_cvt_pk_bf16_f32 v192, v192, v193\n"
"v_cvt_pk_bf16_f32 v193, v194, v195\n"
"v_cvt_pk_bf16_f32 v194, v196, v197\n"
"v_cvt_pk_bf16_f32 v195, v198, v199\n"
"v_accvgpr_read_b32 v200, a232\n"
"v_accvgpr_read_b32 v201, a233\n"
"v_accvgpr_read_b32 v202, a234\n"
"v_accvgpr_read_b32 v203, a235\n"
"v_accvgpr_read_b32 v204, a236\n"
"v_accvgpr_read_b32 v205, a237\n"
"v_accvgpr_read_b32 v206, a238\n"
"v_accvgpr_read_b32 v207, a239\n"
"v_pk_mul_f32 v[200:201], v[200:201], v[28:29] op_sel_hi:[1,0]\n"
"v_pk_mul_f32 v[202:203], v[202:203], v[28:29] op_sel_hi:[1,0]\n"
"v_pk_mul_f32 v[204:205], v[204:205], v[28:29] op_sel_hi:[1,0]\n"
"v_pk_mul_f32 v[206:207], v[206:207], v[28:29] op_sel_hi:[1,0]\n"
"v_cvt_pk_bf16_f32 v200, v200, v201\n"
"v_cvt_pk_bf16_f32 v201, v202, v203\n"
"v_cvt_pk_bf16_f32 v202, v204, v205\n"
"v_cvt_pk_bf16_f32 v203, v206, v207\n"
"v_accvgpr_read_b32 v208, a240\n"
"v_accvgpr_read_b32 v209, a241\n"
"v_accvgpr_read_b32 v210, a242\n"
"v_accvgpr_read_b32 v211, a243\n"
"v_accvgpr_read_b32 v212, a244\n"
"v_accvgpr_read_b32 v213, a245\n"
"v_accvgpr_read_b32 v214, a246\n"
"v_accvgpr_read_b32 v215, a247\n"
"v_pk_mul_f32 v[208:209], v[208:209], v[28:29] op_sel_hi:[1,0]\n"
"v_pk_mul_f32 v[210:211], v[210:211], v[28:29] op_sel_hi:[1,0]\n"
"v_pk_mul_f32 v[212:213], v[212:213], v[28:29] op_sel_hi:[1,0]\n"
"v_pk_mul_f32 v[214:215], v[214:215], v[28:29] op_sel_hi:[1,0]\n"
"v_cvt_pk_bf16_f32 v208, v208, v209\n"
"v_cvt_pk_bf16_f32 v209, v210, v211\n"
"v_cvt_pk_bf16_f32 v210, v212, v213\n"
"v_cvt_pk_bf16_f32 v211, v214, v215\n"
"v_accvgpr_read_b32 v216, a248\n"
"v_accvgpr_read_b32 v217, a249\n"
"v_accvgpr_read_b32 v218, a250\n"
"v_accvgpr_read_b32 v219, a251\n"
"v_accvgpr_read_b32 v220, a252\n"
"v_accvgpr_read_b32 v221, a253\n"
"v_accvgpr_read_b32 v222, a254\n"
"v_accvgpr_read_b32 v223, a255\n"
"v_pk_mul_f32 v[216:217], v[216:217], v[28:29] op_sel_hi:[1,0]\n"
"v_pk_mul_f32 v[218:219], v[218:219], v[28:29] op_sel_hi:[1,0]\n"
"v_pk_mul_f32 v[220:221], v[220:221], v[28:29] op_sel_hi:[1,0]\n"
"v_pk_mul_f32 v[222:223], v[222:223], v[28:29] op_sel_hi:[1,0]\n"
"v_cvt_pk_bf16_f32 v216, v216, v217\n"
"v_cvt_pk_bf16_f32 v217, v218, v219\n"
"v_cvt_pk_bf16_f32 v218, v220, v221\n"
"v_cvt_pk_bf16_f32 v219, v222, v223\n"
"s_waitcnt lgkmcnt(0)\n"
"v_or_b32 v48, v48, v49\n"
"v_or3_b32 v48, v48, v50, v51\n"
"s_nop 0\n"
"v_readfirstlane_b32 s52, v48\n"
"s_waitcnt vmcnt(4)\n"
"s_andn2_b32 s52, s52, s68\n"
"s_cbranch_scc1 .Lmha_redo5\n"
"s_mov_b64 exec, 1\n"
"global_store_dwordx2 v21, v[18:19], s[60:61] sc1\n"
"s_mov_b64 exec, -1\n"
"s_mov_b32 s55, 16384\n"
"buffer_store_dwordx4 v[192:195], v1, s[40:43], s55 offen sc1\n"
"buffer_store_dwordx4 v[200:203], v1, s[40:43], s55 offen offset:1024 sc1\n"
"buffer_store_dwordx4 v[208:211], v1, s[40:43], s55 offen offset:2048 sc1\n"
"buffer_store_dwordx4 v[216:219], v1, s[40:43], s55 offen offset:3072 sc1\n"
"global_load_dwordx2 v[2:3], v15, s[60:61] sc1\n"
"ds_read_b64_tr_b16 a[64:65], v8\n"
"ds_read_b64_tr_b16 a[66:67], v8 offset:2048\n"
"ds_read_b64_tr_b16 a[68:69], v9\n"
"ds_read_b64_tr_b16 a[70:71], v9 offset:2048\n"
"ds_read_b64_tr_b16 a[72:73], v8 offset:512\n"
"ds_read_b64_tr_b16 a[74:75], v8 offset:2560\n"
"ds_read_b64_tr_b16 a[76:77], v9 offset:512\n"
"ds_read_b64_tr_b16 a[78:79], v9 offset:2560\n"
"ds_read_b64_tr_b16 a[80:81], v8 offset:16384\n"
"ds_read_b64_tr_b16 a[82:83], v8 offset:18432\n"
"ds_read_b64_tr_b16 a[84:85], v9 offset:16384\n"
"ds_read_b64_tr_b16 a[86:87], v9 offset:18432\n"
"ds_read_b64_tr_b16 a[88:89], v8 offset:16896\n"
"ds_read_b64_tr_b16 a[90:91], v8 offset:18944\n"
"ds_read_b64_tr_b16 a[92:93], v9 offset:16896\n"
"ds_read_b64_tr_b16 a[94:95], v9 offset:18944\n"
"ds_read_b64_tr_b16 a[96:97], v4\n"
"ds_read_b64_tr_b16 a[98:99], v4 offset:2048\n"
"ds_read_b64_tr_b16 a[100:101], v5\n"
"ds_read_b64_tr_b16 a[102:103], v5 offset:2048\n"
"ds_read_b64_tr_b16 a[104:105], v4 offset:512\n"
"ds_read_b64_tr_b16 a[106:107], v4 offset:2560\n"
"ds_read_b64_tr_b16 a[108:109], v5 offset:512\n"
"ds_read_b64_tr_b16 a[110:111], v5 offset:2560\n"
"ds_read_b64_tr_b16 a[112:113], v6\n"
"ds_read_b64_tr_b16 a[114:115], v6 offset:2048\n"
"ds_read_b64_tr_b16 a[116:117], v7\n"
"ds_read_b64_tr_b16 a[118:119], v7 offset:2048\n"
"ds_read_b64_tr_b16 a[120:121], v6 offset:512\n"
"ds_read_b64_tr_b16 a[122:123], v6 offset:2560\n"
"ds_read_b64_tr_b16 a[124:125], v7 offset:512\n"
"ds_read_b64_tr_b16 a[126:127], v7 offset:2560\n"
"ds_read_b64_tr_b16 a[128:129], v4 offset:16384\n"
"ds_read_b64_tr_b16 a[130:131], v4 offset:18432\n"
"ds_read_b64_tr_b16 a[132:133], v5 offset:16384\n"
"ds_read_b64_tr_b16 a[134:135], v5 offset:18432\n"
"ds_read_b64_tr_b16 a[136:137], v4 offset:16896\n"
"ds_read_b64_tr_b16 a[138:139], v4 offset:18944\n"
"ds_read_b64_tr_b16 a[140:141], v5 offset:16896\n"
"ds_read_b64_tr_b16 a[142:143], v5 offset:18944\n"
"ds_read_b64_tr_b16 a[144:145], v6 offset:16384\n"
"ds_read_b64_tr_b16 a[146:147], v6 offset:18432\n"
"ds_read_b64_tr_b16 a[148:149], v7 offset:16384\n"
"ds_read_b64_tr_b16 a[150:151], v7 offset:18432\n"
"ds_read_b64_tr_b16 a[152:153], v6 offset:16896\n"
"ds_read_b64_tr_b16 a[154:155], v6 offset:18944\n"
"ds_read_b64_tr_b16 a[156:157], v7 offset:16896\n"
"ds_read_b64_tr_b16 a[158:159], v7 offset:18944\n"
"ds_read_b64_tr_b16 a[160:161], v4 offset:32768\n"
"ds_read_b64_tr_b16 a[162:163], v4 offset:34816\n"
"ds_read_b64_tr_b16 a[164:165], v5 offset:32768\n"
"ds_read_b64_tr_b16 a[166:167], v5 offset:34816\n"
"ds_read_b64_tr_b16 a[168:169], v4 offset:33280\n"
"ds_read_b64_tr_b16 a[170:171], v4 offset:35328\n"
"ds_read_b64_tr_b16 a[172:173], v5 offset:33280\n"
"ds_read_b64_tr_b16 a[174:175], v5 offset:35328\n"
"ds_read_b64_tr_b16 a[176:177], v6 offset:32768\n"
"ds_read_b64_tr_b16 a[178:179], v6 offset:34816\n"
"ds_read_b64_tr_b16 a[180:181], v7 offset:32768\n"
"ds_read_b64_tr_b16 a[182:183], v7 offset:34816\n"
"ds_read_b64_tr_b16 a[184:185], v6 offset:33280\n"
"ds_read_b64_tr_b16 a[186:187], v6 offset:35328\n"
"ds_read_b64_tr_b16 a[188:189], v7 offset:33280\n"
"ds_read_b64_tr_b16 a[190:191], v7 offset:35328\n"
"s_waitcnt lgkmcnt(15)\n"
"s_branch .Lmha_tail\n"
".Lmha_redo5:\n"
"s_waitcnt vmcnt(0) lgkmcnt(0)\n"
"s_barrier\n"
"s_mov_b32 s68, -1\n"
"v_and_b32 v1, 15, v0\n"
"v_lshrrev_b32 v2, 4, v0\n"
"v_and_b32 v3, 3, v0\n"
"v_xor_b32 v36, v1, v2\n"
"v_lshlrev_b32 v36, 4, v36\n"
"v_lshl_add_u32 v4, v1, 8, v36\n"
"v_xor_b32 v5, 64, v4\n"
"v_xor_b32 v6, 128, v4\n"
"v_xor_b32 v7, 192, v4\n"
"v_mov_b32 v10, 0x3f803f80\n"
"v_mov_b32 v11, 0x3f803f80\n"
"v_mov_b32 v12, 0x3f803f80\n"
"v_mov_b32 v13, 0x3f803f80\n"
"v_lshrrev_b32 v36, 5, v0\n"
"v_lshlrev_b32 v36, 11, v36\n"
"v_bfe_u32 v37, v0, 2, 3\n"
"v_lshl_add_u32 v36, v37, 6, v36\n"
"v_lshl_add_u32 v36, v3, 3, v36\n"
"v_and_b32 v38, 1, v2\n"
"v_lshl_add_u32 v36, v38, 5, v36\n"
"v_add_u32 v8, 65536, v36\n"
"v_xor_b32 v9, 32, v8\n"
"v_mov_b32 v56, 0xff800000\n"
"v_mov_b32 v57, 0xff800000\n"
"v_mov_b32 v58, 0xff800000\n"
"v_mov_b32 v59, 0xff800000\n"
"v_mov_b32 v60, 0xff800000\n"
"s_mov_b32 s63, 31\n"
".Lmha_fb5:\n"
"s_barrier\n"
"ds_read_b128 a[64:67], v4\n"
"ds_read_b128 a[68:71], v5\n"
"ds_read_b128 a[72:75], v6\n"
"ds_read_b128 a[76:79], v7\n"
"ds_read_b128 a[80:83], v4 offset:4096\n"
"ds_read_b128 a[84:87], v5 offset:4096\n"
"ds_read_b128 a[88:91], v6 offset:4096\n"
"ds_read_b128 a[92:95], v7 offset:4096\n"
"s_waitcnt lgkmcnt(0)\n"
"s_barrier\n"
"v_mfma_f32_16x16x32_bf16 v[64:67], a[64:67], v[80:83], 0\n"
"v_mfma_f32_16x16x32_bf16 v[64:67], a[68:71], v[84:87], v[64:67]\n"
"v_mfma_f32_16x16x32_bf16 v[64:67], a[72:75], v[88:91], v[64:67]\n"
"v_mfma_f32_16x16x32_bf16 v[64:67], a[76:79], v[92:95], v[64:67]\n"
"v_mfma_f32_16x16x32_bf16 v[68:71], a[80:83], v[80:83], 0\n"
"v_mfma_f32_16x16x32_bf16 v[68:71], a[84:87], v[84:87], v[68:71]\n"
"v_mfma_f32_16x16x32_bf16 v[68:71], a[88:91], v[88:91], v[68:71]\n"
"v_mfma_f32_16x16x32_bf16 v[68:71], a[92:95], v[92:95], v[68:71]\n"
"s_nop 7\n"
"v_max3_f32 v56, v56, v64, v65\n"
"v_max3_f32 v56, v56, v66, v67\n"
"v_max3_f32 v56, v56, v68, v69\n"
"v_max3_f32 v56, v56, v70, v71\n"
"v_mfma_f32_16x16x32_bf16 v[64:67], a[64:67], v[96:99], 0\n"
"v_mfma_f32_16x16x32_bf16 v[64:67], a[68:71], v[100:103], v[64:67]\n"
"v_mfma_f32_16x16x32_bf16 v[64:67], a[72:75], v[104:107], v[64:67]\n"
"v_mfma_f32_16x16x32_bf16 v[64:67], a[76:79], v[108:111], v[64:67]\n"
"v_mfma_f32_16x16x32_bf16 v[68:71], a[80:83], v[96:99], 0\n"
"v_mfma_f32_16x16x32_bf16 v[68:71], a[84:87], v[100:103], v[68:71]\n"
"v_mfma_f32_16x16x32_bf16 v[68:71], a[88:91], v[104:107], v[68:71]\n"
"v_mfma_f32_16x16x32_bf16 v[68:71], a[92:95], v[108:111], v[68:71]\n"
"s_nop 7\n"
"v_max3_f32 v57, v57, v64, v65\n"
"v_max3_f32 v57, v57, v66, v67\n"
"v_max3_f32 v57, v57, v68, v69\n"
"v_max3_f32 v57, v57, v70, v71\n"
"v_mfma_f32_16x16x32_bf16 v[64:67], a[64:67], v[112:115], 0\n"
"v_mfma_f32_16x16x32_bf16 v[64:67], a[68:71], v[116:119], v[64:67]\n"
"v_mfma_f32_16x16x32_bf16 v[64:67], a[72:75], v[120:123], v[64:67]\n"
"v_mfma_f32_16x16x32_bf16 v[64:67], a[76:79], v[124:127], v[64:67]\n"
"v_mfma_f32_16x16x32_bf16 v[68:71], a[80:83], v[112:115], 0\n"
"v_mfma_f32_16x16x32_bf16 v[68:71], a[84:87], v[116:119], v[68:71]\n"
"v_mfma_f32_16x16x32_bf16 v[68:71], a[88:91], v[120:123], v[68:71]\n"
"v_mfma_f32_16x16x32_bf16 v[68:71], a[92:95], v[124:127], v[68:71]\n"
"s_nop 7\n"
"v_max3_f32 v58, v58, v64, v65\n"
"v_max3_f32 v58, v58, v66, v67\n"
"v_max3_f32 v58, v58, v68, v69\n"
"v_max3_f32 v58, v58, v70, v71\n"
"v_mfma_f32_16x16x32_bf16 v[64:67], a[64:67], v[128:131], 0\n"
"v_mfma_f32_16x16x32_bf16 v[64:67], a[68:71], v[132:135], v[64:67]\n"
"v_mfma_f32_16x16x32_bf16 v[64:67], a[72:75], v[136:139], v[64:67]\n"
"v_mfma_f32_16x16x32_bf16 v[64:67], a[76:79], v[140:143], v[64:67]\n"
"v_mfma_f32_16x16x32_bf16 v[68:71], a[80:83], v[128:131], 0\n"
"v_mfma_f32_16x16x32_bf16 v[68:71], a[84:87], v[132:135], v[68:71]\n"
"v_mfma_f32_16x16x32_bf16 v[68:71], a[88:91], v[136:139], v[68:71]\n"
"v_mfma_f32_16x16x32_bf16 v[68:71], a[92:95], v[140:143], v[68:71]\n"
"s_nop 7\n"
"v_max3_f32 v59, v59, v64, v65\n"
"v_max3_f32 v59, v59, v66, v67\n"
"v_max3_f32 v59, v59, v68, v69\n"
"v_max3_f32 v59, v59, v70, v71\n"
"v_mfma_f32_16x16x32_bf16 v[64:67], a[64:67], v[144:147], 0\n"
"v_mfma_f32_16x16x32_bf16 v[64:67], a[68:71], v[148:151], v[64:67]\n"
"v_mfma_f32_16x16x32_bf16 v[64:67], a[72:75], v[152:155], v[64:67]\n"
"v_mfma_f32_16x16x32_bf16 v[64:67], a[76:79], v[156:159], v[64:67]\n"
"v_mfma_f32_16x16x32_bf16 v[68:71], a[80:83], v[144:147], 0\n"
"v_mfma_f32_16x16x32_bf16 v[68:71], a[84:87], v[148:151], v[68:71]\n"
"v_mfma_f32_16x16x32_bf16 v[68:71], a[88:91], v[152:155], v[68:71]\n"
"v_mfma_f32_16x16x32_bf16 v[68:71], a[92:95], v[156:159], v[68:71]\n"
"s_nop 7\n"
"v_max3_f32 v60, v60, v64, v65\n"
"v_max3_f32 v60, v60, v66, v67\n"
"v_max3_f32 v60, v60, v68, v69\n"
"v_max3_f32 v60, v60, v70, v71\n"
"s_sub_u32 s63, s63, 1\n"
"s_cbranch_scc0 .Lmha_fb5\n"
"v_mov_b32 v72, v56\n"
"v_mov_b32 v73, v57\n"
"v_mov_b32 v74, v58\n"
"v_mov_b32 v75, v59\n"
"v_mov_b32 v76, v60\n"
"v_permlane32_swap_b32 v56, v72\n"
"v_permlane32_swap_b32 v57, v73\n"
"v_permlane32_swap_b32 v58, v74\n"
"v_permlane32_swap_b32 v59, v75\n"
"v_permlane32_swap_b32 v60, v76\n"
"v_max_f32 v56, v56, v72\n"
"v_max_f32 v57, v57, v73\n"
"v_max_f32 v58, v58, v74\n"
"v_max_f32 v59, v59, v75\n"
"v_max_f32 v60, v60, v76\n"
"v_mov_b32 v72, v56\n"
"v_mov_b32 v73, v57\n"
"v_mov_b32 v74, v58\n"
"v_mov_b32 v75, v59\n"
"v_mov_b32 v76, v60\n"
"v_permlane16_swap_b32 v56, v72\n"
"v_permlane16_swap_b32 v57, v73\n"
"v_permlane16_swap_b32 v58, v74\n"
"v_permlane16_swap_b32 v59, v75\n"
"v_permlane16_swap_b32 v60, v76\n"
"v_max_f32 v56, v56, v72\n"
"v_max_f32 v57, v57, v73\n"
"v_max_f32 v58, v58, v74\n"
"v_max_f32 v59, v59, v75\n"
"v_max_f32 v60, v60, v76\n"
"v_sub_f32 v36, 0, v56\n"
"v_sub_f32 v37, 0, v56\n"
"v_sub_f32 v38, 0, v56\n"
"v_sub_f32 v39, 0, v56\n"
"v_sub_f32 v40, 0, v57\n"
"v_sub_f32 v41, 0, v57\n"
"v_sub_f32 v42, 0, v57\n"
"v_sub_f32 v43, 0, v57\n"
"v_sub_f32 v44, 0, v58\n"
"v_sub_f32 v45, 0, v58\n"
"v_sub_f32 v46, 0, v58\n"
"v_sub_f32 v47, 0, v58\n"
"v_sub_f32 v48, 0, v59\n"
"v_sub_f32 v49, 0, v59\n"
"v_sub_f32 v50, 0, v59\n"
"v_sub_f32 v51, 0, v59\n"
"v_sub_f32 v52, 0, v60\n"
"v_sub_f32 v53, 0, v60\n"
"v_sub_f32 v54, 0, v60\n"
"v_sub_f32 v55, 0, v60\n"
"v_mov_b32 v29, 0\n"
"v_mov_b32 v30, 0\n"
"v_mov_b32 v31, 0\n"
"v_mov_b32 v34, 0\n"
"v_mov_b32 v35, 0\n"
"s_barrier\n"
"ds_read_b128 a[64:67], v4\n"
"ds_read_b128 a[68:71], v5\n"
"ds_read_b128 a[72:75], v6\n"
"ds_read_b128 a[76:79], v7\n"
"ds_read_b128 a[80:83], v4 offset:4096\n"
"ds_read_b128 a[84:87], v5 offset:4096\n"
"ds_read_b128 a[88:91], v6 offset:4096\n"
"ds_read_b128 a[92:95], v7 offset:4096\n"
"ds_read_b128 a[96:99], v4 offset:8192\n"
"ds_read_b128 a[100:103], v5 offset:8192\n"
"ds_read_b128 a[104:107], v6 offset:8192\n"
"ds_read_b128 a[108:111], v7 offset:8192\n"
"ds_read_b128 a[112:115], v4 offset:12288\n"
"ds_read_b128 a[116:119], v5 offset:12288\n"
"ds_read_b128 a[120:123], v6 offset:12288\n"
"ds_read_b128 a[124:127], v7 offset:12288\n"
"ds_read_b64_tr_b16 a[128:129], v8\n"
"ds_read_b64_tr_b16 a[130:131], v8 offset:4096\n"
"ds_read_b64_tr_b16 a[132:133], v9\n"
"ds_read_b64_tr_b16 a[134:135], v9 offset:4096\n"
"ds_read_b64_tr_b16 a[136:137], v8 offset:512\n"
"ds_read_b64_tr_b16 a[138:139], v8 offset:4608\n"
"ds_read_b64_tr_b16 a[140:141], v9 offset:512\n"
"ds_read_b64_tr_b16 a[142:143], v9 offset:4608\n"
"ds_read_b64_tr_b16 a[144:145], v8 offset:1024\n"
"ds_read_b64_tr_b16 a[146:147], v8 offset:5120\n"
"ds_read_b64_tr_b16 a[148:149], v9 offset:1024\n"
"ds_read_b64_tr_b16 a[150:151], v9 offset:5120\n"
"ds_read_b64_tr_b16 a[152:153], v8 offset:1536\n"
"ds_read_b64_tr_b16 a[154:155], v8 offset:5632\n"
"ds_read_b64_tr_b16 a[156:157], v9 offset:1536\n"
"ds_read_b64_tr_b16 a[158:159], v9 offset:5632\n"
"s_waitcnt lgkmcnt(0)\n"
"v_mfma_f32_16x16x32_bf16 v[64:67], a[64:67], v[80:83], v[36:39]\n"
"v_mfma_f32_16x16x32_bf16 v[64:67], a[68:71], v[84:87], v[64:67]\n"
"v_mfma_f32_16x16x32_bf16 v[64:67], a[72:75], v[88:91], v[64:67]\n"
"v_mfma_f32_16x16x32_bf16 v[64:67], a[76:79], v[92:95], v[64:67]\n"
"v_mfma_f32_16x16x32_bf16 v[68:71], a[80:83], v[80:83], v[36:39]\n"
"v_mfma_f32_16x16x32_bf16 v[68:71], a[84:87], v[84:87], v[68:71]\n"
"v_mfma_f32_16x16x32_bf16 v[68:71], a[88:91], v[88:91], v[68:71]\n"
"v_mfma_f32_16x16x32_bf16 v[68:71], a[92:95], v[92:95], v[68:71]\n"
"s_nop 7\n"
"s_branch .Lmha_att5\n"
".Lmha_w3:\n"
"s_mov_b32 s59, 98368\n"
"s_add_u32 m0, s59, 0\n"
"s_add_u32 s66, s56, 0\n"
"buffer_load_dwordx4 v16, s[20:23], s66 offen lds\n"
"buffer_load_dwordx4 v17, s[20:23], s66 offen offset:1024 lds\n"
"s_add_u32 m0, s59, 2048\n"
"s_add_u32 s66, s56, 16384\n"
"buffer_load_dwordx4 v16, s[20:23], s66 offen lds\n"
"buffer_load_dwordx4 v17, s[20:23], s66 offen offset:1024 lds\n"
"buffer_load_dword v33, v30, s[24:27], 0 offen\n"
"s_lshl_b32 s52, s49, 7\n"
"s_add_u32 s52, s52, 25296896\n"
"v_mov_b32 v34, s52\n"
"global_load_dwordx2 v[56:57], v34, s[16:17] sc1\n"
"s_add_u32 m0, s59, 4096\n"
"s_add_u32 s66, s56, 32768\n"
"buffer_load_dwordx4 v16, s[20:23], s66 offen lds\n"
"buffer_load_dwordx4 v17, s[20:23], s66 offen offset:1024 lds\n"
"s_add_u32 m0, s59, 6144\n"
"s_add_u32 s66, s56, 49152\n"
"buffer_load_dwordx4 v16, s[20:23], s66 offen lds\n"
"buffer_load_dwordx4 v17, s[20:23], s66 offen offset:1024 lds\n"
"v_and_b32 v1, 15, v0\n"
"v_lshrrev_b32 v36, 1, v1\n"
"v_xor_b32 v36, v36, v2\n"
"v_lshlrev_b32 v36, 4, v36\n"
"v_lshl_add_u32 v36, v1, 7, v36\n"
"v_xor_b32 v37, 64, v36\n"
"v_add_u32 v18, s59, v36\n"
"v_add_u32 v19, s59, v37\n"
"v_add_u32 v20, s60, v36\n"
"v_add_u32 v21, s60, v37\n"
"v_mul_u32_u24 v36, 2056, v2\n"
"v_lshl_add_u32 v36, v1, 4, v36\n"
"v_add_u32 v26, 16384, v36\n"
"v_add_u32 v27, 147392, v36\n"
"v_add_u32 v28, 81920, v36\n"
"v_and_b32 v1, 15, v0\n"
"v_lshrrev_b32 v2, 4, v0\n"
"v_and_b32 v3, 3, v0\n"
"v_xor_b32 v36, v1, v2\n"
"v_lshlrev_b32 v36, 4, v36\n"
"v_lshl_add_u32 v4, v1, 8, v36\n"
"v_xor_b32 v5, 64, v4\n"
"v_xor_b32 v6, 128, v4\n"
"v_xor_b32 v7, 192, v4\n"
"v_lshl_add_u32 v10, v2, 8, v36\n"
"v_xor_b32 v11, 64, v10\n"
"v_xor_b32 v12, 128, v10\n"
"v_xor_b32 v13, 192, v10\n"
"v_lshrrev_b32 v36, 5, v0\n"
"v_lshlrev_b32 v36, 11, v36\n"
"v_bfe_u32 v37, v0, 2, 3\n"
"v_lshl_add_u32 v36, v37, 6, v36\n"
"v_lshl_add_u32 v36, v3, 3, v36\n"
"v_and_b32 v38, 1, v2\n"
"v_lshl_add_u32 v36, v38, 5, v36\n"
"v_add_u32 v8, 65536, v36\n"
"v_xor_b32 v9, 32, v8\n"
"v_lshrrev_b32 v36, 5, v0\n"
"v_lshlrev_b32 v36, 6, v36\n"
"v_lshl_add_u32 v36, v37, 8, v36\n"
"v_lshlrev_b32 v38, 1, v38\n"
"v_xor_b32 v38, v38, v3\n"
"v_lshl_add_u32 v36, v38, 4, v36\n"
"v_add_u32 v14, 1024, v36\n"
"v_add_u32 v15, 128, v36\n"
"s_lshl_b32 s52, s49, 5\n"
"s_add_u32 s52, s52, s50\n"
"s_mov_b64 s[28:29], s[6:7]\n"
"s_mov_b64 s[36:37], s[8:9]\n"
"s_lshl_b32 s53, s52, 18\n"
"s_add_u32 s28, s28, s53\n"
"s_addc_u32 s29, s29, 0\n"
"s_and_b32 s29, s29, 0xffff\n"
"s_mov_b32 s30, 262144\n"
"s_mov_b32 s31, 0x20000\n"
"s_add_u32 s36, s36, s53\n"
"s_addc_u32 s37, s37, 0\n"
"s_sub_u32 s36, s36, 1024\n"
"s_subb_u32 s37, s37, 0\n"
"s_and_b32 s37, s37, 0xffff\n"
"s_mov_b32 s38, 263168\n"
"s_mov_b32 s39, 0x20000\n"
"s_mul_i32 s61, %1, 2048\n"
"s_and_b32 s61, s61, 0x3e000\n"
"s_mov_b32 s62, s61\n"
"s_mov_b64 s[40:41], s[16:17]\n"
"s_lshl2_add_u32 s53, s49, s48\n"
"s_lshl_b32 s53, s53, 5\n"
"s_add_u32 s53, s53, s50\n"
"s_mul_i32 s53, s53, 20480\n"
"s_add_u32 s40, s40, s53\n"
"s_addc_u32 s41, s41, 0\n"
"s_and_b32 s41, s41, 0xffff\n"
"s_mov_b32 s42, 20480\n"
"s_mov_b32 s43, 0x20000\n"
"s_mov_b64 s[44:45], s[12:13]\n"
"s_and_b32 s53, s50, 7\n"
"s_lshl_b32 s53, s53, 7\n"
"s_andn2_b32 s52, 16, s50\n"
"s_lshl_b32 s52, s52, 17\n"
"s_add_u32 s53, s53, s52\n"
"s_add_u32 s44, s44, s53\n"
"s_addc_u32 s45, s45, 0\n"
"s_and_b32 s45, s45, 0xffff\n"
"s_mov_b32 s46, 0\n"
"s_mov_b32 s47, 0x20000\n"
"s_sub_u32 s46, 4194304, s53\n"
"s_lshr_b32 s69, s50, 3\n"
"s_add_u32 s69, s69, s49\n"
"s_and_b32 s69, s69, 1\n"
"s_lshr_b32 s52, s50, 4\n"
"s_lshl1_add_u32 s69, s69, s52\n"
"s_mov_b32 s64, 0x3e0293ee\n"
"s_mov_b32 s65, 0x3e0293ee\n"
"s_mov_b32 s68, 0\n"
"s_waitcnt vmcnt(10)\n"
"s_barrier\n"
"ds_read_b64_tr_b16 a[0:1], v26\n"
"ds_read_b64_tr_b16 a[2:3], v26 offset:1024\n"
"ds_read_b64_tr_b16 a[4:5], v26 offset:8224\n"
"ds_read_b64_tr_b16 a[6:7], v26 offset:9248\n"
"ds_read_b64_tr_b16 a[8:9], v26 offset:8\n"
"ds_read_b64_tr_b16 a[10:11], v26 offset:1032\n"
"ds_read_b64_tr_b16 a[12:13], v26 offset:8232\n"
"ds_read_b64_tr_b16 a[14:15], v26 offset:9256\n"
"ds_read_b64_tr_b16 a[16:17], v26 offset:256\n"
"ds_read_b64_tr_b16 a[18:19], v26 offset:1280\n"
"ds_read_b64_tr_b16 a[20:21], v26 offset:8480\n"
"ds_read_b64_tr_b16 a[22:23], v26 offset:9504\n"
"ds_read_b64_tr_b16 a[24:25], v26 offset:264\n"
"ds_read_b64_tr_b16 a[26:27], v26 offset:1288\n"
"ds_read_b64_tr_b16 a[28:29], v26 offset:8488\n"
"ds_read_b64_tr_b16 a[30:31], v26 offset:9512\n"
"ds_read_b64_tr_b16 a[32:33], v26 offset:512\n"
"ds_read_b64_tr_b16 a[34:35], v26 offset:1536\n"
"ds_read_b64_tr_b16 a[36:37], v26 offset:8736\n"
"ds_read_b64_tr_b16 a[38:39], v26 offset:9760\n"
"ds_read_b64_tr_b16 a[40:41], v26 offset:520\n"
"ds_read_b64_tr_b16 a[42:43], v26 offset:1544\n"
"ds_read_b64_tr_b16 a[44:45], v26 offset:8744\n"
"ds_read_b64_tr_b16 a[46:47], v26 offset:9768\n"
"ds_read_b64_tr_b16 a[48:49], v26 offset:768\n"
"ds_read_b64_tr_b16 a[50:51], v26 offset:1792\n"
"ds_read_b64_tr_b16 a[52:53], v26 offset:8992\n"
"ds_read_b64_tr_b16 a[54:55], v26 offset:10016\n"
"ds_read_b64_tr_b16 a[56:57], v26 offset:776\n"
"ds_read_b64_tr_b16 a[58:59], v26 offset:1800\n"
"ds_read_b64_tr_b16 a[60:61], v26 offset:9000\n"
"ds_read_b64_tr_b16 a[62:63], v26 offset:10024\n"
"s_waitcnt vmcnt(8)\n"
"ds_read_b128 a[160:163], v18\n"
"ds_read_b128 a[164:167], v19\n"
"s_waitcnt lgkmcnt(0)\n"
"v_mfma_f32_16x16x32_bf16 v[96:99], a[0:3], a[160:163], 0\n"
"s_add_u32 m0, s58, 147392\n"
"s_add_u32 s67, s57, 524288\n"
"v_mfma_f32_16x16x32_bf16 v[96:99], a[4:7], a[164:167], v[96:99]\n"
"buffer_load_dwordx4 v22, s[24:27], s67 offen lds\n"
"v_mfma_f32_16x16x32_bf16 v[100:103], a[8:11], a[160:163], 0\n"
"v_mfma_f32_16x16x32_bf16 v[100:103], a[12:15], a[164:167], v[100:103]\n"
"v_mfma_f32_16x16x32_bf16 v[104:107], a[16:19], a[160:163], 0\n"
"buffer_load_dwordx4 v23, s[24:27], s67 offen offset:1024 lds\n"
"v_mfma_f32_16x16x32_bf16 v[104:107], a[20:23], a[164:167], v[104:107]\n"
"v_mfma_f32_16x16x32_bf16 v[108:111], a[24:27], a[160:163], 0\n"
"v_mfma_f32_16x16x32_bf16 v[108:111], a[28:31], a[164:167], v[108:111]\n"
"buffer_load_dwordx4 v24, s[24:27], s67 offen offset:2056 lds\n"
"v_mfma_f32_16x16x32_bf16 v[112:115], a[32:35], a[160:163], 0\n"
"s_waitcnt vmcnt(9)\n"
"v_mfma_f32_16x16x32_bf16 v[112:115], a[36:39], a[164:167], v[112:115]\n"
"ds_read_b128 a[168:171], v18 offset:2048\n"
"ds_read_b128 a[172:175], v19 offset:2048\n"
"v_mfma_f32_16x16x32_bf16 v[116:119], a[40:43], a[160:163], 0\n"
"buffer_load_dwordx4 v25, s[24:27], s67 offen offset:3080 lds\n"
"v_mfma_f32_16x16x32_bf16 v[116:119], a[44:47], a[164:167], v[116:119]\n"
"v_mfma_f32_16x16x32_bf16 v[120:123], a[48:51], a[160:163], 0\n"
"s_add_u32 m0, s60, 0\n"
"s_add_u32 s66, s56, 128\n"
"v_mfma_f32_16x16x32_bf16 v[120:123], a[52:55], a[164:167], v[120:123]\n"
"buffer_load_dwordx4 v16, s[20:23], s66 offen lds\n"
"v_mfma_f32_16x16x32_bf16 v[124:127], a[56:59], a[160:163], 0\n"
"v_mfma_f32_16x16x32_bf16 v[124:127], a[60:63], a[164:167], v[124:127]\n"
"s_waitcnt lgkmcnt(0)\n"
"v_mfma_f32_16x16x32_bf16 v[128:131], a[0:3], a[168:171], 0\n"
"buffer_load_dwordx4 v17, s[20:23], s66 offen offset:1024 lds\n"
"v_mfma_f32_16x16x32_bf16 v[128:131], a[4:7], a[172:175], v[128:131]\n"
"v_mfma_f32_16x16x32_bf16 v[132:135], a[8:11], a[168:171], 0\n"
"s_add_u32 m0, s60, 2048\n"
"s_add_u32 s66, s56, 16512\n"
"v_mfma_f32_16x16x32_bf16 v[132:135], a[12:15], a[172:175], v[132:135]\n"
"buffer_load_dwordx4 v16, s[20:23], s66 offen lds\n"
"v_mfma_f32_16x16x32_bf16 v[136:139], a[16:19], a[168:171], 0\n"
"v_mfma_f32_16x16x32_bf16 v[136:139], a[20:23], a[172:175], v[136:139]\n"
"v_mfma_f32_16x16x32_bf16 v[140:143], a[24:27], a[168:171], 0\n"
"buffer_load_dwordx4 v17, s[20:23], s66 offen offset:1024 lds\n"
"v_mfma_f32_16x16x32_bf16 v[140:143], a[28:31], a[172:175], v[140:143]\n"
"v_mfma_f32_16x16x32_bf16 v[144:147], a[32:35], a[168:171], 0\n"
"s_waitcnt vmcnt(10)\n"
"v_mfma_f32_16x16x32_bf16 v[144:147], a[36:39], a[172:175], v[144:147]\n"
"ds_read_b128 a[176:179], v18 offset:4096\n"
"ds_read_b128 a[180:183], v19 offset:4096\n"
"v_mfma_f32_16x16x32_bf16 v[148:151], a[40:43], a[168:171], 0\n"
"s_add_u32 m0, s60, 4096\n"
"s_add_u32 s66, s56, 32896\n"
"v_mfma_f32_16x16x32_bf16 v[148:151], a[44:47], a[172:175], v[148:151]\n"
"buffer_load_dwordx4 v16, s[20:23], s66 offen lds\n"
"v_mfma_f32_16x16x32_bf16 v[152:155], a[48:51], a[168:171], 0\n"
"v_mfma_f32_16x16x32_bf16 v[152:155], a[52:55], a[172:175], v[152:155]\n"
"v_mfma_f32_16x16x32_bf16 v[156:159], a[56:59], a[168:171], 0\n"
"buffer_load_dwordx4 v17, s[20:23], s66 offen offset:1024 lds\n"
"v_mfma_f32_16x16x32_bf16 v[156:159], a[60:63], a[172:175], v[156:159]\n"
"s_waitcnt vmcnt(6) lgkmcnt(0)\n"
"s_barrier\n"
"v_mfma_f32_16x16x32_bf16 v[160:163], a[0:3], a[176:179], 0\n"
"ds_read_b64_tr_b16 a[192:193], v27\n"
"ds_read_b64_tr_b16 a[194:195], v27 offset:1024\n"
"s_add_u32 m0, s60, 6144\n"
"s_add_u32 s66, s56, 49280\n"
"v_mfma_f32_16x16x32_bf16 v[160:163], a[4:7], a[180:183], v[160:163]\n"
"ds_read_b64_tr_b16 a[196:197], v27 offset:8224\n"
"buffer_load_dwordx4 v16, s[20:23], s66 offen lds\n"
"v_mfma_f32_16x16x32_bf16 v[164:167], a[8:11], a[176:179], 0\n"
"ds_read_b64_tr_b16 a[198:199], v27 offset:9248\n"
"v_mfma_f32_16x16x32_bf16 v[164:167], a[12:15], a[180:183], v[164:167]\n"
"ds_read_b64_tr_b16 a[200:201], v27 offset:8\n"
"v_mfma_f32_16x16x32_bf16 v[168:171], a[16:19], a[176:179], 0\n"
"ds_read_b64_tr_b16 a[202:203], v27 offset:1032\n"
"buffer_load_dwordx4 v17, s[20:23], s66 offen offset:1024 lds\n"
"v_mfma_f32_16x16x32_bf16 v[168:171], a[20:23], a[180:183], v[168:171]\n"
"ds_read_b64_tr_b16 a[204:205], v27 offset:8232\n"
"ds_read_b64_tr_b16 a[206:207], v27 offset:9256\n"
"v_mfma_f32_16x16x32_bf16 v[172:175], a[24:27], a[176:179], 0\n"
"ds_read_b64_tr_b16 a[208:209], v27 offset:256\n"
"s_add_u32 m0, s58, 81920\n"
"s_add_u32 s67, s57, 1048576\n"
"v_mfma_f32_16x16x32_bf16 v[172:175], a[28:31], a[180:183], v[172:175]\n"
"ds_read_b64_tr_b16 a[210:211], v27 offset:1280\n"
"buffer_load_dwordx4 v22, s[24:27], s67 offen lds\n"
"v_mfma_f32_16x16x32_bf16 v[176:179], a[32:35], a[176:179], 0\n"
"ds_read_b64_tr_b16 a[212:213], v27 offset:8480\n"
"v_mfma_f32_16x16x32_bf16 v[176:179], a[36:39], a[180:183], v[176:179]\n"
"ds_read_b64_tr_b16 a[214:215], v27 offset:9504\n"
"ds_read_b128 a[184:187], v18 offset:6144\n"
"ds_read_b128 a[188:191], v19 offset:6144\n"
"v_mfma_f32_16x16x32_bf16 v[180:183], a[40:43], a[176:179], 0\n"
"ds_read_b64_tr_b16 a[216:217], v27 offset:264\n"
"ds_read_b64_tr_b16 a[218:219], v27 offset:1288\n"
"buffer_load_dwordx4 v23, s[24:27], s67 offen offset:1024 lds\n"
"v_mfma_f32_16x16x32_bf16 v[180:183], a[44:47], a[180:183], v[180:183]\n"
"ds_read_b64_tr_b16 a[220:221], v27 offset:8488\n"
"v_mfma_f32_16x16x32_bf16 v[184:187], a[48:51], a[176:179], 0\n"
"ds_read_b64_tr_b16 a[222:223], v27 offset:9512\n"
"v_mfma_f32_16x16x32_bf16 v[184:187], a[52:55], a[180:183], v[184:187]\n"
"ds_read_b64_tr_b16 a[224:225], v27 offset:512\n"
"buffer_load_dwordx4 v24, s[24:27], s67 offen offset:2056 lds\n"
"v_mfma_f32_16x16x32_bf16 v[188:191], a[56:59], a[176:179], 0\n"
"ds_read_b64_tr_b16 a[226:227], v27 offset:1536\n"
"v_mfma_f32_16x16x32_bf16 v[188:191], a[60:63], a[180:183], v[188:191]\n"
"s_waitcnt lgkmcnt(6)\n"
"v_mfma_f32_16x16x32_bf16 v[192:195], a[0:3], a[184:187], 0\n"
"ds_read_b64_tr_b16 a[228:229], v27 offset:8736\n"
"ds_read_b64_tr_b16 a[230:231], v27 offset:9760\n"
"buffer_load_dwordx4 v25, s[24:27], s67 offen offset:3080 lds\n"
"v_mfma_f32_16x16x32_bf16 v[192:195], a[4:7], a[188:191], v[192:195]\n"
"ds_read_b64_tr_b16 a[232:233], v27 offset:520\n"
"v_mfma_f32_16x16x32_bf16 v[196:199], a[8:11], a[184:187], 0\n"
"ds_read_b64_tr_b16 a[234:235], v27 offset:1544\n"
"s_add_u32 m0, s59, 0\n"
"s_add_u32 s66, s56, 256\n"
"v_mfma_f32_16x16x32_bf16 v[196:199], a[12:15], a[188:191], v[196:199]\n"
"ds_read_b64_tr_b16 a[236:237], v27 offset:8744\n"
"ds_read_b64_tr_b16 a[238:239], v27 offset:9768\n"
"buffer_load_dwordx4 v16, s[20:23], s66 offen lds\n"
"v_mfma_f32_16x16x32_bf16 v[200:203], a[16:19], a[184:187], 0\n"
"ds_read_b64_tr_b16 a[240:241], v27 offset:768\n"
"v_mfma_f32_16x16x32_bf16 v[200:203], a[20:23], a[188:191], v[200:203]\n"
"ds_read_b64_tr_b16 a[242:243], v27 offset:1792\n"
"v_mfma_f32_16x16x32_bf16 v[204:207], a[24:27], a[184:187], 0\n"
"ds_read_b64_tr_b16 a[244:245], v27 offset:8992\n"
"buffer_load_dwordx4 v17, s[20:23], s66 offen offset:1024 lds\n"
"v_mfma_f32_16x16x32_bf16 v[204:207], a[28:31], a[188:191], v[204:207]\n"
"ds_read_b64_tr_b16 a[246:247], v27 offset:10016\n"
"ds_read_b64_tr_b16 a[248:249], v27 offset:776\n"
"v_mfma_f32_16x16x32_bf16 v[208:211], a[32:35], a[184:187], 0\n"
"ds_read_b64_tr_b16 a[250:251], v27 offset:1800\n"
"s_waitcnt vmcnt(12)\n"
"v_mfma_f32_16x16x32_bf16 v[208:211], a[36:39], a[188:191], v[208:211]\n"
"ds_read_b64_tr_b16 a[252:253], v27 offset:9000\n"
"ds_read_b128 a[160:163], v20\n"
"ds_read_b128 a[164:167], v21\n"
"v_mfma_f32_16x16x32_bf16 v[212:215], a[40:43], a[184:187], 0\n"
"ds_read_b64_tr_b16 a[254:255], v27 offset:10024\n"
"s_add_u32 m0, s59, 2048\n"
"s_add_u32 s66, s56, 16640\n"
"v_mfma_f32_16x16x32_bf16 v[212:215], a[44:47], a[188:191], v[212:215]\n"
"buffer_load_dwordx4 v16, s[20:23], s66 offen lds\n"
"v_mfma_f32_16x16x32_bf16 v[216:219], a[48:51], a[184:187], 0\n"
"v_mfma_f32_16x16x32_bf16 v[216:219], a[52:55], a[188:191], v[216:219]\n"
"v_mfma_f32_16x16x32_bf16 v[220:223], a[56:59], a[184:187], 0\n"
"buffer_load_dwordx4 v17, s[20:23], s66 offen offset:1024 lds\n"
"v_mfma_f32_16x16x32_bf16 v[220:223], a[60:63], a[188:191], v[220:223]\n"
"s_waitcnt lgkmcnt(0)\n"
"v_mfma_f32_16x16x32_bf16 v[96:99], a[192:195], a[160:163], v[96:99]\n"
"s_add_u32 m0, s58, 16384\n"
"s_add_u32 s67, s57, 1572864\n"
"v_mfma_f32_16x16x32_bf16 v[96:99], a[196:199], a[164:167], v[96:99]\n"
"buffer_load_dwordx4 v22, s[24:27], s67 offen lds\n"
"v_mfma_f32_16x16x32_bf16 v[100:103], a[200:203], a[160:163], v[100:103]\n"
"v_mfma_f32_16x16x32_bf16 v[100:103], a[204:207], a[164:167], v[100:103]\n"
"v_mfma_f32_16x16x32_bf16 v[104:107], a[208:211], a[160:163], v[104:107]\n"
"buffer_load_dwordx4 v23, s[24:27], s67 offen offset:1024 lds\n"
"v_mfma_f32_16x16x32_bf16 v[104:107], a[212:215], a[164:167], v[104:107]\n"
"v_mfma_f32_16x16x32_bf16 v[108:111], a[216:219], a[160:163], v[108:111]\n"
"v_mfma_f32_16x16x32_bf16 v[108:111], a[220:223], a[164:167], v[108:111]\n"
"buffer_load_dwordx4 v24, s[24:27], s67 offen offset:2056 lds\n"
"v_mfma_f32_16x16x32_bf16 v[112:115], a[224:227], a[160:163], v[112:115]\n"
"s_waitcnt vmcnt(15)\n"
"v_mfma_f32_16x16x32_bf16 v[112:115], a[228:231], a[164:167], v[112:115]\n"
"ds_read_b128 a[168:171], v20 offset:2048\n"
"ds_read_b128 a[172:175], v21 offset:2048\n"
"v_mfma_f32_16x16x32_bf16 v[116:119], a[232:235], a[160:163], v[116:119]\n"
"buffer_load_dwordx4 v25, s[24:27], s67 offen offset:3080 lds\n"
"v_mfma_f32_16x16x32_bf16 v[116:119], a[236:239], a[164:167], v[116:119]\n"
"v_mfma_f32_16x16x32_bf16 v[120:123], a[240:243], a[160:163], v[120:123]\n"
"s_add_u32 m0, s59, 4096\n"
"s_add_u32 s66, s56, 33024\n"
"v_mfma_f32_16x16x32_bf16 v[120:123], a[244:247], a[164:167], v[120:123]\n"
"buffer_load_dwordx4 v16, s[20:23], s66 offen lds\n"
"v_mfma_f32_16x16x32_bf16 v[124:127], a[248:251], a[160:163], v[124:127]\n"
"v_mfma_f32_16x16x32_bf16 v[124:127], a[252:255], a[164:167], v[124:127]\n"
"s_waitcnt vmcnt(9) lgkmcnt(0)\n"
"s_barrier\n"
"v_mfma_f32_16x16x32_bf16 v[128:131], a[192:195], a[168:171], v[128:131]\n"
"ds_read_b64_tr_b16 a[64:65], v28\n"
"buffer_load_dwordx4 v17, s[20:23], s66 offen offset:1024 lds\n"
"v_mfma_f32_16x16x32_bf16 v[128:131], a[196:199], a[172:175], v[128:131]\n"
"ds_read_b64_tr_b16 a[66:67], v28 offset:1024\n"
"v_mfma_f32_16x16x32_bf16 v[132:135], a[200:203], a[168:171], v[132:135]\n"
"ds_read_b64_tr_b16 a[68:69], v28 offset:8224\n"
"s_add_u32 m0, s59, 6144\n"
"s_add_u32 s66, s56, 49408\n"
"v_mfma_f32_16x16x32_bf16 v[132:135], a[204:207], a[172:175], v[132:135]\n"
"ds_read_b64_tr_b16 a[70:71], v28 offset:9248\n"
"buffer_load_dwordx4 v16, s[20:23], s66 offen lds\n"
"v_mfma_f32_16x16x32_bf16 v[136:139], a[208:211], a[168:171], v[136:139]\n"
"ds_read_b64_tr_b16 a[72:73], v28 offset:8\n"
"v_mfma_f32_16x16x32_bf16 v[136:139], a[212:215], a[172:175], v[136:139]\n"
"ds_read_b64_tr_b16 a[74:75], v28 offset:1032\n"
"v_mfma_f32_16x16x32_bf16 v[140:143], a[216:219], a[168:171], v[140:143]\n"
"ds_read_b64_tr_b16 a[76:77], v28 offset:8232\n"
"buffer_load_dwordx4 v17, s[20:23], s66 offen offset:1024 lds\n"
"v_mfma_f32_16x16x32_bf16 v[140:143], a[220:223], a[172:175], v[140:143]\n"
"v_mfma_f32_16x16x32_bf16 v[144:147], a[224:227], a[168:171], v[144:147]\n"
"ds_read_b64_tr_b16 a[78:79], v28 offset:9256\n"
"v_mfma_f32_16x16x32_bf16 v[144:147], a[228:231], a[172:175], v[144:147]\n"
"ds_read_b64_tr_b16 a[80:81], v28 offset:256\n"
"ds_read_b128 a[176:179], v20 offset:4096\n"
"ds_read_b128 a[180:183], v21 offset:4096\n"
"v_mfma_f32_16x16x32_bf16 v[148:151], a[232:235], a[168:171], v[148:151]\n"
"ds_read_b64_tr_b16 a[82:83], v28 offset:1280\n"
"s_add_u32 m0, s60, 0\n"
"s_add_u32 s66, s56, 384\n"
"v_mfma_f32_16x16x32_bf16 v[148:151], a[236:239], a[172:175], v[148:151]\n"
"ds_read_b64_tr_b16 a[84:85], v28 offset:8480\n"
"buffer_load_dwordx4 v16, s[20:23], s66 offen lds\n"
"v_mfma_f32_16x16x32_bf16 v[152:155], a[240:243], a[168:171], v[152:155]\n"
"ds_read_b64_tr_b16 a[86:87], v28 offset:9504\n"
"v_mfma_f32_16x16x32_bf16 v[152:155], a[244:247], a[172:175], v[152:155]\n"
"ds_read_b64_tr_b16 a[88:89], v28 offset:264\n"
"v_mfma_f32_16x16x32_bf16 v[156:159], a[248:251], a[168:171], v[156:159]\n"
"buffer_load_dwordx4 v17, s[20:23], s66 offen offset:1024 lds\n"
"v_mfma_f32_16x16x32_bf16 v[156:159], a[252:255], a[172:175], v[156:159]\n"
"s_waitcnt lgkmcnt(4)\n"
"v_mfma_f32_16x16x32_bf16 v[160:163], a[192:195], a[176:179], v[160:163]\n"
"s_add_u32 m0, s58, 147392\n"
"s_add_u32 s67, s57, 2097152\n"
"v_mfma_f32_16x16x32_bf16 v[160:163], a[196:199], a[180:183], v[160:163]\n"
"buffer_load_dwordx4 v22, s[24:27], s67 offen lds\n"
"v_mfma_f32_16x16x32_bf16 v[164:167], a[200:203], a[176:179], v[164:167]\n"
"ds_read_b64_tr_b16 a[90:91], v28 offset:1288\n"
"ds_read_b64_tr_b16 a[92:93], v28 offset:8488\n"
"v_mfma_f32_16x16x32_bf16 v[164:167], a[204:207], a[180:183], v[164:167]\n"
"ds_read_b64_tr_b16 a[94:95], v28 offset:9512\n"
"ds_read_b64_tr_b16 a[96:97], v28 offset:512\n"
"v_mfma_f32_16x16x32_bf16 v[168:171], a[208:211], a[176:179], v[168:171]\n"
"buffer_load_dwordx4 v23, s[24:27], s67 offen offset:1024 lds\n"
"v_mfma_f32_16x16x32_bf16 v[168:171], a[212:215], a[180:183], v[168:171]\n"
"ds_read_b64_tr_b16 a[98:99], v28 offset:1536\n"
"ds_read_b64_tr_b16 a[100:101], v28 offset:8736\n"
"v_mfma_f32_16x16x32_bf16 v[172:175], a[216:219], a[176:179], v[172:175]\n"
"ds_read_b64_tr_b16 a[102:103], v28 offset:9760\n"
"ds_read_b64_tr_b16 a[104:105], v28 offset:520\n"
"v_mfma_f32_16x16x32_bf16 v[172:175], a[220:223], a[180:183], v[172:175]\n"
"buffer_load_dwordx4 v24, s[24:27], s67 offen offset:2056 lds\n"
"v_mfma_f32_16x16x32_bf16 v[176:179], a[224:227], a[176:179], v[176:179]\n"
"v_mfma_f32_16x16x32_bf16 v[176:179], a[228:231], a[180:183], v[176:179]\n"
"ds_read_b128 a[184:187], v20 offset:6144\n"
"ds_read_b128 a[188:191], v21 offset:6144\n"
"v_mfma_f32_16x16x32_bf16 v[180:183], a[232:235], a[176:179], v[180:183]\n"
"buffer_load_dwordx4 v25, s[24:27], s67 offen offset:3080 lds\n"
"v_mfma_f32_16x16x32_bf16 v[180:183], a[236:239], a[180:183], v[180:183]\n"
"ds_read_b64_tr_b16 a[106:107], v28 offset:1544\n"
"ds_read_b64_tr_b16 a[108:109], v28 offset:8744\n"
"v_mfma_f32_16x16x32_bf16 v[184:187], a[240:243], a[176:179], v[184:187]\n"
"s_add_u32 m0, s60, 2048\n"
"s_add_u32 s66, s56, 16768\n"
"v_mfma_f32_16x16x32_bf16 v[184:187], a[244:247], a[180:183], v[184:187]\n"
"buffer_load_dwordx4 v16, s[20:23], s66 offen lds\n"
"v_mfma_f32_16x16x32_bf16 v[188:191], a[248:251], a[176:179], v[188:191]\n"
"ds_read_b64_tr_b16 a[110:111], v28 offset:9768\n"
"ds_read_b64_tr_b16 a[112:113], v28 offset:768\n"
"v_mfma_f32_16x16x32_bf16 v[188:191], a[252:255], a[180:183], v[188:191]\n"
"s_waitcnt lgkmcnt(4)\n"
"v_mfma_f32_16x16x32_bf16 v[192:195], a[192:195], a[184:187], v[192:195]\n"
"buffer_load_dwordx4 v17, s[20:23], s66 offen offset:1024 lds\n"
"v_mfma_f32_16x16x32_bf16 v[192:195], a[196:199], a[188:191], v[192:195]\n"
"ds_read_b64_tr_b16 a[114:115], v28 offset:1792\n"
"ds_read_b64_tr_b16 a[116:117], v28 offset:8992\n"
"v_mfma_f32_16x16x32_bf16 v[196:199], a[200:203], a[184:187], v[196:199]\n"
"s_add_u32 m0, s60, 4096\n"
"s_add_u32 s66, s56, 33152\n"
"v_mfma_f32_16x16x32_bf16 v[196:199], a[204:207], a[188:191], v[196:199]\n"
"buffer_load_dwordx4 v16, s[20:23], s66 offen lds\n"
"v_mfma_f32_16x16x32_bf16 v[200:203], a[208:211], a[184:187], v[200:203]\n"
"ds_read_b64_tr_b16 a[118:119], v28 offset:10016\n"
"ds_read_b64_tr_b16 a[120:121], v28 offset:776\n"
"v_mfma_f32_16x16x32_bf16 v[200:203], a[212:215], a[188:191], v[200:203]\n"
"ds_read_b64_tr_b16 a[122:123], v28 offset:1800\n"
"ds_read_b64_tr_b16 a[124:125], v28 offset:9000\n"
"v_mfma_f32_16x16x32_bf16 v[204:207], a[216:219], a[184:187], v[204:207]\n"
"buffer_load_dwordx4 v17, s[20:23], s66 offen offset:1024 lds\n"
"v_mfma_f32_16x16x32_bf16 v[204:207], a[220:223], a[188:191], v[204:207]\n"
"ds_read_b64_tr_b16 a[126:127], v28 offset:10024\n"
"v_mfma_f32_16x16x32_bf16 v[208:211], a[224:227], a[184:187], v[208:211]\n"
"s_waitcnt vmcnt(20)\n"
"v_mfma_f32_16x16x32_bf16 v[208:211], a[228:231], a[188:191], v[208:211]\n"
"ds_read_b128 a[160:163], v18\n"
"ds_read_b128 a[164:167], v19\n"
"v_mfma_f32_16x16x32_bf16 v[212:215], a[232:235], a[184:187], v[212:215]\n"
"s_add_u32 m0, s60, 6144\n"
"s_add_u32 s66, s56, 49536\n"
"v_mfma_f32_16x16x32_bf16 v[212:215], a[236:239], a[188:191], v[212:215]\n"
"buffer_load_dwordx4 v16, s[20:23], s66 offen lds\n"
"v_mfma_f32_16x16x32_bf16 v[216:219], a[240:243], a[184:187], v[216:219]\n"
"v_mfma_f32_16x16x32_bf16 v[216:219], a[244:247], a[188:191], v[216:219]\n"
"v_mfma_f32_16x16x32_bf16 v[220:223], a[248:251], a[184:187], v[220:223]\n"
"buffer_load_dwordx4 v17, s[20:23], s66 offen offset:1024 lds\n"
"v_mfma_f32_16x16x32_bf16 v[220:223], a[252:255], a[188:191], v[220:223]\n"
"s_waitcnt vmcnt(20) lgkmcnt(0)\n"
"v_mfma_f32_16x16x32_bf16 v[96:99], a[64:67], a[160:163], v[96:99]\n"
"ds_read_b128 a[168:171], v18 offset:2048\n"
"ds_read_b128 a[172:175], v19 offset:2048\n"
"v_mfma_f32_16x16x32_bf16 v[96:99], a[68:71], a[164:167], v[96:99]\n"
"s_add_u32 m0, s59, 0\n"
"s_add_u32 s66, s56, 512\n"
"v_mfma_f32_16x16x32_bf16 v[100:103], a[72:75], a[160:163], v[100:103]\n"
"buffer_load_dwordx4 v16, s[20:23], s66 offen lds\n"
"v_mfma_f32_16x16x32_bf16 v[100:103], a[76:79], a[164:167], v[100:103]\n"
"v_mfma_f32_16x16x32_bf16 v[104:107], a[80:83], a[160:163], v[104:107]\n"
"v_mfma_f32_16x16x32_bf16 v[104:107], a[84:87], a[164:167], v[104:107]\n"
"buffer_load_dwordx4 v17, s[20:23], s66 offen offset:1024 lds\n"
"v_mfma_f32_16x16x32_bf16 v[108:111], a[88:91], a[160:163], v[108:111]\n"
"v_mfma_f32_16x16x32_bf16 v[108:111], a[92:95], a[164:167], v[108:111]\n"
"v_mfma_f32_16x16x32_bf16 v[112:115], a[96:99], a[160:163], v[112:115]\n"
"v_mfma_f32_16x16x32_bf16 v[112:115], a[100:103], a[164:167], v[112:115]\n"
"v_mfma_f32_16x16x32_bf16 v[116:119], a[104:107], a[160:163], v[116:119]\n"
"v_mfma_f32_16x16x32_bf16 v[116:119], a[108:111], a[164:167], v[116:119]\n"
"v_mfma_f32_16x16x32_bf16 v[120:123], a[112:115], a[160:163], v[120:123]\n"
"v_mfma_f32_16x16x32_bf16 v[120:123], a[116:119], a[164:167], v[120:123]\n"
"v_mfma_f32_16x16x32_bf16 v[124:127], a[120:123], a[160:163], v[124:127]\n"
"v_mfma_f32_16x16x32_bf16 v[124:127], a[124:127], a[164:167], v[124:127]\n"
"s_waitcnt vmcnt(16) lgkmcnt(0)\n"
"s_barrier\n"
"v_mfma_f32_16x16x32_bf16 v[128:131], a[64:67], a[168:171], v[128:131]\n"
"ds_read_b128 a[176:179], v18 offset:4096\n"
"ds_read_b128 a[180:183], v19 offset:4096\n"
"v_mfma_f32_16x16x32_bf16 v[128:131], a[68:71], a[172:175], v[128:131]\n"
"s_add_u32 m0, s58, 81920\n"
"s_add_u32 s67, s57, 2621440\n"
"v_mfma_f32_16x16x32_bf16 v[132:135], a[72:75], a[168:171], v[132:135]\n"
"buffer_load_dwordx4 v22, s[24:27], s67 offen lds\n"
"v_mfma_f32_16x16x32_bf16 v[132:135], a[76:79], a[172:175], v[132:135]\n"
"ds_read_b64_tr_b16 a[0:1], v26\n"
"ds_read_b64_tr_b16 a[2:3], v26 offset:1024\n"
"v_mfma_f32_16x16x32_bf16 v[136:139], a[80:83], a[168:171], v[136:139]\n"
"ds_read_b64_tr_b16 a[4:5], v26 offset:8224\n"
"ds_read_b64_tr_b16 a[6:7], v26 offset:9248\n"
"v_mfma_f32_16x16x32_bf16 v[136:139], a[84:87], a[172:175], v[136:139]\n"
"buffer_load_dwordx4 v23, s[24:27], s67 offen offset:1024 lds\n"
"v_mfma_f32_16x16x32_bf16 v[140:143], a[88:91], a[168:171], v[140:143]\n"
"ds_read_b64_tr_b16 a[8:9], v26 offset:8\n"
"ds_read_b64_tr_b16 a[10:11], v26 offset:1032\n"
"v_mfma_f32_16x16x32_bf16 v[140:143], a[92:95], a[172:175], v[140:143]\n"
"ds_read_b64_tr_b16 a[12:13], v26 offset:8232\n"
"ds_read_b64_tr_b16 a[14:15], v26 offset:9256\n"
"v_mfma_f32_16x16x32_bf16 v[144:147], a[96:99], a[168:171], v[144:147]\n"
"buffer_load_dwordx4 v24, s[24:27], s67 offen offset:2056 lds\n"
"v_mfma_f32_16x16x32_bf16 v[144:147], a[100:103], a[172:175], v[144:147]\n"
"ds_read_b64_tr_b16 a[16:17], v26 offset:256\n"
"ds_read_b64_tr_b16 a[18:19], v26 offset:1280\n"
"v_mfma_f32_16x16x32_bf16 v[148:151], a[104:107], a[168:171], v[148:151]\n"
"ds_read_b64_tr_b16 a[20:21], v26 offset:8480\n"
"ds_read_b64_tr_b16 a[22:23], v26 offset:9504\n"
"v_mfma_f32_16x16x32_bf16 v[148:151], a[108:111], a[172:175], v[148:151]\n"
"buffer_load_dwordx4 v25, s[24:27], s67 offen offset:3080 lds\n"
"v_mfma_f32_16x16x32_bf16 v[152:155], a[112:115], a[168:171], v[152:155]\n"
"ds_read_b64_tr_b16 a[24:25], v26 offset:264\n"
"v_mfma_f32_16x16x32_bf16 v[152:155], a[116:119], a[172:175], v[152:155]\n"
"s_add_u32 m0, s59, 2048\n"
"s_add_u32 s66, s56, 16896\n"
"v_mfma_f32_16x16x32_bf16 v[156:159], a[120:123], a[168:171], v[156:159]\n"
"buffer_load_dwordx4 v16, s[20:23], s66 offen lds\n"
"v_mfma_f32_16x16x32_bf16 v[156:159], a[124:127], a[172:175], v[156:159]\n"
"s_waitcnt vmcnt(19) lgkmcnt(13)\n"
"v_mfma_f32_16x16x32_bf16 v[160:163], a[64:67], a[176:179], v[160:163]\n"
"ds_read_b128 a[184:187], v18 offset:6144\n"
"ds_read_b128 a[188:191], v19 offset:6144\n"
"v_mfma_f32_16x16x32_bf16 v[160:163], a[68:71], a[180:183], v[160:163]\n"
"buffer_load_dwordx4 v17, s[20:23], s66 offen offset:1024 lds\n"
"v_mfma_f32_16x16x32_bf16 v[164:167], a[72:75], a[176:179], v[164:167]\n"
"ds_read_b64_tr_b16 a[26:27], v26 offset:1288\n"
"ds_read_b64_tr_b16 a[28:29], v26 offset:8488\n"
"v_mfma_f32_16x16x32_bf16 v[164:167], a[76:79], a[180:183], v[164:167]\n"
"s_add_u32 m0, s59, 4096\n"
"s_add_u32 s66, s56, 33280\n"
"v_mfma_f32_16x16x32_bf16 v[168:171], a[80:83], a[176:179], v[168:171]\n"
"buffer_load_dwordx4 v16, s[20:23], s66 offen lds\n"
"v_mfma_f32_16x16x32_bf16 v[168:171], a[84:87], a[180:183], v[168:171]\n"
"ds_read_b64_tr_b16 a[30:31], v26 offset:9512\n"
"v_mfma_f32_16x16x32_bf16 v[172:175], a[88:91], a[176:179], v[172:175]\n"
"ds_read_b64_tr_b16 a[32:33], v26 offset:512\n"
"v_mfma_f32_16x16x32_bf16 v[172:175], a[92:95], a[180:183], v[172:175]\n"
"buffer_load_dwordx4 v17, s[20:23], s66 offen offset:1024 lds\n"
"v_mfma_f32_16x16x32_bf16 v[176:179], a[96:99], a[176:179], v[176:179]\n"
"ds_read_b64_tr_b16 a[34:35], v26 offset:1536\n"
"v_mfma_f32_16x16x32_bf16 v[176:179], a[100:103], a[180:183], v[176:179]\n"
"ds_read_b64_tr_b16 a[36:37], v26 offset:8736\n"
"v_mfma_f32_16x16x32_bf16 v[180:183], a[104:107], a[176:179], v[180:183]\n"
"ds_read_b64_tr_b16 a[38:39], v26 offset:9760\n"
"ds_read_b64_tr_b16 a[40:41], v26 offset:520\n"
"v_mfma_f32_16x16x32_bf16 v[180:183], a[108:111], a[180:183], v[180:183]\n"
"ds_read_b64_tr_b16 a[42:43], v26 offset:1544\n"
"v_mfma_f32_16x16x32_bf16 v[184:187], a[112:115], a[176:179], v[184:187]\n"
"ds_read_b64_tr_b16 a[44:45], v26 offset:8744\n"
"v_mfma_f32_16x16x32_bf16 v[184:187], a[116:119], a[180:183], v[184:187]\n"
"ds_read_b64_tr_b16 a[46:47], v26 offset:9768\n"
"v_mfma_f32_16x16x32_bf16 v[188:191], a[120:123], a[176:179], v[188:191]\n"
"ds_read_b64_tr_b16 a[48:49], v26 offset:768\n"
"v_mfma_f32_16x16x32_bf16 v[188:191], a[124:127], a[180:183], v[188:191]\n"
"s_waitcnt vmcnt(14) lgkmcnt(12)\n"
"v_mfma_f32_16x16x32_bf16 v[192:195], a[64:67], a[184:187], v[192:195]\n"
"ds_read_b128 a[160:163], v20\n"
"ds_read_b128 a[164:167], v21\n"
"v_mfma_f32_16x16x32_bf16 v[192:195], a[68:71], a[188:191], v[192:195]\n"
"ds_read_b128 a[168:171], v20 offset:2048\n"
"ds_read_b128 a[172:175], v21 offset:2048\n"
"v_mfma_f32_16x16x32_bf16 v[196:199], a[72:75], a[184:187], v[196:199]\n"
"s_add_u32 m0, s59, 6144\n"
"s_add_u32 s66, s56, 49664\n"
"v_mfma_f32_16x16x32_bf16 v[196:199], a[76:79], a[188:191], v[196:199]\n"
"buffer_load_dwordx4 v16, s[20:23], s66 offen lds\n"
"v_mfma_f32_16x16x32_bf16 v[200:203], a[80:83], a[184:187], v[200:203]\n"
"ds_read_b64_tr_b16 a[50:51], v26 offset:1792\n"
"ds_read_b64_tr_b16 a[52:53], v26 offset:8992\n"
"v_mfma_f32_16x16x32_bf16 v[200:203], a[84:87], a[188:191], v[200:203]\n"
"ds_read_b64_tr_b16 a[54:55], v26 offset:10016\n"
"v_mfma_f32_16x16x32_bf16 v[204:207], a[88:91], a[184:187], v[204:207]\n"
"buffer_load_dwordx4 v17, s[20:23], s66 offen offset:1024 lds\n"
"v_mfma_f32_16x16x32_bf16 v[204:207], a[92:95], a[188:191], v[204:207]\n"
"ds_read_b64_tr_b16 a[56:57], v26 offset:776\n"
"ds_read_b64_tr_b16 a[58:59], v26 offset:1800\n"
"v_mfma_f32_16x16x32_bf16 v[208:211], a[96:99], a[184:187], v[208:211]\n"
"ds_read_b64_tr_b16 a[60:61], v26 offset:9000\n"
"v_mfma_f32_16x16x32_bf16 v[208:211], a[100:103], a[188:191], v[208:211]\n"
"buffer_load_dword v33, v31, s[24:27], 0 offen\n"
"s_nop 0\n"
"v_mfma_f32_16x16x32_bf16 v[212:215], a[104:107], a[184:187], v[212:215]\n"
"ds_read_b64_tr_b16 a[62:63], v26 offset:10024\n"
"v_mfma_f32_16x16x32_bf16 v[212:215], a[108:111], a[188:191], v[212:215]\n"
"v_mfma_f32_16x16x32_bf16 v[216:219], a[112:115], a[184:187], v[216:219]\n"
"v_mfma_f32_16x16x32_bf16 v[216:219], a[116:119], a[188:191], v[216:219]\n"
"v_mfma_f32_16x16x32_bf16 v[220:223], a[120:123], a[184:187], v[220:223]\n"
"v_mfma_f32_16x16x32_bf16 v[220:223], a[124:127], a[188:191], v[220:223]\n"
"s_waitcnt vmcnt(15) lgkmcnt(0)\n"
"v_mfma_f32_16x16x32_bf16 v[96:99], a[0:3], a[160:163], v[96:99]\n"
"ds_read_b128 a[176:179], v20 offset:4096\n"
"ds_read_b128 a[180:183], v21 offset:4096\n"
"v_mfma_f32_16x16x32_bf16 v[96:99], a[4:7], a[164:167], v[96:99]\n"
"s_add_u32 m0, s60, 0\n"
"s_add_u32 s66, s56, 640\n"
"v_mfma_f32_16x16x32_bf16 v[100:103], a[8:11], a[160:163], v[100:103]\n"
"buffer_load_dwordx4 v16, s[20:23], s66 offen lds\n"
"v_mfma_f32_16x16x32_bf16 v[100:103], a[12:15], a[164:167], v[100:103]\n"
"v_mfma_f32_16x16x32_bf16 v[104:107], a[16:19], a[160:163], v[104:107]\n"
"v_mfma_f32_16x16x32_bf16 v[104:107], a[20:23], a[164:167], v[104:107]\n"
"buffer_load_dwordx4 v17, s[20:23], s66 offen offset:1024 lds\n"
"v_mfma_f32_16x16x32_bf16 v[108:111], a[24:27], a[160:163], v[108:111]\n"
"v_mfma_f32_16x16x32_bf16 v[108:111], a[28:31], a[164:167], v[108:111]\n"
"v_mfma_f32_16x16x32_bf16 v[112:115], a[32:35], a[160:163], v[112:115]\n"
"v_mfma_f32_16x16x32_bf16 v[112:115], a[36:39], a[164:167], v[112:115]\n"
"v_mfma_f32_16x16x32_bf16 v[116:119], a[40:43], a[160:163], v[116:119]\n"
"v_mfma_f32_16x16x32_bf16 v[116:119], a[44:47], a[164:167], v[116:119]\n"
"v_mfma_f32_16x16x32_bf16 v[120:123], a[48:51], a[160:163], v[120:123]\n"
"v_mfma_f32_16x16x32_bf16 v[120:123], a[52:55], a[164:167], v[120:123]\n"
"v_mfma_f32_16x16x32_bf16 v[124:127], a[56:59], a[160:163], v[124:127]\n"
"v_mfma_f32_16x16x32_bf16 v[124:127], a[60:63], a[164:167], v[124:127]\n"
"s_waitcnt vmcnt(15) lgkmcnt(0)\n"
"s_barrier\n"
"v_mfma_f32_16x16x32_bf16 v[128:131], a[0:3], a[168:171], v[128:131]\n"
"ds_read_b128 a[184:187], v20 offset:6144\n"
"ds_read_b128 a[188:191], v21 offset:6144\n"
"v_mfma_f32_16x16x32_bf16 v[128:131], a[4:7], a[172:175], v[128:131]\n"
"s_add_u32 m0, s58, 16384\n"
"s_add_u32 s67, s57, 3145728\n"
"v_mfma_f32_16x16x32_bf16 v[132:135], a[8:11], a[168:171], v[132:135]\n"
"buffer_load_dwordx4 v22, s[24:27], s67 offen lds\n"
"v_mfma_f32_16x16x32_bf16 v[132:135], a[12:15], a[172:175], v[132:135]\n"
"ds_read_b64_tr_b16 a[192:193], v27\n"
"ds_read_b64_tr_b16 a[194:195], v27 offset:1024\n"
"v_mfma_f32_16x16x32_bf16 v[136:139], a[16:19], a[168:171], v[136:139]\n"
"ds_read_b64_tr_b16 a[196:197], v27 offset:8224\n"
"ds_read_b64_tr_b16 a[198:199], v27 offset:9248\n"
"v_mfma_f32_16x16x32_bf16 v[136:139], a[20:23], a[172:175], v[136:139]\n"
"buffer_load_dwordx4 v23, s[24:27], s67 offen offset:1024 lds\n"
"v_mfma_f32_16x16x32_bf16 v[140:143], a[24:27], a[168:171], v[140:143]\n"
"ds_read_b64_tr_b16 a[200:201], v27 offset:8\n"
"ds_read_b64_tr_b16 a[202:203], v27 offset:1032\n"
"v_mfma_f32_16x16x32_bf16 v[140:143], a[28:31], a[172:175], v[140:143]\n"
"ds_read_b64_tr_b16 a[204:205], v27 offset:8232\n"
"ds_read_b64_tr_b16 a[206:207], v27 offset:9256\n"
"v_mfma_f32_16x16x32_bf16 v[144:147], a[32:35], a[168:171], v[144:147]\n"
"buffer_load_dwordx4 v24, s[24:27], s67 offen offset:2056 lds\n"
"v_mfma_f32_16x16x32_bf16 v[144:147], a[36:39], a[172:175], v[144:147]\n"
"ds_read_b64_tr_b16 a[208:209], v27 offset:256\n"
"ds_read_b64_tr_b16 a[210:211], v27 offset:1280\n"
"v_mfma_f32_16x16x32_bf16 v[148:151], a[40:43], a[168:171], v[148:151]\n"
"ds_read_b64_tr_b16 a[212:213], v27 offset:8480\n"
"ds_read_b64_tr_b16 a[214:215], v27 offset:9504\n"
"v_mfma_f32_16x16x32_bf16 v[148:151], a[44:47], a[172:175], v[148:151]\n"
"buffer_load_dwordx4 v25, s[24:27], s67 offen offset:3080 lds\n"
"v_mfma_f32_16x16x32_bf16 v[152:155], a[48:51], a[168:171], v[152:155]\n"
"ds_read_b64_tr_b16 a[216:217], v27 offset:264\n"
"v_mfma_f32_16x16x32_bf16 v[152:155], a[52:55], a[172:175], v[152:155]\n"
"s_add_u32 m0, s60, 2048\n"
"s_add_u32 s66, s56, 17024\n"
"v_mfma_f32_16x16x32_bf16 v[156:159], a[56:59], a[168:171], v[156:159]\n"
"buffer_load_dwordx4 v16, s[20:23], s66 offen lds\n"
"v_mfma_f32_16x16x32_bf16 v[156:159], a[60:63], a[172:175], v[156:159]\n"
"s_waitcnt vmcnt(12) lgkmcnt(13)\n"
"v_mfma_f32_16x16x32_bf16 v[160:163], a[0:3], a[176:179], v[160:163]\n"
"ds_read_b128 a[160:163], v18\n"
"ds_read_b128 a[164:167], v19\n"
"v_mfma_f32_16x16x32_bf16 v[160:163], a[4:7], a[180:183], v[160:163]\n"
"ds_read_b128 a[168:171], v18 offset:2048\n"
"ds_read_b128 a[172:175], v19 offset:2048\n"
"v_mfma_f32_16x16x32_bf16 v[164:167], a[8:11], a[176:179], v[164:167]\n"
"buffer_load_dwordx4 v17, s[20:23], s66 offen offset:1024 lds\n"
"v_mfma_f32_16x16x32_bf16 v[164:167], a[12:15], a[180:183], v[164:167]\n"
"ds_read_b64_tr_b16 a[218:219], v27 offset:1288\n"
"ds_read_b64_tr_b16 a[220:221], v27 offset:8488\n"
"v_mfma_f32_16x16x32_bf16 v[168:171], a[16:19], a[176:179], v[168:171]\n"
"s_add_u32 m0, s60, 4096\n"
"s_add_u32 s66, s56, 33408\n"
"v_mfma_f32_16x16x32_bf16 v[168:171], a[20:23], a[180:183], v[168:171]\n"
"buffer_load_dwordx4 v16, s[20:23], s66 offen lds\n"
"v_mfma_f32_16x16x32_bf16 v[172:175], a[24:27], a[176:179], v[172:175]\n"
"ds_read_b64_tr_b16 a[222:223], v27 offset:9512\n"
"ds_read_b64_tr_b16 a[224:225], v27 offset:512\n"
"v_mfma_f32_16x16x32_bf16 v[172:175], a[28:31], a[180:183], v[172:175]\n"
"ds_read_b64_tr_b16 a[226:227], v27 offset:1536\n"
"ds_read_b64_tr_b16 a[228:229], v27 offset:8736\n"
"v_mfma_f32_16x16x32_bf16 v[176:179], a[32:35], a[176:179], v[176:179]\n"
"buffer_load_dwordx4 v17, s[20:23], s66 offen offset:1024 lds\n"
"v_mfma_f32_16x16x32_bf16 v[176:179], a[36:39], a[180:183], v[176:179]\n"
"ds_read_b64_tr_b16 a[230:231], v27 offset:9760\n"
"ds_read_b64_tr_b16 a[232:233], v27 offset:520\n"
"v_mfma_f32_16x16x32_bf16 v[180:183], a[40:43], a[176:179], v[180:183]\n"
"s_add_u32 m0, s60, 6144\n"
"s_add_u32 s66, s56, 49792\n"
"v_mfma_f32_16x16x32_bf16 v[180:183], a[44:47], a[180:183], v[180:183]\n"
"buffer_load_dwordx4 v16, s[20:23], s66 offen lds\n"
"v_mfma_f32_16x16x32_bf16 v[184:187], a[48:51], a[176:179], v[184:187]\n"
"ds_read_b64_tr_b16 a[234:235], v27 offset:1544\n"
"ds_read_b64_tr_b16 a[236:237], v27 offset:8744\n"
"v_mfma_f32_16x16x32_bf16 v[184:187], a[52:55], a[180:183], v[184:187]\n"
"ds_read_b64_tr_b16 a[238:239], v27 offset:9768\n"
"ds_read_b64_tr_b16 a[240:241], v27 offset:768\n"
"v_mfma_f32_16x16x32_bf16 v[188:191], a[56:59], a[176:179], v[188:191]\n"
"global_load_dword v33, v32, s[18:19]\n"
"v_mfma_f32_16x16x32_bf16 v[188:191], a[60:63], a[180:183], v[188:191]\n"
"s_waitcnt vmcnt(15) lgkmcnt(12)\n"
"v_mfma_f32_16x16x32_bf16 v[192:195], a[0:3], a[184:187], v[192:195]\n"
"ds_read_b128 a[176:179], v18 offset:4096\n"
"ds_read_b128 a[180:183], v19 offset:4096\n"
"v_mfma_f32_16x16x32_bf16 v[192:195], a[4:7], a[188:191], v[192:195]\n"
"buffer_load_dwordx4 v17, s[20:23], s66 offen offset:1024 lds\n"
"v_mfma_f32_16x16x32_bf16 v[196:199], a[8:11], a[184:187], v[196:199]\n"
"ds_read_b64_tr_b16 a[242:243], v27 offset:1792\n"
"v_mfma_f32_16x16x32_bf16 v[196:199], a[12:15], a[188:191], v[196:199]\n"
"ds_read_b64_tr_b16 a[244:245], v27 offset:8992\n"
"v_mfma_f32_16x16x32_bf16 v[200:203], a[16:19], a[184:187], v[200:203]\n"
"ds_read_b64_tr_b16 a[246:247], v27 offset:10016\n"
"v_mfma_f32_16x16x32_bf16 v[200:203], a[20:23], a[188:191], v[200:203]\n"
"ds_read_b64_tr_b16 a[248:249], v27 offset:776\n"
"v_mfma_f32_16x16x32_bf16 v[204:207], a[24:27], a[184:187], v[204:207]\n"
"v_mfma_f32_16x16x32_bf16 v[204:207], a[28:31], a[188:191], v[204:207]\n"
"ds_read_b64_tr_b16 a[250:251], v27 offset:1800\n"
"v_mfma_f32_16x16x32_bf16 v[208:211], a[32:35], a[184:187], v[208:211]\n"
"ds_read_b64_tr_b16 a[252:253], v27 offset:9000\n"
"v_mfma_f32_16x16x32_bf16 v[208:211], a[36:39], a[188:191], v[208:211]\n"
"ds_read_b64_tr_b16 a[254:255], v27 offset:10024\n"
"v_mfma_f32_16x16x32_bf16 v[212:215], a[40:43], a[184:187], v[212:215]\n"
"v_mfma_f32_16x16x32_bf16 v[212:215], a[44:47], a[188:191], v[212:215]\n"
"v_mfma_f32_16x16x32_bf16 v[216:219], a[48:51], a[184:187], v[216:219]\n"
"v_mfma_f32_16x16x32_bf16 v[216:219], a[52:55], a[188:191], v[216:219]\n"
"v_mfma_f32_16x16x32_bf16 v[220:223], a[56:59], a[184:187], v[220:223]\n"
"v_mfma_f32_16x16x32_bf16 v[220:223], a[60:63], a[188:191], v[220:223]\n"
"s_waitcnt vmcnt(14) lgkmcnt(0)\n"
"s_barrier\n"
"v_mfma_f32_16x16x32_bf16 v[96:99], a[192:195], a[160:163], v[96:99]\n"
"ds_read_b128 a[184:187], v18 offset:6144\n"
"ds_read_b128 a[188:191], v19 offset:6144\n"
"v_mfma_f32_16x16x32_bf16 v[96:99], a[196:199], a[164:167], v[96:99]\n"
"s_add_u32 m0, s58, 147392\n"
"s_add_u32 s67, s57, 3670016\n"
"v_mfma_f32_16x16x32_bf16 v[100:103], a[200:203], a[160:163], v[100:103]\n"
"buffer_load_dwordx4 v22, s[24:27], s67 offen lds\n"
"v_mfma_f32_16x16x32_bf16 v[100:103], a[204:207], a[164:167], v[100:103]\n"
"ds_read_b64_tr_b16 a[64:65], v28\n"
"ds_read_b64_tr_b16 a[66:67], v28 offset:1024\n"
"v_mfma_f32_16x16x32_bf16 v[104:107], a[208:211], a[160:163], v[104:107]\n"
"ds_read_b64_tr_b16 a[68:69], v28 offset:8224\n"
"ds_read_b64_tr_b16 a[70:71], v28 offset:9248\n"
"v_mfma_f32_16x16x32_bf16 v[104:107], a[212:215], a[164:167], v[104:107]\n"
"buffer_load_dwordx4 v23, s[24:27], s67 offen offset:1024 lds\n"
"v_mfma_f32_16x16x32_bf16 v[108:111], a[216:219], a[160:163], v[108:111]\n"
"ds_read_b64_tr_b16 a[72:73], v28 offset:8\n"
"ds_read_b64_tr_b16 a[74:75], v28 offset:1032\n"
"v_mfma_f32_16x16x32_bf16 v[108:111], a[220:223], a[164:167], v[108:111]\n"
"ds_read_b64_tr_b16 a[76:77], v28 offset:8232\n"
"ds_read_b64_tr_b16 a[78:79], v28 offset:9256\n"
"v_mfma_f32_16x16x32_bf16 v[112:115], a[224:227], a[160:163], v[112:115]\n"
"buffer_load_dwordx4 v24, s[24:27], s67 offen offset:2056 lds\n"
"v_mfma_f32_16x16x32_bf16 v[112:115], a[228:231], a[164:167], v[112:115]\n"
"ds_read_b64_tr_b16 a[80:81], v28 offset:256\n"
"ds_read_b64_tr_b16 a[82:83], v28 offset:1280\n"
"v_mfma_f32_16x16x32_bf16 v[116:119], a[232:235], a[160:163], v[116:119]\n"
"ds_read_b64_tr_b16 a[84:85], v28 offset:8480\n"
"ds_read_b64_tr_b16 a[86:87], v28 offset:9504\n"
"v_mfma_f32_16x16x32_bf16 v[116:119], a[236:239], a[164:167], v[116:119]\n"
"buffer_load_dwordx4 v25, s[24:27], s67 offen offset:3080 lds\n"
"v_mfma_f32_16x16x32_bf16 v[120:123], a[240:243], a[160:163], v[120:123]\n"
"ds_read_b64_tr_b16 a[88:89], v28 offset:264\n"
"v_mfma_f32_16x16x32_bf16 v[120:123], a[244:247], a[164:167], v[120:123]\n"
"s_add_u32 m0, s59, 0\n"
"s_add_u32 s66, s56, 768\n"
"v_mfma_f32_16x16x32_bf16 v[124:127], a[248:251], a[160:163], v[124:127]\n"
"buffer_load_dwordx4 v16, s[20:23], s66 offen lds\n"
"v_mfma_f32_16x16x32_bf16 v[124:127], a[252:255], a[164:167], v[124:127]\n"
"s_waitcnt vmcnt(16) lgkmcnt(13)\n"
"v_mfma_f32_16x16x32_bf16 v[128:131], a[192:195], a[168:171], v[128:131]\n"
"ds_read_b128 a[160:163], v20\n"
"ds_read_b128 a[164:167], v21\n"
"v_mfma_f32_16x16x32_bf16 v[128:131], a[196:199], a[172:175], v[128:131]\n"
"buffer_load_dwordx4 v17, s[20:23], s66 offen offset:1024 lds\n"
"v_mfma_f32_16x16x32_bf16 v[132:135], a[200:203], a[168:171], v[132:135]\n"
"ds_read_b64_tr_b16 a[90:91], v28 offset:1288\n"
"ds_read_b64_tr_b16 a[92:93], v28 offset:8488\n"
"v_mfma_f32_16x16x32_bf16 v[132:135], a[204:207], a[172:175], v[132:135]\n"
"s_add_u32 m0, s59, 2048\n"
"s_add_u32 s66, s56, 17152\n"
"v_mfma_f32_16x16x32_bf16 v[136:139], a[208:211], a[168:171], v[136:139]\n"
"buffer_load_dwordx4 v16, s[20:23], s66 offen lds\n"
"v_mfma_f32_16x16x32_bf16 v[136:139], a[212:215], a[172:175], v[136:139]\n"
"ds_read_b64_tr_b16 a[94:95], v28 offset:9512\n"
"ds_read_b64_tr_b16 a[96:97], v28 offset:512\n"
"v_mfma_f32_16x16x32_bf16 v[140:143], a[216:219], a[168:171], v[140:143]\n"
"ds_read_b64_tr_b16 a[98:99], v28 offset:1536\n"
"ds_read_b64_tr_b16 a[100:101], v28 offset:8736\n"
"v_mfma_f32_16x16x32_bf16 v[140:143], a[220:223], a[172:175], v[140:143]\n"
"buffer_load_dwordx4 v17, s[20:23], s66 offen offset:1024 lds\n"
"v_mfma_f32_16x16x32_bf16 v[144:147], a[224:227], a[168:171], v[144:147]\n"
"ds_read_b64_tr_b16 a[102:103], v28 offset:9760\n"
"v_mfma_f32_16x16x32_bf16 v[144:147], a[228:231], a[172:175], v[144:147]\n"
"s_add_u32 m0, s59, 4096\n"
"s_add_u32 s66, s56, 33536\n"
"v_mfma_f32_16x16x32_bf16 v[148:151], a[232:235], a[168:171], v[148:151]\n"
"buffer_load_dwordx4 v16, s[20:23], s66 offen lds\n"
"v_mfma_f32_16x16x32_bf16 v[148:151], a[236:239], a[172:175], v[148:151]\n"
"ds_read_b64_tr_b16 a[104:105], v28 offset:520\n"
"ds_read_b64_tr_b16 a[106:107], v28 offset:1544\n"
"v_mfma_f32_16x16x32_bf16 v[152:155], a[240:243], a[168:171], v[152:155]\n"
"ds_read_b64_tr_b16 a[108:109], v28 offset:8744\n"
"ds_read_b64_tr_b16 a[110:111], v28 offset:9768\n"
"v_mfma_f32_16x16x32_bf16 v[152:155], a[244:247], a[172:175], v[152:155]\n"
"buffer_load_dwordx4 v17, s[20:23], s66 offen offset:1024 lds\n"
"v_mfma_f32_16x16x32_bf16 v[156:159], a[248:251], a[168:171], v[156:159]\n"
"ds_read_b64_tr_b16 a[112:113], v28 offset:768\n"
"v_mfma_f32_16x16x32_bf16 v[156:159], a[252:255], a[172:175], v[156:159]\n"
"s_waitcnt vmcnt(15) lgkmcnt(12)\n"
"v_mfma_f32_16x16x32_bf16 v[160:163], a[192:195], a[176:179], v[160:163]\n"
"ds_read_b128 a[168:171], v20 offset:2048\n"
"ds_read_b128 a[172:175], v21 offset:2048\n"
"v_mfma_f32_16x16x32_bf16 v[160:163], a[196:199], a[180:183], v[160:163]\n"
"s_add_u32 m0, s59, 6144\n"
"s_add_u32 s66, s56, 49920\n"
"v_mfma_f32_16x16x32_bf16 v[164:167], a[200:203], a[176:179], v[164:167]\n"
"buffer_load_dwordx4 v16, s[20:23], s66 offen lds\n"
"v_mfma_f32_16x16x32_bf16 v[164:167], a[204:207], a[180:183], v[164:167]\n"
"ds_read_b64_tr_b16 a[114:115], v28 offset:1792\n"
"ds_read_b64_tr_b16 a[116:117], v28 offset:8992\n"
"v_mfma_f32_16x16x32_bf16 v[168:171], a[208:211], a[176:179], v[168:171]\n"
"ds_read_b64_tr_b16 a[118:119], v28 offset:10016\n"
"v_mfma_f32_16x16x32_bf16 v[168:171], a[212:215], a[180:183], v[168:171]\n"
"buffer_load_dwordx4 v17, s[20:23], s66 offen offset:1024 lds\n"
"v_mfma_f32_16x16x32_bf16 v[172:175], a[216:219], a[176:179], v[172:175]\n"
"ds_read_b64_tr_b16 a[120:121], v28 offset:776\n"
"ds_read_b64_tr_b16 a[122:123], v28 offset:1800\n"
"v_mfma_f32_16x16x32_bf16 v[172:175], a[220:223], a[180:183], v[172:175]\n"
"s_add_u32 m0, s60, 0\n"
"s_add_u32 s66, s56, 896\n"
"v_mfma_f32_16x16x32_bf16 v[176:179], a[224:227], a[176:179], v[176:179]\n"
"buffer_load_dwordx4 v16, s[20:23], s66 offen lds\n"
"v_mfma_f32_16x16x32_bf16 v[176:179], a[228:231], a[180:183], v[176:179]\n"
"ds_read_b64_tr_b16 a[124:125], v28 offset:9000\n"
"v_mfma_f32_16x16x32_bf16 v[180:183], a[232:235], a[176:179], v[180:183]\n"
"ds_read_b64_tr_b16 a[126:127], v28 offset:10024\n"
"v_mfma_f32_16x16x32_bf16 v[180:183], a[236:239], a[180:183], v[180:183]\n"
"buffer_load_dwordx4 v17, s[20:23], s66 offen offset:1024 lds\n"
"v_mfma_f32_16x16x32_bf16 v[184:187], a[240:243], a[176:179], v[184:187]\n"
"s_mov_b32 m0, 0\n"
"s_waitcnt vmcnt(49)\n"
"v_mfma_f32_16x16x32_bf16 v[184:187], a[244:247], a[180:183], v[184:187]\n"
"buffer_load_dwordx4 v10, s[28:31], s61 offen lds\n"
"v_mfma_f32_16x16x32_bf16 v[188:191], a[248:251], a[176:179], v[188:191]\n"
"buffer_load_dwordx4 v11, s[28:31], s61 offen offset:1024 lds\n"
"v_mfma_f32_16x16x32_bf16 v[188:191], a[252:255], a[180:183], v[188:191]\n"
"s_waitcnt vmcnt(19) lgkmcnt(0)\n"
"s_barrier\n"
"v_mfma_f32_16x16x32_bf16 v[192:195], a[192:195], a[184:187], v[192:195]\n"
"ds_read_b64_tr_b16 a[0:1], v26\n"
"ds_read_b128 a[176:179], v20 offset:4096\n"
"ds_read_b128 a[180:183], v21 offset:4096\n"
"v_mfma_f32_16x16x32_bf16 v[192:195], a[196:199], a[188:191], v[192:195]\n"
"ds_read_b64_tr_b16 a[2:3], v26 offset:1024\n"
"buffer_load_dwordx4 v12, s[28:31], s61 offen offset:2048 lds\n"
"v_mfma_f32_16x16x32_bf16 v[196:199], a[200:203], a[184:187], v[196:199]\n"
"ds_read_b64_tr_b16 a[4:5], v26 offset:8224\n"
"buffer_load_dwordx4 v13, s[28:31], s61 offen offset:3072 lds\n"
"s_add_u32 s61, s61, 4096\n"
"s_and_b32 s61, s61, 0x3ffff\n"
"v_mfma_f32_16x16x32_bf16 v[196:199], a[204:207], a[188:191], v[196:199]\n"
"ds_read_b64_tr_b16 a[6:7], v26 offset:9248\n"
"v_mfma_f32_16x16x32_bf16 v[200:203], a[208:211], a[184:187], v[200:203]\n"
"ds_read_b64_tr_b16 a[8:9], v26 offset:8\n"
"s_add_u32 m0, s60, 2048\n"
"s_add_u32 s66, s56, 17280\n"
"v_mfma_f32_16x16x32_bf16 v[200:203], a[212:215], a[188:191], v[200:203]\n"
"ds_read_b64_tr_b16 a[10:11], v26 offset:1032\n"
"buffer_load_dwordx4 v16, s[20:23], s66 offen lds\n"
"v_mfma_f32_16x16x32_bf16 v[204:207], a[216:219], a[184:187], v[204:207]\n"
"ds_read_b64_tr_b16 a[12:13], v26 offset:8232\n"
"v_mfma_f32_16x16x32_bf16 v[204:207], a[220:223], a[188:191], v[204:207]\n"
"v_mfma_f32_16x16x32_bf16 v[208:211], a[224:227], a[184:187], v[208:211]\n"
"ds_read_b64_tr_b16 a[14:15], v26 offset:9256\n"
"buffer_load_dwordx4 v17, s[20:23], s66 offen offset:1024 lds\n"
"v_mfma_f32_16x16x32_bf16 v[208:211], a[228:231], a[188:191], v[208:211]\n"
"ds_read_b64_tr_b16 a[16:17], v26 offset:256\n"
"s_mov_b32 m0, 4096\n"
"s_waitcnt vmcnt(49)\n"
"v_mfma_f32_16x16x32_bf16 v[212:215], a[232:235], a[184:187], v[212:215]\n"
"ds_read_b64_tr_b16 a[18:19], v26 offset:1280\n"
"buffer_load_dwordx4 v10, s[28:31], s61 offen lds\n"
"v_mfma_f32_16x16x32_bf16 v[212:215], a[236:239], a[188:191], v[212:215]\n"
"ds_read_b64_tr_b16 a[20:21], v26 offset:8480\n"
"buffer_load_dwordx4 v11, s[28:31], s61 offen offset:1024 lds\n"
"v_mfma_f32_16x16x32_bf16 v[216:219], a[240:243], a[184:187], v[216:219]\n"
"ds_read_b64_tr_b16 a[22:23], v26 offset:9504\n"
"buffer_load_dwordx4 v12, s[28:31], s61 offen offset:2048 lds\n"
"v_mfma_f32_16x16x32_bf16 v[216:219], a[244:247], a[188:191], v[216:219]\n"
"ds_read_b64_tr_b16 a[24:25], v26 offset:264\n"
"buffer_load_dwordx4 v13, s[28:31], s61 offen offset:3072 lds\n"
"s_add_u32 s61, s61, 4096\n"
"s_and_b32 s61, s61, 0x3ffff\n"
"v_mfma_f32_16x16x32_bf16 v[220:223], a[248:251], a[184:187], v[220:223]\n"
"s_mov_b32 m0, 65536\n"
"s_waitcnt vmcnt(49)\n"
"v_mfma_f32_16x16x32_bf16 v[220:223], a[252:255], a[188:191], v[220:223]\n"
"s_waitcnt vmcnt(24) lgkmcnt(12)\n"
"v_mfma_f32_16x16x32_bf16 v[96:99], a[64:67], a[160:163], v[96:99]\n"
"ds_read_b64_tr_b16 a[26:27], v26 offset:1288\n"
"ds_read_b128 a[184:187], v20 offset:6144\n"
"ds_read_b128 a[188:191], v21 offset:6144\n"
"v_mfma_f32_16x16x32_bf16 v[96:99], a[68:71], a[164:167], v[96:99]\n"
"ds_read_b64_tr_b16 a[28:29], v26 offset:8488\n"
"buffer_load_dwordx4 v14, s[36:39], s62 offen lds\n"
"v_mfma_f32_16x16x32_bf16 v[100:103], a[72:75], a[160:163], v[100:103]\n"
"ds_read_b64_tr_b16 a[30:31], v26 offset:9512\n"
"buffer_load_dwordx4 v15, s[36:39], s62 offen offset:1024 lds\n"
"v_mfma_f32_16x16x32_bf16 v[100:103], a[76:79], a[164:167], v[100:103]\n"
"ds_read_b64_tr_b16 a[32:33], v26 offset:512\n"
"buffer_load_dwordx4 v14, s[36:39], s62 offen offset:2048 lds\n"
"v_mfma_f32_16x16x32_bf16 v[104:107], a[80:83], a[160:163], v[104:107]\n"
"buffer_load_dwordx4 v15, s[36:39], s62 offen offset:3072 lds\n"
"s_add_u32 s62, s62, 4096\n"
"s_and_b32 s62, s62, 0x3ffff\n"
"v_mfma_f32_16x16x32_bf16 v[104:107], a[84:87], a[164:167], v[104:107]\n"
"ds_read_b64_tr_b16 a[34:35], v26 offset:1536\n"
"v_mfma_f32_16x16x32_bf16 v[108:111], a[88:91], a[160:163], v[108:111]\n"
"ds_read_b64_tr_b16 a[36:37], v26 offset:8736\n"
"s_add_u32 m0, s60, 4096\n"
"s_add_u32 s66, s56, 33664\n"
"v_mfma_f32_16x16x32_bf16 v[108:111], a[92:95], a[164:167], v[108:111]\n"
"ds_read_b64_tr_b16 a[38:39], v26 offset:9760\n"
"buffer_load_dwordx4 v16, s[20:23], s66 offen lds\n"
"v_mfma_f32_16x16x32_bf16 v[112:115], a[96:99], a[160:163], v[112:115]\n"
"ds_read_b64_tr_b16 a[40:41], v26 offset:520\n"
"v_mfma_f32_16x16x32_bf16 v[112:115], a[100:103], a[164:167], v[112:115]\n"
"v_mfma_f32_16x16x32_bf16 v[116:119], a[104:107], a[160:163], v[116:119]\n"
"ds_read_b64_tr_b16 a[42:43], v26 offset:1544\n"
"buffer_load_dwordx4 v17, s[20:23], s66 offen offset:1024 lds\n"
"v_mfma_f32_16x16x32_bf16 v[116:119], a[108:111], a[164:167], v[116:119]\n"
"ds_read_b64_tr_b16 a[44:45], v26 offset:8744\n"
"s_mov_b32 m0, 69632\n"
"s_waitcnt vmcnt(49)\n"
"v_mfma_f32_16x16x32_bf16 v[120:123], a[112:115], a[160:163], v[120:123]\n"
"ds_read_b64_tr_b16 a[46:47], v26 offset:9768\n"
"buffer_load_dwordx4 v14, s[36:39], s62 offen lds\n"
"v_mfma_f32_16x16x32_bf16 v[120:123], a[116:119], a[164:167], v[120:123]\n"
"ds_read_b64_tr_b16 a[48:49], v26 offset:768\n"
"buffer_load_dwordx4 v15, s[36:39], s62 offen offset:1024 lds\n"
"v_mfma_f32_16x16x32_bf16 v[124:127], a[120:123], a[160:163], v[124:127]\n"
"buffer_load_dwordx4 v14, s[36:39], s62 offen offset:2048 lds\n"
"v_mfma_f32_16x16x32_bf16 v[124:127], a[124:127], a[164:167], v[124:127]\n"
"s_waitcnt vmcnt(27) lgkmcnt(11)\n"
"v_mfma_f32_16x16x32_bf16 v[128:131], a[64:67], a[168:171], v[128:131]\n"
"ds_read_b64_tr_b16 a[50:51], v26 offset:1792\n"
"ds_read_b128 a[160:163], v18\n"
"ds_read_b128 a[164:167], v19\n"
"v_mfma_f32_16x16x32_bf16 v[128:131], a[68:71], a[172:175], v[128:131]\n"
"ds_read_b64_tr_b16 a[52:53], v26 offset:8992\n"
"buffer_load_dwordx4 v15, s[36:39], s62 offen offset:3072 lds\n"
"s_add_u32 s62, s62, 4096\n"
"s_and_b32 s62, s62, 0x3ffff\n"
"v_mfma_f32_16x16x32_bf16 v[132:135], a[72:75], a[168:171], v[132:135]\n"
"v_mfma_f32_16x16x32_bf16 v[132:135], a[76:79], a[172:175], v[132:135]\n"
"ds_read_b64_tr_b16 a[54:55], v26 offset:10016\n"
"s_add_u32 m0, s60, 6144\n"
"s_add_u32 s66, s56, 50048\n"
"v_mfma_f32_16x16x32_bf16 v[136:139], a[80:83], a[168:171], v[136:139]\n"
"ds_read_b64_tr_b16 a[56:57], v26 offset:776\n"
"buffer_load_dwordx4 v16, s[20:23], s66 offen lds\n"
"v_mfma_f32_16x16x32_bf16 v[136:139], a[84:87], a[172:175], v[136:139]\n"
"v_mfma_f32_16x16x32_bf16 v[140:143], a[88:91], a[168:171], v[140:143]\n"
"ds_read_b64_tr_b16 a[58:59], v26 offset:1800\n"
"v_mfma_f32_16x16x32_bf16 v[140:143], a[92:95], a[172:175], v[140:143]\n"
"ds_read_b64_tr_b16 a[60:61], v26 offset:9000\n"
"buffer_load_dwordx4 v17, s[20:23], s66 offen offset:1024 lds\n"
"v_mfma_f32_16x16x32_bf16 v[144:147], a[96:99], a[168:171], v[144:147]\n"
"s_mov_b32 m0, 8192\n"
"s_waitcnt vmcnt(49)\n"
"v_mfma_f32_16x16x32_bf16 v[144:147], a[100:103], a[172:175], v[144:147]\n"
"ds_read_b64_tr_b16 a[62:63], v26 offset:10024\n"
"buffer_load_dwordx4 v10, s[28:31], s61 offen lds\n"
"v_mfma_f32_16x16x32_bf16 v[148:151], a[104:107], a[168:171], v[148:151]\n"
"buffer_load_dwordx4 v11, s[28:31], s61 offen offset:1024 lds\n"
"v_mfma_f32_16x16x32_bf16 v[148:151], a[108:111], a[172:175], v[148:151]\n"
"buffer_load_dwordx4 v12, s[28:31], s61 offen offset:2048 lds\n"
"v_mfma_f32_16x16x32_bf16 v[152:155], a[112:115], a[168:171], v[152:155]\n"
"buffer_load_dwordx4 v13, s[28:31], s61 offen offset:3072 lds\n"
"s_add_u32 s61, s61, 4096\n"
"s_and_b32 s61, s61, 0x3ffff\n"
"v_mfma_f32_16x16x32_bf16 v[152:155], a[116:119], a[172:175], v[152:155]\n"
"s_mov_b32 m0, 12288\n"
"s_waitcnt vmcnt(49)\n"
"v_mfma_f32_16x16x32_bf16 v[156:159], a[120:123], a[168:171], v[156:159]\n"
"buffer_load_dwordx4 v10, s[28:31], s61 offen lds\n"
"v_mfma_f32_16x16x32_bf16 v[156:159], a[124:127], a[172:175], v[156:159]\n"
"s_waitcnt vmcnt(27) lgkmcnt(0)\n"
"s_barrier\n"
"v_mfma_f32_16x16x32_bf16 v[160:163], a[64:67], a[176:179], v[160:163]\n"
"ds_read_b64_tr_b16 a[192:193], v27\n"
"ds_read_b128 a[168:171], v20\n"
"ds_read_b128 a[172:175], v21\n"
"v_mfma_f32_16x16x32_bf16 v[160:163], a[68:71], a[180:183], v[160:163]\n"
"ds_read_b64_tr_b16 a[194:195], v27 offset:1024\n"
"buffer_load_dwordx4 v11, s[28:31], s61 offen offset:1024 lds\n"
"v_mfma_f32_16x16x32_bf16 v[164:167], a[72:75], a[176:179], v[164:167]\n"
"ds_read_b64_tr_b16 a[196:197], v27 offset:8224\n"
"buffer_load_dwordx4 v12, s[28:31], s61 offen offset:2048 lds\n"
"v_mfma_f32_16x16x32_bf16 v[164:167], a[76:79], a[180:183], v[164:167]\n"
"ds_read_b64_tr_b16 a[198:199], v27 offset:9248\n"
"buffer_load_dwordx4 v13, s[28:31], s61 offen offset:3072 lds\n"
"s_add_u32 s61, s61, 4096\n"
"s_and_b32 s61, s61, 0x3ffff\n"
"v_mfma_f32_16x16x32_bf16 v[168:171], a[80:83], a[176:179], v[168:171]\n"
"ds_read_b64_tr_b16 a[200:201], v27 offset:8\n"
"s_mov_b32 m0, 73728\n"
"s_waitcnt vmcnt(49)\n"
"v_mfma_f32_16x16x32_bf16 v[168:171], a[84:87], a[180:183], v[168:171]\n"
"ds_read_b64_tr_b16 a[202:203], v27 offset:1032\n"
"buffer_load_dwordx4 v14, s[36:39], s62 offen lds\n"
"v_mfma_f32_16x16x32_bf16 v[172:175], a[88:91], a[176:179], v[172:175]\n"
"ds_read_b64_tr_b16 a[204:205], v27 offset:8232\n"
"buffer_load_dwordx4 v15, s[36:39], s62 offen offset:1024 lds\n"
"v_mfma_f32_16x16x32_bf16 v[172:175], a[92:95], a[180:183], v[172:175]\n"
"buffer_load_dwordx4 v14, s[36:39], s62 offen offset:2048 lds\n"
"v_mfma_f32_16x16x32_bf16 v[176:179], a[96:99], a[176:179], v[176:179]\n"
"ds_read_b64_tr_b16 a[206:207], v27 offset:9256\n"
"buffer_load_dwordx4 v15, s[36:39], s62 offen offset:3072 lds\n"
"s_add_u32 s62, s62, 4096\n"
"s_and_b32 s62, s62, 0x3ffff\n"
"v_mfma_f32_16x16x32_bf16 v[176:179], a[100:103], a[180:183], v[176:179]\n"
"ds_read_b64_tr_b16 a[208:209], v27 offset:256\n"
"s_mov_b32 m0, 77824\n"
"s_waitcnt vmcnt(49)\n"
"v_mfma_f32_16x16x32_bf16 v[180:183], a[104:107], a[176:179], v[180:183]\n"
"ds_read_b64_tr_b16 a[210:211], v27 offset:1280\n"
"buffer_load_dwordx4 v14, s[36:39], s62 offen lds\n"
"v_mfma_f32_16x16x32_bf16 v[180:183], a[108:111], a[180:183], v[180:183]\n"
"ds_read_b64_tr_b16 a[212:213], v27 offset:8480\n"
"buffer_load_dwordx4 v15, s[36:39], s62 offen offset:1024 lds\n"
"v_mfma_f32_16x16x32_bf16 v[184:187], a[112:115], a[176:179], v[184:187]\n"
"ds_read_b64_tr_b16 a[214:215], v27 offset:9504\n"
"buffer_load_dwordx4 v14, s[36:39], s62 offen offset:2048 lds\n"
"v_mfma_f32_16x16x32_bf16 v[184:187], a[116:119], a[180:183], v[184:187]\n"
"ds_read_b64_tr_b16 a[216:217], v27 offset:264\n"
"buffer_load_dwordx4 v15, s[36:39], s62 offen offset:3072 lds\n"
"s_add_u32 s62, s62, 4096\n"
"s_and_b32 s62, s62, 0x3ffff\n"
"v_mfma_f32_16x16x32_bf16 v[188:191], a[120:123], a[176:179], v[188:191]\n"
"s_mov_b32 m0, 81920\n"
"s_waitcnt vmcnt(49)\n"
"v_mfma_f32_16x16x32_bf16 v[188:191], a[124:127], a[180:183], v[188:191]\n"
"s_waitcnt lgkmcnt(12)\n"
"v_mfma_f32_16x16x32_bf16 v[192:195], a[64:67], a[184:187], v[192:195]\n"
"ds_read_b64_tr_b16 a[218:219], v27 offset:1288\n"
"ds_read_b128 a[176:179], v18 offset:2048\n"
"ds_read_b128 a[180:183], v19 offset:2048\n"
"v_mfma_f32_16x16x32_bf16 v[192:195], a[68:71], a[188:191], v[192:195]\n"
"ds_read_b64_tr_b16 a[220:221], v27 offset:8488\n"
"buffer_load_dwordx4 v14, s[36:39], s62 offen lds\n"
"v_mfma_f32_16x16x32_bf16 v[196:199], a[72:75], a[184:187], v[196:199]\n"
"ds_read_b64_tr_b16 a[222:223], v27 offset:9512\n"
"buffer_load_dwordx4 v15, s[36:39], s62 offen offset:1024 lds\n"
"v_mfma_f32_16x16x32_bf16 v[196:199], a[76:79], a[188:191], v[196:199]\n"
"ds_read_b64_tr_b16 a[224:225], v27 offset:512\n"
"buffer_load_dwordx4 v14, s[36:39], s62 offen offset:2048 lds\n"
"v_mfma_f32_16x16x32_bf16 v[200:203], a[80:83], a[184:187], v[200:203]\n"
"buffer_load_dwordx4 v15, s[36:39], s62 offen offset:3072 lds\n"
"s_add_u32 s62, s62, 4096\n"
"s_and_b32 s62, s62, 0x3ffff\n"
"v_mfma_f32_16x16x32_bf16 v[200:203], a[84:87], a[188:191], v[200:203]\n"
"ds_read_b64_tr_b16 a[226:227], v27 offset:1536\n"
"s_mov_b32 m0, 86016\n"
"s_waitcnt vmcnt(49)\n"
"v_mfma_f32_16x16x32_bf16 v[204:207], a[88:91], a[184:187], v[204:207]\n"
"ds_read_b64_tr_b16 a[228:229], v27 offset:8736\n"
"buffer_load_dwordx4 v14, s[36:39], s62 offen lds\n"
"v_mfma_f32_16x16x32_bf16 v[204:207], a[92:95], a[188:191], v[204:207]\n"
"ds_read_b64_tr_b16 a[230:231], v27 offset:9760\n"
"buffer_load_dwordx4 v15, s[36:39], s62 offen offset:1024 lds\n"
"v_mfma_f32_16x16x32_bf16 v[208:211], a[96:99], a[184:187], v[208:211]\n"
"ds_read_b64_tr_b16 a[232:233], v27 offset:520\n"
"buffer_load_dwordx4 v14, s[36:39], s62 offen offset:2048 lds\n"
"v_mfma_f32_16x16x32_bf16 v[208:211], a[100:103], a[188:191], v[208:211]\n"
"buffer_load_dwordx4 v15, s[36:39], s62 offen offset:3072 lds\n"
"s_add_u32 s62, s62, 4096\n"
"s_and_b32 s62, s62, 0x3ffff\n"
"v_mfma_f32_16x16x32_bf16 v[212:215], a[104:107], a[184:187], v[212:215]\n"
"ds_read_b64_tr_b16 a[234:235], v27 offset:1544\n"
"v_mfma_f32_16x16x32_bf16 v[212:215], a[108:111], a[188:191], v[212:215]\n"
"ds_read_b64_tr_b16 a[236:237], v27 offset:8744\n"
"v_mfma_f32_16x16x32_bf16 v[216:219], a[112:115], a[184:187], v[216:219]\n"
"ds_read_b64_tr_b16 a[238:239], v27 offset:9768\n"
"v_mfma_f32_16x16x32_bf16 v[216:219], a[116:119], a[188:191], v[216:219]\n"
"ds_read_b64_tr_b16 a[240:241], v27 offset:768\n"
"v_mfma_f32_16x16x32_bf16 v[220:223], a[120:123], a[184:187], v[220:223]\n"
"v_mfma_f32_16x16x32_bf16 v[220:223], a[124:127], a[188:191], v[220:223]\n"
"s_waitcnt vmcnt(40) lgkmcnt(11)\n"
"v_mfma_f32_16x16x32_bf16 v[96:99], a[0:3], a[160:163], v[96:99]\n"
"ds_read_b128 a[184:187], v20 offset:2048\n"
"ds_read_b128 a[188:191], v21 offset:2048\n"
"v_mfma_f32_16x16x32_bf16 v[96:99], a[4:7], a[164:167], v[96:99]\n"
"s_mov_b32 m0, 16384\n"
"s_waitcnt vmcnt(49)\n"
"v_mfma_f32_16x16x32_bf16 v[100:103], a[8:11], a[160:163], v[100:103]\n"
"buffer_load_dwordx4 v10, s[28:31], s61 offen lds\n"
"v_mfma_f32_16x16x32_bf16 v[100:103], a[12:15], a[164:167], v[100:103]\n"
"buffer_load_dwordx4 v11, s[28:31], s61 offen offset:1024 lds\n"
"v_mfma_f32_16x16x32_bf16 v[104:107], a[16:19], a[160:163], v[104:107]\n"
"buffer_load_dwordx4 v12, s[28:31], s61 offen offset:2048 lds\n"
"v_mfma_f32_16x16x32_bf16 v[104:107], a[20:23], a[164:167], v[104:107]\n"
"buffer_load_dwordx4 v13, s[28:31], s61 offen offset:3072 lds\n"
"s_add_u32 s61, s61, 4096\n"
"s_and_b32 s61, s61, 0x3ffff\n"
"v_mfma_f32_16x16x32_bf16 v[108:111], a[24:27], a[160:163], v[108:111]\n"
"ds_read_b64_tr_b16 a[242:243], v27 offset:1792\n"
"ds_read_b64_tr_b16 a[244:245], v27 offset:8992\n"
"v_mfma_f32_16x16x32_bf16 v[108:111], a[28:31], a[164:167], v[108:111]\n"
"ds_read_b64_tr_b16 a[246:247], v27 offset:10016\n"
"v_mfma_f32_16x16x32_bf16 v[112:115], a[32:35], a[160:163], v[112:115]\n"
"ds_read_b64_tr_b16 a[248:249], v27 offset:776\n"
"ds_read_b64_tr_b16 a[250:251], v27 offset:1800\n"
"v_mfma_f32_16x16x32_bf16 v[112:115], a[36:39], a[164:167], v[112:115]\n"
"ds_read_b64_tr_b16 a[252:253], v27 offset:9000\n"
"v_mfma_f32_16x16x32_bf16 v[116:119], a[40:43], a[160:163], v[116:119]\n"
"ds_read_b64_tr_b16 a[254:255], v27 offset:10024\n"
"v_mfma_f32_16x16x32_bf16 v[116:119], a[44:47], a[164:167], v[116:119]\n"
"v_mfma_f32_16x16x32_bf16 v[120:123], a[48:51], a[160:163], v[120:123]\n"
"v_mfma_f32_16x16x32_bf16 v[120:123], a[52:55], a[164:167], v[120:123]\n"
"v_mfma_f32_16x16x32_bf16 v[124:127], a[56:59], a[160:163], v[124:127]\n"
"v_mfma_f32_16x16x32_bf16 v[124:127], a[60:63], a[164:167], v[124:127]\n"
"s_waitcnt lgkmcnt(0)\n"
"v_mfma_f32_16x16x32_bf16 v[96:99], a[192:195], a[168:171], v[96:99]\n"
"ds_read_b128 a[160:163], v18 offset:4096\n"
"ds_read_b128 a[164:167], v19 offset:4096\n"
"v_readfirstlane_b32 s34, v56\n"
"v_readfirstlane_b32 s35, v57\n"
"v_mfma_f32_16x16x32_bf16 v[96:99], a[196:199], a[172:175], v[96:99]\n"
"s_mov_b32 m0, 20480\n"
"s_waitcnt vmcnt(49)\n"
"v_mov_b32 v29, 0\n"
"v_mov_b32 v30, 0\n"
"v_mfma_f32_16x16x32_bf16 v[100:103], a[200:203], a[168:171], v[100:103]\n"
"buffer_load_dwordx4 v10, s[28:31], s61 offen lds\n"
"v_mov_b32 v31, 0\n"
"v_mov_b32 v34, 0\n"
"v_mfma_f32_16x16x32_bf16 v[100:103], a[204:207], a[172:175], v[100:103]\n"
"buffer_load_dwordx4 v11, s[28:31], s61 offen offset:1024 lds\n"
"s_mul_i32 s70, s34, 0x9e3779b1\n"
"s_add_u32 s70, s70, 0x7f4a7c15\n"
"v_mfma_f32_16x16x32_bf16 v[104:107], a[208:211], a[168:171], v[104:107]\n"
"buffer_load_dwordx4 v12, s[28:31], s61 offen offset:2048 lds\n"
"s_mul_i32 s71, s35, 0x85ebca6b\n"
"s_xor_b32 s71, s71, s70\n"
"v_mfma_f32_16x16x32_bf16 v[104:107], a[212:215], a[172:175], v[104:107]\n"
"buffer_load_dwordx4 v13, s[28:31], s61 offen offset:3072 lds\n"
"s_add_u32 s61, s61, 4096\n"
"s_and_b32 s61, s61, 0x3ffff\n"
"s_add_u32 s71, s71, 0x165667b1\n"
"v_lshrrev_b32 v2, 5, v0\n"
"v_mfma_f32_16x16x32_bf16 v[108:111], a[216:219], a[168:171], v[108:111]\n"
"s_mov_b32 m0, 24576\n"
"s_waitcnt vmcnt(49)\n"
"v_lshlrev_b32 v16, 10, v2\n"
"v_bfe_u32 v2, v0, 2, 3\n"
"v_mfma_f32_16x16x32_bf16 v[108:111], a[220:223], a[172:175], v[108:111]\n"
"buffer_load_dwordx4 v10, s[28:31], s61 offen lds\n"
"v_lshl_add_u32 v16, v2, 6, v16\n"
"v_and_b32 v2, 3, v0\n"
"v_mfma_f32_16x16x32_bf16 v[112:115], a[224:227], a[168:171], v[112:115]\n"
"buffer_load_dwordx4 v11, s[28:31], s61 offen offset:1024 lds\n"
"v_lshl_add_u32 v16, v2, 3, v16\n"
"v_bfe_u32 v2, v0, 4, 1\n"
"v_mfma_f32_16x16x32_bf16 v[112:115], a[228:231], a[172:175], v[112:115]\n"
"buffer_load_dwordx4 v12, s[28:31], s61 offen offset:2048 lds\n"
"v_lshl_add_u32 v16, v2, 5, v16\n"
"s_lshl_b32 s58, s48, 12\n"
"v_mfma_f32_16x16x32_bf16 v[116:119], a[232:235], a[168:171], v[116:119]\n"
"buffer_load_dwordx4 v13, s[28:31], s61 offen offset:3072 lds\n"
"s_add_u32 s61, s61, 4096\n"
"s_and_b32 s61, s61, 0x3ffff\n"
"v_add_u32 v16, 131072, v16\n"
"v_add_u32 v16, s58, v16\n"
"v_mfma_f32_16x16x32_bf16 v[116:119], a[236:239], a[172:175], v[116:119]\n"
"v_xor_b32 v17, 32, v16\n"
"v_bfe_u32 v2, v0, 2, 3\n"
"v_mfma_f32_16x16x32_bf16 v[120:123], a[240:243], a[168:171], v[120:123]\n"
"v_lshlrev_b32 v22, 10, v2\n"
"v_lshrrev_b32 v2, 5, v0\n"
"v_mfma_f32_16x16x32_bf16 v[120:123], a[244:247], a[172:175], v[120:123]\n"
"v_lshl_add_u32 v22, v2, 6, v22\n"
"v_bfe_u32 v2, v0, 4, 1\n"
"v_mfma_f32_16x16x32_bf16 v[124:127], a[248:251], a[168:171], v[124:127]\n"
"v_lshlrev_b32 v2, 1, v2\n"
"v_and_b32 v3, 3, v0\n"
"v_mfma_f32_16x16x32_bf16 v[124:127], a[252:255], a[172:175], v[124:127]\n"
"s_waitcnt vmcnt(42) lgkmcnt(0)\n"
"v_xor_b32 v2, v2, v3\n"
"v_lshl_add_u32 v22, v2, 4, v22\n"
"v_add_u32 v23, 7168, v22\n"
"v_mfma_f32_16x16x32_bf16 v[128:131], a[0:3], a[176:179], v[128:131]\n"
"ds_read_b128 a[168:171], v20 offset:4096\n"
"ds_read_b128 a[172:175], v21 offset:4096\n"
"v_add_u32 v24, 14336, v22\n"
"v_add_u32 v25, 21504, v22\n"
"v_mfma_f32_16x16x32_bf16 v[128:131], a[4:7], a[180:183], v[128:131]\n"
"s_mov_b32 m0, 28672\n"
"s_waitcnt vmcnt(49)\n"
"v_mul_f32 v96, s64, v96\n"
"v_mul_f32 v97, s64, v97\n"
"v_mfma_f32_16x16x32_bf16 v[132:135], a[8:11], a[176:179], v[132:135]\n"
"buffer_load_dwordx4 v10, s[28:31], s61 offen lds\n"
"v_mul_f32 v98, s64, v98\n"
"v_mul_f32 v99, s64, v99\n"
"v_mfma_f32_16x16x32_bf16 v[132:135], a[12:15], a[180:183], v[132:135]\n"
"buffer_load_dwordx4 v11, s[28:31], s61 offen offset:1024 lds\n"
"v_mul_f32 v100, s64, v100\n"
"v_mul_f32 v101, s64, v101\n"
"v_mfma_f32_16x16x32_bf16 v[136:139], a[16:19], a[176:179], v[136:139]\n"
"buffer_load_dwordx4 v12, s[28:31], s61 offen offset:2048 lds\n"
"v_mul_f32 v102, s64, v102\n"
"v_mul_f32 v103, s64, v103\n"
"v_mfma_f32_16x16x32_bf16 v[136:139], a[20:23], a[180:183], v[136:139]\n"
"buffer_load_dwordx4 v13, s[28:31], s61 offen offset:3072 lds\n"
"s_add_u32 s61, s61, 4096\n"
"s_and_b32 s61, s61, 0x3ffff\n"
"v_cvt_pk_bf16_f32 v80, v96, v97\n"
"v_cvt_pk_bf16_f32 v81, v98, v99\n"
"v_mfma_f32_16x16x32_bf16 v[140:143], a[24:27], a[176:179], v[140:143]\n"
"v_mul_f32 v104, s64, v104\n"
"v_mul_f32 v105, s64, v105\n"
"v_mfma_f32_16x16x32_bf16 v[140:143], a[28:31], a[180:183], v[140:143]\n"
"v_mul_f32 v106, s64, v106\n"
"v_mul_f32 v107, s64, v107\n"
"v_mfma_f32_16x16x32_bf16 v[144:147], a[32:35], a[176:179], v[144:147]\n"
"v_cvt_pk_bf16_f32 v82, v100, v101\n"
"v_cvt_pk_bf16_f32 v83, v102, v103\n"
"v_mfma_f32_16x16x32_bf16 v[144:147], a[36:39], a[180:183], v[144:147]\n"
"v_mul_f32 v108, s64, v108\n"
"v_mul_f32 v109, s64, v109\n"
"v_mfma_f32_16x16x32_bf16 v[148:151], a[40:43], a[176:179], v[148:151]\n"
"v_mul_f32 v110, s64, v110\n"
"v_mul_f32 v111, s64, v111\n"
"v_mfma_f32_16x16x32_bf16 v[148:151], a[44:47], a[180:183], v[148:151]\n"
"v_cvt_pk_bf16_f32 v84, v104, v105\n"
"v_cvt_pk_bf16_f32 v85, v106, v107\n"
"v_mfma_f32_16x16x32_bf16 v[152:155], a[48:51], a[176:179], v[152:155]\n"
"v_mul_f32 v112, s64, v112\n"
"v_mul_f32 v113, s64, v113\n"
"v_mfma_f32_16x16x32_bf16 v[152:155], a[52:55], a[180:183], v[152:155]\n"
"v_mul_f32 v114, s64, v114\n"
"v_mul_f32 v115, s64, v115\n"
"v_mfma_f32_16x16x32_bf16 v[156:159], a[56:59], a[176:179], v[156:159]\n"
"v_cvt_pk_bf16_f32 v86, v108, v109\n"
"v_cvt_pk_bf16_f32 v87, v110, v111\n"
"v_mfma_f32_16x16x32_bf16 v[156:159], a[60:63], a[180:183], v[156:159]\n"
"s_waitcnt lgkmcnt(0)\n"
"v_mul_f32 v116, s64, v116\n"
"v_mul_f32 v117, s64, v117\n"
"v_mul_f32 v118, s64, v118\n"
"v_mfma_f32_16x16x32_bf16 v[128:131], a[192:195], a[184:187], v[128:131]\n"
"ds_read_b128 a[176:179], v18 offset:6144\n"
"ds_read_b128 a[180:183], v19 offset:6144\n"
"v_mul_f32 v119, s64, v119\n"
"v_cvt_pk_bf16_f32 v88, v112, v113\n"
"v_mfma_f32_16x16x32_bf16 v[128:131], a[196:199], a[188:191], v[128:131]\n"
"s_mov_b32 m0, 90112\n"
"s_waitcnt vmcnt(49)\n"
"v_cvt_pk_bf16_f32 v89, v114, v115\n"
"v_mul_f32 v120, s64, v120\n"
"v_mfma_f32_16x16x32_bf16 v[132:135], a[200:203], a[184:187], v[132:135]\n"
"buffer_load_dwordx4 v14, s[36:39], s62 offen lds\n"
"v_mul_f32 v121, s64, v121\n"
"v_mul_f32 v122, s64, v122\n"
"v_mfma_f32_16x16x32_bf16 v[132:135], a[204:207], a[188:191], v[132:135]\n"
"buffer_load_dwordx4 v15, s[36:39], s62 offen offset:1024 lds\n"
"v_mul_f32 v123, s64, v123\n"
"v_cvt_pk_bf16_f32 v90, v116, v117\n"
"v_mfma_f32_16x16x32_bf16 v[136:139], a[208:211], a[184:187], v[136:139]\n"
"buffer_load_dwordx4 v14, s[36:39], s62 offen offset:2048 lds\n"
"v_cvt_pk_bf16_f32 v91, v118, v119\n"
"v_mul_f32 v124, s64, v124\n"
"v_mfma_f32_16x16x32_bf16 v[136:139], a[212:215], a[188:191], v[136:139]\n"
"buffer_load_dwordx4 v15, s[36:39], s62 offen offset:3072 lds\n"
"s_add_u32 s62, s62, 4096\n"
"s_and_b32 s62, s62, 0x3ffff\n"
"v_mul_f32 v125, s64, v125\n"
"v_mul_f32 v126, s64, v126\n"
"v_mfma_f32_16x16x32_bf16 v[140:143], a[216:219], a[184:187], v[140:143]\n"
"v_mul_f32 v127, s64, v127\n"
"v_cvt_pk_bf16_f32 v92, v120, v121\n"
"v_mfma_f32_16x16x32_bf16 v[140:143], a[220:223], a[188:191], v[140:143]\n"
"v_cvt_pk_bf16_f32 v93, v122, v123\n"
"v_cvt_pk_bf16_f32 v94, v124, v125\n"
"v_mfma_f32_16x16x32_bf16 v[144:147], a[224:227], a[184:187], v[144:147]\n"
"v_cvt_pk_bf16_f32 v95, v126, v127\n"
"v_mfma_f32_16x16x32_bf16 v[144:147], a[228:231], a[188:191], v[144:147]\n"
"v_mfma_f32_16x16x32_bf16 v[148:151], a[232:235], a[184:187], v[148:151]\n"
"v_mfma_f32_16x16x32_bf16 v[148:151], a[236:239], a[188:191], v[148:151]\n"
"v_mfma_f32_16x16x32_bf16 v[152:155], a[240:243], a[184:187], v[152:155]\n"
"v_mfma_f32_16x16x32_bf16 v[152:155], a[244:247], a[188:191], v[152:155]\n"
"v_mfma_f32_16x16x32_bf16 v[156:159], a[248:251], a[184:187], v[156:159]\n"
"global_load_dword v33, v32, s[18:19]\n"
"v_mfma_f32_16x16x32_bf16 v[156:159], a[252:255], a[188:191], v[156:159]\n"
"s_waitcnt vmcnt(45) lgkmcnt(0)\n"
"s_barrier\n"
"v_mfma_f32_16x16x32_bf16 v[160:163], a[0:3], a[160:163], v[160:163]\n"
"ds_read_b128 a[184:187], v20 offset:6144\n"
"ds_read_b128 a[188:191], v21 offset:6144\n"
"v_mul_f32 v128, s64, v128\n"
"v_mul_f32 v129, s64, v129\n"
"v_mfma_f32_16x16x32_bf16 v[160:163], a[4:7], a[164:167], v[160:163]\n"
"s_mov_b32 m0, 94208\n"
"s_waitcnt vmcnt(49)\n"
"v_mul_f32 v130, s64, v130\n"
"v_mul_f32 v131, s64, v131\n"
"v_mfma_f32_16x16x32_bf16 v[164:167], a[8:11], a[160:163], v[164:167]\n"
"ds_read_b128 a[64:67], v4\n"
"ds_read_b128 a[68:71], v5\n"
"v_mul_f32 v132, s64, v132\n"
"v_mul_f32 v133, s64, v133\n"
"v_mfma_f32_16x16x32_bf16 v[164:167], a[12:15], a[164:167], v[164:167]\n"
"ds_read_b128 a[72:75], v6\n"
"ds_read_b128 a[76:79], v7\n"
"v_mul_f32 v134, s64, v134\n"
"v_mul_f32 v135, s64, v135\n"
"v_mfma_f32_16x16x32_bf16 v[168:171], a[16:19], a[160:163], v[168:171]\n"
"ds_read_b128 a[80:83], v4 offset:4096\n"
"ds_read_b128 a[84:87], v5 offset:4096\n"
"v_cvt_pk_bf16_f32 v96, v128, v129\n"
"v_cvt_pk_bf16_f32 v97, v130, v131\n"
"v_mfma_f32_16x16x32_bf16 v[168:171], a[20:23], a[164:167], v[168:171]\n"
"ds_read_b128 a[88:91], v6 offset:4096\n"
"ds_read_b128 a[92:95], v7 offset:4096\n"
"v_mul_f32 v136, s64, v136\n"
"v_mul_f32 v137, s64, v137\n"
"v_mfma_f32_16x16x32_bf16 v[172:175], a[24:27], a[160:163], v[172:175]\n"
"buffer_load_dwordx4 v14, s[36:39], s62 offen lds\n"
"v_mul_f32 v138, s64, v138\n"
"v_mul_f32 v139, s64, v139\n"
"v_mfma_f32_16x16x32_bf16 v[172:175], a[28:31], a[164:167], v[172:175]\n"
"buffer_load_dwordx4 v15, s[36:39], s62 offen offset:1024 lds\n"
"v_cvt_pk_bf16_f32 v98, v132, v133\n"
"v_cvt_pk_bf16_f32 v99, v134, v135\n"
"v_mfma_f32_16x16x32_bf16 v[176:179], a[32:35], a[160:163], v[176:179]\n"
"buffer_load_dwordx4 v14, s[36:39], s62 offen offset:2048 lds\n"
"v_mul_f32 v140, s64, v140\n"
"v_mul_f32 v141, s64, v141\n"
"v_mfma_f32_16x16x32_bf16 v[176:179], a[36:39], a[164:167], v[176:179]\n"
"buffer_load_dwordx4 v15, s[36:39], s62 offen offset:3072 lds\n"
"s_add_u32 s62, s62, 4096\n"
"s_and_b32 s62, s62, 0x3ffff\n"
"v_mul_f32 v142, s64, v142\n"
"v_mul_f32 v143, s64, v143\n"
"v_mfma_f32_16x16x32_bf16 v[180:183], a[40:43], a[160:163], v[180:183]\n"
"v_cvt_pk_bf16_f32 v100, v136, v137\n"
"v_cvt_pk_bf16_f32 v101, v138, v139\n"
"v_mfma_f32_16x16x32_bf16 v[180:183], a[44:47], a[164:167], v[180:183]\n"
"v_mul_f32 v144, s64, v144\n"
"v_mul_f32 v145, s64, v145\n"
"v_mfma_f32_16x16x32_bf16 v[184:187], a[48:51], a[160:163], v[184:187]\n"
"v_mul_f32 v146, s64, v146\n"
"v_mul_f32 v147, s64, v147\n"
"v_mfma_f32_16x16x32_bf16 v[184:187], a[52:55], a[164:167], v[184:187]\n"
"v_cvt_pk_bf16_f32 v102, v140, v141\n"
"v_cvt_pk_bf16_f32 v103, v142, v143\n"
"v_mfma_f32_16x16x32_bf16 v[188:191], a[56:59], a[160:163], v[188:191]\n"
"v_mul_f32 v148, s64, v148\n"
"v_mul_f32 v149, s64, v149\n"
"v_mfma_f32_16x16x32_bf16 v[188:191], a[60:63], a[164:167], v[188:191]\n"
"s_waitcnt lgkmcnt(0)\n"
"v_mul_f32 v150, s64, v150\n"
"v_mul_f32 v151, s64, v151\n"
"v_cvt_pk_bf16_f32 v104, v144, v145\n"
"v_mfma_f32_16x16x32_bf16 v[36:39], a[64:67], v[80:83], 0\n"
"v_cvt_pk_bf16_f32 v105, v146, v147\n"
"v_mul_f32 v152, s64, v152\n"
"v_mfma_f32_16x16x32_bf16 v[36:39], a[68:71], v[84:87], v[36:39]\n"
"v_mul_f32 v153, s64, v153\n"
"v_mul_f32 v154, s64, v154\n"
"v_mfma_f32_16x16x32_bf16 v[36:39], a[72:75], v[88:91], v[36:39]\n"
"v_mul_f32 v155, s64, v155\n"
"v_cvt_pk_bf16_f32 v106, v148, v149\n"
"v_mfma_f32_16x16x32_bf16 v[36:39], a[76:79], v[92:95], v[36:39]\n"
"v_cvt_pk_bf16_f32 v107, v150, v151\n"
"v_mul_f32 v156, s64, v156\n"
"v_mfma_f32_16x16x32_bf16 v[160:163], a[192:195], a[168:171], v[160:163]\n"
"s_mov_b32 m0, 32768\n"
"s_waitcnt vmcnt(49)\n"
"v_mul_f32 v157, s64, v157\n"
"v_mul_f32 v158, s64, v158\n"
"v_mfma_f32_16x16x32_bf16 v[160:163], a[196:199], a[172:175], v[160:163]\n"
"buffer_load_dwordx4 v10, s[28:31], s61 offen lds\n"
"v_mul_f32 v159, s64, v159\n"
"v_max3_f32 v56, v36, v37, v38\n"
"v_mfma_f32_16x16x32_bf16 v[164:167], a[200:203], a[168:171], v[164:167]\n"
"buffer_load_dwordx4 v11, s[28:31], s61 offen offset:1024 lds\n"
"v_cvt_pk_bf16_f32 v108, v152, v153\n"
"v_max_f32 v56, v56, v39\n"
"v_mfma_f32_16x16x32_bf16 v[164:167], a[204:207], a[172:175], v[164:167]\n"
"buffer_load_dwordx4 v12, s[28:31], s61 offen offset:2048 lds\n"
"v_cvt_pk_bf16_f32 v109, v154, v155\n"
"v_mov_b32 v72, v56\n"
"v_mfma_f32_16x16x32_bf16 v[168:171], a[208:211], a[168:171], v[168:171]\n"
"buffer_load_dwordx4 v13, s[28:31], s61 offen offset:3072 lds\n"
"s_add_u32 s61, s61, 4096\n"
"s_and_b32 s61, s61, 0x3ffff\n"
"v_cvt_pk_bf16_f32 v110, v156, v157\n"
"v_permlane32_swap_b32 v56, v72\n"
"v_mfma_f32_16x16x32_bf16 v[168:171], a[212:215], a[172:175], v[168:171]\n"
"v_cvt_pk_bf16_f32 v111, v158, v159\n"
"v_max_f32 v56, v56, v72\n"
"v_mfma_f32_16x16x32_bf16 v[172:175], a[216:219], a[168:171], v[172:175]\n"
"v_mov_b32 v72, v56\n"
"v_mfma_f32_16x16x32_bf16 v[40:43], a[64:67], v[96:99], 0\n"
"v_mfma_f32_16x16x32_bf16 v[40:43], a[68:71], v[100:103], v[40:43]\n"
"v_permlane16_swap_b32 v56, v72\n"
"v_max_f32 v56, v56, v72\n"
"v_mfma_f32_16x16x32_bf16 v[40:43], a[72:75], v[104:107], v[40:43]\n"
"v_sub_f32 v36, 0, v56\n"
"v_sub_f32 v37, 0, v56\n"
"v_mfma_f32_16x16x32_bf16 v[40:43], a[76:79], v[108:111], v[40:43]\n"
"v_sub_f32 v38, 0, v56\n"
"v_sub_f32 v39, 0, v56\n"
"v_mfma_f32_16x16x32_bf16 v[172:175], a[220:223], a[172:175], v[172:175]\n"
"v_mfma_f32_16x16x32_bf16 v[176:179], a[224:227], a[168:171], v[176:179]\n"
"v_mfma_f32_16x16x32_bf16 v[176:179], a[228:231], a[172:175], v[176:179]\n"
"v_mfma_f32_16x16x32_bf16 v[180:183], a[232:235], a[168:171], v[180:183]\n"
"v_mfma_f32_16x16x32_bf16 v[180:183], a[236:239], a[172:175], v[180:183]\n"
"v_mfma_f32_16x16x32_bf16 v[184:187], a[240:243], a[168:171], v[184:187]\n"
"v_max3_f32 v57, v40, v41, v42\n"
"v_max_f32 v57, v57, v43\n"
"v_mfma_f32_16x16x32_bf16 v[184:187], a[244:247], a[172:175], v[184:187]\n"
"v_mov_b32 v73, v57\n"
"v_mfma_f32_16x16x32_bf16 v[188:191], a[248:251], a[168:171], v[188:191]\n"
"v_mfma_f32_16x16x32_bf16 v[188:191], a[252:255], a[172:175], v[188:191]\n"
"v_permlane32_swap_b32 v57, v73\n"
"v_max_f32 v57, v57, v73\n"
"v_mov_b32 v73, v57\n"
"v_mfma_f32_16x16x32_bf16 v[192:195], a[0:3], a[176:179], v[192:195]\n"
"s_mov_b32 m0, 36864\n"
"s_waitcnt vmcnt(49)\n"
"v_mul_f32 v160, s64, v160\n"
"v_permlane16_swap_b32 v57, v73\n"
"v_mfma_f32_16x16x32_bf16 v[192:195], a[4:7], a[180:183], v[192:195]\n"
"buffer_load_dwordx4 v10, s[28:31], s61 offen lds\n"
"v_mul_f32 v161, s64, v161\n"
"v_max_f32 v57, v57, v73\n"
"v_mfma_f32_16x16x32_bf16 v[196:199], a[8:11], a[176:179], v[196:199]\n"
"buffer_load_dwordx4 v11, s[28:31], s61 offen offset:1024 lds\n"
"v_mul_f32 v162, s64, v162\n"
"v_sub_f32 v40, 0, v57\n"
"v_mfma_f32_16x16x32_bf16 v[196:199], a[12:15], a[180:183], v[196:199]\n"
"buffer_load_dwordx4 v12, s[28:31], s61 offen offset:2048 lds\n"
"v_mul_f32 v163, s64, v163\n"
"v_sub_f32 v41, 0, v57\n"
"v_mfma_f32_16x16x32_bf16 v[200:203], a[16:19], a[176:179], v[200:203]\n"
"buffer_load_dwordx4 v13, s[28:31], s61 offen offset:3072 lds\n"
"s_add_u32 s61, s61, 4096\n"
"s_and_b32 s61, s61, 0x3ffff\n"
"v_mul_f32 v164, s64, v164\n"
"v_sub_f32 v42, 0, v57\n"
"v_mfma_f32_16x16x32_bf16 v[200:203], a[20:23], a[180:183], v[200:203]\n"
"s_mov_b32 m0, 98304\n"
"s_waitcnt vmcnt(49)\n"
"v_mul_f32 v165, s64, v165\n"
"v_sub_f32 v43, 0, v57\n"
"v_mfma_f32_16x16x32_bf16 v[204:207], a[24:27], a[176:179], v[204:207]\n"
"buffer_load_dwordx4 v14, s[36:39], s62 offen lds\n"
"v_mul_f32 v166, s64, v166\n"
"v_mul_f32 v167, s64, v167\n"
"v_mfma_f32_16x16x32_bf16 v[204:207], a[28:31], a[180:183], v[204:207]\n"
"buffer_load_dwordx4 v15, s[36:39], s62 offen offset:1024 lds\n"
"v_cvt_pk_bf16_f32 v112, v160, v161\n"
"v_cvt_pk_bf16_f32 v113, v162, v163\n"
"v_mfma_f32_16x16x32_bf16 v[208:211], a[32:35], a[176:179], v[208:211]\n"
"buffer_load_dwordx4 v14, s[36:39], s62 offen offset:2048 lds\n"
"v_mul_f32 v168, s64, v168\n"
"v_mul_f32 v169, s64, v169\n"
"v_mfma_f32_16x16x32_bf16 v[208:211], a[36:39], a[180:183], v[208:211]\n"
"buffer_load_dwordx4 v15, s[36:39], s62 offen offset:3072 lds\n"
"s_add_u32 s62, s62, 4096\n"
"s_and_b32 s62, s62, 0x3ffff\n"
"v_mul_f32 v170, s64, v170\n"
"v_mul_f32 v171, s64, v171\n"
"v_mfma_f32_16x16x32_bf16 v[212:215], a[40:43], a[176:179], v[212:215]\n"
"v_cvt_pk_bf16_f32 v114, v164, v165\n"
"v_cvt_pk_bf16_f32 v115, v166, v167\n"
"v_mfma_f32_16x16x32_bf16 v[212:215], a[44:47], a[180:183], v[212:215]\n"
"v_mul_f32 v172, s64, v172\n"
"v_mul_f32 v173, s64, v173\n"
"v_mfma_f32_16x16x32_bf16 v[216:219], a[48:51], a[176:179], v[216:219]\n"
"v_mul_f32 v174, s64, v174\n"
"v_mul_f32 v175, s64, v175\n"
"v_mfma_f32_16x16x32_bf16 v[216:219], a[52:55], a[180:183], v[216:219]\n"
"v_cvt_pk_bf16_f32 v116, v168, v169\n"
"v_cvt_pk_bf16_f32 v117, v170, v171\n"
"v_mfma_f32_16x16x32_bf16 v[220:223], a[56:59], a[176:179], v[220:223]\n"
"v_mul_f32 v176, s64, v176\n"
"v_mul_f32 v177, s64, v177\n"
"v_mfma_f32_16x16x32_bf16 v[220:223], a[60:63], a[180:183], v[220:223]\n"
"v_mul_f32 v178, s64, v178\n"
"v_mul_f32 v179, s64, v179\n"
"v_cvt_pk_bf16_f32 v118, v172, v173\n"
"v_cvt_pk_bf16_f32 v119, v174, v175\n"
"v_mfma_f32_16x16x32_bf16 v[192:195], a[192:195], a[184:187], v[192:195]\n"
"s_mov_b32 m0, 102400\n"
"s_waitcnt vmcnt(49)\n"
"v_mul_f32 v180, s64, v180\n"
"v_mul_f32 v181, s64, v181\n"
"v_mfma_f32_16x16x32_bf16 v[192:195], a[196:199], a[188:191], v[192:195]\n"
"buffer_load_dwordx4 v14, s[36:39], s62 offen lds\n"
"v_mul_f32 v182, s64, v182\n"
"v_mul_f32 v183, s64, v183\n"
"v_mfma_f32_16x16x32_bf16 v[196:199], a[200:203], a[184:187], v[196:199]\n"
"buffer_load_dwordx4 v15, s[36:39], s62 offen offset:1024 lds\n"
"v_cvt_pk_bf16_f32 v120, v176, v177\n"
"v_cvt_pk_bf16_f32 v121, v178, v179\n"
"v_mfma_f32_16x16x32_bf16 v[196:199], a[204:207], a[188:191], v[196:199]\n"
"buffer_load_dwordx4 v14, s[36:39], s62 offen offset:2048 lds\n"
"v_mul_f32 v184, s64, v184\n"
"v_mul_f32 v185, s64, v185\n"
"v_mfma_f32_16x16x32_bf16 v[200:203], a[208:211], a[184:187], v[200:203]\n"
"buffer_load_dwordx4 v15, s[36:39], s62 offen offset:3072 lds\n"
"s_add_u32 s62, s62, 4096\n"
"s_and_b32 s62, s62, 0x3ffff\n"
"v_mul_f32 v186, s64, v186\n"
"v_mul_f32 v187, s64, v187\n"
"v_mfma_f32_16x16x32_bf16 v[200:203], a[212:215], a[188:191], v[200:203]\n"
"v_cvt_pk_bf16_f32 v122, v180, v181\n"
"v_cvt_pk_bf16_f32 v123, v182, v183\n"
"v_mfma_f32_16x16x32_bf16 v[204:207], a[216:219], a[184:187], v[204:207]\n"
"v_mul_f32 v188, s64, v188\n"
"v_mul_f32 v189, s64, v189\n"
"v_mfma_f32_16x16x32_bf16 v[204:207], a[220:223], a[188:191], v[204:207]\n"
"v_mul_f32 v190, s64, v190\n"
"v_mul_f32 v191, s64, v191\n"
"v_mfma_f32_16x16x32_bf16 v[208:211], a[224:227], a[184:187], v[208:211]\n"
"v_cvt_pk_bf16_f32 v124, v184, v185\n"
"v_cvt_pk_bf16_f32 v125, v186, v187\n"
"v_mfma_f32_16x16x32_bf16 v[208:211], a[228:231], a[188:191], v[208:211]\n"
"v_cvt_pk_bf16_f32 v126, v188, v189\n"
"v_cvt_pk_bf16_f32 v127, v190, v191\n"
"v_mfma_f32_16x16x32_bf16 v[212:215], a[232:235], a[184:187], v[212:215]\n"
"v_mfma_f32_16x16x32_bf16 v[44:47], a[64:67], v[112:115], 0\n"
"v_mfma_f32_16x16x32_bf16 v[44:47], a[68:71], v[116:119], v[44:47]\n"
"v_mfma_f32_16x16x32_bf16 v[44:47], a[72:75], v[120:123], v[44:47]\n"
"v_mfma_f32_16x16x32_bf16 v[44:47], a[76:79], v[124:127], v[44:47]\n"
"v_mfma_f32_16x16x32_bf16 v[212:215], a[236:239], a[188:191], v[212:215]\n"
"v_mfma_f32_16x16x32_bf16 v[216:219], a[240:243], a[184:187], v[216:219]\n"
"v_mfma_f32_16x16x32_bf16 v[216:219], a[244:247], a[188:191], v[216:219]\n"
"v_mfma_f32_16x16x32_bf16 v[220:223], a[248:251], a[184:187], v[220:223]\n"
"v_mfma_f32_16x16x32_bf16 v[220:223], a[252:255], a[188:191], v[220:223]\n"
"s_mov_b32 m0, 40960\n"
"s_waitcnt vmcnt(49)\n"
"buffer_load_dwordx4 v10, s[28:31], s61 offen lds\n"
"buffer_load_dwordx4 v11, s[28:31], s61 offen offset:1024 lds\n"
"buffer_load_dwordx4 v12, s[28:31], s61 offen offset:2048 lds\n"
"buffer_load_dwordx4 v13, s[28:31], s61 offen offset:3072 lds\n"
"s_add_u32 s61, s61, 4096\n"
"s_and_b32 s61, s61, 0x3ffff\n"
"v_pk_mul_f32 v[192:193], v[192:193], s[64:65]\n"
"v_pk_mul_f32 v[194:195], v[194:195], s[64:65]\n"
"v_pk_mul_f32 v[196:197], v[196:197], s[64:65]\n"
"v_pk_mul_f32 v[198:199], v[198:199], s[64:65]\n"
"v_pk_mul_f32 v[200:201], v[200:201], s[64:65]\n"
"v_pk_mul_f32 v[202:203], v[202:203], s[64:65]\n"
"v_pk_mul_f32 v[204:205], v[204:205], s[64:65]\n"
"v_pk_mul_f32 v[206:207], v[206:207], s[64:65]\n"
"v_pk_mul_f32 v[208:209], v[208:209], s[64:65]\n"
"v_pk_mul_f32 v[210:211], v[210:211], s[64:65]\n"
"v_pk_mul_f32 v[212:213], v[212:213], s[64:65]\n"
"v_pk_mul_f32 v[214:215], v[214:215], s[64:65]\n"
"v_pk_mul_f32 v[216:217], v[216:217], s[64:65]\n"
"v_pk_mul_f32 v[218:219], v[218:219], s[64:65]\n"
"v_pk_mul_f32 v[220:221], v[220:221], s[64:65]\n"
"v_pk_mul_f32 v[222:223], v[222:223], s[64:65]\n"
"v_mfma_f32_16x16x32_bf16 v[64:67], a[64:67], v[80:83], v[36:39]\n"
"v_cvt_pk_bf16_f32 v128, v192, v193\n"
"v_cvt_pk_bf16_f32 v129, v194, v195\n"
"v_mfma_f32_16x16x32_bf16 v[64:67], a[68:71], v[84:87], v[64:67]\n"
"v_cvt_pk_bf16_f32 v130, v196, v197\n"
"v_cvt_pk_bf16_f32 v131, v198, v199\n"
"v_mfma_f32_16x16x32_bf16 v[64:67], a[72:75], v[88:91], v[64:67]\n"
"v_cvt_pk_bf16_f32 v132, v200, v201\n"
"v_cvt_pk_bf16_f32 v133, v202, v203\n"
"v_mfma_f32_16x16x32_bf16 v[64:67], a[76:79], v[92:95], v[64:67]\n"
"v_cvt_pk_bf16_f32 v134, v204, v205\n"
"v_cvt_pk_bf16_f32 v135, v206, v207\n"
"v_mfma_f32_16x16x32_bf16 v[68:71], a[80:83], v[80:83], v[36:39]\n"
"v_cvt_pk_bf16_f32 v136, v208, v209\n"
"v_cvt_pk_bf16_f32 v137, v210, v211\n"
"v_mfma_f32_16x16x32_bf16 v[68:71], a[84:87], v[84:87], v[68:71]\n"
"v_cvt_pk_bf16_f32 v138, v212, v213\n"
"v_cvt_pk_bf16_f32 v139, v214, v215\n"
"v_mfma_f32_16x16x32_bf16 v[68:71], a[88:91], v[88:91], v[68:71]\n"
"v_cvt_pk_bf16_f32 v140, v216, v217\n"
"v_cvt_pk_bf16_f32 v141, v218, v219\n"
"v_mfma_f32_16x16x32_bf16 v[68:71], a[92:95], v[92:95], v[68:71]\n"
"v_cvt_pk_bf16_f32 v142, v220, v221\n"
"v_cvt_pk_bf16_f32 v143, v222, v223\n"
"s_barrier\n"
"v_mfma_f32_16x16x32_bf16 v[48:51], a[64:67], v[128:131], 0\n"
"ds_read_b64_tr_b16 a[128:129], v8\n"
"ds_read_b64_tr_b16 a[130:131], v8 offset:4096\n"
"v_mfma_f32_16x16x32_bf16 v[48:51], a[68:71], v[132:135], v[48:51]\n"
"ds_read_b64_tr_b16 a[132:133], v9\n"
"ds_read_b64_tr_b16 a[134:135], v9 offset:4096\n"
"v_mfma_f32_16x16x32_bf16 v[48:51], a[72:75], v[136:139], v[48:51]\n"
"ds_read_b64_tr_b16 a[136:137], v8 offset:512\n"
"ds_read_b64_tr_b16 a[138:139], v8 offset:4608\n"
"v_mfma_f32_16x16x32_bf16 v[48:51], a[76:79], v[140:143], v[48:51]\n"
"ds_read_b64_tr_b16 a[140:141], v9 offset:512\n"
"ds_read_b64_tr_b16 a[142:143], v9 offset:4608\n"
"ds_read_b64_tr_b16 a[144:145], v8 offset:1024\n"
"ds_read_b64_tr_b16 a[146:147], v8 offset:5120\n"
"ds_read_b64_tr_b16 a[148:149], v9 offset:1024\n"
"ds_read_b64_tr_b16 a[150:151], v9 offset:5120\n"
"ds_read_b64_tr_b16 a[152:153], v8 offset:1536\n"
"ds_read_b64_tr_b16 a[154:155], v8 offset:5632\n"
"ds_read_b64_tr_b16 a[156:157], v9 offset:1536\n"
"ds_read_b64_tr_b16 a[158:159], v9 offset:5632\n"
"ds_read_b128 a[96:99], v4 offset:8192\n"
"ds_read_b128 a[100:103], v5 offset:8192\n"
"ds_read_b128 a[104:107], v6 offset:8192\n"
"ds_read_b128 a[108:111], v7 offset:8192\n"
"ds_read_b128 a[112:115], v4 offset:12288\n"
"ds_read_b128 a[116:119], v5 offset:12288\n"
"ds_read_b128 a[120:123], v6 offset:12288\n"
"ds_read_b128 a[124:127], v7 offset:12288\n"
"s_mov_b32 m0, 45056\n"
"s_waitcnt vmcnt(49)\n"
"buffer_load_dwordx4 v10, s[28:31], s61 offen lds\n"
"buffer_load_dwordx4 v11, s[28:31], s61 offen offset:1024 lds\n"
"buffer_load_dwordx4 v12, s[28:31], s61 offen offset:2048 lds\n"
"buffer_load_dwordx4 v13, s[28:31], s61 offen offset:3072 lds\n"
"s_add_u32 s61, s61, 4096\n"
"s_and_b32 s61, s61, 0x3ffff\n"
"v_max3_f32 v59, v48, v49, v50\n"
"v_max3_f32 v58, v44, v45, v46\n"
"v_max_f32 v59, v59, v51\n"
"v_max_f32 v58, v58, v47\n"
"v_mov_b32 v75, v59\n"
"v_mov_b32 v74, v58\n"
"s_nop 0\n"
"v_permlane32_swap_b32 v59, v75\n"
"v_permlane32_swap_b32 v58, v74\n"
"v_max_f32 v59, v59, v75\n"
"v_max_f32 v58, v58, v74\n"
"v_mov_b32 v75, v59\n"
"v_mov_b32 v74, v58\n"
"s_nop 0\n"
"v_permlane16_swap_b32 v59, v75\n"
"v_permlane16_swap_b32 v58, v74\n"
"v_max_f32 v59, v59, v75\n"
"v_max_f32 v58, v58, v74\n"
"v_sub_f32 v48, 0, v59\n"
"v_sub_f32 v44, 0, v58\n"
"v_sub_f32 v49, 0, v59\n"
"v_sub_f32 v45, 0, v58\n"
"v_sub_f32 v50, 0, v59\n"
"v_sub_f32 v46, 0, v58\n"
"v_sub_f32 v51, 0, v59\n"
"v_sub_f32 v47, 0, v58\n"
".Lmha_att4:\n"
"v_mfma_f32_16x16x32_bf16 v[72:75], a[64:67], v[96:99], v[40:43]\n"
"s_mov_b32 s51, 0\n"
"v_exp_f32 v64, v64\n"
"v_mfma_f32_16x16x32_bf16 v[72:75], a[68:71], v[100:103], v[72:75]\n"
"s_add_u32 s61, s62, 4096\n"
"v_mfma_f32_16x16x32_bf16 v[72:75], a[72:75], v[104:107], v[72:75]\n"
"v_mov_b32 v27, 0x3f803f80\n"
"v_exp_f32 v65, v65\n"
"v_mfma_f32_16x16x32_bf16 v[72:75], a[76:79], v[108:111], v[72:75]\n"
"v_accvgpr_write_b32 a224, v27\n"
"v_accvgpr_write_b32 a225, v27\n"
"v_accvgpr_write_b32 a226, v27\n"
"v_accvgpr_write_b32 a227, v27\n"
"v_add_f32 v29, v29, v64\n"
"v_exp_f32 v66, v66\n"
"v_cvt_pk_bf16_f32 v56, v64, v65\n"
"v_add_f32 v29, v29, v65\n"
"v_exp_f32 v67, v67\n"
"v_add_f32 v29, v29, v66\n"
"v_mfma_f32_16x16x32_bf16 v[76:79], a[80:83], v[96:99], v[40:43]\n"
"v_exp_f32 v68, v68\n"
"v_mfma_f32_16x16x32_bf16 v[76:79], a[84:87], v[100:103], v[76:79]\n"
"v_cvt_pk_bf16_f32 v57, v66, v67\n"
"v_add_f32 v29, v29, v67\n"
"v_mfma_f32_16x16x32_bf16 v[76:79], a[88:91], v[104:107], v[76:79]\n"
"v_exp_f32 v69, v69\n"
"v_mfma_f32_16x16x32_bf16 v[76:79], a[92:95], v[108:111], v[76:79]\n"
"v_add_f32 v29, v29, v68\n"
"v_exp_f32 v70, v70\n"
"v_cvt_pk_bf16_f32 v58, v68, v69\n"
"v_add_f32 v29, v29, v69\n"
"v_exp_f32 v71, v71\n"
"v_add_f32 v29, v29, v70\n"
"v_mfma_f32_16x16x32_bf16 v[64:67], a[64:67], v[112:115], v[44:47]\n"
"v_exp_f32 v72, v72\n"
"v_mfma_f32_16x16x32_bf16 v[64:67], a[68:71], v[116:119], v[64:67]\n"
"v_cvt_pk_bf16_f32 v59, v70, v71\n"
"v_add_f32 v29, v29, v71\n"
"v_mfma_f32_16x16x32_bf16 v[64:67], a[72:75], v[120:123], v[64:67]\n"
"v_exp_f32 v73, v73\n"
"v_mfma_f32_16x16x32_bf16 v[64:67], a[76:79], v[124:127], v[64:67]\n"
"s_waitcnt vmcnt(41) lgkmcnt(0)\n"
"s_barrier\n"
"v_mfma_f32_16x16x32_bf16 v[160:163], a[128:131], v[56:59], 0\n"
"s_mov_b32 m0, 106496\n"
"v_exp_f32 v74, v74\n"
"v_mfma_f32_16x16x32_bf16 v[164:167], a[132:135], v[56:59], 0\n"
"ds_read_b64_tr_b16 a[160:161], v8 offset:8192\n"
"v_cvt_pk_bf16_f32 v60, v72, v73\n"
"v_mfma_f32_16x16x32_bf16 v[168:171], a[136:139], v[56:59], 0\n"
"buffer_load_dwordx4 v14, s[36:39], s62 offen lds\n"
"v_exp_f32 v75, v75\n"
"v_mfma_f32_16x16x32_bf16 v[172:175], a[140:143], v[56:59], 0\n"
"ds_read_b64_tr_b16 a[162:163], v8 offset:12288\n"
"ds_read_b64_tr_b16 a[164:165], v9 offset:8192\n"
"v_mfma_f32_16x16x32_bf16 v[68:71], a[80:83], v[112:115], v[44:47]\n"
"buffer_load_dwordx4 v15, s[36:39], s62 offen offset:1024 lds\n"
"v_exp_f32 v76, v76\n"
"v_mfma_f32_16x16x32_bf16 v[68:71], a[84:87], v[116:119], v[68:71]\n"
"ds_read_b64_tr_b16 a[166:167], v9 offset:12288\n"
"v_cvt_pk_bf16_f32 v61, v74, v75\n"
"v_mfma_f32_16x16x32_bf16 v[68:71], a[88:91], v[120:123], v[68:71]\n"
"buffer_load_dwordx4 v14, s[36:39], s62 offen offset:2048 lds\n"
"v_exp_f32 v77, v77\n"
"v_mfma_f32_16x16x32_bf16 v[68:71], a[92:95], v[124:127], v[68:71]\n"
"ds_read_b64_tr_b16 a[168:169], v8 offset:8704\n"
"ds_read_b64_tr_b16 a[170:171], v8 offset:12800\n"
"v_mfma_f32_16x16x32_bf16 v[176:179], a[144:147], v[56:59], 0\n"
"buffer_load_dwordx4 v15, s[36:39], s62 offen offset:3072 lds\n"
"v_exp_f32 v78, v78\n"
"v_mfma_f32_16x16x32_bf16 v[180:183], a[148:151], v[56:59], 0\n"
"ds_read_b64_tr_b16 a[172:173], v9 offset:8704\n"
"v_cvt_pk_bf16_f32 v62, v76, v77\n"
"v_mfma_f32_16x16x32_bf16 v[184:187], a[152:155], v[56:59], 0\n"
"v_exp_f32 v79, v79\n"
"v_mfma_f32_16x16x32_bf16 v[188:191], a[156:159], v[56:59], 0\n"
"ds_read_b64_tr_b16 a[174:175], v9 offset:12800\n"
"v_mfma_f32_16x16x32_bf16 v[72:75], a[64:67], v[128:131], v[48:51]\n"
"v_exp_f32 v64, v64\n"
"v_mfma_f32_16x16x32_bf16 v[72:75], a[68:71], v[132:135], v[72:75]\n"
"ds_read_b64_tr_b16 a[176:177], v8 offset:9216\n"
"v_cvt_pk_bf16_f32 v63, v78, v79\n"
"v_mfma_f32_16x16x32_bf16 v[72:75], a[72:75], v[136:139], v[72:75]\n"
"v_exp_f32 v65, v65\n"
"v_mfma_f32_16x16x32_bf16 v[72:75], a[76:79], v[140:143], v[72:75]\n"
"ds_read_b64_tr_b16 a[178:179], v8 offset:13312\n"
"v_add_f32 v31, v31, v64\n"
"v_mfma_f32_16x16x32_bf16 v[192:195], a[128:131], v[60:63], 0\n"
"s_mov_b32 m0, 110592\n"
"v_exp_f32 v66, v66\n"
"v_mfma_f32_16x16x32_bf16 v[196:199], a[132:135], v[60:63], 0\n"
"ds_read_b64_tr_b16 a[180:181], v9 offset:9216\n"
"v_cvt_pk_bf16_f32 v56, v64, v65\n"
"v_add_f32 v31, v31, v65\n"
"v_mfma_f32_16x16x32_bf16 v[200:203], a[136:139], v[60:63], 0\n"
"buffer_load_dwordx4 v14, s[36:39], s61 offen lds\n"
"v_exp_f32 v67, v67\n"
"v_mfma_f32_16x16x32_bf16 v[204:207], a[140:143], v[60:63], 0\n"
"ds_read_b64_tr_b16 a[182:183], v9 offset:13312\n"
"buffer_load_dwordx4 v15, s[36:39], s61 offen offset:1024 lds\n"
"v_add_f32 v31, v31, v66\n"
"v_mfma_f32_16x16x32_bf16 v[18:21], a[224:227], v[60:63], 0\n"
"v_mfma_f32_16x16x32_bf16 v[76:79], a[80:83], v[128:131], v[48:51]\n"
"buffer_load_dwordx4 v14, s[36:39], s61 offen offset:2048 lds\n"
"v_exp_f32 v68, v68\n"
"v_mfma_f32_16x16x32_bf16 v[76:79], a[84:87], v[132:135], v[76:79]\n"
"ds_read_b64_tr_b16 a[184:185], v8 offset:9728\n"
"buffer_load_dwordx4 v15, s[36:39], s61 offen offset:3072 lds\n"
"v_cvt_pk_bf16_f32 v57, v66, v67\n"
"v_add_f32 v31, v31, v67\n"
"v_mfma_f32_16x16x32_bf16 v[76:79], a[88:91], v[136:139], v[76:79]\n"
"s_add_u32 s62, s62, 8192\n"
"s_and_b32 s62, s62, 0x3ffff\n"
"s_add_u32 s61, s62, 4096\n"
"s_mov_b32 m0, 131072\n"
"s_add_u32 s52, s51, 0\n"
"v_exp_f32 v69, v69\n"
"v_mfma_f32_16x16x32_bf16 v[76:79], a[92:95], v[140:143], v[76:79]\n"
"ds_read_b64_tr_b16 a[186:187], v8 offset:13824\n"
"buffer_load_dwordx4 v22, s[44:47], s52 offen lds\n"
"v_add_f32 v31, v31, v68\n"
"v_mfma_f32_16x16x32_bf16 v[208:211], a[144:147], v[60:63], 0\n"
"buffer_load_dwordx4 v23, s[44:47], s52 offen offset:1024 lds\n"
"v_exp_f32 v70, v70\n"
"v_mfma_f32_16x16x32_bf16 v[212:215], a[148:151], v[60:63], 0\n"
"ds_read_b64_tr_b16 a[188:189], v9 offset:9728\n"
"buffer_load_dwordx4 v24, s[44:47], s52 offen offset:2048 lds\n"
"v_cvt_pk_bf16_f32 v58, v68, v69\n"
"v_add_f32 v31, v31, v69\n"
"v_mfma_f32_16x16x32_bf16 v[216:219], a[152:155], v[60:63], 0\n"
"buffer_load_dwordx4 v25, s[44:47], s52 offen offset:3072 lds\n"
"v_exp_f32 v71, v71\n"
"v_mfma_f32_16x16x32_bf16 v[220:223], a[156:159], v[60:63], 0\n"
"ds_read_b64_tr_b16 a[190:191], v9 offset:13824\n"
"v_add_f32 v31, v31, v70\n"
"v_mfma_f32_16x16x32_bf16 v[64:67], a[96:99], v[80:83], v[36:39]\n"
"v_lshrrev_b32 v26, 5, v0\n"
"v_exp_f32 v72, v72\n"
"v_mfma_f32_16x16x32_bf16 v[64:67], a[100:103], v[84:87], v[64:67]\n"
"v_cvt_pk_bf16_f32 v59, v70, v71\n"
"v_add_f32 v31, v31, v71\n"
"v_mfma_f32_16x16x32_bf16 v[64:67], a[104:107], v[88:91], v[64:67]\n"
"v_and_b32 v27, 31, v0\n"
"v_exp_f32 v73, v73\n"
"v_mfma_f32_16x16x32_bf16 v[64:67], a[108:111], v[92:95], v[64:67]\n"
"ds_read_b128 a[64:67], v4 offset:16384\n"
"v_add_f32 v34, v34, v72\n"
"v_mfma_f32_16x16x32_bf16 v[224:227], a[128:131], v[56:59], 0\n"
"s_mov_b32 m0, 49152\n"
"v_lshlrev_b32 v26, 19, v26\n"
"v_exp_f32 v74, v74\n"
"v_mfma_f32_16x16x32_bf16 v[228:231], a[132:135], v[56:59], 0\n"
"ds_read_b128 a[68:71], v5 offset:16384\n"
"v_cvt_pk_bf16_f32 v60, v72, v73\n"
"v_add_f32 v34, v34, v73\n"
"v_mfma_f32_16x16x32_bf16 v[232:235], a[136:139], v[56:59], 0\n"
"buffer_load_dwordx4 v10, s[28:31], s62 offen lds\n"
"v_lshl_add_u32 v26, v27, 10, v26\n"
"v_exp_f32 v75, v75\n"
"v_mfma_f32_16x16x32_bf16 v[236:239], a[140:143], v[56:59], 0\n"
"ds_read_b128 a[72:75], v6 offset:16384\n"
"v_add_f32 v34, v34, v74\n"
"v_mfma_f32_16x16x32_bf16 v[68:71], a[112:115], v[80:83], v[36:39]\n"
"buffer_load_dwordx4 v11, s[28:31], s62 offen offset:1024 lds\n"
"s_lshl_b32 s53, s49, 15\n"
"v_exp_f32 v76, v76\n"
"v_mfma_f32_16x16x32_bf16 v[68:71], a[116:119], v[84:87], v[68:71]\n"
"ds_read_b128 a[76:79], v7 offset:16384\n"
"v_cvt_pk_bf16_f32 v61, v74, v75\n"
"v_add_f32 v34, v34, v75\n"
"v_mfma_f32_16x16x32_bf16 v[68:71], a[120:123], v[88:91], v[68:71]\n"
"buffer_load_dwordx4 v12, s[28:31], s62 offen offset:2048 lds\n"
"s_and_b32 s53, s53, 0x18000\n"
"v_exp_f32 v77, v77\n"
"v_mfma_f32_16x16x32_bf16 v[68:71], a[124:127], v[92:95], v[68:71]\n"
"ds_read_b128 a[80:83], v4 offset:20480\n"
"v_add_f32 v34, v34, v76\n"
"v_mfma_f32_16x16x32_bf16 v[240:243], a[144:147], v[56:59], 0\n"
"buffer_load_dwordx4 v13, s[28:31], s62 offen offset:3072 lds\n"
"v_add_u32 v26, s53, v26\n"
"v_exp_f32 v78, v78\n"
"v_mfma_f32_16x16x32_bf16 v[244:247], a[148:151], v[56:59], 0\n"
"ds_read_b128 a[84:87], v5 offset:20480\n"
"v_cvt_pk_bf16_f32 v62, v76, v77\n"
"v_add_f32 v34, v34, v77\n"
"v_mfma_f32_16x16x32_bf16 v[248:251], a[152:155], v[56:59], 0\n"
"v_exp_f32 v79, v79\n"
"v_mfma_f32_16x16x32_bf16 v[252:255], a[156:159], v[56:59], 0\n"
"v_add_f32 v34, v34, v78\n"
"v_mfma_f32_16x16x32_bf16 v[72:75], a[96:99], v[96:99], v[40:43]\n"
"v_exp_f32 v64, v64\n"
"v_mfma_f32_16x16x32_bf16 v[72:75], a[100:103], v[100:103], v[72:75]\n"
"v_cvt_pk_bf16_f32 v63, v78, v79\n"
"v_add_f32 v34, v34, v79\n"
"v_mfma_f32_16x16x32_bf16 v[72:75], a[104:107], v[104:107], v[72:75]\n"
"v_exp_f32 v65, v65\n"
"v_mfma_f32_16x16x32_bf16 v[72:75], a[108:111], v[108:111], v[72:75]\n"
"ds_read_b128 a[88:91], v6 offset:20480\n"
"v_add_f32 v29, v29, v64\n"
"v_mfma_f32_16x16x32_bf16 a[192:195], a[128:131], v[60:63], 0\n"
"s_mov_b32 m0, 53248\n"
"v_exp_f32 v66, v66\n"
"v_mfma_f32_16x16x32_bf16 a[196:199], a[132:135], v[60:63], 0\n"
"s_waitcnt vmcnt(52)\n"
"v_cvt_pk_bf16_f32 v56, v64, v65\n"
"v_add_f32 v29, v29, v65\n"
"v_mfma_f32_16x16x32_bf16 a[200:203], a[136:139], v[60:63], 0\n"
"buffer_load_dwordx4 v10, s[28:31], s61 offen lds\n"
"v_exp_f32 v67, v67\n"
"v_mfma_f32_16x16x32_bf16 a[204:207], a[140:143], v[60:63], 0\n"
"ds_read_b128 a[92:95], v7 offset:20480\n"
"v_add_f32 v29, v29, v66\n"
"v_mfma_f32_16x16x32_bf16 v[76:79], a[112:115], v[96:99], v[40:43]\n"
"buffer_load_dwordx4 v11, s[28:31], s61 offen offset:1024 lds\n"
"v_exp_f32 v68, v68\n"
"v_mfma_f32_16x16x32_bf16 v[76:79], a[116:119], v[100:103], v[76:79]\n"
"v_cvt_pk_bf16_f32 v57, v66, v67\n"
"v_add_f32 v29, v29, v67\n"
"v_mfma_f32_16x16x32_bf16 v[76:79], a[120:123], v[104:107], v[76:79]\n"
"buffer_load_dwordx4 v12, s[28:31], s61 offen offset:2048 lds\n"
"v_exp_f32 v69, v69\n"
"v_mfma_f32_16x16x32_bf16 v[76:79], a[124:127], v[108:111], v[76:79]\n"
"v_add_f32 v29, v29, v68\n"
"v_mfma_f32_16x16x32_bf16 a[208:211], a[144:147], v[60:63], 0\n"
"buffer_load_dwordx4 v13, s[28:31], s61 offen offset:3072 lds\n"
"v_exp_f32 v70, v70\n"
"v_mfma_f32_16x16x32_bf16 a[212:215], a[148:151], v[60:63], 0\n"
"v_cvt_pk_bf16_f32 v58, v68, v69\n"
"v_add_f32 v29, v29, v69\n"
"v_mfma_f32_16x16x32_bf16 a[216:219], a[152:155], v[60:63], 0\n"
"v_exp_f32 v71, v71\n"
"v_mfma_f32_16x16x32_bf16 a[220:223], a[156:159], v[60:63], 0\n"
"v_add_f32 v29, v29, v70\n"
"v_mfma_f32_16x16x32_bf16 v[64:67], a[96:99], v[112:115], v[44:47]\n"
"v_exp_f32 v72, v72\n"
"v_mfma_f32_16x16x32_bf16 v[64:67], a[100:103], v[116:119], v[64:67]\n"
"v_cvt_pk_bf16_f32 v59, v70, v71\n"
"v_add_f32 v29, v29, v71\n"
"v_mfma_f32_16x16x32_bf16 v[64:67], a[104:107], v[120:123], v[64:67]\n"
"global_load_dword v33, v32, s[18:19]\n"
"v_exp_f32 v73, v73\n"
"v_mfma_f32_16x16x32_bf16 v[64:67], a[108:111], v[124:127], v[64:67]\n"
"s_waitcnt vmcnt(54) lgkmcnt(0)\n"
"s_barrier\n"
"v_mfma_f32_16x16x32_bf16 v[160:163], a[160:163], v[56:59], v[160:163]\n"
"s_mov_b32 m0, 114688\n"
"v_exp_f32 v74, v74\n"
"v_mfma_f32_16x16x32_bf16 v[164:167], a[164:167], v[56:59], v[164:167]\n"
"ds_read_b64_tr_b16 a[128:129], v8 offset:16384\n"
"s_waitcnt vmcnt(52)\n"
"v_cvt_pk_bf16_f32 v60, v72, v73\n"
"v_mfma_f32_16x16x32_bf16 v[168:171], a[168:171], v[56:59], v[168:171]\n"
"buffer_load_dwordx4 v14, s[36:39], s62 offen lds\n"
"v_exp_f32 v75, v75\n"
"v_mfma_f32_16x16x32_bf16 v[172:175], a[172:175], v[56:59], v[172:175]\n"
"ds_read_b64_tr_b16 a[130:131], v8 offset:20480\n"
"ds_read_b64_tr_b16 a[132:133], v9 offset:16384\n"
"buffer_load_dwordx4 v15, s[36:39], s62 offen offset:1024 lds\n"
"v_mfma_f32_16x16x32_bf16 v[68:71], a[112:115], v[112:115], v[44:47]\n"
"buffer_load_dwordx4 v14, s[36:39], s62 offen offset:2048 lds\n"
"v_exp_f32 v76, v76\n"
"v_mfma_f32_16x16x32_bf16 v[68:71], a[116:119], v[116:119], v[68:71]\n"
"ds_read_b64_tr_b16 a[134:135], v9 offset:20480\n"
"buffer_load_dwordx4 v15, s[36:39], s62 offen offset:3072 lds\n"
"v_cvt_pk_bf16_f32 v61, v74, v75\n"
"v_mfma_f32_16x16x32_bf16 v[68:71], a[120:123], v[120:123], v[68:71]\n"
"s_mov_b32 m0, 135168\n"
"s_add_u32 s52, s51, 524288\n"
"v_exp_f32 v77, v77\n"
"v_mfma_f32_16x16x32_bf16 v[68:71], a[124:127], v[124:127], v[68:71]\n"
"ds_read_b64_tr_b16 a[136:137], v8 offset:16896\n"
"ds_read_b64_tr_b16 a[138:139], v8 offset:20992\n"
"buffer_load_dwordx4 v22, s[44:47], s52 offen lds\n"
"v_mfma_f32_16x16x32_bf16 v[176:179], a[176:179], v[56:59], v[176:179]\n"
"buffer_load_dwordx4 v23, s[44:47], s52 offen offset:1024 lds\n"
"v_exp_f32 v78, v78\n"
"v_mfma_f32_16x16x32_bf16 v[180:183], a[180:183], v[56:59], v[180:183]\n"
"ds_read_b64_tr_b16 a[140:141], v9 offset:16896\n"
"buffer_load_dwordx4 v24, s[44:47], s52 offen offset:2048 lds\n"
"v_cvt_pk_bf16_f32 v62, v76, v77\n"
"v_mfma_f32_16x16x32_bf16 v[184:187], a[184:187], v[56:59], v[184:187]\n"
"buffer_load_dwordx4 v25, s[44:47], s52 offen offset:3072 lds\n"
"v_exp_f32 v79, v79\n"
"v_mfma_f32_16x16x32_bf16 v[188:191], a[188:191], v[56:59], v[188:191]\n"
"ds_read_b64_tr_b16 a[142:143], v9 offset:20992\n"
"v_mfma_f32_16x16x32_bf16 v[72:75], a[96:99], v[128:131], v[48:51]\n"
"v_exp_f32 v64, v64\n"
"v_mfma_f32_16x16x32_bf16 v[72:75], a[100:103], v[132:135], v[72:75]\n"
"ds_read_b64_tr_b16 a[144:145], v8 offset:17408\n"
"v_cvt_pk_bf16_f32 v63, v78, v79\n"
"v_mfma_f32_16x16x32_bf16 v[72:75], a[104:107], v[136:139], v[72:75]\n"
"v_exp_f32 v65, v65\n"
"v_mfma_f32_16x16x32_bf16 v[72:75], a[108:111], v[140:143], v[72:75]\n"
"ds_read_b64_tr_b16 a[146:147], v8 offset:21504\n"
"v_add_f32 v31, v31, v64\n"
"v_mfma_f32_16x16x32_bf16 v[192:195], a[160:163], v[60:63], v[192:195]\n"
"s_mov_b32 m0, 118784\n"
"v_exp_f32 v66, v66\n"
"v_mfma_f32_16x16x32_bf16 v[196:199], a[164:167], v[60:63], v[196:199]\n"
"ds_read_b64_tr_b16 a[148:149], v9 offset:17408\n"
"s_waitcnt vmcnt(52)\n"
"v_cvt_pk_bf16_f32 v56, v64, v65\n"
"v_add_f32 v31, v31, v65\n"
"v_mfma_f32_16x16x32_bf16 v[200:203], a[168:171], v[60:63], v[200:203]\n"
"buffer_load_dwordx4 v14, s[36:39], s61 offen lds\n"
"v_exp_f32 v67, v67\n"
"v_mfma_f32_16x16x32_bf16 v[204:207], a[172:175], v[60:63], v[204:207]\n"
"ds_read_b64_tr_b16 a[150:151], v9 offset:21504\n"
"v_add_f32 v31, v31, v66\n"
"v_mfma_f32_16x16x32_bf16 v[18:21], a[224:227], v[60:63], v[18:21]\n"
"v_mfma_f32_16x16x32_bf16 v[76:79], a[112:115], v[128:131], v[48:51]\n"
"buffer_load_dwordx4 v15, s[36:39], s61 offen offset:1024 lds\n"
"v_exp_f32 v68, v68\n"
"v_mfma_f32_16x16x32_bf16 v[76:79], a[116:119], v[132:135], v[76:79]\n"
"ds_read_b64_tr_b16 a[152:153], v8 offset:17920\n"
"v_cvt_pk_bf16_f32 v57, v66, v67\n"
"v_add_f32 v31, v31, v67\n"
"v_mfma_f32_16x16x32_bf16 v[76:79], a[120:123], v[136:139], v[76:79]\n"
"buffer_load_dwordx4 v14, s[36:39], s61 offen offset:2048 lds\n"
"v_exp_f32 v69, v69\n"
"v_mfma_f32_16x16x32_bf16 v[76:79], a[124:127], v[140:143], v[76:79]\n"
"ds_read_b64_tr_b16 a[154:155], v8 offset:22016\n"
"v_add_f32 v31, v31, v68\n"
"v_mfma_f32_16x16x32_bf16 v[208:211], a[176:179], v[60:63], v[208:211]\n"
"buffer_load_dwordx4 v15, s[36:39], s61 offen offset:3072 lds\n"
"v_exp_f32 v70, v70\n"
"v_mfma_f32_16x16x32_bf16 v[212:215], a[180:183], v[60:63], v[212:215]\n"
"ds_read_b64_tr_b16 a[156:157], v9 offset:17920\n"
"v_cvt_pk_bf16_f32 v58, v68, v69\n"
"v_add_f32 v31, v31, v69\n"
"v_mfma_f32_16x16x32_bf16 v[216:219], a[184:187], v[60:63], v[216:219]\n"
"s_add_u32 s62, s62, 8192\n"
"s_and_b32 s62, s62, 0x3ffff\n"
"s_add_u32 s61, s62, 4096\n"
"v_exp_f32 v71, v71\n"
"v_mfma_f32_16x16x32_bf16 v[220:223], a[188:191], v[60:63], v[220:223]\n"
"ds_read_b64_tr_b16 a[158:159], v9 offset:22016\n"
"v_add_f32 v31, v31, v70\n"
"v_mfma_f32_16x16x32_bf16 v[64:67], a[64:67], v[80:83], v[36:39]\n"
"v_exp_f32 v72, v72\n"
"v_mfma_f32_16x16x32_bf16 v[64:67], a[68:71], v[84:87], v[64:67]\n"
"v_cvt_pk_bf16_f32 v59, v70, v71\n"
"v_add_f32 v31, v31, v71\n"
"v_mfma_f32_16x16x32_bf16 v[64:67], a[72:75], v[88:91], v[64:67]\n"
"v_exp_f32 v73, v73\n"
"v_mfma_f32_16x16x32_bf16 v[64:67], a[76:79], v[92:95], v[64:67]\n"
"ds_read_b128 a[96:99], v4 offset:24576\n"
"v_add_f32 v34, v34, v72\n"
"v_mfma_f32_16x16x32_bf16 v[224:227], a[160:163], v[56:59], v[224:227]\n"
"s_mov_b32 m0, 57344\n"
"v_exp_f32 v74, v74\n"
"v_mfma_f32_16x16x32_bf16 v[228:231], a[164:167], v[56:59], v[228:231]\n"
"ds_read_b128 a[100:103], v5 offset:24576\n"
"v_cvt_pk_bf16_f32 v60, v72, v73\n"
"v_add_f32 v34, v34, v73\n"
"v_mfma_f32_16x16x32_bf16 v[232:235], a[168:171], v[56:59], v[232:235]\n"
"buffer_load_dwordx4 v10, s[28:31], s62 offen lds\n"
"v_exp_f32 v75, v75\n"
"v_mfma_f32_16x16x32_bf16 v[236:239], a[172:175], v[56:59], v[236:239]\n"
"ds_read_b128 a[104:107], v6 offset:24576\n"
"v_add_f32 v34, v34, v74\n"
"v_mfma_f32_16x16x32_bf16 v[68:71], a[80:83], v[80:83], v[36:39]\n"
"buffer_load_dwordx4 v11, s[28:31], s62 offen offset:1024 lds\n"
"v_exp_f32 v76, v76\n"
"v_mfma_f32_16x16x32_bf16 v[68:71], a[84:87], v[84:87], v[68:71]\n"
"ds_read_b128 a[108:111], v7 offset:24576\n"
"v_cvt_pk_bf16_f32 v61, v74, v75\n"
"v_add_f32 v34, v34, v75\n"
"v_mfma_f32_16x16x32_bf16 v[68:71], a[88:91], v[88:91], v[68:71]\n"
"buffer_load_dwordx4 v12, s[28:31], s62 offen offset:2048 lds\n"
"v_exp_f32 v77, v77\n"
"v_mfma_f32_16x16x32_bf16 v[68:71], a[92:95], v[92:95], v[68:71]\n"
"ds_read_b128 a[112:115], v4 offset:28672\n"
"v_add_f32 v34, v34, v76\n"
"v_mfma_f32_16x16x32_bf16 v[240:243], a[176:179], v[56:59], v[240:243]\n"
"buffer_load_dwordx4 v13, s[28:31], s62 offen offset:3072 lds\n"
"v_exp_f32 v78, v78\n"
"v_mfma_f32_16x16x32_bf16 v[244:247], a[180:183], v[56:59], v[244:247]\n"
"ds_read_b128 a[116:119], v5 offset:28672\n"
"v_cvt_pk_bf16_f32 v62, v76, v77\n"
"v_add_f32 v34, v34, v77\n"
"v_mfma_f32_16x16x32_bf16 v[248:251], a[184:187], v[56:59], v[248:251]\n"
"v_exp_f32 v79, v79\n"
"v_mfma_f32_16x16x32_bf16 v[252:255], a[188:191], v[56:59], v[252:255]\n"
"v_add_f32 v34, v34, v78\n"
"v_mfma_f32_16x16x32_bf16 v[72:75], a[64:67], v[96:99], v[40:43]\n"
"v_exp_f32 v64, v64\n"
"v_mfma_f32_16x16x32_bf16 v[72:75], a[68:71], v[100:103], v[72:75]\n"
"v_cvt_pk_bf16_f32 v63, v78, v79\n"
"v_add_f32 v34, v34, v79\n"
"v_mfma_f32_16x16x32_bf16 v[72:75], a[72:75], v[104:107], v[72:75]\n"
"v_exp_f32 v65, v65\n"
"v_mfma_f32_16x16x32_bf16 v[72:75], a[76:79], v[108:111], v[72:75]\n"
"ds_read_b128 a[120:123], v6 offset:28672\n"
"v_add_f32 v29, v29, v64\n"
"v_mfma_f32_16x16x32_bf16 a[192:195], a[160:163], v[60:63], a[192:195]\n"
"s_mov_b32 m0, 61440\n"
"v_exp_f32 v66, v66\n"
"v_mfma_f32_16x16x32_bf16 a[196:199], a[164:167], v[60:63], a[196:199]\n"
"s_waitcnt vmcnt(52)\n"
"v_cvt_pk_bf16_f32 v56, v64, v65\n"
"v_add_f32 v29, v29, v65\n"
"v_mfma_f32_16x16x32_bf16 a[200:203], a[168:171], v[60:63], a[200:203]\n"
"buffer_load_dwordx4 v10, s[28:31], s61 offen lds\n"
"v_exp_f32 v67, v67\n"
"v_mfma_f32_16x16x32_bf16 a[204:207], a[172:175], v[60:63], a[204:207]\n"
"ds_read_b128 a[124:127], v7 offset:28672\n"
"buffer_load_dwordx4 v11, s[28:31], s61 offen offset:1024 lds\n"
"v_add_f32 v29, v29, v66\n"
"v_mfma_f32_16x16x32_bf16 v[76:79], a[80:83], v[96:99], v[40:43]\n"
"buffer_load_dwordx4 v12, s[28:31], s61 offen offset:2048 lds\n"
"v_exp_f32 v68, v68\n"
"v_mfma_f32_16x16x32_bf16 v[76:79], a[84:87], v[100:103], v[76:79]\n"
"buffer_load_dwordx4 v13, s[28:31], s61 offen offset:3072 lds\n"
"v_cvt_pk_bf16_f32 v57, v66, v67\n"
"v_add_f32 v29, v29, v67\n"
"v_mfma_f32_16x16x32_bf16 v[76:79], a[88:91], v[104:107], v[76:79]\n"
"s_mov_b32 m0, 139264\n"
"s_add_u32 s52, s51, 1048576\n"
"v_exp_f32 v69, v69\n"
"v_mfma_f32_16x16x32_bf16 v[76:79], a[92:95], v[108:111], v[76:79]\n"
"buffer_load_dwordx4 v22, s[44:47], s52 offen lds\n"
"v_add_f32 v29, v29, v68\n"
"v_mfma_f32_16x16x32_bf16 a[208:211], a[176:179], v[60:63], a[208:211]\n"
"buffer_load_dwordx4 v23, s[44:47], s52 offen offset:1024 lds\n"
"v_exp_f32 v70, v70\n"
"v_mfma_f32_16x16x32_bf16 a[212:215], a[180:183], v[60:63], a[212:215]\n"
"buffer_load_dwordx4 v24, s[44:47], s52 offen offset:2048 lds\n"
"v_cvt_pk_bf16_f32 v58, v68, v69\n"
"v_add_f32 v29, v29, v69\n"
"v_mfma_f32_16x16x32_bf16 a[216:219], a[184:187], v[60:63], a[216:219]\n"
"buffer_load_dwordx4 v25, s[44:47], s52 offen offset:3072 lds\n"
"v_exp_f32 v71, v71\n"
"v_mfma_f32_16x16x32_bf16 a[220:223], a[188:191], v[60:63], a[220:223]\n"
"v_add_f32 v29, v29, v70\n"
"v_mfma_f32_16x16x32_bf16 v[64:67], a[64:67], v[112:115], v[44:47]\n"
"v_exp_f32 v72, v72\n"
"v_mfma_f32_16x16x32_bf16 v[64:67], a[68:71], v[116:119], v[64:67]\n"
"v_cvt_pk_bf16_f32 v59, v70, v71\n"
"v_add_f32 v29, v29, v71\n"
"v_mfma_f32_16x16x32_bf16 v[64:67], a[72:75], v[120:123], v[64:67]\n"
"v_exp_f32 v73, v73\n"
"v_mfma_f32_16x16x32_bf16 v[64:67], a[76:79], v[124:127], v[64:67]\n"
"s_waitcnt lgkmcnt(0)\n"
"s_barrier\n"
"v_mfma_f32_16x16x32_bf16 v[160:163], a[128:131], v[56:59], v[160:163]\n"
"s_mov_b32 m0, 122880\n"
"v_exp_f32 v74, v74\n"
"v_mfma_f32_16x16x32_bf16 v[164:167], a[132:135], v[56:59], v[164:167]\n"
"ds_read_b64_tr_b16 a[160:161], v8 offset:24576\n"
"s_waitcnt vmcnt(52)\n"
"v_cvt_pk_bf16_f32 v60, v72, v73\n"
"v_mfma_f32_16x16x32_bf16 v[168:171], a[136:139], v[56:59], v[168:171]\n"
"buffer_load_dwordx4 v14, s[36:39], s62 offen lds\n"
"v_exp_f32 v75, v75\n"
"v_mfma_f32_16x16x32_bf16 v[172:175], a[140:143], v[56:59], v[172:175]\n"
"ds_read_b64_tr_b16 a[162:163], v8 offset:28672\n"
"ds_read_b64_tr_b16 a[164:165], v9 offset:24576\n"
"v_mfma_f32_16x16x32_bf16 v[68:71], a[80:83], v[112:115], v[44:47]\n"
"buffer_load_dwordx4 v15, s[36:39], s62 offen offset:1024 lds\n"
"v_exp_f32 v76, v76\n"
"v_mfma_f32_16x16x32_bf16 v[68:71], a[84:87], v[116:119], v[68:71]\n"
"ds_read_b64_tr_b16 a[166:167], v9 offset:28672\n"
"v_cvt_pk_bf16_f32 v61, v74, v75\n"
"v_mfma_f32_16x16x32_bf16 v[68:71], a[88:91], v[120:123], v[68:71]\n"
"buffer_load_dwordx4 v14, s[36:39], s62 offen offset:2048 lds\n"
"v_exp_f32 v77, v77\n"
"v_mfma_f32_16x16x32_bf16 v[68:71], a[92:95], v[124:127], v[68:71]\n"
"ds_read_b64_tr_b16 a[168:169], v8 offset:25088\n"
"ds_read_b64_tr_b16 a[170:171], v8 offset:29184\n"
"v_mfma_f32_16x16x32_bf16 v[176:179], a[144:147], v[56:59], v[176:179]\n"
"buffer_load_dwordx4 v15, s[36:39], s62 offen offset:3072 lds\n"
"v_exp_f32 v78, v78\n"
"v_mfma_f32_16x16x32_bf16 v[180:183], a[148:151], v[56:59], v[180:183]\n"
"ds_read_b64_tr_b16 a[172:173], v9 offset:25088\n"
"v_cvt_pk_bf16_f32 v62, v76, v77\n"
"v_mfma_f32_16x16x32_bf16 v[184:187], a[152:155], v[56:59], v[184:187]\n"
"v_exp_f32 v79, v79\n"
"v_mfma_f32_16x16x32_bf16 v[188:191], a[156:159], v[56:59], v[188:191]\n"
"ds_read_b64_tr_b16 a[174:175], v9 offset:29184\n"
"v_mfma_f32_16x16x32_bf16 v[72:75], a[64:67], v[128:131], v[48:51]\n"
"v_exp_f32 v64, v64\n"
"v_mfma_f32_16x16x32_bf16 v[72:75], a[68:71], v[132:135], v[72:75]\n"
"ds_read_b64_tr_b16 a[176:177], v8 offset:25600\n"
"v_cvt_pk_bf16_f32 v63, v78, v79\n"
"v_mfma_f32_16x16x32_bf16 v[72:75], a[72:75], v[136:139], v[72:75]\n"
"v_exp_f32 v65, v65\n"
"v_mfma_f32_16x16x32_bf16 v[72:75], a[76:79], v[140:143], v[72:75]\n"
"ds_read_b64_tr_b16 a[178:179], v8 offset:29696\n"
"v_add_f32 v31, v31, v64\n"
"v_mfma_f32_16x16x32_bf16 v[192:195], a[128:131], v[60:63], v[192:195]\n"
"s_mov_b32 m0, 126976\n"
"v_exp_f32 v66, v66\n"
"v_mfma_f32_16x16x32_bf16 v[196:199], a[132:135], v[60:63], v[196:199]\n"
"ds_read_b64_tr_b16 a[180:181], v9 offset:25600\n"
"v_cvt_pk_bf16_f32 v56, v64, v65\n"
"v_add_f32 v31, v31, v65\n"
"v_mfma_f32_16x16x32_bf16 v[200:203], a[136:139], v[60:63], v[200:203]\n"
"buffer_load_dwordx4 v14, s[36:39], s61 offen lds\n"
"v_exp_f32 v67, v67\n"
"v_mfma_f32_16x16x32_bf16 v[204:207], a[140:143], v[60:63], v[204:207]\n"
"ds_read_b64_tr_b16 a[182:183], v9 offset:29696\n"
"v_add_f32 v31, v31, v66\n"
"v_mfma_f32_16x16x32_bf16 v[18:21], a[224:227], v[60:63], v[18:21]\n"
"v_mfma_f32_16x16x32_bf16 v[76:79], a[80:83], v[128:131], v[48:51]\n"
"buffer_load_dwordx4 v15, s[36:39], s61 offen offset:1024 lds\n"
"v_exp_f32 v68, v68\n"
"v_mfma_f32_16x16x32_bf16 v[76:79], a[84:87], v[132:135], v[76:79]\n"
"ds_read_b64_tr_b16 a[184:185], v8 offset:26112\n"
"v_cvt_pk_bf16_f32 v57, v66, v67\n"
"v_add_f32 v31, v31, v67\n"
"v_mfma_f32_16x16x32_bf16 v[76:79], a[88:91], v[136:139], v[76:79]\n"
"buffer_load_dwordx4 v14, s[36:39], s61 offen offset:2048 lds\n"
"v_exp_f32 v69, v69\n"
"v_mfma_f32_16x16x32_bf16 v[76:79], a[92:95], v[140:143], v[76:79]\n"
"ds_read_b64_tr_b16 a[186:187], v8 offset:30208\n"
"v_add_f32 v31, v31, v68\n"
"v_mfma_f32_16x16x32_bf16 v[208:211], a[144:147], v[60:63], v[208:211]\n"
"buffer_load_dwordx4 v15, s[36:39], s61 offen offset:3072 lds\n"
"v_exp_f32 v70, v70\n"
"v_mfma_f32_16x16x32_bf16 v[212:215], a[148:151], v[60:63], v[212:215]\n"
"ds_read_b64_tr_b16 a[188:189], v9 offset:26112\n"
"v_cvt_pk_bf16_f32 v58, v68, v69\n"
"v_add_f32 v31, v31, v69\n"
"v_mfma_f32_16x16x32_bf16 v[216:219], a[152:155], v[60:63], v[216:219]\n"
"s_add_u32 s62, s62, 8192\n"
"s_and_b32 s62, s62, 0x3ffff\n"
"s_add_u32 s61, s62, 4096\n"
"v_exp_f32 v71, v71\n"
"v_mfma_f32_16x16x32_bf16 v[220:223], a[156:159], v[60:63], v[220:223]\n"
"ds_read_b64_tr_b16 a[190:191], v9 offset:30208\n"
"v_add_f32 v31, v31, v70\n"
"v_mfma_f32_16x16x32_bf16 v[64:67], a[96:99], v[80:83], v[36:39]\n"
"v_exp_f32 v72, v72\n"
"v_mfma_f32_16x16x32_bf16 v[64:67], a[100:103], v[84:87], v[64:67]\n"
"v_cvt_pk_bf16_f32 v59, v70, v71\n"
"v_add_f32 v31, v31, v71\n"
"v_mfma_f32_16x16x32_bf16 v[64:67], a[104:107], v[88:91], v[64:67]\n"
"v_exp_f32 v73, v73\n"
"v_mfma_f32_16x16x32_bf16 v[64:67], a[108:111], v[92:95], v[64:67]\n"
"ds_read_b128 a[64:67], v4 offset:32768\n"
"v_add_f32 v34, v34, v72\n"
"v_mfma_f32_16x16x32_bf16 v[224:227], a[128:131], v[56:59], v[224:227]\n"
"s_mov_b32 m0, 0\n"
"v_exp_f32 v74, v74\n"
"v_mfma_f32_16x16x32_bf16 v[228:231], a[132:135], v[56:59], v[228:231]\n"
"ds_read_b128 a[68:71], v5 offset:32768\n"
"s_waitcnt vmcnt(52)\n"
"v_cvt_pk_bf16_f32 v60, v72, v73\n"
"v_add_f32 v34, v34, v73\n"
"v_mfma_f32_16x16x32_bf16 v[232:235], a[136:139], v[56:59], v[232:235]\n"
"buffer_load_dwordx4 v10, s[28:31], s62 offen lds\n"
"v_exp_f32 v75, v75\n"
"v_mfma_f32_16x16x32_bf16 v[236:239], a[140:143], v[56:59], v[236:239]\n"
"ds_read_b128 a[72:75], v6 offset:32768\n"
"buffer_load_dwordx4 v11, s[28:31], s62 offen offset:1024 lds\n"
"v_add_f32 v34, v34, v74\n"
"v_mfma_f32_16x16x32_bf16 v[68:71], a[112:115], v[80:83], v[36:39]\n"
"buffer_load_dwordx4 v12, s[28:31], s62 offen offset:2048 lds\n"
"v_exp_f32 v76, v76\n"
"v_mfma_f32_16x16x32_bf16 v[68:71], a[116:119], v[84:87], v[68:71]\n"
"ds_read_b128 a[76:79], v7 offset:32768\n"
"buffer_load_dwordx4 v13, s[28:31], s62 offen offset:3072 lds\n"
"v_cvt_pk_bf16_f32 v61, v74, v75\n"
"v_add_f32 v34, v34, v75\n"
"v_mfma_f32_16x16x32_bf16 v[68:71], a[120:123], v[88:91], v[68:71]\n"
"s_mov_b32 m0, 143360\n"
"s_add_u32 s52, s51, 1572864\n"
"v_exp_f32 v77, v77\n"
"v_mfma_f32_16x16x32_bf16 v[68:71], a[124:127], v[92:95], v[68:71]\n"
"ds_read_b128 a[80:83], v4 offset:36864\n"
"buffer_load_dwordx4 v22, s[44:47], s52 offen lds\n"
"v_add_f32 v34, v34, v76\n"
"v_mfma_f32_16x16x32_bf16 v[240:243], a[144:147], v[56:59], v[240:243]\n"
"buffer_load_dwordx4 v23, s[44:47], s52 offen offset:1024 lds\n"
"v_exp_f32 v78, v78\n"
"v_mfma_f32_16x16x32_bf16 v[244:247], a[148:151], v[56:59], v[244:247]\n"
"ds_read_b128 a[84:87], v5 offset:36864\n"
"buffer_load_dwordx4 v24, s[44:47], s52 offen offset:2048 lds\n"
"v_cvt_pk_bf16_f32 v62, v76, v77\n"
"v_add_f32 v34, v34, v77\n"
"v_mfma_f32_16x16x32_bf16 v[248:251], a[152:155], v[56:59], v[248:251]\n"
"buffer_load_dwordx4 v25, s[44:47], s52 offen offset:3072 lds\n"
"v_exp_f32 v79, v79\n"
"v_mfma_f32_16x16x32_bf16 v[252:255], a[156:159], v[56:59], v[252:255]\n"
"v_add_f32 v34, v34, v78\n"
"v_mfma_f32_16x16x32_bf16 v[72:75], a[96:99], v[96:99], v[40:43]\n"
"v_exp_f32 v64, v64\n"
"v_mfma_f32_16x16x32_bf16 v[72:75], a[100:103], v[100:103], v[72:75]\n"
"v_cvt_pk_bf16_f32 v63, v78, v79\n"
"v_add_f32 v34, v34, v79\n"
"v_mfma_f32_16x16x32_bf16 v[72:75], a[104:107], v[104:107], v[72:75]\n"
"v_exp_f32 v65, v65\n"
"v_mfma_f32_16x16x32_bf16 v[72:75], a[108:111], v[108:111], v[72:75]\n"
"ds_read_b128 a[88:91], v6 offset:36864\n"
"v_add_f32 v29, v29, v64\n"
"v_mfma_f32_16x16x32_bf16 a[192:195], a[128:131], v[60:63], a[192:195]\n"
"s_mov_b32 m0, 4096\n"
"v_exp_f32 v66, v66\n"
"v_mfma_f32_16x16x32_bf16 a[196:199], a[132:135], v[60:63], a[196:199]\n"
"s_waitcnt vmcnt(52)\n"
"v_cvt_pk_bf16_f32 v56, v64, v65\n"
"v_add_f32 v29, v29, v65\n"
"v_mfma_f32_16x16x32_bf16 a[200:203], a[136:139], v[60:63], a[200:203]\n"
"buffer_load_dwordx4 v10, s[28:31], s61 offen lds\n"
"v_exp_f32 v67, v67\n"
"v_mfma_f32_16x16x32_bf16 a[204:207], a[140:143], v[60:63], a[204:207]\n"
"ds_read_b128 a[92:95], v7 offset:36864\n"
"v_add_f32 v29, v29, v66\n"
"v_mfma_f32_16x16x32_bf16 v[76:79], a[112:115], v[96:99], v[40:43]\n"
"buffer_load_dwordx4 v11, s[28:31], s61 offen offset:1024 lds\n"
"v_exp_f32 v68, v68\n"
"v_mfma_f32_16x16x32_bf16 v[76:79], a[116:119], v[100:103], v[76:79]\n"
"v_cvt_pk_bf16_f32 v57, v66, v67\n"
"v_add_f32 v29, v29, v67\n"
"v_mfma_f32_16x16x32_bf16 v[76:79], a[120:123], v[104:107], v[76:79]\n"
"buffer_load_dwordx4 v12, s[28:31], s61 offen offset:2048 lds\n"
"v_exp_f32 v69, v69\n"
"v_mfma_f32_16x16x32_bf16 v[76:79], a[124:127], v[108:111], v[76:79]\n"
"v_add_f32 v29, v29, v68\n"
"v_mfma_f32_16x16x32_bf16 a[208:211], a[144:147], v[60:63], a[208:211]\n"
"buffer_load_dwordx4 v13, s[28:31], s61 offen offset:3072 lds\n"
"v_exp_f32 v70, v70\n"
"v_mfma_f32_16x16x32_bf16 a[212:215], a[148:151], v[60:63], a[212:215]\n"
"v_cvt_pk_bf16_f32 v58, v68, v69\n"
"v_add_f32 v29, v29, v69\n"
"v_mfma_f32_16x16x32_bf16 a[216:219], a[152:155], v[60:63], a[216:219]\n"
"v_exp_f32 v71, v71\n"
"v_mfma_f32_16x16x32_bf16 a[220:223], a[156:159], v[60:63], a[220:223]\n"
"v_add_f32 v29, v29, v70\n"
"v_mfma_f32_16x16x32_bf16 v[64:67], a[96:99], v[112:115], v[44:47]\n"
"v_exp_f32 v72, v72\n"
"v_mfma_f32_16x16x32_bf16 v[64:67], a[100:103], v[116:119], v[64:67]\n"
"v_cvt_pk_bf16_f32 v59, v70, v71\n"
"v_add_f32 v29, v29, v71\n"
"v_mfma_f32_16x16x32_bf16 v[64:67], a[104:107], v[120:123], v[64:67]\n"
"global_load_dword v33, v32, s[18:19]\n"
"v_exp_f32 v73, v73\n"
"v_mfma_f32_16x16x32_bf16 v[64:67], a[108:111], v[124:127], v[64:67]\n"
"s_waitcnt lgkmcnt(0)\n"
"s_barrier\n"
"v_mfma_f32_16x16x32_bf16 v[160:163], a[160:163], v[56:59], v[160:163]\n"
"s_mov_b32 m0, 65536\n"
"v_exp_f32 v74, v74\n"
"v_mfma_f32_16x16x32_bf16 v[164:167], a[164:167], v[56:59], v[164:167]\n"
"ds_read_b64_tr_b16 a[128:129], v8 offset:32768\n"
"s_waitcnt vmcnt(52)\n"
"v_cvt_pk_bf16_f32 v60, v72, v73\n"
"v_mfma_f32_16x16x32_bf16 v[168:171], a[168:171], v[56:59], v[168:171]\n"
"buffer_load_dwordx4 v14, s[36:39], s62 offen lds\n"
"v_exp_f32 v75, v75\n"
"v_mfma_f32_16x16x32_bf16 v[172:175], a[172:175], v[56:59], v[172:175]\n"
"ds_read_b64_tr_b16 a[130:131], v8 offset:36864\n"
"ds_read_b64_tr_b16 a[132:133], v9 offset:32768\n"
"v_mfma_f32_16x16x32_bf16 v[68:71], a[112:115], v[112:115], v[44:47]\n"
"buffer_load_dwordx4 v15, s[36:39], s62 offen offset:1024 lds\n"
"v_exp_f32 v76, v76\n"
"v_mfma_f32_16x16x32_bf16 v[68:71], a[116:119], v[116:119], v[68:71]\n"
"ds_read_b64_tr_b16 a[134:135], v9 offset:36864\n"
"v_cvt_pk_bf16_f32 v61, v74, v75\n"
"v_mfma_f32_16x16x32_bf16 v[68:71], a[120:123], v[120:123], v[68:71]\n"
"buffer_load_dwordx4 v14, s[36:39], s62 offen offset:2048 lds\n"
"v_exp_f32 v77, v77\n"
"v_mfma_f32_16x16x32_bf16 v[68:71], a[124:127], v[124:127], v[68:71]\n"
"ds_read_b64_tr_b16 a[136:137], v8 offset:33280\n"
"ds_read_b64_tr_b16 a[138:139], v8 offset:37376\n"
"v_mfma_f32_16x16x32_bf16 v[176:179], a[176:179], v[56:59], v[176:179]\n"
"buffer_load_dwordx4 v15, s[36:39], s62 offen offset:3072 lds\n"
"v_exp_f32 v78, v78\n"
"v_mfma_f32_16x16x32_bf16 v[180:183], a[180:183], v[56:59], v[180:183]\n"
"ds_read_b64_tr_b16 a[140:141], v9 offset:33280\n"
"v_cvt_pk_bf16_f32 v62, v76, v77\n"
"v_mfma_f32_16x16x32_bf16 v[184:187], a[184:187], v[56:59], v[184:187]\n"
"v_exp_f32 v79, v79\n"
"v_mfma_f32_16x16x32_bf16 v[188:191], a[188:191], v[56:59], v[188:191]\n"
"ds_read_b64_tr_b16 a[142:143], v9 offset:37376\n"
"v_mfma_f32_16x16x32_bf16 v[72:75], a[96:99], v[128:131], v[48:51]\n"
"v_exp_f32 v64, v64\n"
"v_mfma_f32_16x16x32_bf16 v[72:75], a[100:103], v[132:135], v[72:75]\n"
"ds_read_b64_tr_b16 a[144:145], v8 offset:33792\n"
"v_cvt_pk_bf16_f32 v63, v78, v79\n"
"v_mfma_f32_16x16x32_bf16 v[72:75], a[104:107], v[136:139], v[72:75]\n"
"v_exp_f32 v65, v65\n"
"v_mfma_f32_16x16x32_bf16 v[72:75], a[108:111], v[140:143], v[72:75]\n"
"ds_read_b64_tr_b16 a[146:147], v8 offset:37888\n"
"v_add_f32 v31, v31, v64\n"
"v_mfma_f32_16x16x32_bf16 v[192:195], a[160:163], v[60:63], v[192:195]\n"
"s_mov_b32 m0, 69632\n"
"v_exp_f32 v66, v66\n"
"v_mfma_f32_16x16x32_bf16 v[196:199], a[164:167], v[60:63], v[196:199]\n"
"ds_read_b64_tr_b16 a[148:149], v9 offset:33792\n"
"s_waitcnt vmcnt(52)\n"
"v_cvt_pk_bf16_f32 v56, v64, v65\n"
"v_add_f32 v31, v31, v65\n"
"v_mfma_f32_16x16x32_bf16 v[200:203], a[168:171], v[60:63], v[200:203]\n"
"buffer_load_dwordx4 v14, s[36:39], s61 offen lds\n"
"v_exp_f32 v67, v67\n"
"v_mfma_f32_16x16x32_bf16 v[204:207], a[172:175], v[60:63], v[204:207]\n"
"ds_read_b64_tr_b16 a[150:151], v9 offset:37888\n"
"buffer_load_dwordx4 v15, s[36:39], s61 offen offset:1024 lds\n"
"v_add_f32 v31, v31, v66\n"
"v_mfma_f32_16x16x32_bf16 v[18:21], a[224:227], v[60:63], v[18:21]\n"
"v_mfma_f32_16x16x32_bf16 v[76:79], a[112:115], v[128:131], v[48:51]\n"
"buffer_load_dwordx4 v14, s[36:39], s61 offen offset:2048 lds\n"
"v_exp_f32 v68, v68\n"
"v_mfma_f32_16x16x32_bf16 v[76:79], a[116:119], v[132:135], v[76:79]\n"
"ds_read_b64_tr_b16 a[152:153], v8 offset:34304\n"
"buffer_load_dwordx4 v15, s[36:39], s61 offen offset:3072 lds\n"
"v_cvt_pk_bf16_f32 v57, v66, v67\n"
"v_add_f32 v31, v31, v67\n"
"v_mfma_f32_16x16x32_bf16 v[76:79], a[120:123], v[136:139], v[76:79]\n"
"s_add_u32 s62, s62, 8192\n"
"s_and_b32 s62, s62, 0x3ffff\n"
"s_add_u32 s61, s62, 4096\n"
"s_mov_b32 m0, 147456\n"
"s_add_u32 s52, s51, 32768\n"
"v_exp_f32 v69, v69\n"
"v_mfma_f32_16x16x32_bf16 v[76:79], a[124:127], v[140:143], v[76:79]\n"
"ds_read_b64_tr_b16 a[154:155], v8 offset:38400\n"
"buffer_load_dwordx4 v22, s[44:47], s52 offen lds\n"
"v_add_f32 v31, v31, v68\n"
"v_mfma_f32_16x16x32_bf16 v[208:211], a[176:179], v[60:63], v[208:211]\n"
"buffer_load_dwordx4 v23, s[44:47], s52 offen offset:1024 lds\n"
"v_exp_f32 v70, v70\n"
"v_mfma_f32_16x16x32_bf16 v[212:215], a[180:183], v[60:63], v[212:215]\n"
"ds_read_b64_tr_b16 a[156:157], v9 offset:34304\n"
"buffer_load_dwordx4 v24, s[44:47], s52 offen offset:2048 lds\n"
"v_cvt_pk_bf16_f32 v58, v68, v69\n"
"v_add_f32 v31, v31, v69\n"
"v_mfma_f32_16x16x32_bf16 v[216:219], a[184:187], v[60:63], v[216:219]\n"
"buffer_load_dwordx4 v25, s[44:47], s52 offen offset:3072 lds\n"
"v_exp_f32 v71, v71\n"
"v_mfma_f32_16x16x32_bf16 v[220:223], a[188:191], v[60:63], v[220:223]\n"
"ds_read_b64_tr_b16 a[158:159], v9 offset:38400\n"
"v_add_f32 v31, v31, v70\n"
"v_mfma_f32_16x16x32_bf16 v[64:67], a[64:67], v[80:83], v[36:39]\n"
"v_exp_f32 v72, v72\n"
"v_mfma_f32_16x16x32_bf16 v[64:67], a[68:71], v[84:87], v[64:67]\n"
"v_cvt_pk_bf16_f32 v59, v70, v71\n"
"v_add_f32 v31, v31, v71\n"
"v_mfma_f32_16x16x32_bf16 v[64:67], a[72:75], v[88:91], v[64:67]\n"
"v_exp_f32 v73, v73\n"
"v_mfma_f32_16x16x32_bf16 v[64:67], a[76:79], v[92:95], v[64:67]\n"
"ds_read_b128 a[96:99], v4 offset:40960\n"
"v_add_f32 v34, v34, v72\n"
"v_mfma_f32_16x16x32_bf16 v[224:227], a[160:163], v[56:59], v[224:227]\n"
"s_mov_b32 m0, 8192\n"
"v_exp_f32 v74, v74\n"
"v_mfma_f32_16x16x32_bf16 v[228:231], a[164:167], v[56:59], v[228:231]\n"
"ds_read_b128 a[100:103], v5 offset:40960\n"
"s_waitcnt vmcnt(52)\n"
"v_cvt_pk_bf16_f32 v60, v72, v73\n"
"v_add_f32 v34, v34, v73\n"
"v_mfma_f32_16x16x32_bf16 v[232:235], a[168:171], v[56:59], v[232:235]\n"
"buffer_load_dwordx4 v10, s[28:31], s62 offen lds\n"
"v_exp_f32 v75, v75\n"
"v_mfma_f32_16x16x32_bf16 v[236:239], a[172:175], v[56:59], v[236:239]\n"
"ds_read_b128 a[104:107], v6 offset:40960\n"
"v_add_f32 v34, v34, v74\n"
"v_mfma_f32_16x16x32_bf16 v[68:71], a[80:83], v[80:83], v[36:39]\n"
"buffer_load_dwordx4 v11, s[28:31], s62 offen offset:1024 lds\n"
"v_exp_f32 v76, v76\n"
"v_mfma_f32_16x16x32_bf16 v[68:71], a[84:87], v[84:87], v[68:71]\n"
"ds_read_b128 a[108:111], v7 offset:40960\n"
"v_cvt_pk_bf16_f32 v61, v74, v75\n"
"v_add_f32 v34, v34, v75\n"
"v_mfma_f32_16x16x32_bf16 v[68:71], a[88:91], v[88:91], v[68:71]\n"
"buffer_load_dwordx4 v12, s[28:31], s62 offen offset:2048 lds\n"
"v_exp_f32 v77, v77\n"
"v_mfma_f32_16x16x32_bf16 v[68:71], a[92:95], v[92:95], v[68:71]\n"
"ds_read_b128 a[112:115], v4 offset:45056\n"
"v_add_f32 v34, v34, v76\n"
"v_mfma_f32_16x16x32_bf16 v[240:243], a[176:179], v[56:59], v[240:243]\n"
"buffer_load_dwordx4 v13, s[28:31], s62 offen offset:3072 lds\n"
"v_exp_f32 v78, v78\n"
"v_mfma_f32_16x16x32_bf16 v[244:247], a[180:183], v[56:59], v[244:247]\n"
"ds_read_b128 a[116:119], v5 offset:45056\n"
"v_cvt_pk_bf16_f32 v62, v76, v77\n"
"v_add_f32 v34, v34, v77\n"
"v_mfma_f32_16x16x32_bf16 v[248:251], a[184:187], v[56:59], v[248:251]\n"
"v_exp_f32 v79, v79\n"
"v_mfma_f32_16x16x32_bf16 v[252:255], a[188:191], v[56:59], v[252:255]\n"
"v_add_f32 v34, v34, v78\n"
"v_mfma_f32_16x16x32_bf16 v[72:75], a[64:67], v[96:99], v[40:43]\n"
"v_exp_f32 v64, v64\n"
"v_mfma_f32_16x16x32_bf16 v[72:75], a[68:71], v[100:103], v[72:75]\n"
"v_cvt_pk_bf16_f32 v63, v78, v79\n"
"v_add_f32 v34, v34, v79\n"
"v_mfma_f32_16x16x32_bf16 v[72:75], a[72:75], v[104:107], v[72:75]\n"
"v_exp_f32 v65, v65\n"
"v_mfma_f32_16x16x32_bf16 v[72:75], a[76:79], v[108:111], v[72:75]\n"
"ds_read_b128 a[120:123], v6 offset:45056\n"
"v_add_f32 v29, v29, v64\n"
"v_mfma_f32_16x16x32_bf16 a[192:195], a[160:163], v[60:63], a[192:195]\n"
"s_mov_b32 m0, 12288\n"
"v_exp_f32 v66, v66\n"
"v_mfma_f32_16x16x32_bf16 a[196:199], a[164:167], v[60:63], a[196:199]\n"
"v_cvt_pk_bf16_f32 v56, v64, v65\n"
"v_add_f32 v29, v29, v65\n"
"v_mfma_f32_16x16x32_bf16 a[200:203], a[168:171], v[60:63], a[200:203]\n"
"buffer_load_dwordx4 v10, s[28:31], s61 offen lds\n"
"v_exp_f32 v67, v67\n"
"v_mfma_f32_16x16x32_bf16 a[204:207], a[172:175], v[60:63], a[204:207]\n"
"ds_read_b128 a[124:127], v7 offset:45056\n"
"v_add_f32 v29, v29, v66\n"
"v_mfma_f32_16x16x32_bf16 v[76:79], a[80:83], v[96:99], v[40:43]\n"
"buffer_load_dwordx4 v11, s[28:31], s61 offen offset:1024 lds\n"
"v_exp_f32 v68, v68\n"
"v_mfma_f32_16x16x32_bf16 v[76:79], a[84:87], v[100:103], v[76:79]\n"
"v_cvt_pk_bf16_f32 v57, v66, v67\n"
"v_add_f32 v29, v29, v67\n"
"v_mfma_f32_16x16x32_bf16 v[76:79], a[88:91], v[104:107], v[76:79]\n"
"buffer_load_dwordx4 v12, s[28:31], s61 offen offset:2048 lds\n"
"v_exp_f32 v69, v69\n"
"v_mfma_f32_16x16x32_bf16 v[76:79], a[92:95], v[108:111], v[76:79]\n"
"v_add_f32 v29, v29, v68\n"
"v_mfma_f32_16x16x32_bf16 a[208:211], a[176:179], v[60:63], a[208:211]\n"
"buffer_load_dwordx4 v13, s[28:31], s61 offen offset:3072 lds\n"
"v_exp_f32 v70, v70\n"
"v_mfma_f32_16x16x32_bf16 a[212:215], a[180:183], v[60:63], a[212:215]\n"
"v_cvt_pk_bf16_f32 v58, v68, v69\n"
"v_add_f32 v29, v29, v69\n"
"v_mfma_f32_16x16x32_bf16 a[216:219], a[184:187], v[60:63], a[216:219]\n"
"v_exp_f32 v71, v71\n"
"v_mfma_f32_16x16x32_bf16 a[220:223], a[188:191], v[60:63], a[220:223]\n"
"v_add_f32 v29, v29, v70\n"
"v_mfma_f32_16x16x32_bf16 v[64:67], a[64:67], v[112:115], v[44:47]\n"
"v_exp_f32 v72, v72\n"
"v_mfma_f32_16x16x32_bf16 v[64:67], a[68:71], v[116:119], v[64:67]\n"
"v_cvt_pk_bf16_f32 v59, v70, v71\n"
"v_add_f32 v29, v29, v71\n"
"v_mfma_f32_16x16x32_bf16 v[64:67], a[72:75], v[120:123], v[64:67]\n"
"v_exp_f32 v73, v73\n"
"v_mfma_f32_16x16x32_bf16 v[64:67], a[76:79], v[124:127], v[64:67]\n"
"s_waitcnt lgkmcnt(0)\n"
"s_barrier\n"
"v_mfma_f32_16x16x32_bf16 v[160:163], a[128:131], v[56:59], v[160:163]\n"
"s_mov_b32 m0, 73728\n"
"v_exp_f32 v74, v74\n"
"v_mfma_f32_16x16x32_bf16 v[164:167], a[132:135], v[56:59], v[164:167]\n"
"ds_read_b64_tr_b16 a[160:161], v8 offset:40960\n"
"s_waitcnt vmcnt(52)\n"
"v_cvt_pk_bf16_f32 v60, v72, v73\n"
"v_mfma_f32_16x16x32_bf16 v[168:171], a[136:139], v[56:59], v[168:171]\n"
"buffer_load_dwordx4 v14, s[36:39], s62 offen lds\n"
"v_exp_f32 v75, v75\n"
"v_mfma_f32_16x16x32_bf16 v[172:175], a[140:143], v[56:59], v[172:175]\n"
"ds_read_b64_tr_b16 a[162:163], v8 offset:45056\n"
"ds_read_b64_tr_b16 a[164:165], v9 offset:40960\n"
"buffer_load_dwordx4 v15, s[36:39], s62 offen offset:1024 lds\n"
"v_mfma_f32_16x16x32_bf16 v[68:71], a[80:83], v[112:115], v[44:47]\n"
"buffer_load_dwordx4 v14, s[36:39], s62 offen offset:2048 lds\n"
"v_exp_f32 v76, v76\n"
"v_mfma_f32_16x16x32_bf16 v[68:71], a[84:87], v[116:119], v[68:71]\n"
"ds_read_b64_tr_b16 a[166:167], v9 offset:45056\n"
"buffer_load_dwordx4 v15, s[36:39], s62 offen offset:3072 lds\n"
"v_cvt_pk_bf16_f32 v61, v74, v75\n"
"v_mfma_f32_16x16x32_bf16 v[68:71], a[88:91], v[120:123], v[68:71]\n"
"s_mov_b32 m0, 151552\n"
"s_add_u32 s52, s51, 557056\n"
"v_exp_f32 v77, v77\n"
"v_mfma_f32_16x16x32_bf16 v[68:71], a[92:95], v[124:127], v[68:71]\n"
"ds_read_b64_tr_b16 a[168:169], v8 offset:41472\n"
"ds_read_b64_tr_b16 a[170:171], v8 offset:45568\n"
"buffer_load_dwordx4 v22, s[44:47], s52 offen lds\n"
"v_mfma_f32_16x16x32_bf16 v[176:179], a[144:147], v[56:59], v[176:179]\n"
"buffer_load_dwordx4 v23, s[44:47], s52 offen offset:1024 lds\n"
"v_exp_f32 v78, v78\n"
"v_mfma_f32_16x16x32_bf16 v[180:183], a[148:151], v[56:59], v[180:183]\n"
"ds_read_b64_tr_b16 a[172:173], v9 offset:41472\n"
"buffer_load_dwordx4 v24, s[44:47], s52 offen offset:2048 lds\n"
"v_cvt_pk_bf16_f32 v62, v76, v77\n"
"v_mfma_f32_16x16x32_bf16 v[184:187], a[152:155], v[56:59], v[184:187]\n"
"buffer_load_dwordx4 v25, s[44:47], s52 offen offset:3072 lds\n"
"v_exp_f32 v79, v79\n"
"v_mfma_f32_16x16x32_bf16 v[188:191], a[156:159], v[56:59], v[188:191]\n"
"ds_read_b64_tr_b16 a[174:175], v9 offset:45568\n"
"v_mfma_f32_16x16x32_bf16 v[72:75], a[64:67], v[128:131], v[48:51]\n"
"v_exp_f32 v64, v64\n"
"v_mfma_f32_16x16x32_bf16 v[72:75], a[68:71], v[132:135], v[72:75]\n"
"ds_read_b64_tr_b16 a[176:177], v8 offset:41984\n"
"v_cvt_pk_bf16_f32 v63, v78, v79\n"
"v_mfma_f32_16x16x32_bf16 v[72:75], a[72:75], v[136:139], v[72:75]\n"
"v_exp_f32 v65, v65\n"
"v_mfma_f32_16x16x32_bf16 v[72:75], a[76:79], v[140:143], v[72:75]\n"
"ds_read_b64_tr_b16 a[178:179], v8 offset:46080\n"
"v_add_f32 v31, v31, v64\n"
"v_mfma_f32_16x16x32_bf16 v[192:195], a[128:131], v[60:63], v[192:195]\n"
"s_mov_b32 m0, 77824\n"
"v_exp_f32 v66, v66\n"
"v_mfma_f32_16x16x32_bf16 v[196:199], a[132:135], v[60:63], v[196:199]\n"
"ds_read_b64_tr_b16 a[180:181], v9 offset:41984\n"
"s_waitcnt vmcnt(52)\n"
"v_cvt_pk_bf16_f32 v56, v64, v65\n"
"v_add_f32 v31, v31, v65\n"
"v_mfma_f32_16x16x32_bf16 v[200:203], a[136:139], v[60:63], v[200:203]\n"
"buffer_load_dwordx4 v14, s[36:39], s61 offen lds\n"
"v_exp_f32 v67, v67\n"
"v_mfma_f32_16x16x32_bf16 v[204:207], a[140:143], v[60:63], v[204:207]\n"
"ds_read_b64_tr_b16 a[182:183], v9 offset:46080\n"
"v_add_f32 v31, v31, v66\n"
"v_mfma_f32_16x16x32_bf16 v[18:21], a[224:227], v[60:63], v[18:21]\n"
"v_mfma_f32_16x16x32_bf16 v[76:79], a[80:83], v[128:131], v[48:51]\n"
"buffer_load_dwordx4 v15, s[36:39], s61 offen offset:1024 lds\n"
"v_exp_f32 v68, v68\n"
"v_mfma_f32_16x16x32_bf16 v[76:79], a[84:87], v[132:135], v[76:79]\n"
"ds_read_b64_tr_b16 a[184:185], v8 offset:42496\n"
"v_cvt_pk_bf16_f32 v57, v66, v67\n"
"v_add_f32 v31, v31, v67\n"
"v_mfma_f32_16x16x32_bf16 v[76:79], a[88:91], v[136:139], v[76:79]\n"
"buffer_load_dwordx4 v14, s[36:39], s61 offen offset:2048 lds\n"
"v_exp_f32 v69, v69\n"
"v_mfma_f32_16x16x32_bf16 v[76:79], a[92:95], v[140:143], v[76:79]\n"
"ds_read_b64_tr_b16 a[186:187], v8 offset:46592\n"
"v_add_f32 v31, v31, v68\n"
"v_mfma_f32_16x16x32_bf16 v[208:211], a[144:147], v[60:63], v[208:211]\n"
"buffer_load_dwordx4 v15, s[36:39], s61 offen offset:3072 lds\n"
"v_exp_f32 v70, v70\n"
"v_mfma_f32_16x16x32_bf16 v[212:215], a[148:151], v[60:63], v[212:215]\n"
"ds_read_b64_tr_b16 a[188:189], v9 offset:42496\n"
"v_cvt_pk_bf16_f32 v58, v68, v69\n"
"v_add_f32 v31, v31, v69\n"
"v_mfma_f32_16x16x32_bf16 v[216:219], a[152:155], v[60:63], v[216:219]\n"
"s_add_u32 s62, s62, 8192\n"
"s_and_b32 s62, s62, 0x3ffff\n"
"s_add_u32 s61, s62, 4096\n"
"v_exp_f32 v71, v71\n"
"v_mfma_f32_16x16x32_bf16 v[220:223], a[156:159], v[60:63], v[220:223]\n"
"ds_read_b64_tr_b16 a[190:191], v9 offset:46592\n"
"v_add_f32 v31, v31, v70\n"
"v_mfma_f32_16x16x32_bf16 v[64:67], a[96:99], v[80:83], v[36:39]\n"
"v_exp_f32 v72, v72\n"
"v_mfma_f32_16x16x32_bf16 v[64:67], a[100:103], v[84:87], v[64:67]\n"
"v_cvt_pk_bf16_f32 v59, v70, v71\n"
"v_add_f32 v31, v31, v71\n"
"v_mfma_f32_16x16x32_bf16 v[64:67], a[104:107], v[88:91], v[64:67]\n"
"v_exp_f32 v73, v73\n"
"v_mfma_f32_16x16x32_bf16 v[64:67], a[108:111], v[92:95], v[64:67]\n"
"ds_read_b128 a[64:67], v4 offset:49152\n"
"v_add_f32 v34, v34, v72\n"
"v_mfma_f32_16x16x32_bf16 v[224:227], a[128:131], v[56:59], v[224:227]\n"
"s_mov_b32 m0, 16384\n"
"v_exp_f32 v74, v74\n"
"v_mfma_f32_16x16x32_bf16 v[228:231], a[132:135], v[56:59], v[228:231]\n"
"ds_read_b128 a[68:71], v5 offset:49152\n"
"v_cvt_pk_bf16_f32 v60, v72, v73\n"
"v_add_f32 v34, v34, v73\n"
"v_mfma_f32_16x16x32_bf16 v[232:235], a[136:139], v[56:59], v[232:235]\n"
"buffer_load_dwordx4 v10, s[28:31], s62 offen lds\n"
"v_exp_f32 v75, v75\n"
"v_mfma_f32_16x16x32_bf16 v[236:239], a[140:143], v[56:59], v[236:239]\n"
"ds_read_b128 a[72:75], v6 offset:49152\n"
"v_add_f32 v34, v34, v74\n"
"v_mfma_f32_16x16x32_bf16 v[68:71], a[112:115], v[80:83], v[36:39]\n"
"buffer_load_dwordx4 v11, s[28:31], s62 offen offset:1024 lds\n"
"v_exp_f32 v76, v76\n"
"v_mfma_f32_16x16x32_bf16 v[68:71], a[116:119], v[84:87], v[68:71]\n"
"ds_read_b128 a[76:79], v7 offset:49152\n"
"v_cvt_pk_bf16_f32 v61, v74, v75\n"
"v_add_f32 v34, v34, v75\n"
"v_mfma_f32_16x16x32_bf16 v[68:71], a[120:123], v[88:91], v[68:71]\n"
"buffer_load_dwordx4 v12, s[28:31], s62 offen offset:2048 lds\n"
"v_exp_f32 v77, v77\n"
"v_mfma_f32_16x16x32_bf16 v[68:71], a[124:127], v[92:95], v[68:71]\n"
"ds_read_b128 a[80:83], v4 offset:53248\n"
"v_add_f32 v34, v34, v76\n"
"v_mfma_f32_16x16x32_bf16 v[240:243], a[144:147], v[56:59], v[240:243]\n"
"buffer_load_dwordx4 v13, s[28:31], s62 offen offset:3072 lds\n"
"v_exp_f32 v78, v78\n"
"v_mfma_f32_16x16x32_bf16 v[244:247], a[148:151], v[56:59], v[244:247]\n"
"ds_read_b128 a[84:87], v5 offset:53248\n"
"v_cvt_pk_bf16_f32 v62, v76, v77\n"
"v_add_f32 v34, v34, v77\n"
"v_mfma_f32_16x16x32_bf16 v[248:251], a[152:155], v[56:59], v[248:251]\n"
"v_exp_f32 v79, v79\n"
"v_mfma_f32_16x16x32_bf16 v[252:255], a[156:159], v[56:59], v[252:255]\n"
"v_add_f32 v34, v34, v78\n"
"v_mfma_f32_16x16x32_bf16 v[72:75], a[96:99], v[96:99], v[40:43]\n"
"v_exp_f32 v64, v64\n"
"v_mfma_f32_16x16x32_bf16 v[72:75], a[100:103], v[100:103], v[72:75]\n"
"v_cvt_pk_bf16_f32 v63, v78, v79\n"
"v_add_f32 v34, v34, v79\n"
"v_mfma_f32_16x16x32_bf16 v[72:75], a[104:107], v[104:107], v[72:75]\n"
"v_exp_f32 v65, v65\n"
"v_mfma_f32_16x16x32_bf16 v[72:75], a[108:111], v[108:111], v[72:75]\n"
"ds_read_b128 a[88:91], v6 offset:53248\n"
"v_add_f32 v29, v29, v64\n"
"v_mfma_f32_16x16x32_bf16 a[192:195], a[128:131], v[60:63], a[192:195]\n"
"s_mov_b32 m0, 20480\n"
"v_exp_f32 v66, v66\n"
"v_mfma_f32_16x16x32_bf16 a[196:199], a[132:135], v[60:63], a[196:199]\n"
"s_waitcnt vmcnt(52)\n"
"v_cvt_pk_bf16_f32 v56, v64, v65\n"
"v_add_f32 v29, v29, v65\n"
"v_mfma_f32_16x16x32_bf16 a[200:203], a[136:139], v[60:63], a[200:203]\n"
"buffer_load_dwordx4 v10, s[28:31], s61 offen lds\n"
"v_exp_f32 v67, v67\n"
"v_mfma_f32_16x16x32_bf16 a[204:207], a[140:143], v[60:63], a[204:207]\n"
"ds_read_b128 a[92:95], v7 offset:53248\n"
"buffer_load_dwordx4 v11, s[28:31], s61 offen offset:1024 lds\n"
"v_add_f32 v29, v29, v66\n"
"v_mfma_f32_16x16x32_bf16 v[76:79], a[112:115], v[96:99], v[40:43]\n"
"buffer_load_dwordx4 v12, s[28:31], s61 offen offset:2048 lds\n"
"v_exp_f32 v68, v68\n"
"v_mfma_f32_16x16x32_bf16 v[76:79], a[116:119], v[100:103], v[76:79]\n"
"buffer_load_dwordx4 v13, s[28:31], s61 offen offset:3072 lds\n"
"v_cvt_pk_bf16_f32 v57, v66, v67\n"
"v_add_f32 v29, v29, v67\n"
"v_mfma_f32_16x16x32_bf16 v[76:79], a[120:123], v[104:107], v[76:79]\n"
"s_mov_b32 m0, 155648\n"
"s_add_u32 s52, s51, 1081344\n"
"v_exp_f32 v69, v69\n"
"v_mfma_f32_16x16x32_bf16 v[76:79], a[124:127], v[108:111], v[76:79]\n"
"buffer_load_dwordx4 v22, s[44:47], s52 offen lds\n"
"v_add_f32 v29, v29, v68\n"
"v_mfma_f32_16x16x32_bf16 a[208:211], a[144:147], v[60:63], a[208:211]\n"
"buffer_load_dwordx4 v23, s[44:47], s52 offen offset:1024 lds\n"
"v_exp_f32 v70, v70\n"
"v_mfma_f32_16x16x32_bf16 a[212:215], a[148:151], v[60:63], a[212:215]\n"
"buffer_load_dwordx4 v24, s[44:47], s52 offen offset:2048 lds\n"
"v_cvt_pk_bf16_f32 v58, v68, v69\n"
"v_add_f32 v29, v29, v69\n"
"v_mfma_f32_16x16x32_bf16 a[216:219], a[152:155], v[60:63], a[216:219]\n"
"buffer_load_dwordx4 v25, s[44:47], s52 offen offset:3072 lds\n"
"v_exp_f32 v71, v71\n"
"v_mfma_f32_16x16x32_bf16 a[220:223], a[156:159], v[60:63], a[220:223]\n"
"v_add_f32 v29, v29, v70\n"
"v_mfma_f32_16x16x32_bf16 v[64:67], a[96:99], v[112:115], v[44:47]\n"
"v_exp_f32 v72, v72\n"
"v_mfma_f32_16x16x32_bf16 v[64:67], a[100:103], v[116:119], v[64:67]\n"
"v_cvt_pk_bf16_f32 v59, v70, v71\n"
"v_add_f32 v29, v29, v71\n"
"v_mfma_f32_16x16x32_bf16 v[64:67], a[104:107], v[120:123], v[64:67]\n"
"global_load_dword v33, v32, s[18:19]\n"
"v_exp_f32 v73, v73\n"
"v_mfma_f32_16x16x32_bf16 v[64:67], a[108:111], v[124:127], v[64:67]\n"
"s_waitcnt lgkmcnt(0)\n"
"s_barrier\n"
"v_mfma_f32_16x16x32_bf16 v[160:163], a[160:163], v[56:59], v[160:163]\n"
"s_mov_b32 m0, 81920\n"
"v_exp_f32 v74, v74\n"
"v_mfma_f32_16x16x32_bf16 v[164:167], a[164:167], v[56:59], v[164:167]\n"
"ds_read_b64_tr_b16 a[128:129], v8 offset:49152\n"
"s_waitcnt vmcnt(52)\n"
"v_cvt_pk_bf16_f32 v60, v72, v73\n"
"v_mfma_f32_16x16x32_bf16 v[168:171], a[168:171], v[56:59], v[168:171]\n"
"buffer_load_dwordx4 v14, s[36:39], s62 offen lds\n"
"v_exp_f32 v75, v75\n"
"v_mfma_f32_16x16x32_bf16 v[172:175], a[172:175], v[56:59], v[172:175]\n"
"ds_read_b64_tr_b16 a[130:131], v8 offset:53248\n"
"ds_read_b64_tr_b16 a[132:133], v9 offset:49152\n"
"v_mfma_f32_16x16x32_bf16 v[68:71], a[112:115], v[112:115], v[44:47]\n"
"buffer_load_dwordx4 v15, s[36:39], s62 offen offset:1024 lds\n"
"v_exp_f32 v76, v76\n"
"v_mfma_f32_16x16x32_bf16 v[68:71], a[116:119], v[116:119], v[68:71]\n"
"ds_read_b64_tr_b16 a[134:135], v9 offset:53248\n"
"v_cvt_pk_bf16_f32 v61, v74, v75\n"
"v_mfma_f32_16x16x32_bf16 v[68:71], a[120:123], v[120:123], v[68:71]\n"
"buffer_load_dwordx4 v14, s[36:39], s62 offen offset:2048 lds\n"
"v_exp_f32 v77, v77\n"
"v_mfma_f32_16x16x32_bf16 v[68:71], a[124:127], v[124:127], v[68:71]\n"
"ds_read_b64_tr_b16 a[136:137], v8 offset:49664\n"
"ds_read_b64_tr_b16 a[138:139], v8 offset:53760\n"
"v_mfma_f32_16x16x32_bf16 v[176:179], a[176:179], v[56:59], v[176:179]\n"
"buffer_load_dwordx4 v15, s[36:39], s62 offen offset:3072 lds\n"
"v_exp_f32 v78, v78\n"
"v_mfma_f32_16x16x32_bf16 v[180:183], a[180:183], v[56:59], v[180:183]\n"
"ds_read_b64_tr_b16 a[140:141], v9 offset:49664\n"
"v_cvt_pk_bf16_f32 v62, v76, v77\n"
"v_mfma_f32_16x16x32_bf16 v[184:187], a[184:187], v[56:59], v[184:187]\n"
"v_exp_f32 v79, v79\n"
"v_mfma_f32_16x16x32_bf16 v[188:191], a[188:191], v[56:59], v[188:191]\n"
"ds_read_b64_tr_b16 a[142:143], v9 offset:53760\n"
"v_mfma_f32_16x16x32_bf16 v[72:75], a[96:99], v[128:131], v[48:51]\n"
"v_exp_f32 v64, v64\n"
"v_mfma_f32_16x16x32_bf16 v[72:75], a[100:103], v[132:135], v[72:75]\n"
"ds_read_b64_tr_b16 a[144:145], v8 offset:50176\n"
"v_cvt_pk_bf16_f32 v63, v78, v79\n"
"v_mfma_f32_16x16x32_bf16 v[72:75], a[104:107], v[136:139], v[72:75]\n"
"v_exp_f32 v65, v65\n"
"v_mfma_f32_16x16x32_bf16 v[72:75], a[108:111], v[140:143], v[72:75]\n"
"ds_read_b64_tr_b16 a[146:147], v8 offset:54272\n"
"v_add_f32 v31, v31, v64\n"
"v_mfma_f32_16x16x32_bf16 v[192:195], a[160:163], v[60:63], v[192:195]\n"
"s_mov_b32 m0, 86016\n"
"v_exp_f32 v66, v66\n"
"v_mfma_f32_16x16x32_bf16 v[196:199], a[164:167], v[60:63], v[196:199]\n"
"ds_read_b64_tr_b16 a[148:149], v9 offset:50176\n"
"v_cvt_pk_bf16_f32 v56, v64, v65\n"
"v_add_f32 v31, v31, v65\n"
"v_mfma_f32_16x16x32_bf16 v[200:203], a[168:171], v[60:63], v[200:203]\n"
"buffer_load_dwordx4 v14, s[36:39], s61 offen lds\n"
"v_exp_f32 v67, v67\n"
"v_mfma_f32_16x16x32_bf16 v[204:207], a[172:175], v[60:63], v[204:207]\n"
"ds_read_b64_tr_b16 a[150:151], v9 offset:54272\n"
"v_add_f32 v31, v31, v66\n"
"v_mfma_f32_16x16x32_bf16 v[18:21], a[224:227], v[60:63], v[18:21]\n"
"v_mfma_f32_16x16x32_bf16 v[76:79], a[112:115], v[128:131], v[48:51]\n"
"buffer_load_dwordx4 v15, s[36:39], s61 offen offset:1024 lds\n"
"v_exp_f32 v68, v68\n"
"v_mfma_f32_16x16x32_bf16 v[76:79], a[116:119], v[132:135], v[76:79]\n"
"ds_read_b64_tr_b16 a[152:153], v8 offset:50688\n"
"v_cvt_pk_bf16_f32 v57, v66, v67\n"
"v_add_f32 v31, v31, v67\n"
"v_mfma_f32_16x16x32_bf16 v[76:79], a[120:123], v[136:139], v[76:79]\n"
"buffer_load_dwordx4 v14, s[36:39], s61 offen offset:2048 lds\n"
"v_exp_f32 v69, v69\n"
"v_mfma_f32_16x16x32_bf16 v[76:79], a[124:127], v[140:143], v[76:79]\n"
"ds_read_b64_tr_b16 a[154:155], v8 offset:54784\n"
"v_add_f32 v31, v31, v68\n"
"v_mfma_f32_16x16x32_bf16 v[208:211], a[176:179], v[60:63], v[208:211]\n"
"buffer_load_dwordx4 v15, s[36:39], s61 offen offset:3072 lds\n"
"v_exp_f32 v70, v70\n"
"v_mfma_f32_16x16x32_bf16 v[212:215], a[180:183], v[60:63], v[212:215]\n"
"ds_read_b64_tr_b16 a[156:157], v9 offset:50688\n"
"v_cvt_pk_bf16_f32 v58, v68, v69\n"
"v_add_f32 v31, v31, v69\n"
"v_mfma_f32_16x16x32_bf16 v[216:219], a[184:187], v[60:63], v[216:219]\n"
"s_add_u32 s62, s62, 8192\n"
"s_and_b32 s62, s62, 0x3ffff\n"
"s_add_u32 s61, s62, 4096\n"
"v_exp_f32 v71, v71\n"
"v_mfma_f32_16x16x32_bf16 v[220:223], a[188:191], v[60:63], v[220:223]\n"
"ds_read_b64_tr_b16 a[158:159], v9 offset:54784\n"
"v_add_f32 v31, v31, v70\n"
"v_mfma_f32_16x16x32_bf16 v[64:67], a[64:67], v[80:83], v[36:39]\n"
"v_exp_f32 v72, v72\n"
"v_mfma_f32_16x16x32_bf16 v[64:67], a[68:71], v[84:87], v[64:67]\n"
"v_cvt_pk_bf16_f32 v59, v70, v71\n"
"v_add_f32 v31, v31, v71\n"
"v_mfma_f32_16x16x32_bf16 v[64:67], a[72:75], v[88:91], v[64:67]\n"
"v_exp_f32 v73, v73\n"
"v_mfma_f32_16x16x32_bf16 v[64:67], a[76:79], v[92:95], v[64:67]\n"
"ds_read_b128 a[96:99], v4 offset:57344\n"
"v_add_f32 v34, v34, v72\n"
"v_mfma_f32_16x16x32_bf16 v[224:227], a[160:163], v[56:59], v[224:227]\n"
"s_mov_b32 m0, 24576\n"
"v_exp_f32 v74, v74\n"
"v_mfma_f32_16x16x32_bf16 v[228:231], a[164:167], v[56:59], v[228:231]\n"
"ds_read_b128 a[100:103], v5 offset:57344\n"
"s_waitcnt vmcnt(52)\n"
"v_cvt_pk_bf16_f32 v60, v72, v73\n"
"v_add_f32 v34, v34, v73\n"
"v_mfma_f32_16x16x32_bf16 v[232:235], a[168:171], v[56:59], v[232:235]\n"
"buffer_load_dwordx4 v10, s[28:31], s62 offen lds\n"
"v_exp_f32 v75, v75\n"
"v_mfma_f32_16x16x32_bf16 v[236:239], a[172:175], v[56:59], v[236:239]\n"
"ds_read_b128 a[104:107], v6 offset:57344\n"
"buffer_load_dwordx4 v11, s[28:31], s62 offen offset:1024 lds\n"
"v_add_f32 v34, v34, v74\n"
"v_mfma_f32_16x16x32_bf16 v[68:71], a[80:83], v[80:83], v[36:39]\n"
"buffer_load_dwordx4 v12, s[28:31], s62 offen offset:2048 lds\n"
"v_exp_f32 v76, v76\n"
"v_mfma_f32_16x16x32_bf16 v[68:71], a[84:87], v[84:87], v[68:71]\n"
"ds_read_b128 a[108:111], v7 offset:57344\n"
"buffer_load_dwordx4 v13, s[28:31], s62 offen offset:3072 lds\n"
"v_cvt_pk_bf16_f32 v61, v74, v75\n"
"v_add_f32 v34, v34, v75\n"
"v_mfma_f32_16x16x32_bf16 v[68:71], a[88:91], v[88:91], v[68:71]\n"
"s_mov_b32 m0, 159744\n"
"s_add_u32 s52, s51, 1605632\n"
"v_exp_f32 v77, v77\n"
"v_mfma_f32_16x16x32_bf16 v[68:71], a[92:95], v[92:95], v[68:71]\n"
"ds_read_b128 a[112:115], v4 offset:61440\n"
"buffer_load_dwordx4 v22, s[44:47], s52 offen lds\n"
"v_add_f32 v34, v34, v76\n"
"v_mfma_f32_16x16x32_bf16 v[240:243], a[176:179], v[56:59], v[240:243]\n"
"buffer_load_dwordx4 v23, s[44:47], s52 offen offset:1024 lds\n"
"v_exp_f32 v78, v78\n"
"v_mfma_f32_16x16x32_bf16 v[244:247], a[180:183], v[56:59], v[244:247]\n"
"ds_read_b128 a[116:119], v5 offset:61440\n"
"buffer_load_dwordx4 v24, s[44:47], s52 offen offset:2048 lds\n"
"v_cvt_pk_bf16_f32 v62, v76, v77\n"
"v_add_f32 v34, v34, v77\n"
"v_mfma_f32_16x16x32_bf16 v[248:251], a[184:187], v[56:59], v[248:251]\n"
"buffer_load_dwordx4 v25, s[44:47], s52 offen offset:3072 lds\n"
"v_exp_f32 v79, v79\n"
"v_mfma_f32_16x16x32_bf16 v[252:255], a[188:191], v[56:59], v[252:255]\n"
"s_add_u32 s51, s51, 65536\n"
"v_add_f32 v34, v34, v78\n"
"v_mfma_f32_16x16x32_bf16 v[72:75], a[64:67], v[96:99], v[40:43]\n"
"v_exp_f32 v64, v64\n"
"v_mfma_f32_16x16x32_bf16 v[72:75], a[68:71], v[100:103], v[72:75]\n"
"v_cvt_pk_bf16_f32 v63, v78, v79\n"
"v_add_f32 v34, v34, v79\n"
"v_mfma_f32_16x16x32_bf16 v[72:75], a[72:75], v[104:107], v[72:75]\n"
"v_exp_f32 v65, v65\n"
"v_mfma_f32_16x16x32_bf16 v[72:75], a[76:79], v[108:111], v[72:75]\n"
"ds_read_b128 a[120:123], v6 offset:61440\n"
"v_add_f32 v29, v29, v64\n"
"v_mfma_f32_16x16x32_bf16 a[192:195], a[160:163], v[60:63], a[192:195]\n"
"s_mov_b32 m0, 28672\n"
"v_exp_f32 v66, v66\n"
"v_mfma_f32_16x16x32_bf16 a[196:199], a[164:167], v[60:63], a[196:199]\n"
"s_waitcnt vmcnt(52)\n"
"v_cvt_pk_bf16_f32 v56, v64, v65\n"
"v_add_f32 v29, v29, v65\n"
"v_mfma_f32_16x16x32_bf16 a[200:203], a[168:171], v[60:63], a[200:203]\n"
"buffer_load_dwordx4 v10, s[28:31], s61 offen lds\n"
"v_exp_f32 v67, v67\n"
"v_mfma_f32_16x16x32_bf16 a[204:207], a[172:175], v[60:63], a[204:207]\n"
"ds_read_b128 a[124:127], v7 offset:61440\n"
"v_add_f32 v29, v29, v66\n"
"v_mfma_f32_16x16x32_bf16 v[76:79], a[80:83], v[96:99], v[40:43]\n"
"buffer_load_dwordx4 v11, s[28:31], s61 offen offset:1024 lds\n"
"v_exp_f32 v68, v68\n"
"v_mfma_f32_16x16x32_bf16 v[76:79], a[84:87], v[100:103], v[76:79]\n"
"v_cvt_pk_bf16_f32 v57, v66, v67\n"
"v_add_f32 v29, v29, v67\n"
"v_mfma_f32_16x16x32_bf16 v[76:79], a[88:91], v[104:107], v[76:79]\n"
"buffer_load_dwordx4 v12, s[28:31], s61 offen offset:2048 lds\n"
"v_exp_f32 v69, v69\n"
"v_mfma_f32_16x16x32_bf16 v[76:79], a[92:95], v[108:111], v[76:79]\n"
"v_add_f32 v29, v29, v68\n"
"v_mfma_f32_16x16x32_bf16 a[208:211], a[176:179], v[60:63], a[208:211]\n"
"buffer_load_dwordx4 v13, s[28:31], s61 offen offset:3072 lds\n"
"v_exp_f32 v70, v70\n"
"v_mfma_f32_16x16x32_bf16 a[212:215], a[180:183], v[60:63], a[212:215]\n"
"v_cvt_pk_bf16_f32 v58, v68, v69\n"
"v_add_f32 v29, v29, v69\n"
"v_mfma_f32_16x16x32_bf16 a[216:219], a[184:187], v[60:63], a[216:219]\n"
"v_exp_f32 v71, v71\n"
"v_mfma_f32_16x16x32_bf16 a[220:223], a[188:191], v[60:63], a[220:223]\n"
"v_add_f32 v29, v29, v70\n"
"v_mfma_f32_16x16x32_bf16 v[64:67], a[64:67], v[112:115], v[44:47]\n"
"v_exp_f32 v72, v72\n"
"v_mfma_f32_16x16x32_bf16 v[64:67], a[68:71], v[116:119], v[64:67]\n"
"v_cvt_pk_bf16_f32 v59, v70, v71\n"
"v_add_f32 v29, v29, v71\n"
"v_mfma_f32_16x16x32_bf16 v[64:67], a[72:75], v[120:123], v[64:67]\n"
"v_exp_f32 v73, v73\n"
"v_mfma_f32_16x16x32_bf16 v[64:67], a[76:79], v[124:127], v[64:67]\n"
"s_waitcnt lgkmcnt(0)\n"
"s_barrier\n"
"v_mfma_f32_16x16x32_bf16 v[160:163], a[128:131], v[56:59], v[160:163]\n"
"s_mov_b32 m0, 90112\n"
"v_exp_f32 v74, v74\n"
"v_mfma_f32_16x16x32_bf16 v[164:167], a[132:135], v[56:59], v[164:167]\n"
"ds_read_b64_tr_b16 a[160:161], v8 offset:57344\n"
"v_cvt_pk_bf16_f32 v60, v72, v73\n"
"v_mfma_f32_16x16x32_bf16 v[168:171], a[136:139], v[56:59], v[168:171]\n"
"buffer_load_dwordx4 v14, s[36:39], s62 offen lds\n"
"v_exp_f32 v75, v75\n"
"v_mfma_f32_16x16x32_bf16 v[172:175], a[140:143], v[56:59], v[172:175]\n"
"ds_read_b64_tr_b16 a[162:163], v8 offset:61440\n"
"ds_read_b64_tr_b16 a[164:165], v9 offset:57344\n"
"v_mfma_f32_16x16x32_bf16 v[68:71], a[80:83], v[112:115], v[44:47]\n"
"buffer_load_dwordx4 v15, s[36:39], s62 offen offset:1024 lds\n"
"v_exp_f32 v76, v76\n"
"v_mfma_f32_16x16x32_bf16 v[68:71], a[84:87], v[116:119], v[68:71]\n"
"ds_read_b64_tr_b16 a[166:167], v9 offset:61440\n"
"v_cvt_pk_bf16_f32 v61, v74, v75\n"
"v_mfma_f32_16x16x32_bf16 v[68:71], a[88:91], v[120:123], v[68:71]\n"
"buffer_load_dwordx4 v14, s[36:39], s62 offen offset:2048 lds\n"
"v_exp_f32 v77, v77\n"
"v_mfma_f32_16x16x32_bf16 v[68:71], a[92:95], v[124:127], v[68:71]\n"
"ds_read_b64_tr_b16 a[168:169], v8 offset:57856\n"
"ds_read_b64_tr_b16 a[170:171], v8 offset:61952\n"
"v_mfma_f32_16x16x32_bf16 v[176:179], a[144:147], v[56:59], v[176:179]\n"
"buffer_load_dwordx4 v15, s[36:39], s62 offen offset:3072 lds\n"
"v_exp_f32 v78, v78\n"
"v_mfma_f32_16x16x32_bf16 v[180:183], a[148:151], v[56:59], v[180:183]\n"
"ds_read_b64_tr_b16 a[172:173], v9 offset:57856\n"
"v_cvt_pk_bf16_f32 v62, v76, v77\n"
"v_mfma_f32_16x16x32_bf16 v[184:187], a[152:155], v[56:59], v[184:187]\n"
"v_exp_f32 v79, v79\n"
"v_mfma_f32_16x16x32_bf16 v[188:191], a[156:159], v[56:59], v[188:191]\n"
"ds_read_b64_tr_b16 a[174:175], v9 offset:61952\n"
"v_mfma_f32_16x16x32_bf16 v[72:75], a[64:67], v[128:131], v[48:51]\n"
"v_exp_f32 v64, v64\n"
"v_mfma_f32_16x16x32_bf16 v[72:75], a[68:71], v[132:135], v[72:75]\n"
"ds_read_b64_tr_b16 a[176:177], v8 offset:58368\n"
"v_cvt_pk_bf16_f32 v63, v78, v79\n"
"v_mfma_f32_16x16x32_bf16 v[72:75], a[72:75], v[136:139], v[72:75]\n"
"v_exp_f32 v65, v65\n"
"v_mfma_f32_16x16x32_bf16 v[72:75], a[76:79], v[140:143], v[72:75]\n"
"ds_read_b64_tr_b16 a[178:179], v8 offset:62464\n"
"v_add_f32 v31, v31, v64\n"
"v_mfma_f32_16x16x32_bf16 v[192:195], a[128:131], v[60:63], v[192:195]\n"
"s_mov_b32 m0, 94208\n"
"v_exp_f32 v66, v66\n"
"v_mfma_f32_16x16x32_bf16 v[196:199], a[132:135], v[60:63], v[196:199]\n"
"ds_read_b64_tr_b16 a[180:181], v9 offset:58368\n"
"s_waitcnt vmcnt(52)\n"
"v_cvt_pk_bf16_f32 v56, v64, v65\n"
"v_add_f32 v31, v31, v65\n"
"v_mfma_f32_16x16x32_bf16 v[200:203], a[136:139], v[60:63], v[200:203]\n"
"buffer_load_dwordx4 v14, s[36:39], s61 offen lds\n"
"v_exp_f32 v67, v67\n"
"v_mfma_f32_16x16x32_bf16 v[204:207], a[140:143], v[60:63], v[204:207]\n"
"ds_read_b64_tr_b16 a[182:183], v9 offset:62464\n"
"v_add_f32 v31, v31, v66\n"
"v_mfma_f32_16x16x32_bf16 v[18:21], a[224:227], v[60:63], v[18:21]\n"
"v_mfma_f32_16x16x32_bf16 v[76:79], a[80:83], v[128:131], v[48:51]\n"
"buffer_load_dwordx4 v15, s[36:39], s61 offen offset:1024 lds\n"
"v_exp_f32 v68, v68\n"
"v_mfma_f32_16x16x32_bf16 v[76:79], a[84:87], v[132:135], v[76:79]\n"
"ds_read_b64_tr_b16 a[184:185], v8 offset:58880\n"
"v_cvt_pk_bf16_f32 v57, v66, v67\n"
"v_add_f32 v31, v31, v67\n"
"v_mfma_f32_16x16x32_bf16 v[76:79], a[88:91], v[136:139], v[76:79]\n"
"buffer_load_dwordx4 v14, s[36:39], s61 offen offset:2048 lds\n"
"v_exp_f32 v69, v69\n"
"v_mfma_f32_16x16x32_bf16 v[76:79], a[92:95], v[140:143], v[76:79]\n"
"ds_read_b64_tr_b16 a[186:187], v8 offset:62976\n"
"v_add_f32 v31, v31, v68\n"
"v_mfma_f32_16x16x32_bf16 v[208:211], a[144:147], v[60:63], v[208:211]\n"
"buffer_load_dwordx4 v15, s[36:39], s61 offen offset:3072 lds\n"
"v_exp_f32 v70, v70\n"
"v_mfma_f32_16x16x32_bf16 v[212:215], a[148:151], v[60:63], v[212:215]\n"
"ds_read_b64_tr_b16 a[188:189], v9 offset:58880\n"
"v_cvt_pk_bf16_f32 v58, v68, v69\n"
"v_add_f32 v31, v31, v69\n"
"v_mfma_f32_16x16x32_bf16 v[216:219], a[152:155], v[60:63], v[216:219]\n"
"s_add_u32 s62, s62, 8192\n"
"s_and_b32 s62, s62, 0x3ffff\n"
"s_add_u32 s61, s62, 4096\n"
"v_exp_f32 v71, v71\n"
"v_mfma_f32_16x16x32_bf16 v[220:223], a[156:159], v[60:63], v[220:223]\n"
"ds_read_b64_tr_b16 a[190:191], v9 offset:62976\n"
"v_add_f32 v31, v31, v70\n"
"v_mfma_f32_16x16x32_bf16 v[64:67], a[96:99], v[80:83], v[36:39]\n"
"v_exp_f32 v72, v72\n"
"v_mfma_f32_16x16x32_bf16 v[64:67], a[100:103], v[84:87], v[64:67]\n"
"v_cvt_pk_bf16_f32 v59, v70, v71\n"
"v_add_f32 v31, v31, v71\n"
"v_mfma_f32_16x16x32_bf16 v[64:67], a[104:107], v[88:91], v[64:67]\n"
"v_exp_f32 v73, v73\n"
"v_mfma_f32_16x16x32_bf16 v[64:67], a[108:111], v[92:95], v[64:67]\n"
"ds_read_b128 a[64:67], v4\n"
"v_add_f32 v34, v34, v72\n"
"v_mfma_f32_16x16x32_bf16 v[224:227], a[128:131], v[56:59], v[224:227]\n"
"s_mov_b32 m0, 32768\n"
"v_exp_f32 v74, v74\n"
"v_mfma_f32_16x16x32_bf16 v[228:231], a[132:135], v[56:59], v[228:231]\n"
"ds_read_b128 a[68:71], v5\n"
"v_cvt_pk_bf16_f32 v60, v72, v73\n"
"v_add_f32 v34, v34, v73\n"
"v_mfma_f32_16x16x32_bf16 v[232:235], a[136:139], v[56:59], v[232:235]\n"
"buffer_load_dwordx4 v10, s[28:31], s62 offen lds\n"
"v_exp_f32 v75, v75\n"
"v_mfma_f32_16x16x32_bf16 v[236:239], a[140:143], v[56:59], v[236:239]\n"
"ds_read_b128 a[72:75], v6\n"
"v_add_f32 v34, v34, v74\n"
"v_mfma_f32_16x16x32_bf16 v[68:71], a[112:115], v[80:83], v[36:39]\n"
"buffer_load_dwordx4 v11, s[28:31], s62 offen offset:1024 lds\n"
"v_exp_f32 v76, v76\n"
"v_mfma_f32_16x16x32_bf16 v[68:71], a[116:119], v[84:87], v[68:71]\n"
"ds_read_b128 a[76:79], v7\n"
"v_cvt_pk_bf16_f32 v61, v74, v75\n"
"v_add_f32 v34, v34, v75\n"
"v_mfma_f32_16x16x32_bf16 v[68:71], a[120:123], v[88:91], v[68:71]\n"
"buffer_load_dwordx4 v12, s[28:31], s62 offen offset:2048 lds\n"
"v_exp_f32 v77, v77\n"
"v_mfma_f32_16x16x32_bf16 v[68:71], a[124:127], v[92:95], v[68:71]\n"
"ds_read_b128 a[80:83], v4 offset:4096\n"
"v_add_f32 v34, v34, v76\n"
"v_mfma_f32_16x16x32_bf16 v[240:243], a[144:147], v[56:59], v[240:243]\n"
"buffer_load_dwordx4 v13, s[28:31], s62 offen offset:3072 lds\n"
"v_exp_f32 v78, v78\n"
"v_mfma_f32_16x16x32_bf16 v[244:247], a[148:151], v[56:59], v[244:247]\n"
"ds_read_b128 a[84:87], v5 offset:4096\n"
"v_cvt_pk_bf16_f32 v62, v76, v77\n"
"v_add_f32 v34, v34, v77\n"
"v_mfma_f32_16x16x32_bf16 v[248:251], a[152:155], v[56:59], v[248:251]\n"
"v_exp_f32 v79, v79\n"
"v_mfma_f32_16x16x32_bf16 v[252:255], a[156:159], v[56:59], v[252:255]\n"
"v_add_f32 v34, v34, v78\n"
"v_mfma_f32_16x16x32_bf16 v[72:75], a[96:99], v[96:99], v[40:43]\n"
"v_exp_f32 v64, v64\n"
"v_mfma_f32_16x16x32_bf16 v[72:75], a[100:103], v[100:103], v[72:75]\n"
"v_cvt_pk_bf16_f32 v63, v78, v79\n"
"v_add_f32 v34, v34, v79\n"
"v_mfma_f32_16x16x32_bf16 v[72:75], a[104:107], v[104:107], v[72:75]\n"
"v_exp_f32 v65, v65\n"
"v_mfma_f32_16x16x32_bf16 v[72:75], a[108:111], v[108:111], v[72:75]\n"
"ds_read_b128 a[88:91], v6 offset:4096\n"
"v_add_f32 v29, v29, v64\n"
"v_mfma_f32_16x16x32_bf16 a[192:195], a[128:131], v[60:63], a[192:195]\n"
"s_mov_b32 m0, 36864\n"
"v_exp_f32 v66, v66\n"
"v_mfma_f32_16x16x32_bf16 a[196:199], a[132:135], v[60:63], a[196:199]\n"
"s_waitcnt vmcnt(52)\n"
"v_cvt_pk_bf16_f32 v56, v64, v65\n"
"v_add_f32 v29, v29, v65\n"
"v_mfma_f32_16x16x32_bf16 a[200:203], a[136:139], v[60:63], a[200:203]\n"
"buffer_load_dwordx4 v10, s[28:31], s61 offen lds\n"
"v_exp_f32 v67, v67\n"
"v_mfma_f32_16x16x32_bf16 a[204:207], a[140:143], v[60:63], a[204:207]\n"
"ds_read_b128 a[92:95], v7 offset:4096\n"
"v_add_f32 v29, v29, v66\n"
"v_mfma_f32_16x16x32_bf16 v[76:79], a[112:115], v[96:99], v[40:43]\n"
"buffer_load_dwordx4 v11, s[28:31], s61 offen offset:1024 lds\n"
"v_exp_f32 v68, v68\n"
"v_mfma_f32_16x16x32_bf16 v[76:79], a[116:119], v[100:103], v[76:79]\n"
"v_cvt_pk_bf16_f32 v57, v66, v67\n"
"v_add_f32 v29, v29, v67\n"
"v_mfma_f32_16x16x32_bf16 v[76:79], a[120:123], v[104:107], v[76:79]\n"
"buffer_load_dwordx4 v12, s[28:31], s61 offen offset:2048 lds\n"
"v_exp_f32 v69, v69\n"
"v_mfma_f32_16x16x32_bf16 v[76:79], a[124:127], v[108:111], v[76:79]\n"
"v_add_f32 v29, v29, v68\n"
"v_mfma_f32_16x16x32_bf16 a[208:211], a[144:147], v[60:63], a[208:211]\n"
"buffer_load_dwordx4 v13, s[28:31], s61 offen offset:3072 lds\n"
"v_exp_f32 v70, v70\n"
"v_mfma_f32_16x16x32_bf16 a[212:215], a[148:151], v[60:63], a[212:215]\n"
"v_cvt_pk_bf16_f32 v58, v68, v69\n"
"v_add_f32 v29, v29, v69\n"
"v_mfma_f32_16x16x32_bf16 a[216:219], a[152:155], v[60:63], a[216:219]\n"
"v_exp_f32 v71, v71\n"
"v_mfma_f32_16x16x32_bf16 a[220:223], a[156:159], v[60:63], a[220:223]\n"
"v_add_f32 v29, v29, v70\n"
"v_mfma_f32_16x16x32_bf16 v[64:67], a[96:99], v[112:115], v[44:47]\n"
"v_exp_f32 v72, v72\n"
"v_mfma_f32_16x16x32_bf16 v[64:67], a[100:103], v[116:119], v[64:67]\n"
"v_cvt_pk_bf16_f32 v59, v70, v71\n"
"v_add_f32 v29, v29, v71\n"
"v_mfma_f32_16x16x32_bf16 v[64:67], a[104:107], v[120:123], v[64:67]\n"
"global_load_dword v33, v32, s[18:19]\n"
"v_exp_f32 v73, v73\n"
"v_mfma_f32_16x16x32_bf16 v[64:67], a[108:111], v[124:127], v[64:67]\n"
"s_waitcnt vmcnt(21) lgkmcnt(0)\n"
"s_barrier\n"
"v_mfma_f32_16x16x32_bf16 v[160:163], a[160:163], v[56:59], v[160:163]\n"
"s_mov_b32 m0, 98304\n"
"v_exp_f32 v74, v74\n"
"v_mfma_f32_16x16x32_bf16 v[164:167], a[164:167], v[56:59], v[164:167]\n"
"ds_read_b64_tr_b16 a[128:129], v8\n"
"v_cvt_pk_bf16_f32 v60, v72, v73\n"
"v_mfma_f32_16x16x32_bf16 v[168:171], a[168:171], v[56:59], v[168:171]\n"
"buffer_load_dwordx4 v14, s[36:39], s62 offen lds\n"
"v_exp_f32 v75, v75\n"
"v_mfma_f32_16x16x32_bf16 v[172:175], a[172:175], v[56:59], v[172:175]\n"
"ds_read_b64_tr_b16 a[130:131], v8 offset:4096\n"
"ds_read_b64_tr_b16 a[132:133], v9\n"
"v_mfma_f32_16x16x32_bf16 v[68:71], a[112:115], v[112:115], v[44:47]\n"
"buffer_load_dwordx4 v15, s[36:39], s62 offen offset:1024 lds\n"
"v_exp_f32 v76, v76\n"
"v_mfma_f32_16x16x32_bf16 v[68:71], a[116:119], v[116:119], v[68:71]\n"
"ds_read_b64_tr_b16 a[134:135], v9 offset:4096\n"
"v_cvt_pk_bf16_f32 v61, v74, v75\n"
"v_mfma_f32_16x16x32_bf16 v[68:71], a[120:123], v[120:123], v[68:71]\n"
"buffer_load_dwordx4 v14, s[36:39], s62 offen offset:2048 lds\n"
"v_exp_f32 v77, v77\n"
"v_mfma_f32_16x16x32_bf16 v[68:71], a[124:127], v[124:127], v[68:71]\n"
"ds_read_b64_tr_b16 a[136:137], v8 offset:512\n"
"ds_read_b64_tr_b16 a[138:139], v8 offset:4608\n"
"v_mfma_f32_16x16x32_bf16 v[176:179], a[176:179], v[56:59], v[176:179]\n"
"buffer_load_dwordx4 v15, s[36:39], s62 offen offset:3072 lds\n"
"v_exp_f32 v78, v78\n"
"v_mfma_f32_16x16x32_bf16 v[180:183], a[180:183], v[56:59], v[180:183]\n"
"ds_read_b64_tr_b16 a[140:141], v9 offset:512\n"
"v_cvt_pk_bf16_f32 v62, v76, v77\n"
"v_mfma_f32_16x16x32_bf16 v[184:187], a[184:187], v[56:59], v[184:187]\n"
"v_exp_f32 v79, v79\n"
"v_mfma_f32_16x16x32_bf16 v[188:191], a[188:191], v[56:59], v[188:191]\n"
"ds_read_b64_tr_b16 a[142:143], v9 offset:4608\n"
"v_mfma_f32_16x16x32_bf16 v[72:75], a[96:99], v[128:131], v[48:51]\n"
"v_exp_f32 v64, v64\n"
"v_mfma_f32_16x16x32_bf16 v[72:75], a[100:103], v[132:135], v[72:75]\n"
"ds_read_b64_tr_b16 a[144:145], v8 offset:1024\n"
"v_cvt_pk_bf16_f32 v63, v78, v79\n"
"v_mfma_f32_16x16x32_bf16 v[72:75], a[104:107], v[136:139], v[72:75]\n"
"v_exp_f32 v65, v65\n"
"v_mfma_f32_16x16x32_bf16 v[72:75], a[108:111], v[140:143], v[72:75]\n"
"ds_read_b64_tr_b16 a[146:147], v8 offset:5120\n"
"v_add_f32 v31, v31, v64\n"
"v_mfma_f32_16x16x32_bf16 v[192:195], a[160:163], v[60:63], v[192:195]\n"
"s_mov_b32 m0, 102400\n"
"v_exp_f32 v66, v66\n"
"v_mfma_f32_16x16x32_bf16 v[196:199], a[164:167], v[60:63], v[196:199]\n"
"ds_read_b64_tr_b16 a[148:149], v9 offset:1024\n"
"v_cvt_pk_bf16_f32 v56, v64, v65\n"
"v_add_f32 v31, v31, v65\n"
"v_mfma_f32_16x16x32_bf16 v[200:203], a[168:171], v[60:63], v[200:203]\n"
"buffer_load_dwordx4 v14, s[36:39], s61 offen lds\n"
"v_exp_f32 v67, v67\n"
"v_mfma_f32_16x16x32_bf16 v[204:207], a[172:175], v[60:63], v[204:207]\n"
"ds_read_b64_tr_b16 a[150:151], v9 offset:5120\n"
"v_add_f32 v31, v31, v66\n"
"v_mfma_f32_16x16x32_bf16 v[18:21], a[224:227], v[60:63], v[18:21]\n"
"v_mfma_f32_16x16x32_bf16 v[76:79], a[112:115], v[128:131], v[48:51]\n"
"buffer_load_dwordx4 v15, s[36:39], s61 offen offset:1024 lds\n"
"v_exp_f32 v68, v68\n"
"v_mfma_f32_16x16x32_bf16 v[76:79], a[116:119], v[132:135], v[76:79]\n"
"ds_read_b64_tr_b16 a[152:153], v8 offset:1536\n"
"v_cvt_pk_bf16_f32 v57, v66, v67\n"
"v_add_f32 v31, v31, v67\n"
"v_mfma_f32_16x16x32_bf16 v[76:79], a[120:123], v[136:139], v[76:79]\n"
"buffer_load_dwordx4 v14, s[36:39], s61 offen offset:2048 lds\n"
"v_exp_f32 v69, v69\n"
"v_mfma_f32_16x16x32_bf16 v[76:79], a[124:127], v[140:143], v[76:79]\n"
"ds_read_b64_tr_b16 a[154:155], v8 offset:5632\n"
"v_add_f32 v31, v31, v68\n"
"v_mfma_f32_16x16x32_bf16 v[208:211], a[176:179], v[60:63], v[208:211]\n"
"buffer_load_dwordx4 v15, s[36:39], s61 offen offset:3072 lds\n"
"v_exp_f32 v70, v70\n"
"v_mfma_f32_16x16x32_bf16 v[212:215], a[180:183], v[60:63], v[212:215]\n"
"ds_read_b64_tr_b16 a[156:157], v9 offset:1536\n"
"v_cvt_pk_bf16_f32 v58, v68, v69\n"
"v_add_f32 v31, v31, v69\n"
"v_mfma_f32_16x16x32_bf16 v[216:219], a[184:187], v[60:63], v[216:219]\n"
"s_add_u32 s62, s62, 8192\n"
"s_and_b32 s62, s62, 0x3ffff\n"
"s_add_u32 s61, s62, 4096\n"
"v_exp_f32 v71, v71\n"
"v_mfma_f32_16x16x32_bf16 v[220:223], a[188:191], v[60:63], v[220:223]\n"
"ds_read_b64_tr_b16 a[158:159], v9 offset:5632\n"
"v_add_f32 v31, v31, v70\n"
"ds_read_b64_tr_b16 a[0:1], v16\n"
"ds_read_b64_tr_b16 a[2:3], v16 offset:2048\n"
"ds_read_b64_tr_b16 a[4:5], v17\n"
"ds_read_b64_tr_b16 a[6:7], v17 offset:2048\n"
"ds_read_b64_tr_b16 a[8:9], v16 offset:512\n"
"ds_read_b64_tr_b16 a[10:11], v16 offset:2560\n"
"ds_read_b64_tr_b16 a[12:13], v17 offset:512\n"
"ds_read_b64_tr_b16 a[14:15], v17 offset:2560\n"
"ds_read_b64_tr_b16 a[16:17], v16 offset:16384\n"
"ds_read_b64_tr_b16 a[18:19], v16 offset:18432\n"
"ds_read_b64_tr_b16 a[20:21], v17 offset:16384\n"
"ds_read_b64_tr_b16 a[22:23], v17 offset:18432\n"
"ds_read_b64_tr_b16 a[24:25], v16 offset:16896\n"
"ds_read_b64_tr_b16 a[26:27], v16 offset:18944\n"
"ds_read_b64_tr_b16 a[28:29], v17 offset:16896\n"
"ds_read_b64_tr_b16 a[30:31], v17 offset:18944\n"
"s_mov_b32 s63, 1\n"
"s_branch .Lmha_ent4\n"
".Lmha_back4:\n"
"ds_read_b64_tr_b16 a[32:33], v16\n"
"ds_read_b64_tr_b16 a[34:35], v16 offset:2048\n"
"ds_read_b64_tr_b16 a[36:37], v17\n"
"ds_read_b64_tr_b16 a[38:39], v17 offset:2048\n"
"ds_read_b64_tr_b16 a[40:41], v16 offset:512\n"
"ds_read_b64_tr_b16 a[42:43], v16 offset:2560\n"
"ds_read_b64_tr_b16 a[44:45], v17 offset:512\n"
"ds_read_b64_tr_b16 a[46:47], v17 offset:2560\n"
"ds_read_b64_tr_b16 a[48:49], v16 offset:16384\n"
"ds_read_b64_tr_b16 a[50:51], v16 offset:18432\n"
"ds_read_b64_tr_b16 a[52:53], v17 offset:16384\n"
"ds_read_b64_tr_b16 a[54:55], v17 offset:18432\n"
"ds_read_b64_tr_b16 a[56:57], v16 offset:16896\n"
"ds_read_b64_tr_b16 a[58:59], v16 offset:18944\n"
"ds_read_b64_tr_b16 a[60:61], v17 offset:16896\n"
"ds_read_b64_tr_b16 a[62:63], v17 offset:18944\n"
"v_lshlrev_b32 v27, 7, v0\n"
"s_lshl_b32 s54, s49, 14\n"
"s_add_u32 s54, s54, 25165824\n"
"s_add_u32 s54, s16, s54\n"
"s_addc_u32 s55, s17, 0\n"
"s_waitcnt vmcnt(50)\n"
"global_load_dword v33, v27, s[54:55] sc1\n"
"v_add_u32 v27, 8192, v27\n"
"global_load_dword v33, v27, s[54:55] sc1\n"
".Lmha_top4:\n"
".Lmha_ent4:\n"
"v_mfma_f32_16x16x32_bf16 v[64:67], a[64:67], v[80:83], v[36:39]\n"
"v_exp_f32 v72, v72\n"
"v_mfma_f32_16x16x32_bf16 v[64:67], a[68:71], v[84:87], v[64:67]\n"
"v_cvt_pk_bf16_f32 v59, v70, v71\n"
"v_add_f32 v31, v31, v71\n"
"v_mfma_f32_16x16x32_bf16 v[64:67], a[72:75], v[88:91], v[64:67]\n"
"v_exp_f32 v73, v73\n"
"v_mfma_f32_16x16x32_bf16 v[64:67], a[76:79], v[92:95], v[64:67]\n"
"ds_read_b128 a[96:99], v4 offset:8192\n"
"v_add_f32 v34, v34, v72\n"
"v_mfma_f32_16x16x32_bf16 v[224:227], a[160:163], v[56:59], v[224:227]\n"
"s_mov_b32 m0, 40960\n"
"v_exp_f32 v74, v74\n"
"v_mfma_f32_16x16x32_bf16 v[228:231], a[164:167], v[56:59], v[228:231]\n"
"ds_read_b128 a[100:103], v5 offset:8192\n"
"v_cvt_pk_bf16_f32 v60, v72, v73\n"
"v_add_f32 v34, v34, v73\n"
"v_mfma_f32_16x16x32_bf16 v[232:235], a[168:171], v[56:59], v[232:235]\n"
"buffer_load_dwordx4 v10, s[28:31], s62 offen nt lds\n"
"v_exp_f32 v75, v75\n"
"v_mfma_f32_16x16x32_bf16 v[236:239], a[172:175], v[56:59], v[236:239]\n"
"ds_read_b128 a[104:107], v6 offset:8192\n"
"v_add_f32 v34, v34, v74\n"
"v_mfma_f32_16x16x32_bf16 v[68:71], a[80:83], v[80:83], v[36:39]\n"
"buffer_load_dwordx4 v11, s[28:31], s62 offen offset:1024 nt lds\n"
"v_exp_f32 v76, v76\n"
"v_mfma_f32_16x16x32_bf16 v[68:71], a[84:87], v[84:87], v[68:71]\n"
"ds_read_b128 a[108:111], v7 offset:8192\n"
"v_cvt_pk_bf16_f32 v61, v74, v75\n"
"v_add_f32 v34, v34, v75\n"
"v_mfma_f32_16x16x32_bf16 v[68:71], a[88:91], v[88:91], v[68:71]\n"
"buffer_load_dwordx4 v12, s[28:31], s62 offen offset:2048 nt lds\n"
"v_exp_f32 v77, v77\n"
"v_mfma_f32_16x16x32_bf16 v[68:71], a[92:95], v[92:95], v[68:71]\n"
"ds_read_b128 a[112:115], v4 offset:12288\n"
"v_add_f32 v34, v34, v76\n"
"v_mfma_f32_16x16x32_bf16 v[240:243], a[176:179], v[56:59], v[240:243]\n"
"buffer_load_dwordx4 v13, s[28:31], s62 offen offset:3072 nt lds\n"
"v_exp_f32 v78, v78\n"
"v_mfma_f32_16x16x32_bf16 v[244:247], a[180:183], v[56:59], v[244:247]\n"
"ds_read_b128 a[116:119], v5 offset:12288\n"
"v_cvt_pk_bf16_f32 v62, v76, v77\n"
"v_add_f32 v34, v34, v77\n"
"v_mfma_f32_16x16x32_bf16 v[248:251], a[184:187], v[56:59], v[248:251]\n"
"v_exp_f32 v79, v79\n"
"v_mfma_f32_16x16x32_bf16 v[252:255], a[188:191], v[56:59], v[252:255]\n"
"v_add_f32 v34, v34, v78\n"
"v_mfma_f32_16x16x32_bf16 v[72:75], a[64:67], v[96:99], v[40:43]\n"
"v_exp_f32 v64, v64\n"
"v_mfma_f32_16x16x32_bf16 v[72:75], a[68:71], v[100:103], v[72:75]\n"
"v_cvt_pk_bf16_f32 v63, v78, v79\n"
"v_add_f32 v34, v34, v79\n"
"v_mfma_f32_16x16x32_bf16 v[72:75], a[72:75], v[104:107], v[72:75]\n"
"v_exp_f32 v65, v65\n"
"v_mfma_f32_16x16x32_bf16 v[72:75], a[76:79], v[108:111], v[72:75]\n"
"ds_read_b128 a[120:123], v6 offset:12288\n"
"v_add_f32 v29, v29, v64\n"
"v_mfma_f32_16x16x32_bf16 a[192:195], a[160:163], v[60:63], a[192:195]\n"
"s_mov_b32 m0, 45056\n"
"v_exp_f32 v66, v66\n"
"v_mfma_f32_16x16x32_bf16 a[196:199], a[164:167], v[60:63], a[196:199]\n"
"v_cvt_pk_bf16_f32 v56, v64, v65\n"
"v_add_f32 v29, v29, v65\n"
"v_mfma_f32_16x16x32_bf16 a[200:203], a[168:171], v[60:63], a[200:203]\n"
"buffer_load_dwordx4 v10, s[28:31], s61 offen nt lds\n"
"v_exp_f32 v67, v67\n"
"v_mfma_f32_16x16x32_bf16 a[204:207], a[172:175], v[60:63], a[204:207]\n"
"ds_read_b128 a[124:127], v7 offset:12288\n"
"v_add_f32 v29, v29, v66\n"
"v_mfma_f32_16x16x32_bf16 v[76:79], a[80:83], v[96:99], v[40:43]\n"
"buffer_load_dwordx4 v11, s[28:31], s61 offen offset:1024 nt lds\n"
"v_exp_f32 v68, v68\n"
"v_mfma_f32_16x16x32_bf16 v[76:79], a[84:87], v[100:103], v[76:79]\n"
"v_cvt_pk_bf16_f32 v57, v66, v67\n"
"v_add_f32 v29, v29, v67\n"
"v_mfma_f32_16x16x32_bf16 v[76:79], a[88:91], v[104:107], v[76:79]\n"
"buffer_load_dwordx4 v12, s[28:31], s61 offen offset:2048 nt lds\n"
"v_exp_f32 v69, v69\n"
"v_mfma_f32_16x16x32_bf16 v[76:79], a[92:95], v[108:111], v[76:79]\n"
"v_add_f32 v29, v29, v68\n"
"v_mfma_f32_16x16x32_bf16 a[208:211], a[176:179], v[60:63], a[208:211]\n"
"buffer_load_dwordx4 v13, s[28:31], s61 offen offset:3072 nt lds\n"
"v_exp_f32 v70, v70\n"
"v_mfma_f32_16x16x32_bf16 a[212:215], a[180:183], v[60:63], a[212:215]\n"
"v_cvt_pk_bf16_f32 v58, v68, v69\n"
"v_add_f32 v29, v29, v69\n"
"v_mfma_f32_16x16x32_bf16 a[216:219], a[184:187], v[60:63], a[216:219]\n"
"v_exp_f32 v71, v71\n"
"v_mfma_f32_16x16x32_bf16 a[220:223], a[188:191], v[60:63], a[220:223]\n"
"v_add_f32 v29, v29, v70\n"
"v_mfma_f32_16x16x32_bf16 v[64:67], a[64:67], v[112:115], v[44:47]\n"
"v_exp_f32 v72, v72\n"
"v_mfma_f32_16x16x32_bf16 v[64:67], a[68:71], v[116:119], v[64:67]\n"
"v_cvt_pk_bf16_f32 v59, v70, v71\n"
"v_add_f32 v29, v29, v71\n"
"v_mfma_f32_16x16x32_bf16 v[64:67], a[72:75], v[120:123], v[64:67]\n"
"v_exp_f32 v73, v73\n"
"v_mfma_f32_16x16x32_bf16 v[64:67], a[76:79], v[124:127], v[64:67]\n"
"s_waitcnt vmcnt(58) lgkmcnt(0)\n"
"s_barrier\n"
"v_mfma_f32_16x16x32_bf16 v[160:163], a[128:131], v[56:59], v[160:163]\n"
"s_mov_b32 m0, 106496\n"
"v_exp_f32 v74, v74\n"
"v_mfma_f32_16x16x32_bf16 v[164:167], a[132:135], v[56:59], v[164:167]\n"
"ds_read_b64_tr_b16 a[160:161], v8 offset:8192\n"
"v_cvt_pk_bf16_f32 v60, v72, v73\n"
"v_mfma_f32_16x16x32_bf16 v[168:171], a[136:139], v[56:59], v[168:171]\n"
"buffer_load_dwordx4 v14, s[36:39], s62 offen nt lds\n"
"v_exp_f32 v75, v75\n"
"v_mfma_f32_16x16x32_bf16 v[172:175], a[140:143], v[56:59], v[172:175]\n"
"ds_read_b64_tr_b16 a[162:163], v8 offset:12288\n"
"ds_read_b64_tr_b16 a[164:165], v9 offset:8192\n"
"v_mfma_f32_16x16x32_bf16 v[68:71], a[80:83], v[112:115], v[44:47]\n"
"buffer_load_dwordx4 v15, s[36:39], s62 offen offset:1024 nt lds\n"
"v_exp_f32 v76, v76\n"
"v_mfma_f32_16x16x32_bf16 v[68:71], a[84:87], v[116:119], v[68:71]\n"
"ds_read_b64_tr_b16 a[166:167], v9 offset:12288\n"
"v_cvt_pk_bf16_f32 v61, v74, v75\n"
"v_mfma_f32_16x16x32_bf16 v[68:71], a[88:91], v[120:123], v[68:71]\n"
"buffer_load_dwordx4 v14, s[36:39], s62 offen offset:2048 nt lds\n"
"v_exp_f32 v77, v77\n"
"v_mfma_f32_16x16x32_bf16 v[68:71], a[92:95], v[124:127], v[68:71]\n"
"ds_read_b64_tr_b16 a[168:169], v8 offset:8704\n"
"ds_read_b64_tr_b16 a[170:171], v8 offset:12800\n"
"v_mfma_f32_16x16x32_bf16 v[176:179], a[144:147], v[56:59], v[176:179]\n"
"buffer_load_dwordx4 v15, s[36:39], s62 offen offset:3072 nt lds\n"
"v_exp_f32 v78, v78\n"
"v_mfma_f32_16x16x32_bf16 v[180:183], a[148:151], v[56:59], v[180:183]\n"
"ds_read_b64_tr_b16 a[172:173], v9 offset:8704\n"
"v_cvt_pk_bf16_f32 v62, v76, v77\n"
"v_mfma_f32_16x16x32_bf16 v[184:187], a[152:155], v[56:59], v[184:187]\n"
"v_exp_f32 v79, v79\n"
"v_mfma_f32_16x16x32_bf16 v[188:191], a[156:159], v[56:59], v[188:191]\n"
"ds_read_b64_tr_b16 a[174:175], v9 offset:12800\n"
"v_mfma_f32_16x16x32_bf16 v[72:75], a[64:67], v[128:131], v[48:51]\n"
"v_exp_f32 v64, v64\n"
"v_mfma_f32_16x16x32_bf16 v[72:75], a[68:71], v[132:135], v[72:75]\n"
"ds_read_b64_tr_b16 a[176:177], v8 offset:9216\n"
"v_cvt_pk_bf16_f32 v63, v78, v79\n"
"v_mfma_f32_16x16x32_bf16 v[72:75], a[72:75], v[136:139], v[72:75]\n"
"v_exp_f32 v65, v65\n"
"v_mfma_f32_16x16x32_bf16 v[72:75], a[76:79], v[140:143], v[72:75]\n"
"ds_read_b64_tr_b16 a[178:179], v8 offset:13312\n"
"v_add_f32 v31, v31, v64\n"
"v_mfma_f32_16x16x32_bf16 v[192:195], a[128:131], v[60:63], v[192:195]\n"
"s_mov_b32 m0, 110592\n"
"v_exp_f32 v66, v66\n"
"v_mfma_f32_16x16x32_bf16 v[196:199], a[132:135], v[60:63], v[196:199]\n"
"ds_read_b64_tr_b16 a[180:181], v9 offset:9216\n"
"v_cvt_pk_bf16_f32 v56, v64, v65\n"
"v_add_f32 v31, v31, v65\n"
"v_mfma_f32_16x16x32_bf16 v[200:203], a[136:139], v[60:63], v[200:203]\n"
"buffer_load_dwordx4 v14, s[36:39], s61 offen nt lds\n"
"v_exp_f32 v67, v67\n"
"v_mfma_f32_16x16x32_bf16 v[204:207], a[140:143], v[60:63], v[204:207]\n"
"ds_read_b64_tr_b16 a[182:183], v9 offset:13312\n"
"buffer_load_dwordx4 v15, s[36:39], s61 offen offset:1024 nt lds\n"
"v_add_f32 v31, v31, v66\n"
"v_mfma_f32_16x16x32_bf16 v[18:21], a[224:227], v[60:63], v[18:21]\n"
"v_mfma_f32_16x16x32_bf16 v[76:79], a[80:83], v[128:131], v[48:51]\n"
"buffer_load_dwordx4 v14, s[36:39], s61 offen offset:2048 nt lds\n"
"v_exp_f32 v68, v68\n"
"v_mfma_f32_16x16x32_bf16 v[76:79], a[84:87], v[132:135], v[76:79]\n"
"ds_read_b64_tr_b16 a[184:185], v8 offset:9728\n"
"buffer_load_dwordx4 v15, s[36:39], s61 offen offset:3072 nt lds\n"
"v_cvt_pk_bf16_f32 v57, v66, v67\n"
"v_add_f32 v31, v31, v67\n"
"v_mfma_f32_16x16x32_bf16 v[76:79], a[88:91], v[136:139], v[76:79]\n"
"s_add_u32 s62, s62, 8192\n"
"s_and_b32 s62, s62, 0x3ffff\n"
"s_add_u32 s61, s62, 4096\n"
"s_mov_b32 m0, 131072\n"
"s_add_u32 s52, s51, 0\n"
"v_exp_f32 v69, v69\n"
"v_mfma_f32_16x16x32_bf16 v[76:79], a[92:95], v[140:143], v[76:79]\n"
"ds_read_b64_tr_b16 a[186:187], v8 offset:13824\n"
"buffer_load_dwordx4 v22, s[44:47], s52 offen lds\n"
"v_add_f32 v31, v31, v68\n"
"v_mfma_f32_16x16x32_bf16 v[208:211], a[144:147], v[60:63], v[208:211]\n"
"buffer_load_dwordx4 v23, s[44:47], s52 offen offset:1024 lds\n"
"v_exp_f32 v70, v70\n"
"v_mfma_f32_16x16x32_bf16 v[212:215], a[148:151], v[60:63], v[212:215]\n"
"ds_read_b64_tr_b16 a[188:189], v9 offset:9728\n"
"buffer_load_dwordx4 v24, s[44:47], s52 offen offset:2048 lds\n"
"v_cvt_pk_bf16_f32 v58, v68, v69\n"
"v_add_f32 v31, v31, v69\n"
"v_mfma_f32_16x16x32_bf16 v[216:219], a[152:155], v[60:63], v[216:219]\n"
"buffer_load_dwordx4 v25, s[44:47], s52 offen offset:3072 lds\n"
"v_exp_f32 v71, v71\n"
"v_mfma_f32_16x16x32_bf16 v[220:223], a[156:159], v[60:63], v[220:223]\n"
"ds_read_b64_tr_b16 a[190:191], v9 offset:13824\n"
"v_add_f32 v31, v31, v70\n"
"v_mfma_f32_16x16x32_bf16 v[64:67], a[96:99], v[80:83], v[36:39]\n"
"v_exp_f32 v72, v72\n"
"v_mfma_f32_16x16x32_bf16 v[64:67], a[100:103], v[84:87], v[64:67]\n"
"v_cvt_pk_bf16_f32 v59, v70, v71\n"
"v_add_f32 v31, v31, v71\n"
"v_mfma_f32_16x16x32_bf16 v[64:67], a[104:107], v[88:91], v[64:67]\n"
"v_exp_f32 v73, v73\n"
"v_mfma_f32_16x16x32_bf16 v[64:67], a[108:111], v[92:95], v[64:67]\n"
"ds_read_b128 a[64:67], v4 offset:16384\n"
"v_add_f32 v34, v34, v72\n"
"v_mfma_f32_16x16x32_bf16 v[224:227], a[128:131], v[56:59], v[224:227]\n"
"s_mov_b32 m0, 49152\n"
"v_exp_f32 v74, v74\n"
"v_mfma_f32_16x16x32_bf16 v[228:231], a[132:135], v[56:59], v[228:231]\n"
"ds_read_b128 a[68:71], v5 offset:16384\n"
"v_cvt_pk_bf16_f32 v60, v72, v73\n"
"v_add_f32 v34, v34, v73\n"
"v_mfma_f32_16x16x32_bf16 v[232:235], a[136:139], v[56:59], v[232:235]\n"
"buffer_load_dwordx4 v10, s[28:31], s62 offen nt lds\n"
"v_exp_f32 v75, v75\n"
"v_mfma_f32_16x16x32_bf16 v[236:239], a[140:143], v[56:59], v[236:239]\n"
"ds_read_b128 a[72:75], v6 offset:16384\n"
"v_add_f32 v34, v34, v74\n"
"v_mfma_f32_16x16x32_bf16 v[68:71], a[112:115], v[80:83], v[36:39]\n"
"buffer_load_dwordx4 v11, s[28:31], s62 offen offset:1024 nt lds\n"
"v_exp_f32 v76, v76\n"
"v_mfma_f32_16x16x32_bf16 v[68:71], a[116:119], v[84:87], v[68:71]\n"
"ds_read_b128 a[76:79], v7 offset:16384\n"
"v_cvt_pk_bf16_f32 v61, v74, v75\n"
"v_add_f32 v34, v34, v75\n"
"v_mfma_f32_16x16x32_bf16 v[68:71], a[120:123], v[88:91], v[68:71]\n"
"buffer_load_dwordx4 v12, s[28:31], s62 offen offset:2048 nt lds\n"
"v_exp_f32 v77, v77\n"
"v_mfma_f32_16x16x32_bf16 v[68:71], a[124:127], v[92:95], v[68:71]\n"
"ds_read_b128 a[80:83], v4 offset:20480\n"
"v_add_f32 v34, v34, v76\n"
"v_mfma_f32_16x16x32_bf16 v[240:243], a[144:147], v[56:59], v[240:243]\n"
"buffer_load_dwordx4 v13, s[28:31], s62 offen offset:3072 nt lds\n"
"v_exp_f32 v78, v78\n"
"v_mfma_f32_16x16x32_bf16 v[244:247], a[148:151], v[56:59], v[244:247]\n"
"ds_read_b128 a[84:87], v5 offset:20480\n"
"v_cvt_pk_bf16_f32 v62, v76, v77\n"
"v_add_f32 v34, v34, v77\n"
"v_mfma_f32_16x16x32_bf16 v[248:251], a[152:155], v[56:59], v[248:251]\n"
"v_exp_f32 v79, v79\n"
"v_mfma_f32_16x16x32_bf16 v[252:255], a[156:159], v[56:59], v[252:255]\n"
"v_add_f32 v34, v34, v78\n"
"v_mfma_f32_16x16x32_bf16 v[72:75], a[96:99], v[96:99], v[40:43]\n"
"v_exp_f32 v64, v64\n"
"v_mfma_f32_16x16x32_bf16 v[72:75], a[100:103], v[100:103], v[72:75]\n"
"v_cvt_pk_bf16_f32 v63, v78, v79\n"
"v_add_f32 v34, v34, v79\n"
"v_mfma_f32_16x16x32_bf16 v[72:75], a[104:107], v[104:107], v[72:75]\n"
"v_exp_f32 v65, v65\n"
"v_mfma_f32_16x16x32_bf16 v[72:75], a[108:111], v[108:111], v[72:75]\n"
"ds_read_b128 a[88:91], v6 offset:20480\n"
"v_add_f32 v29, v29, v64\n"
"v_mfma_f32_16x16x32_bf16 a[192:195], a[128:131], v[60:63], a[192:195]\n"
"s_mov_b32 m0, 53248\n"
"v_exp_f32 v66, v66\n"
"v_mfma_f32_16x16x32_bf16 a[196:199], a[132:135], v[60:63], a[196:199]\n"
"v_cvt_pk_bf16_f32 v56, v64, v65\n"
"v_add_f32 v29, v29, v65\n"
"v_mfma_f32_16x16x32_bf16 a[200:203], a[136:139], v[60:63], a[200:203]\n"
"buffer_load_dwordx4 v10, s[28:31], s61 offen nt lds\n"
"v_exp_f32 v67, v67\n"
"v_mfma_f32_16x16x32_bf16 a[204:207], a[140:143], v[60:63], a[204:207]\n"
"ds_read_b128 a[92:95], v7 offset:20480\n"
"v_add_f32 v29, v29, v66\n"
"v_mfma_f32_16x16x32_bf16 v[76:79], a[112:115], v[96:99], v[40:43]\n"
"buffer_load_dwordx4 v11, s[28:31], s61 offen offset:1024 nt lds\n"
"v_exp_f32 v68, v68\n"
"v_mfma_f32_16x16x32_bf16 v[76:79], a[116:119], v[100:103], v[76:79]\n"
"v_cvt_pk_bf16_f32 v57, v66, v67\n"
"v_add_f32 v29, v29, v67\n"
"v_mfma_f32_16x16x32_bf16 v[76:79], a[120:123], v[104:107], v[76:79]\n"
"buffer_load_dwordx4 v12, s[28:31], s61 offen offset:2048 nt lds\n"
"v_exp_f32 v69, v69\n"
"v_mfma_f32_16x16x32_bf16 v[76:79], a[124:127], v[108:111], v[76:79]\n"
"v_add_f32 v29, v29, v68\n"
"v_mfma_f32_16x16x32_bf16 a[208:211], a[144:147], v[60:63], a[208:211]\n"
"buffer_load_dwordx4 v13, s[28:31], s61 offen offset:3072 nt lds\n"
"v_exp_f32 v70, v70\n"
"v_mfma_f32_16x16x32_bf16 a[212:215], a[148:151], v[60:63], a[212:215]\n"
"v_cvt_pk_bf16_f32 v58, v68, v69\n"
"v_add_f32 v29, v29, v69\n"
"v_mfma_f32_16x16x32_bf16 a[216:219], a[152:155], v[60:63], a[216:219]\n"
"v_exp_f32 v71, v71\n"
"v_mfma_f32_16x16x32_bf16 a[220:223], a[156:159], v[60:63], a[220:223]\n"
"v_add_f32 v29, v29, v70\n"
"v_mfma_f32_16x16x32_bf16 v[64:67], a[96:99], v[112:115], v[44:47]\n"
"v_exp_f32 v72, v72\n"
"v_mfma_f32_16x16x32_bf16 v[64:67], a[100:103], v[116:119], v[64:67]\n"
"v_cvt_pk_bf16_f32 v59, v70, v71\n"
"v_add_f32 v29, v29, v71\n"
"v_mfma_f32_16x16x32_bf16 v[64:67], a[104:107], v[120:123], v[64:67]\n"
"global_load_dword v33, v32, s[18:19]\n"
"v_exp_f32 v73, v73\n"
"v_mfma_f32_16x16x32_bf16 v[64:67], a[108:111], v[124:127], v[64:67]\n"
"s_waitcnt vmcnt(54) lgkmcnt(0)\n"
"s_barrier\n"
"v_mfma_f32_16x16x32_bf16 v[160:163], a[160:163], v[56:59], v[160:163]\n"
"s_mov_b32 m0, 114688\n"
"v_exp_f32 v74, v74\n"
"v_mfma_f32_16x16x32_bf16 v[164:167], a[164:167], v[56:59], v[164:167]\n"
"ds_read_b64_tr_b16 a[128:129], v8 offset:16384\n"
"s_waitcnt vmcnt(52)\n"
"v_cvt_pk_bf16_f32 v60, v72, v73\n"
"v_mfma_f32_16x16x32_bf16 v[168:171], a[168:171], v[56:59], v[168:171]\n"
"buffer_load_dwordx4 v14, s[36:39], s62 offen nt lds\n"
"v_exp_f32 v75, v75\n"
"v_mfma_f32_16x16x32_bf16 v[172:175], a[172:175], v[56:59], v[172:175]\n"
"ds_read_b64_tr_b16 a[130:131], v8 offset:20480\n"
"ds_read_b64_tr_b16 a[132:133], v9 offset:16384\n"
"buffer_load_dwordx4 v15, s[36:39], s62 offen offset:1024 nt lds\n"
"v_mfma_f32_16x16x32_bf16 v[68:71], a[112:115], v[112:115], v[44:47]\n"
"buffer_load_dwordx4 v14, s[36:39], s62 offen offset:2048 nt lds\n"
"v_exp_f32 v76, v76\n"
"v_mfma_f32_16x16x32_bf16 v[68:71], a[116:119], v[116:119], v[68:71]\n"
"ds_read_b64_tr_b16 a[134:135], v9 offset:20480\n"
"buffer_load_dwordx4 v15, s[36:39], s62 offen offset:3072 nt lds\n"
"v_cvt_pk_bf16_f32 v61, v74, v75\n"
"v_mfma_f32_16x16x32_bf16 v[68:71], a[120:123], v[120:123], v[68:71]\n"
"s_mov_b32 m0, 135168\n"
"s_add_u32 s52, s51, 524288\n"
"v_exp_f32 v77, v77\n"
"v_mfma_f32_16x16x32_bf16 v[68:71], a[124:127], v[124:127], v[68:71]\n"
"ds_read_b64_tr_b16 a[136:137], v8 offset:16896\n"
"ds_read_b64_tr_b16 a[138:139], v8 offset:20992\n"
"buffer_load_dwordx4 v22, s[44:47], s52 offen lds\n"
"v_mfma_f32_16x16x32_bf16 v[176:179], a[176:179], v[56:59], v[176:179]\n"
"buffer_load_dwordx4 v23, s[44:47], s52 offen offset:1024 lds\n"
"v_exp_f32 v78, v78\n"
"v_mfma_f32_16x16x32_bf16 v[180:183], a[180:183], v[56:59], v[180:183]\n"
"ds_read_b64_tr_b16 a[140:141], v9 offset:16896\n"
"buffer_load_dwordx4 v24, s[44:47], s52 offen offset:2048 lds\n"
"v_cvt_pk_bf16_f32 v62, v76, v77\n"
"v_mfma_f32_16x16x32_bf16 v[184:187], a[184:187], v[56:59], v[184:187]\n"
"buffer_load_dwordx4 v25, s[44:47], s52 offen offset:3072 lds\n"
"v_exp_f32 v79, v79\n"
"v_mfma_f32_16x16x32_bf16 v[188:191], a[188:191], v[56:59], v[188:191]\n"
"ds_read_b64_tr_b16 a[142:143], v9 offset:20992\n"
"v_mfma_f32_16x16x32_bf16 v[72:75], a[96:99], v[128:131], v[48:51]\n"
"v_exp_f32 v64, v64\n"
"v_mfma_f32_16x16x32_bf16 v[72:75], a[100:103], v[132:135], v[72:75]\n"
"ds_read_b64_tr_b16 a[144:145], v8 offset:17408\n"
"v_cvt_pk_bf16_f32 v63, v78, v79\n"
"v_mfma_f32_16x16x32_bf16 v[72:75], a[104:107], v[136:139], v[72:75]\n"
"v_exp_f32 v65, v65\n"
"v_mfma_f32_16x16x32_bf16 v[72:75], a[108:111], v[140:143], v[72:75]\n"
"ds_read_b64_tr_b16 a[146:147], v8 offset:21504\n"
"v_add_f32 v31, v31, v64\n"
"v_mfma_f32_16x16x32_bf16 v[192:195], a[160:163], v[60:63], v[192:195]\n"
"s_mov_b32 m0, 118784\n"
"v_exp_f32 v66, v66\n"
"v_mfma_f32_16x16x32_bf16 v[196:199], a[164:167], v[60:63], v[196:199]\n"
"ds_read_b64_tr_b16 a[148:149], v9 offset:17408\n"
"s_waitcnt vmcnt(52)\n"
"v_cvt_pk_bf16_f32 v56, v64, v65\n"
"v_add_f32 v31, v31, v65\n"
"v_mfma_f32_16x16x32_bf16 v[200:203], a[168:171], v[60:63], v[200:203]\n"
"buffer_load_dwordx4 v14, s[36:39], s61 offen nt lds\n"
"v_exp_f32 v67, v67\n"
"v_mfma_f32_16x16x32_bf16 v[204:207], a[172:175], v[60:63], v[204:207]\n"
"ds_read_b64_tr_b16 a[150:151], v9 offset:21504\n"
"v_add_f32 v31, v31, v66\n"
"v_mfma_f32_16x16x32_bf16 v[18:21], a[224:227], v[60:63], v[18:21]\n"
"v_mfma_f32_16x16x32_bf16 v[76:79], a[112:115], v[128:131], v[48:51]\n"
"buffer_load_dwordx4 v15, s[36:39], s61 offen offset:1024 nt lds\n"
"v_exp_f32 v68, v68\n"
"v_mfma_f32_16x16x32_bf16 v[76:79], a[116:119], v[132:135], v[76:79]\n"
"ds_read_b64_tr_b16 a[152:153], v8 offset:17920\n"
"v_cvt_pk_bf16_f32 v57, v66, v67\n"
"v_add_f32 v31, v31, v67\n"
"v_mfma_f32_16x16x32_bf16 v[76:79], a[120:123], v[136:139], v[76:79]\n"
"buffer_load_dwordx4 v14, s[36:39], s61 offen offset:2048 nt lds\n"
"v_exp_f32 v69, v69\n"
"v_mfma_f32_16x16x32_bf16 v[76:79], a[124:127], v[140:143], v[76:79]\n"
"ds_read_b64_tr_b16 a[154:155], v8 offset:22016\n"
"v_add_f32 v31, v31, v68\n"
"v_mfma_f32_16x16x32_bf16 v[208:211], a[176:179], v[60:63], v[208:211]\n"
"buffer_load_dwordx4 v15, s[36:39], s61 offen offset:3072 nt lds\n"
"v_exp_f32 v70, v70\n"
"v_mfma_f32_16x16x32_bf16 v[212:215], a[180:183], v[60:63], v[212:215]\n"
"ds_read_b64_tr_b16 a[156:157], v9 offset:17920\n"
"v_cvt_pk_bf16_f32 v58, v68, v69\n"
"v_add_f32 v31, v31, v69\n"
"v_mfma_f32_16x16x32_bf16 v[216:219], a[184:187], v[60:63], v[216:219]\n"
"s_add_u32 s62, s62, 8192\n"
"s_and_b32 s62, s62, 0x3ffff\n"
"s_add_u32 s61, s62, 4096\n"
"v_exp_f32 v71, v71\n"
"v_mfma_f32_16x16x32_bf16 v[220:223], a[188:191], v[60:63], v[220:223]\n"
"ds_read_b64_tr_b16 a[158:159], v9 offset:22016\n"
"v_add_f32 v31, v31, v70\n"
"v_mfma_f32_16x16x32_bf16 v[64:67], a[64:67], v[80:83], v[36:39]\n"
"v_exp_f32 v72, v72\n"
"v_mfma_f32_16x16x32_bf16 v[64:67], a[68:71], v[84:87], v[64:67]\n"
"v_cvt_pk_bf16_f32 v59, v70, v71\n"
"v_add_f32 v31, v31, v71\n"
"v_mfma_f32_16x16x32_bf16 v[64:67], a[72:75], v[88:91], v[64:67]\n"
"v_exp_f32 v73, v73\n"
"v_mfma_f32_16x16x32_bf16 v[64:67], a[76:79], v[92:95], v[64:67]\n"
"ds_read_b128 a[96:99], v4 offset:24576\n"
"v_add_f32 v34, v34, v72\n"
"v_mfma_f32_16x16x32_bf16 v[224:227], a[160:163], v[56:59], v[224:227]\n"
"s_mov_b32 m0, 57344\n"
"v_exp_f32 v74, v74\n"
"v_mfma_f32_16x16x32_bf16 v[228:231], a[164:167], v[56:59], v[228:231]\n"
"ds_read_b128 a[100:103], v5 offset:24576\n"
"v_cvt_pk_bf16_f32 v60, v72, v73\n"
"v_add_f32 v34, v34, v73\n"
"v_mfma_f32_16x16x32_bf16 v[232:235], a[168:171], v[56:59], v[232:235]\n"
"buffer_load_dwordx4 v10, s[28:31], s62 offen nt lds\n"
"v_exp_f32 v75, v75\n"
"v_mfma_f32_16x16x32_bf16 v[236:239], a[172:175], v[56:59], v[236:239]\n"
"ds_read_b128 a[104:107], v6 offset:24576\n"
"v_add_f32 v34, v34, v74\n"
"v_mfma_f32_16x16x32_bf16 v[68:71], a[80:83], v[80:83], v[36:39]\n"
"buffer_load_dwordx4 v11, s[28:31], s62 offen offset:1024 nt lds\n"
"v_exp_f32 v76, v76\n"
"v_mfma_f32_16x16x32_bf16 v[68:71], a[84:87], v[84:87], v[68:71]\n"
"ds_read_b128 a[108:111], v7 offset:24576\n"
"v_cvt_pk_bf16_f32 v61, v74, v75\n"
"v_add_f32 v34, v34, v75\n"
"v_mfma_f32_16x16x32_bf16 v[68:71], a[88:91], v[88:91], v[68:71]\n"
"buffer_load_dwordx4 v12, s[28:31], s62 offen offset:2048 nt lds\n"
"v_exp_f32 v77, v77\n"
"v_mfma_f32_16x16x32_bf16 v[68:71], a[92:95], v[92:95], v[68:71]\n"
"ds_read_b128 a[112:115], v4 offset:28672\n"
"v_add_f32 v34, v34, v76\n"
"v_mfma_f32_16x16x32_bf16 v[240:243], a[176:179], v[56:59], v[240:243]\n"
"buffer_load_dwordx4 v13, s[28:31], s62 offen offset:3072 nt lds\n"
"v_exp_f32 v78, v78\n"
"v_mfma_f32_16x16x32_bf16 v[244:247], a[180:183], v[56:59], v[244:247]\n"
"ds_read_b128 a[116:119], v5 offset:28672\n"
"v_cvt_pk_bf16_f32 v62, v76, v77\n"
"v_add_f32 v34, v34, v77\n"
"v_mfma_f32_16x16x32_bf16 v[248:251], a[184:187], v[56:59], v[248:251]\n"
"v_exp_f32 v79, v79\n"
"v_mfma_f32_16x16x32_bf16 v[252:255], a[188:191], v[56:59], v[252:255]\n"
"v_add_f32 v34, v34, v78\n"
"v_mfma_f32_16x16x32_bf16 v[72:75], a[64:67], v[96:99], v[40:43]\n"
"v_exp_f32 v64, v64\n"
"v_mfma_f32_16x16x32_bf16 v[72:75], a[68:71], v[100:103], v[72:75]\n"
"v_cvt_pk_bf16_f32 v63, v78, v79\n"
"v_add_f32 v34, v34, v79\n"
"v_mfma_f32_16x16x32_bf16 v[72:75], a[72:75], v[104:107], v[72:75]\n"
"v_exp_f32 v65, v65\n"
"v_mfma_f32_16x16x32_bf16 v[72:75], a[76:79], v[108:111], v[72:75]\n"
"ds_read_b128 a[120:123], v6 offset:28672\n"
"v_add_f32 v29, v29, v64\n"
"v_mfma_f32_16x16x32_bf16 a[192:195], a[160:163], v[60:63], a[192:195]\n"
"s_mov_b32 m0, 61440\n"
"v_exp_f32 v66, v66\n"
"v_mfma_f32_16x16x32_bf16 a[196:199], a[164:167], v[60:63], a[196:199]\n"
"s_waitcnt vmcnt(52)\n"
"v_cvt_pk_bf16_f32 v56, v64, v65\n"
"v_add_f32 v29, v29, v65\n"
"v_mfma_f32_16x16x32_bf16 a[200:203], a[168:171], v[60:63], a[200:203]\n"
"buffer_load_dwordx4 v10, s[28:31], s61 offen nt lds\n"
"v_exp_f32 v67, v67\n"
"v_mfma_f32_16x16x32_bf16 a[204:207], a[172:175], v[60:63], a[204:207]\n"
"ds_read_b128 a[124:127], v7 offset:28672\n"
"buffer_load_dwordx4 v11, s[28:31], s61 offen offset:1024 nt lds\n"
"v_add_f32 v29, v29, v66\n"
"v_mfma_f32_16x16x32_bf16 v[76:79], a[80:83], v[96:99], v[40:43]\n"
"buffer_load_dwordx4 v12, s[28:31], s61 offen offset:2048 nt lds\n"
"v_exp_f32 v68, v68\n"
"v_mfma_f32_16x16x32_bf16 v[76:79], a[84:87], v[100:103], v[76:79]\n"
"buffer_load_dwordx4 v13, s[28:31], s61 offen offset:3072 nt lds\n"
"v_cvt_pk_bf16_f32 v57, v66, v67\n"
"v_add_f32 v29, v29, v67\n"
"v_mfma_f32_16x16x32_bf16 v[76:79], a[88:91], v[104:107], v[76:79]\n"
"s_mov_b32 m0, 139264\n"
"s_add_u32 s52, s51, 1048576\n"
"v_exp_f32 v69, v69\n"
"v_mfma_f32_16x16x32_bf16 v[76:79], a[92:95], v[108:111], v[76:79]\n"
"buffer_load_dwordx4 v22, s[44:47], s52 offen lds\n"
"v_add_f32 v29, v29, v68\n"
"v_mfma_f32_16x16x32_bf16 a[208:211], a[176:179], v[60:63], a[208:211]\n"
"buffer_load_dwordx4 v23, s[44:47], s52 offen offset:1024 lds\n"
"v_exp_f32 v70, v70\n"
"v_mfma_f32_16x16x32_bf16 a[212:215], a[180:183], v[60:63], a[212:215]\n"
"buffer_load_dwordx4 v24, s[44:47], s52 offen offset:2048 lds\n"
"v_cvt_pk_bf16_f32 v58, v68, v69\n"
"v_add_f32 v29, v29, v69\n"
"v_mfma_f32_16x16x32_bf16 a[216:219], a[184:187], v[60:63], a[216:219]\n"
"buffer_load_dwordx4 v25, s[44:47], s52 offen offset:3072 lds\n"
"v_exp_f32 v71, v71\n"
"v_mfma_f32_16x16x32_bf16 a[220:223], a[188:191], v[60:63], a[220:223]\n"
"v_add_f32 v29, v29, v70\n"
"v_mfma_f32_16x16x32_bf16 v[64:67], a[64:67], v[112:115], v[44:47]\n"
"v_exp_f32 v72, v72\n"
"v_mfma_f32_16x16x32_bf16 v[64:67], a[68:71], v[116:119], v[64:67]\n"
"v_cvt_pk_bf16_f32 v59, v70, v71\n"
"v_add_f32 v29, v29, v71\n"
"v_mfma_f32_16x16x32_bf16 v[64:67], a[72:75], v[120:123], v[64:67]\n"
"v_exp_f32 v73, v73\n"
"v_mfma_f32_16x16x32_bf16 v[64:67], a[76:79], v[124:127], v[64:67]\n"
"s_waitcnt vmcnt(62) lgkmcnt(0)\n"
"s_barrier\n"
"v_mfma_f32_16x16x32_bf16 v[160:163], a[128:131], v[56:59], v[160:163]\n"
"s_mov_b32 m0, 122880\n"
"v_exp_f32 v74, v74\n"
"v_mfma_f32_16x16x32_bf16 v[164:167], a[132:135], v[56:59], v[164:167]\n"
"ds_read_b64_tr_b16 a[160:161], v8 offset:24576\n"
"s_waitcnt vmcnt(52)\n"
"v_cvt_pk_bf16_f32 v60, v72, v73\n"
"v_mfma_f32_16x16x32_bf16 v[168:171], a[136:139], v[56:59], v[168:171]\n"
"buffer_load_dwordx4 v14, s[36:39], s62 offen nt lds\n"
"v_exp_f32 v75, v75\n"
"v_mfma_f32_16x16x32_bf16 v[172:175], a[140:143], v[56:59], v[172:175]\n"
"ds_read_b64_tr_b16 a[162:163], v8 offset:28672\n"
"ds_read_b64_tr_b16 a[164:165], v9 offset:24576\n"
"v_mfma_f32_16x16x32_bf16 v[68:71], a[80:83], v[112:115], v[44:47]\n"
"buffer_load_dwordx4 v15, s[36:39], s62 offen offset:1024 nt lds\n"
"v_exp_f32 v76, v76\n"
"v_mfma_f32_16x16x32_bf16 v[68:71], a[84:87], v[116:119], v[68:71]\n"
"ds_read_b64_tr_b16 a[166:167], v9 offset:28672\n"
"v_cvt_pk_bf16_f32 v61, v74, v75\n"
"v_mfma_f32_16x16x32_bf16 v[68:71], a[88:91], v[120:123], v[68:71]\n"
"buffer_load_dwordx4 v14, s[36:39], s62 offen offset:2048 nt lds\n"
"v_exp_f32 v77, v77\n"
"v_mfma_f32_16x16x32_bf16 v[68:71], a[92:95], v[124:127], v[68:71]\n"
"ds_read_b64_tr_b16 a[168:169], v8 offset:25088\n"
"ds_read_b64_tr_b16 a[170:171], v8 offset:29184\n"
"v_mfma_f32_16x16x32_bf16 v[176:179], a[144:147], v[56:59], v[176:179]\n"
"buffer_load_dwordx4 v15, s[36:39], s62 offen offset:3072 nt lds\n"
"v_exp_f32 v78, v78\n"
"v_mfma_f32_16x16x32_bf16 v[180:183], a[148:151], v[56:59], v[180:183]\n"
"ds_read_b64_tr_b16 a[172:173], v9 offset:25088\n"
"v_cvt_pk_bf16_f32 v62, v76, v77\n"
"v_mfma_f32_16x16x32_bf16 v[184:187], a[152:155], v[56:59], v[184:187]\n"
"v_exp_f32 v79, v79\n"
"v_mfma_f32_16x16x32_bf16 v[188:191], a[156:159], v[56:59], v[188:191]\n"
"ds_read_b64_tr_b16 a[174:175], v9 offset:29184\n"
"v_mfma_f32_16x16x32_bf16 v[72:75], a[64:67], v[128:131], v[48:51]\n"
"v_exp_f32 v64, v64\n"
"v_mfma_f32_16x16x32_bf16 v[72:75], a[68:71], v[132:135], v[72:75]\n"
"ds_read_b64_tr_b16 a[176:177], v8 offset:25600\n"
"v_cvt_pk_bf16_f32 v63, v78, v79\n"
"v_mfma_f32_16x16x32_bf16 v[72:75], a[72:75], v[136:139], v[72:75]\n"
"v_exp_f32 v65, v65\n"
"v_mfma_f32_16x16x32_bf16 v[72:75], a[76:79], v[140:143], v[72:75]\n"
"ds_read_b64_tr_b16 a[178:179], v8 offset:29696\n"
"v_add_f32 v31, v31, v64\n"
"v_mfma_f32_16x16x32_bf16 v[192:195], a[128:131], v[60:63], v[192:195]\n"
"s_mov_b32 m0, 126976\n"
"v_exp_f32 v66, v66\n"
"v_mfma_f32_16x16x32_bf16 v[196:199], a[132:135], v[60:63], v[196:199]\n"
"ds_read_b64_tr_b16 a[180:181], v9 offset:25600\n"
"v_cvt_pk_bf16_f32 v56, v64, v65\n"
"v_add_f32 v31, v31, v65\n"
"v_mfma_f32_16x16x32_bf16 v[200:203], a[136:139], v[60:63], v[200:203]\n"
"buffer_load_dwordx4 v14, s[36:39], s61 offen nt lds\n"
"v_exp_f32 v67, v67\n"
"v_mfma_f32_16x16x32_bf16 v[204:207], a[140:143], v[60:63], v[204:207]\n"
"ds_read_b64_tr_b16 a[182:183], v9 offset:29696\n"
"v_add_f32 v31, v31, v66\n"
"v_mfma_f32_16x16x32_bf16 v[18:21], a[224:227], v[60:63], v[18:21]\n"
"v_mfma_f32_16x16x32_bf16 v[76:79], a[80:83], v[128:131], v[48:51]\n"
"buffer_load_dwordx4 v15, s[36:39], s61 offen offset:1024 nt lds\n"
"v_exp_f32 v68, v68\n"
"v_mfma_f32_16x16x32_bf16 v[76:79], a[84:87], v[132:135], v[76:79]\n"
"ds_read_b64_tr_b16 a[184:185], v8 offset:26112\n"
"v_cvt_pk_bf16_f32 v57, v66, v67\n"
"v_add_f32 v31, v31, v67\n"
"v_mfma_f32_16x16x32_bf16 v[76:79], a[88:91], v[136:139], v[76:79]\n"
"buffer_load_dwordx4 v14, s[36:39], s61 offen offset:2048 nt lds\n"
"v_exp_f32 v69, v69\n"
"v_mfma_f32_16x16x32_bf16 v[76:79], a[92:95], v[140:143], v[76:79]\n"
"ds_read_b64_tr_b16 a[186:187], v8 offset:30208\n"
"v_add_f32 v31, v31, v68\n"
"v_mfma_f32_16x16x32_bf16 v[208:211], a[144:147], v[60:63], v[208:211]\n"
"buffer_load_dwordx4 v15, s[36:39], s61 offen offset:3072 nt lds\n"
"v_exp_f32 v70, v70\n"
"v_mfma_f32_16x16x32_bf16 v[212:215], a[148:151], v[60:63], v[212:215]\n"
"ds_read_b64_tr_b16 a[188:189], v9 offset:26112\n"
"v_cvt_pk_bf16_f32 v58, v68, v69\n"
"v_add_f32 v31, v31, v69\n"
"v_mfma_f32_16x16x32_bf16 v[216:219], a[152:155], v[60:63], v[216:219]\n"
"s_add_u32 s62, s62, 8192\n"
"s_and_b32 s62, s62, 0x3ffff\n"
"s_add_u32 s61, s62, 4096\n"
"v_exp_f32 v71, v71\n"
"v_mfma_f32_16x16x32_bf16 v[220:223], a[156:159], v[60:63], v[220:223]\n"
"ds_read_b64_tr_b16 a[190:191], v9 offset:30208\n"
"v_add_f32 v31, v31, v70\n"
"v_mfma_f32_16x16x32_bf16 v[64:67], a[96:99], v[80:83], v[36:39]\n"
"v_exp_f32 v72, v72\n"
"v_mfma_f32_16x16x32_bf16 v[64:67], a[100:103], v[84:87], v[64:67]\n"
"v_cvt_pk_bf16_f32 v59, v70, v71\n"
"v_add_f32 v31, v31, v71\n"
"v_mfma_f32_16x16x32_bf16 v[64:67], a[104:107], v[88:91], v[64:67]\n"
"v_exp_f32 v73, v73\n"
"v_mfma_f32_16x16x32_bf16 v[64:67], a[108:111], v[92:95], v[64:67]\n"
"ds_read_b128 a[64:67], v4 offset:32768\n"
"v_add_f32 v34, v34, v72\n"
"v_mfma_f32_16x16x32_bf16 v[224:227], a[128:131], v[56:59], v[224:227]\n"
"s_mov_b32 m0, 0\n"
"v_exp_f32 v74, v74\n"
"v_mfma_f32_16x16x32_bf16 v[228:231], a[132:135], v[56:59], v[228:231]\n"
"ds_read_b128 a[68:71], v5 offset:32768\n"
"s_waitcnt vmcnt(52)\n"
"v_cvt_pk_bf16_f32 v60, v72, v73\n"
"v_add_f32 v34, v34, v73\n"
"v_mfma_f32_16x16x32_bf16 v[232:235], a[136:139], v[56:59], v[232:235]\n"
"buffer_load_dwordx4 v10, s[28:31], s62 offen nt lds\n"
"v_exp_f32 v75, v75\n"
"v_mfma_f32_16x16x32_bf16 v[236:239], a[140:143], v[56:59], v[236:239]\n"
"ds_read_b128 a[72:75], v6 offset:32768\n"
"buffer_load_dwordx4 v11, s[28:31], s62 offen offset:1024 nt lds\n"
"v_add_f32 v34, v34, v74\n"
"v_mfma_f32_16x16x32_bf16 v[68:71], a[112:115], v[80:83], v[36:39]\n"
"buffer_load_dwordx4 v12, s[28:31], s62 offen offset:2048 nt lds\n"
"v_exp_f32 v76, v76\n"
"v_mfma_f32_16x16x32_bf16 v[68:71], a[116:119], v[84:87], v[68:71]\n"
"ds_read_b128 a[76:79], v7 offset:32768\n"
"buffer_load_dwordx4 v13, s[28:31], s62 offen offset:3072 nt lds\n"
"v_cvt_pk_bf16_f32 v61, v74, v75\n"
"v_add_f32 v34, v34, v75\n"
"v_mfma_f32_16x16x32_bf16 v[68:71], a[120:123], v[88:91], v[68:71]\n"
"s_mov_b32 m0, 143360\n"
"s_add_u32 s52, s51, 1572864\n"
"v_exp_f32 v77, v77\n"
"v_mfma_f32_16x16x32_bf16 v[68:71], a[124:127], v[92:95], v[68:71]\n"
"ds_read_b128 a[80:83], v4 offset:36864\n"
"buffer_load_dwordx4 v22, s[44:47], s52 offen lds\n"
"v_add_f32 v34, v34, v76\n"
"v_mfma_f32_16x16x32_bf16 v[240:243], a[144:147], v[56:59], v[240:243]\n"
"buffer_load_dwordx4 v23, s[44:47], s52 offen offset:1024 lds\n"
"v_exp_f32 v78, v78\n"
"v_mfma_f32_16x16x32_bf16 v[244:247], a[148:151], v[56:59], v[244:247]\n"
"ds_read_b128 a[84:87], v5 offset:36864\n"
"buffer_load_dwordx4 v24, s[44:47], s52 offen offset:2048 lds\n"
"v_cvt_pk_bf16_f32 v62, v76, v77\n"
"v_add_f32 v34, v34, v77\n"
"v_mfma_f32_16x16x32_bf16 v[248:251], a[152:155], v[56:59], v[248:251]\n"
"buffer_load_dwordx4 v25, s[44:47], s52 offen offset:3072 lds\n"
"v_exp_f32 v79, v79\n"
"v_mfma_f32_16x16x32_bf16 v[252:255], a[156:159], v[56:59], v[252:255]\n"
"v_add_f32 v34, v34, v78\n"
"v_mfma_f32_16x16x32_bf16 v[72:75], a[96:99], v[96:99], v[40:43]\n"
"v_exp_f32 v64, v64\n"
"v_mfma_f32_16x16x32_bf16 v[72:75], a[100:103], v[100:103], v[72:75]\n"
"v_cvt_pk_bf16_f32 v63, v78, v79\n"
"v_add_f32 v34, v34, v79\n"
"v_mfma_f32_16x16x32_bf16 v[72:75], a[104:107], v[104:107], v[72:75]\n"
"v_exp_f32 v65, v65\n"
"v_mfma_f32_16x16x32_bf16 v[72:75], a[108:111], v[108:111], v[72:75]\n"
"ds_read_b128 a[88:91], v6 offset:36864\n"
"v_add_f32 v29, v29, v64\n"
"v_mfma_f32_16x16x32_bf16 a[192:195], a[128:131], v[60:63], a[192:195]\n"
"s_mov_b32 m0, 4096\n"
"v_exp_f32 v66, v66\n"
"v_mfma_f32_16x16x32_bf16 a[196:199], a[132:135], v[60:63], a[196:199]\n"
"s_waitcnt vmcnt(52)\n"
"v_cvt_pk_bf16_f32 v56, v64, v65\n"
"v_add_f32 v29, v29, v65\n"
"v_mfma_f32_16x16x32_bf16 a[200:203], a[136:139], v[60:63], a[200:203]\n"
"buffer_load_dwordx4 v10, s[28:31], s61 offen nt lds\n"
"v_exp_f32 v67, v67\n"
"v_mfma_f32_16x16x32_bf16 a[204:207], a[140:143], v[60:63], a[204:207]\n"
"ds_read_b128 a[92:95], v7 offset:36864\n"
"v_add_f32 v29, v29, v66\n"
"v_mfma_f32_16x16x32_bf16 v[76:79], a[112:115], v[96:99], v[40:43]\n"
"buffer_load_dwordx4 v11, s[28:31], s61 offen offset:1024 nt lds\n"
"v_exp_f32 v68, v68\n"
"v_mfma_f32_16x16x32_bf16 v[76:79], a[116:119], v[100:103], v[76:79]\n"
"v_cvt_pk_bf16_f32 v57, v66, v67\n"
"v_add_f32 v29, v29, v67\n"
"v_mfma_f32_16x16x32_bf16 v[76:79], a[120:123], v[104:107], v[76:79]\n"
"buffer_load_dwordx4 v12, s[28:31], s61 offen offset:2048 nt lds\n"
"v_exp_f32 v69, v69\n"
"v_mfma_f32_16x16x32_bf16 v[76:79], a[124:127], v[108:111], v[76:79]\n"
"v_add_f32 v29, v29, v68\n"
"v_mfma_f32_16x16x32_bf16 a[208:211], a[144:147], v[60:63], a[208:211]\n"
"buffer_load_dwordx4 v13, s[28:31], s61 offen offset:3072 nt lds\n"
"v_exp_f32 v70, v70\n"
"v_mfma_f32_16x16x32_bf16 a[212:215], a[148:151], v[60:63], a[212:215]\n"
"v_cvt_pk_bf16_f32 v58, v68, v69\n"
"v_add_f32 v29, v29, v69\n"
"v_mfma_f32_16x16x32_bf16 a[216:219], a[152:155], v[60:63], a[216:219]\n"
"v_exp_f32 v71, v71\n"
"v_mfma_f32_16x16x32_bf16 a[220:223], a[156:159], v[60:63], a[220:223]\n"
"v_add_f32 v29, v29, v70\n"
"v_mfma_f32_16x16x32_bf16 v[64:67], a[96:99], v[112:115], v[44:47]\n"
"v_exp_f32 v72, v72\n"
"v_mfma_f32_16x16x32_bf16 v[64:67], a[100:103], v[116:119], v[64:67]\n"
"v_cvt_pk_bf16_f32 v59, v70, v71\n"
"v_add_f32 v29, v29, v71\n"
"v_mfma_f32_16x16x32_bf16 v[64:67], a[104:107], v[120:123], v[64:67]\n"
"v_exp_f32 v73, v73\n"
"v_mfma_f32_16x16x32_bf16 v[64:67], a[108:111], v[124:127], v[64:67]\n"
"s_waitcnt vmcnt(63) lgkmcnt(0)\n"
"s_barrier\n"
"v_mfma_f32_16x16x32_bf16 v[160:163], a[160:163], v[56:59], v[160:163]\n"
"s_mov_b32 m0, 65536\n"
"v_exp_f32 v74, v74\n"
"v_mfma_f32_16x16x32_bf16 v[164:167], a[164:167], v[56:59], v[164:167]\n"
"ds_read_b64_tr_b16 a[128:129], v8 offset:32768\n"
"v_cvt_pk_bf16_f32 v60, v72, v73\n"
"v_mfma_f32_16x16x32_bf16 v[168:171], a[168:171], v[56:59], v[168:171]\n"
"buffer_load_dwordx4 v14, s[36:39], s62 offen nt lds\n"
"v_exp_f32 v75, v75\n"
"v_mfma_f32_16x16x32_bf16 v[172:175], a[172:175], v[56:59], v[172:175]\n"
"ds_read_b64_tr_b16 a[130:131], v8 offset:36864\n"
"ds_read_b64_tr_b16 a[132:133], v9 offset:32768\n"
"v_mfma_f32_16x16x32_bf16 v[68:71], a[112:115], v[112:115], v[44:47]\n"
"buffer_load_dwordx4 v15, s[36:39], s62 offen offset:1024 nt lds\n"
"v_exp_f32 v76, v76\n"
"v_mfma_f32_16x16x32_bf16 v[68:71], a[116:119], v[116:119], v[68:71]\n"
"ds_read_b64_tr_b16 a[134:135], v9 offset:36864\n"
"v_cvt_pk_bf16_f32 v61, v74, v75\n"
"v_mfma_f32_16x16x32_bf16 v[68:71], a[120:123], v[120:123], v[68:71]\n"
"buffer_load_dwordx4 v14, s[36:39], s62 offen offset:2048 nt lds\n"
"v_exp_f32 v77, v77\n"
"v_mfma_f32_16x16x32_bf16 v[68:71], a[124:127], v[124:127], v[68:71]\n"
"ds_read_b64_tr_b16 a[136:137], v8 offset:33280\n"
"ds_read_b64_tr_b16 a[138:139], v8 offset:37376\n"
"v_mfma_f32_16x16x32_bf16 v[176:179], a[176:179], v[56:59], v[176:179]\n"
"buffer_load_dwordx4 v15, s[36:39], s62 offen offset:3072 nt lds\n"
"v_exp_f32 v78, v78\n"
"v_mfma_f32_16x16x32_bf16 v[180:183], a[180:183], v[56:59], v[180:183]\n"
"ds_read_b64_tr_b16 a[140:141], v9 offset:33280\n"
"v_cvt_pk_bf16_f32 v62, v76, v77\n"
"v_mfma_f32_16x16x32_bf16 v[184:187], a[184:187], v[56:59], v[184:187]\n"
"v_exp_f32 v79, v79\n"
"v_mfma_f32_16x16x32_bf16 v[188:191], a[188:191], v[56:59], v[188:191]\n"
"ds_read_b64_tr_b16 a[142:143], v9 offset:37376\n"
"v_mfma_f32_16x16x32_bf16 v[72:75], a[96:99], v[128:131], v[48:51]\n"
"v_exp_f32 v64, v64\n"
"v_mfma_f32_16x16x32_bf16 v[72:75], a[100:103], v[132:135], v[72:75]\n"
"ds_read_b64_tr_b16 a[144:145], v8 offset:33792\n"
"v_cvt_pk_bf16_f32 v63, v78, v79\n"
"v_mfma_f32_16x16x32_bf16 v[72:75], a[104:107], v[136:139], v[72:75]\n"
"v_exp_f32 v65, v65\n"
"v_mfma_f32_16x16x32_bf16 v[72:75], a[108:111], v[140:143], v[72:75]\n"
"ds_read_b64_tr_b16 a[146:147], v8 offset:37888\n"
"v_add_f32 v31, v31, v64\n"
"v_mfma_f32_16x16x32_bf16 v[192:195], a[160:163], v[60:63], v[192:195]\n"
"s_mov_b32 m0, 69632\n"
"v_exp_f32 v66, v66\n"
"v_mfma_f32_16x16x32_bf16 v[196:199], a[164:167], v[60:63], v[196:199]\n"
"ds_read_b64_tr_b16 a[148:149], v9 offset:33792\n"
"s_waitcnt vmcnt(52)\n"
"v_cvt_pk_bf16_f32 v56, v64, v65\n"
"v_add_f32 v31, v31, v65\n"
"v_mfma_f32_16x16x32_bf16 v[200:203], a[168:171], v[60:63], v[200:203]\n"
"buffer_load_dwordx4 v14, s[36:39], s61 offen nt lds\n"
"v_exp_f32 v67, v67\n"
"v_mfma_f32_16x16x32_bf16 v[204:207], a[172:175], v[60:63], v[204:207]\n"
"ds_read_b64_tr_b16 a[150:151], v9 offset:37888\n"
"buffer_load_dwordx4 v15, s[36:39], s61 offen offset:1024 nt lds\n"
"v_add_f32 v31, v31, v66\n"
"v_mfma_f32_16x16x32_bf16 v[18:21], a[224:227], v[60:63], v[18:21]\n"
"v_mfma_f32_16x16x32_bf16 v[76:79], a[112:115], v[128:131], v[48:51]\n"
"buffer_load_dwordx4 v14, s[36:39], s61 offen offset:2048 nt lds\n"
"v_exp_f32 v68, v68\n"
"v_mfma_f32_16x16x32_bf16 v[76:79], a[116:119], v[132:135], v[76:79]\n"
"ds_read_b64_tr_b16 a[152:153], v8 offset:34304\n"
"buffer_load_dwordx4 v15, s[36:39], s61 offen offset:3072 nt lds\n"
"v_cvt_pk_bf16_f32 v57, v66, v67\n"
"v_add_f32 v31, v31, v67\n"
"v_mfma_f32_16x16x32_bf16 v[76:79], a[120:123], v[136:139], v[76:79]\n"
"s_add_u32 s62, s62, 8192\n"
"s_and_b32 s62, s62, 0x3ffff\n"
"s_add_u32 s61, s62, 4096\n"
"s_mov_b32 m0, 147456\n"
"s_add_u32 s52, s51, 32768\n"
"v_exp_f32 v69, v69\n"
"v_mfma_f32_16x16x32_bf16 v[76:79], a[124:127], v[140:143], v[76:79]\n"
"ds_read_b64_tr_b16 a[154:155], v8 offset:38400\n"
"buffer_load_dwordx4 v22, s[44:47], s52 offen lds\n"
"v_add_f32 v31, v31, v68\n"
"v_mfma_f32_16x16x32_bf16 v[208:211], a[176:179], v[60:63], v[208:211]\n"
"buffer_load_dwordx4 v23, s[44:47], s52 offen offset:1024 lds\n"
"v_exp_f32 v70, v70\n"
"v_mfma_f32_16x16x32_bf16 v[212:215], a[180:183], v[60:63], v[212:215]\n"
"ds_read_b64_tr_b16 a[156:157], v9 offset:34304\n"
"buffer_load_dwordx4 v24, s[44:47], s52 offen offset:2048 lds\n"
"v_cvt_pk_bf16_f32 v58, v68, v69\n"
"v_add_f32 v31, v31, v69\n"
"v_mfma_f32_16x16x32_bf16 v[216:219], a[184:187], v[60:63], v[216:219]\n"
"buffer_load_dwordx4 v25, s[44:47], s52 offen offset:3072 lds\n"
"v_exp_f32 v71, v71\n"
"v_mfma_f32_16x16x32_bf16 v[220:223], a[188:191], v[60:63], v[220:223]\n"
"ds_read_b64_tr_b16 a[158:159], v9 offset:38400\n"
"v_add_f32 v31, v31, v70\n"
"v_mfma_f32_16x16x32_bf16 v[64:67], a[64:67], v[80:83], v[36:39]\n"
"v_exp_f32 v72, v72\n"
"v_mfma_f32_16x16x32_bf16 v[64:67], a[68:71], v[84:87], v[64:67]\n"
"v_cvt_pk_bf16_f32 v59, v70, v71\n"
"v_add_f32 v31, v31, v71\n"
"v_mfma_f32_16x16x32_bf16 v[64:67], a[72:75], v[88:91], v[64:67]\n"
"v_exp_f32 v73, v73\n"
"v_mfma_f32_16x16x32_bf16 v[64:67], a[76:79], v[92:95], v[64:67]\n"
"ds_read_b128 a[96:99], v4 offset:40960\n"
"v_add_f32 v34, v34, v72\n"
"v_mfma_f32_16x16x32_bf16 v[224:227], a[160:163], v[56:59], v[224:227]\n"
"s_mov_b32 m0, 8192\n"
"v_exp_f32 v74, v74\n"
"v_mfma_f32_16x16x32_bf16 v[228:231], a[164:167], v[56:59], v[228:231]\n"
"ds_read_b128 a[100:103], v5 offset:40960\n"
"s_waitcnt vmcnt(52)\n"
"v_cvt_pk_bf16_f32 v60, v72, v73\n"
"v_add_f32 v34, v34, v73\n"
"v_mfma_f32_16x16x32_bf16 v[232:235], a[168:171], v[56:59], v[232:235]\n"
"buffer_load_dwordx4 v10, s[28:31], s62 offen nt lds\n"
"v_exp_f32 v75, v75\n"
"v_mfma_f32_16x16x32_bf16 v[236:239], a[172:175], v[56:59], v[236:239]\n"
"ds_read_b128 a[104:107], v6 offset:40960\n"
"v_add_f32 v34, v34, v74\n"
"v_mfma_f32_16x16x32_bf16 v[68:71], a[80:83], v[80:83], v[36:39]\n"
"buffer_load_dwordx4 v11, s[28:31], s62 offen offset:1024 nt lds\n"
"v_exp_f32 v76, v76\n"
"v_mfma_f32_16x16x32_bf16 v[68:71], a[84:87], v[84:87], v[68:71]\n"
"ds_read_b128 a[108:111], v7 offset:40960\n"
"v_cvt_pk_bf16_f32 v61, v74, v75\n"
"v_add_f32 v34, v34, v75\n"
"v_mfma_f32_16x16x32_bf16 v[68:71], a[88:91], v[88:91], v[68:71]\n"
"buffer_load_dwordx4 v12, s[28:31], s62 offen offset:2048 nt lds\n"
"v_exp_f32 v77, v77\n"
"v_mfma_f32_16x16x32_bf16 v[68:71], a[92:95], v[92:95], v[68:71]\n"
"ds_read_b128 a[112:115], v4 offset:45056\n"
"v_add_f32 v34, v34, v76\n"
"v_mfma_f32_16x16x32_bf16 v[240:243], a[176:179], v[56:59], v[240:243]\n"
"buffer_load_dwordx4 v13, s[28:31], s62 offen offset:3072 nt lds\n"
"v_exp_f32 v78, v78\n"
"v_mfma_f32_16x16x32_bf16 v[244:247], a[180:183], v[56:59], v[244:247]\n"
"ds_read_b128 a[116:119], v5 offset:45056\n"
"v_cvt_pk_bf16_f32 v62, v76, v77\n"
"v_add_f32 v34, v34, v77\n"
"v_mfma_f32_16x16x32_bf16 v[248:251], a[184:187], v[56:59], v[248:251]\n"
"v_exp_f32 v79, v79\n"
"v_mfma_f32_16x16x32_bf16 v[252:255], a[188:191], v[56:59], v[252:255]\n"
"v_add_f32 v34, v34, v78\n"
"v_mfma_f32_16x16x32_bf16 v[72:75], a[64:67], v[96:99], v[40:43]\n"
"v_exp_f32 v64, v64\n"
"v_mfma_f32_16x16x32_bf16 v[72:75], a[68:71], v[100:103], v[72:75]\n"
"v_cvt_pk_bf16_f32 v63, v78, v79\n"
"v_add_f32 v34, v34, v79\n"
"v_mfma_f32_16x16x32_bf16 v[72:75], a[72:75], v[104:107], v[72:75]\n"
"v_exp_f32 v65, v65\n"
"v_mfma_f32_16x16x32_bf16 v[72:75], a[76:79], v[108:111], v[72:75]\n"
"ds_read_b128 a[120:123], v6 offset:45056\n"
"v_add_f32 v29, v29, v64\n"
"v_mfma_f32_16x16x32_bf16 a[192:195], a[160:163], v[60:63], a[192:195]\n"
"s_mov_b32 m0, 12288\n"
"v_exp_f32 v66, v66\n"
"v_mfma_f32_16x16x32_bf16 a[196:199], a[164:167], v[60:63], a[196:199]\n"
"v_cvt_pk_bf16_f32 v56, v64, v65\n"
"v_add_f32 v29, v29, v65\n"
"v_mfma_f32_16x16x32_bf16 a[200:203], a[168:171], v[60:63], a[200:203]\n"
"buffer_load_dwordx4 v10, s[28:31], s61 offen nt lds\n"
"v_exp_f32 v67, v67\n"
"v_mfma_f32_16x16x32_bf16 a[204:207], a[172:175], v[60:63], a[204:207]\n"
"ds_read_b128 a[124:127], v7 offset:45056\n"
"v_add_f32 v29, v29, v66\n"
"v_mfma_f32_16x16x32_bf16 v[76:79], a[80:83], v[96:99], v[40:43]\n"
"buffer_load_dwordx4 v11, s[28:31], s61 offen offset:1024 nt lds\n"
"v_exp_f32 v68, v68\n"
"v_mfma_f32_16x16x32_bf16 v[76:79], a[84:87], v[100:103], v[76:79]\n"
"v_cvt_pk_bf16_f32 v57, v66, v67\n"
"v_add_f32 v29, v29, v67\n"
"v_mfma_f32_16x16x32_bf16 v[76:79], a[88:91], v[104:107], v[76:79]\n"
"buffer_load_dwordx4 v12, s[28:31], s61 offen offset:2048 nt lds\n"
"v_exp_f32 v69, v69\n"
"v_mfma_f32_16x16x32_bf16 v[76:79], a[92:95], v[108:111], v[76:79]\n"
"v_add_f32 v29, v29, v68\n"
"v_mfma_f32_16x16x32_bf16 a[208:211], a[176:179], v[60:63], a[208:211]\n"
"buffer_load_dwordx4 v13, s[28:31], s61 offen offset:3072 nt lds\n"
"v_exp_f32 v70, v70\n"
"v_mfma_f32_16x16x32_bf16 a[212:215], a[180:183], v[60:63], a[212:215]\n"
"v_cvt_pk_bf16_f32 v58, v68, v69\n"
"v_add_f32 v29, v29, v69\n"
"v_mfma_f32_16x16x32_bf16 a[216:219], a[184:187], v[60:63], a[216:219]\n"
"v_exp_f32 v71, v71\n"
"v_mfma_f32_16x16x32_bf16 a[220:223], a[188:191], v[60:63], a[220:223]\n"
"v_add_f32 v29, v29, v70\n"
"v_mfma_f32_16x16x32_bf16 v[64:67], a[64:67], v[112:115], v[44:47]\n"
"v_exp_f32 v72, v72\n"
"v_mfma_f32_16x16x32_bf16 v[64:67], a[68:71], v[116:119], v[64:67]\n"
"v_cvt_pk_bf16_f32 v59, v70, v71\n"
"v_add_f32 v29, v29, v71\n"
"v_mfma_f32_16x16x32_bf16 v[64:67], a[72:75], v[120:123], v[64:67]\n"
"v_exp_f32 v73, v73\n"
"v_mfma_f32_16x16x32_bf16 v[64:67], a[76:79], v[124:127], v[64:67]\n"
"s_waitcnt vmcnt(63) lgkmcnt(0)\n"
"s_barrier\n"
"v_mfma_f32_16x16x32_bf16 v[160:163], a[128:131], v[56:59], v[160:163]\n"
"s_mov_b32 m0, 73728\n"
"v_exp_f32 v74, v74\n"
"v_mfma_f32_16x16x32_bf16 v[164:167], a[132:135], v[56:59], v[164:167]\n"
"ds_read_b64_tr_b16 a[160:161], v8 offset:40960\n"
"s_waitcnt vmcnt(52)\n"
"v_cvt_pk_bf16_f32 v60, v72, v73\n"
"v_mfma_f32_16x16x32_bf16 v[168:171], a[136:139], v[56:59], v[168:171]\n"
"buffer_load_dwordx4 v14, s[36:39], s62 offen nt lds\n"
"v_exp_f32 v75, v75\n"
"v_mfma_f32_16x16x32_bf16 v[172:175], a[140:143], v[56:59], v[172:175]\n"
"ds_read_b64_tr_b16 a[162:163], v8 offset:45056\n"
"ds_read_b64_tr_b16 a[164:165], v9 offset:40960\n"
"buffer_load_dwordx4 v15, s[36:39], s62 offen offset:1024 nt lds\n"
"v_mfma_f32_16x16x32_bf16 v[68:71], a[80:83], v[112:115], v[44:47]\n"
"buffer_load_dwordx4 v14, s[36:39], s62 offen offset:2048 nt lds\n"
"v_exp_f32 v76, v76\n"
"v_mfma_f32_16x16x32_bf16 v[68:71], a[84:87], v[116:119], v[68:71]\n"
"ds_read_b64_tr_b16 a[166:167], v9 offset:45056\n"
"buffer_load_dwordx4 v15, s[36:39], s62 offen offset:3072 nt lds\n"
"v_cvt_pk_bf16_f32 v61, v74, v75\n"
"v_mfma_f32_16x16x32_bf16 v[68:71], a[88:91], v[120:123], v[68:71]\n"
"s_mov_b32 m0, 151552\n"
"s_add_u32 s52, s51, 557056\n"
"v_exp_f32 v77, v77\n"
"v_mfma_f32_16x16x32_bf16 v[68:71], a[92:95], v[124:127], v[68:71]\n"
"ds_read_b64_tr_b16 a[168:169], v8 offset:41472\n"
"ds_read_b64_tr_b16 a[170:171], v8 offset:45568\n"
"buffer_load_dwordx4 v22, s[44:47], s52 offen lds\n"
"v_mfma_f32_16x16x32_bf16 v[176:179], a[144:147], v[56:59], v[176:179]\n"
"buffer_load_dwordx4 v23, s[44:47], s52 offen offset:1024 lds\n"
"v_exp_f32 v78, v78\n"
"v_mfma_f32_16x16x32_bf16 v[180:183], a[148:151], v[56:59], v[180:183]\n"
"ds_read_b64_tr_b16 a[172:173], v9 offset:41472\n"
"buffer_load_dwordx4 v24, s[44:47], s52 offen offset:2048 lds\n"
"v_cvt_pk_bf16_f32 v62, v76, v77\n"
"v_mfma_f32_16x16x32_bf16 v[184:187], a[152:155], v[56:59], v[184:187]\n"
"buffer_load_dwordx4 v25, s[44:47], s52 offen offset:3072 lds\n"
"v_exp_f32 v79, v79\n"
"v_mfma_f32_16x16x32_bf16 v[188:191], a[156:159], v[56:59], v[188:191]\n"
"ds_read_b64_tr_b16 a[174:175], v9 offset:45568\n"
"v_mfma_f32_16x16x32_bf16 v[72:75], a[64:67], v[128:131], v[48:51]\n"
"v_exp_f32 v64, v64\n"
"v_mfma_f32_16x16x32_bf16 v[72:75], a[68:71], v[132:135], v[72:75]\n"
"ds_read_b64_tr_b16 a[176:177], v8 offset:41984\n"
"v_cvt_pk_bf16_f32 v63, v78, v79\n"
"v_mfma_f32_16x16x32_bf16 v[72:75], a[72:75], v[136:139], v[72:75]\n"
"v_exp_f32 v65, v65\n"
"v_mfma_f32_16x16x32_bf16 v[72:75], a[76:79], v[140:143], v[72:75]\n"
"ds_read_b64_tr_b16 a[178:179], v8 offset:46080\n"
"v_add_f32 v31, v31, v64\n"
"v_mfma_f32_16x16x32_bf16 v[192:195], a[128:131], v[60:63], v[192:195]\n"
"s_mov_b32 m0, 77824\n"
"v_exp_f32 v66, v66\n"
"v_mfma_f32_16x16x32_bf16 v[196:199], a[132:135], v[60:63], v[196:199]\n"
"ds_read_b64_tr_b16 a[180:181], v9 offset:41984\n"
"s_waitcnt vmcnt(52)\n"
"v_cvt_pk_bf16_f32 v56, v64, v65\n"
"v_add_f32 v31, v31, v65\n"
"v_mfma_f32_16x16x32_bf16 v[200:203], a[136:139], v[60:63], v[200:203]\n"
"buffer_load_dwordx4 v14, s[36:39], s61 offen nt lds\n"
"v_exp_f32 v67, v67\n"
"v_mfma_f32_16x16x32_bf16 v[204:207], a[140:143], v[60:63], v[204:207]\n"
"ds_read_b64_tr_b16 a[182:183], v9 offset:46080\n"
"v_add_f32 v31, v31, v66\n"
"v_mfma_f32_16x16x32_bf16 v[18:21], a[224:227], v[60:63], v[18:21]\n"
"v_mfma_f32_16x16x32_bf16 v[76:79], a[80:83], v[128:131], v[48:51]\n"
"buffer_load_dwordx4 v15, s[36:39], s61 offen offset:1024 nt lds\n"
"v_exp_f32 v68, v68\n"
"v_mfma_f32_16x16x32_bf16 v[76:79], a[84:87], v[132:135], v[76:79]\n"
"ds_read_b64_tr_b16 a[184:185], v8 offset:42496\n"
"v_cvt_pk_bf16_f32 v57, v66, v67\n"
"v_add_f32 v31, v31, v67\n"
"v_mfma_f32_16x16x32_bf16 v[76:79], a[88:91], v[136:139], v[76:79]\n"
"buffer_load_dwordx4 v14, s[36:39], s61 offen offset:2048 nt lds\n"
"v_exp_f32 v69, v69\n"
"v_mfma_f32_16x16x32_bf16 v[76:79], a[92:95], v[140:143], v[76:79]\n"
"ds_read_b64_tr_b16 a[186:187], v8 offset:46592\n"
"v_add_f32 v31, v31, v68\n"
"v_mfma_f32_16x16x32_bf16 v[208:211], a[144:147], v[60:63], v[208:211]\n"
"buffer_load_dwordx4 v15, s[36:39], s61 offen offset:3072 nt lds\n"
"v_exp_f32 v70, v70\n"
"v_mfma_f32_16x16x32_bf16 v[212:215], a[148:151], v[60:63], v[212:215]\n"
"ds_read_b64_tr_b16 a[188:189], v9 offset:42496\n"
"v_cvt_pk_bf16_f32 v58, v68, v69\n"
"v_add_f32 v31, v31, v69\n"
"v_mfma_f32_16x16x32_bf16 v[216:219], a[152:155], v[60:63], v[216:219]\n"
"s_add_u32 s62, s62, 8192\n"
"s_and_b32 s62, s62, 0x3ffff\n"
"s_add_u32 s61, s62, 4096\n"
"v_exp_f32 v71, v71\n"
"v_mfma_f32_16x16x32_bf16 v[220:223], a[156:159], v[60:63], v[220:223]\n"
"ds_read_b64_tr_b16 a[190:191], v9 offset:46592\n"
"v_add_f32 v31, v31, v70\n"
"v_mfma_f32_16x16x32_bf16 v[64:67], a[96:99], v[80:83], v[36:39]\n"
"v_exp_f32 v72, v72\n"
"v_mfma_f32_16x16x32_bf16 v[64:67], a[100:103], v[84:87], v[64:67]\n"
"v_cvt_pk_bf16_f32 v59, v70, v71\n"
"v_add_f32 v31, v31, v71\n"
"v_mfma_f32_16x16x32_bf16 v[64:67], a[104:107], v[88:91], v[64:67]\n"
"v_exp_f32 v73, v73\n"
"v_mfma_f32_16x16x32_bf16 v[64:67], a[108:111], v[92:95], v[64:67]\n"
"ds_read_b128 a[64:67], v4 offset:49152\n"
"v_add_f32 v34, v34, v72\n"
"v_mfma_f32_16x16x32_bf16 v[224:227], a[128:131], v[56:59], v[224:227]\n"
"s_mov_b32 m0, 16384\n"
"v_exp_f32 v74, v74\n"
"v_mfma_f32_16x16x32_bf16 v[228:231], a[132:135], v[56:59], v[228:231]\n"
"ds_read_b128 a[68:71], v5 offset:49152\n"
"v_cvt_pk_bf16_f32 v60, v72, v73\n"
"v_add_f32 v34, v34, v73\n"
"v_mfma_f32_16x16x32_bf16 v[232:235], a[136:139], v[56:59], v[232:235]\n"
"buffer_load_dwordx4 v10, s[28:31], s62 offen nt lds\n"
"v_exp_f32 v75, v75\n"
"v_mfma_f32_16x16x32_bf16 v[236:239], a[140:143], v[56:59], v[236:239]\n"
"ds_read_b128 a[72:75], v6 offset:49152\n"
"v_add_f32 v34, v34, v74\n"
"v_mfma_f32_16x16x32_bf16 v[68:71], a[112:115], v[80:83], v[36:39]\n"
"buffer_load_dwordx4 v11, s[28:31], s62 offen offset:1024 nt lds\n"
"v_exp_f32 v76, v76\n"
"v_mfma_f32_16x16x32_bf16 v[68:71], a[116:119], v[84:87], v[68:71]\n"
"ds_read_b128 a[76:79], v7 offset:49152\n"
"v_cvt_pk_bf16_f32 v61, v74, v75\n"
"v_add_f32 v34, v34, v75\n"
"v_mfma_f32_16x16x32_bf16 v[68:71], a[120:123], v[88:91], v[68:71]\n"
"buffer_load_dwordx4 v12, s[28:31], s62 offen offset:2048 nt lds\n"
"v_exp_f32 v77, v77\n"
"v_mfma_f32_16x16x32_bf16 v[68:71], a[124:127], v[92:95], v[68:71]\n"
"ds_read_b128 a[80:83], v4 offset:53248\n"
"v_add_f32 v34, v34, v76\n"
"v_mfma_f32_16x16x32_bf16 v[240:243], a[144:147], v[56:59], v[240:243]\n"
"buffer_load_dwordx4 v13, s[28:31], s62 offen offset:3072 nt lds\n"
"v_exp_f32 v78, v78\n"
"v_mfma_f32_16x16x32_bf16 v[244:247], a[148:151], v[56:59], v[244:247]\n"
"ds_read_b128 a[84:87], v5 offset:53248\n"
"v_cvt_pk_bf16_f32 v62, v76, v77\n"
"v_add_f32 v34, v34, v77\n"
"v_mfma_f32_16x16x32_bf16 v[248:251], a[152:155], v[56:59], v[248:251]\n"
"v_exp_f32 v79, v79\n"
"v_mfma_f32_16x16x32_bf16 v[252:255], a[156:159], v[56:59], v[252:255]\n"
"v_add_f32 v34, v34, v78\n"
"v_mfma_f32_16x16x32_bf16 v[72:75], a[96:99], v[96:99], v[40:43]\n"
"v_exp_f32 v64, v64\n"
"v_mfma_f32_16x16x32_bf16 v[72:75], a[100:103], v[100:103], v[72:75]\n"
"v_cvt_pk_bf16_f32 v63, v78, v79\n"
"v_add_f32 v34, v34, v79\n"
"v_mfma_f32_16x16x32_bf16 v[72:75], a[104:107], v[104:107], v[72:75]\n"
"v_exp_f32 v65, v65\n"
"v_mfma_f32_16x16x32_bf16 v[72:75], a[108:111], v[108:111], v[72:75]\n"
"ds_read_b128 a[88:91], v6 offset:53248\n"
"v_add_f32 v29, v29, v64\n"
"v_mfma_f32_16x16x32_bf16 a[192:195], a[128:131], v[60:63], a[192:195]\n"
"s_mov_b32 m0, 20480\n"
"v_exp_f32 v66, v66\n"
"v_mfma_f32_16x16x32_bf16 a[196:199], a[132:135], v[60:63], a[196:199]\n"
"s_waitcnt vmcnt(52)\n"
"v_cvt_pk_bf16_f32 v56, v64, v65\n"
"v_add_f32 v29, v29, v65\n"
"v_mfma_f32_16x16x32_bf16 a[200:203], a[136:139], v[60:63], a[200:203]\n"
"buffer_load_dwordx4 v10, s[28:31], s61 offen nt lds\n"
"v_exp_f32 v67, v67\n"
"v_mfma_f32_16x16x32_bf16 a[204:207], a[140:143], v[60:63], a[204:207]\n"
"ds_read_b128 a[92:95], v7 offset:53248\n"
"buffer_load_dwordx4 v11, s[28:31], s61 offen offset:1024 nt lds\n"
"v_add_f32 v29, v29, v66\n"
"v_mfma_f32_16x16x32_bf16 v[76:79], a[112:115], v[96:99], v[40:43]\n"
"buffer_load_dwordx4 v12, s[28:31], s61 offen offset:2048 nt lds\n"
"v_exp_f32 v68, v68\n"
"v_mfma_f32_16x16x32_bf16 v[76:79], a[116:119], v[100:103], v[76:79]\n"
"buffer_load_dwordx4 v13, s[28:31], s61 offen offset:3072 nt lds\n"
"v_cvt_pk_bf16_f32 v57, v66, v67\n"
"v_add_f32 v29, v29, v67\n"
"v_mfma_f32_16x16x32_bf16 v[76:79], a[120:123], v[104:107], v[76:79]\n"
"s_mov_b32 m0, 155648\n"
"s_add_u32 s52, s51, 1081344\n"
"v_exp_f32 v69, v69\n"
"v_mfma_f32_16x16x32_bf16 v[76:79], a[124:127], v[108:111], v[76:79]\n"
"buffer_load_dwordx4 v22, s[44:47], s52 offen lds\n"
"v_add_f32 v29, v29, v68\n"
"v_mfma_f32_16x16x32_bf16 a[208:211], a[144:147], v[60:63], a[208:211]\n"
"buffer_load_dwordx4 v23, s[44:47], s52 offen offset:1024 lds\n"
"v_exp_f32 v70, v70\n"
"v_mfma_f32_16x16x32_bf16 a[212:215], a[148:151], v[60:63], a[212:215]\n"
"buffer_load_dwordx4 v24, s[44:47], s52 offen offset:2048 lds\n"
"v_cvt_pk_bf16_f32 v58, v68, v69\n"
"v_add_f32 v29, v29, v69\n"
"v_mfma_f32_16x16x32_bf16 a[216:219], a[152:155], v[60:63], a[216:219]\n"
"buffer_load_dwordx4 v25, s[44:47], s52 offen offset:3072 lds\n"
"v_exp_f32 v71, v71\n"
"v_mfma_f32_16x16x32_bf16 a[220:223], a[156:159], v[60:63], a[220:223]\n"
"v_add_f32 v29, v29, v70\n"
"v_mfma_f32_16x16x32_bf16 v[64:67], a[96:99], v[112:115], v[44:47]\n"
"v_exp_f32 v72, v72\n"
"v_mfma_f32_16x16x32_bf16 v[64:67], a[100:103], v[116:119], v[64:67]\n"
"v_cvt_pk_bf16_f32 v59, v70, v71\n"
"v_add_f32 v29, v29, v71\n"
"v_mfma_f32_16x16x32_bf16 v[64:67], a[104:107], v[120:123], v[64:67]\n"
"v_exp_f32 v73, v73\n"
"v_mfma_f32_16x16x32_bf16 v[64:67], a[108:111], v[124:127], v[64:67]\n"
"s_waitcnt vmcnt(63) lgkmcnt(0)\n"
"s_barrier\n"
"v_mfma_f32_16x16x32_bf16 v[160:163], a[160:163], v[56:59], v[160:163]\n"
"s_mov_b32 m0, 81920\n"
"v_exp_f32 v74, v74\n"
"v_mfma_f32_16x16x32_bf16 v[164:167], a[164:167], v[56:59], v[164:167]\n"
"ds_read_b64_tr_b16 a[128:129], v8 offset:49152\n"
"s_waitcnt vmcnt(52)\n"
"v_cvt_pk_bf16_f32 v60, v72, v73\n"
"v_mfma_f32_16x16x32_bf16 v[168:171], a[168:171], v[56:59], v[168:171]\n"
"buffer_load_dwordx4 v14, s[36:39], s62 offen nt lds\n"
"v_exp_f32 v75, v75\n"
"v_mfma_f32_16x16x32_bf16 v[172:175], a[172:175], v[56:59], v[172:175]\n"
"ds_read_b64_tr_b16 a[130:131], v8 offset:53248\n"
"ds_read_b64_tr_b16 a[132:133], v9 offset:49152\n"
"v_mfma_f32_16x16x32_bf16 v[68:71], a[112:115], v[112:115], v[44:47]\n"
"buffer_load_dwordx4 v15, s[36:39], s62 offen offset:1024 nt lds\n"
"v_exp_f32 v76, v76\n"
"v_mfma_f32_16x16x32_bf16 v[68:71], a[116:119], v[116:119], v[68:71]\n"
"ds_read_b64_tr_b16 a[134:135], v9 offset:53248\n"
"v_cvt_pk_bf16_f32 v61, v74, v75\n"
"v_mfma_f32_16x16x32_bf16 v[68:71], a[120:123], v[120:123], v[68:71]\n"
"buffer_load_dwordx4 v14, s[36:39], s62 offen offset:2048 nt lds\n"
"v_exp_f32 v77, v77\n"
"v_mfma_f32_16x16x32_bf16 v[68:71], a[124:127], v[124:127], v[68:71]\n"
"ds_read_b64_tr_b16 a[136:137], v8 offset:49664\n"
"ds_read_b64_tr_b16 a[138:139], v8 offset:53760\n"
"v_mfma_f32_16x16x32_bf16 v[176:179], a[176:179], v[56:59], v[176:179]\n"
"buffer_load_dwordx4 v15, s[36:39], s62 offen offset:3072 nt lds\n"
"v_exp_f32 v78, v78\n"
"v_mfma_f32_16x16x32_bf16 v[180:183], a[180:183], v[56:59], v[180:183]\n"
"ds_read_b64_tr_b16 a[140:141], v9 offset:49664\n"
"v_cvt_pk_bf16_f32 v62, v76, v77\n"
"v_mfma_f32_16x16x32_bf16 v[184:187], a[184:187], v[56:59], v[184:187]\n"
"v_exp_f32 v79, v79\n"
"v_mfma_f32_16x16x32_bf16 v[188:191], a[188:191], v[56:59], v[188:191]\n"
"ds_read_b64_tr_b16 a[142:143], v9 offset:53760\n"
"v_mfma_f32_16x16x32_bf16 v[72:75], a[96:99], v[128:131], v[48:51]\n"
"v_exp_f32 v64, v64\n"
"v_mfma_f32_16x16x32_bf16 v[72:75], a[100:103], v[132:135], v[72:75]\n"
"ds_read_b64_tr_b16 a[144:145], v8 offset:50176\n"
"v_cvt_pk_bf16_f32 v63, v78, v79\n"
"v_mfma_f32_16x16x32_bf16 v[72:75], a[104:107], v[136:139], v[72:75]\n"
"v_exp_f32 v65, v65\n"
"v_mfma_f32_16x16x32_bf16 v[72:75], a[108:111], v[140:143], v[72:75]\n"
"ds_read_b64_tr_b16 a[146:147], v8 offset:54272\n"
"v_add_f32 v31, v31, v64\n"
"v_mfma_f32_16x16x32_bf16 v[192:195], a[160:163], v[60:63], v[192:195]\n"
"s_mov_b32 m0, 86016\n"
"v_exp_f32 v66, v66\n"
"v_mfma_f32_16x16x32_bf16 v[196:199], a[164:167], v[60:63], v[196:199]\n"
"ds_read_b64_tr_b16 a[148:149], v9 offset:50176\n"
"v_cvt_pk_bf16_f32 v56, v64, v65\n"
"v_add_f32 v31, v31, v65\n"
"v_mfma_f32_16x16x32_bf16 v[200:203], a[168:171], v[60:63], v[200:203]\n"
"buffer_load_dwordx4 v14, s[36:39], s61 offen nt lds\n"
"v_exp_f32 v67, v67\n"
"v_mfma_f32_16x16x32_bf16 v[204:207], a[172:175], v[60:63], v[204:207]\n"
"ds_read_b64_tr_b16 a[150:151], v9 offset:54272\n"
"v_add_f32 v31, v31, v66\n"
"v_mfma_f32_16x16x32_bf16 v[18:21], a[224:227], v[60:63], v[18:21]\n"
"v_mfma_f32_16x16x32_bf16 v[76:79], a[112:115], v[128:131], v[48:51]\n"
"buffer_load_dwordx4 v15, s[36:39], s61 offen offset:1024 nt lds\n"
"v_exp_f32 v68, v68\n"
"v_mfma_f32_16x16x32_bf16 v[76:79], a[116:119], v[132:135], v[76:79]\n"
"ds_read_b64_tr_b16 a[152:153], v8 offset:50688\n"
"v_cvt_pk_bf16_f32 v57, v66, v67\n"
"v_add_f32 v31, v31, v67\n"
"v_mfma_f32_16x16x32_bf16 v[76:79], a[120:123], v[136:139], v[76:79]\n"
"buffer_load_dwordx4 v14, s[36:39], s61 offen offset:2048 nt lds\n"
"v_exp_f32 v69, v69\n"
"v_mfma_f32_16x16x32_bf16 v[76:79], a[124:127], v[140:143], v[76:79]\n"
"ds_read_b64_tr_b16 a[154:155], v8 offset:54784\n"
"v_add_f32 v31, v31, v68\n"
"v_mfma_f32_16x16x32_bf16 v[208:211], a[176:179], v[60:63], v[208:211]\n"
"buffer_load_dwordx4 v15, s[36:39], s61 offen offset:3072 nt lds\n"
"v_exp_f32 v70, v70\n"
"v_mfma_f32_16x16x32_bf16 v[212:215], a[180:183], v[60:63], v[212:215]\n"
"ds_read_b64_tr_b16 a[156:157], v9 offset:50688\n"
"v_cvt_pk_bf16_f32 v58, v68, v69\n"
"v_add_f32 v31, v31, v69\n"
"v_mfma_f32_16x16x32_bf16 v[216:219], a[184:187], v[60:63], v[216:219]\n"
"s_add_u32 s62, s62, 8192\n"
"s_and_b32 s62, s62, 0x3ffff\n"
"s_add_u32 s61, s62, 4096\n"
"v_exp_f32 v71, v71\n"
"v_mfma_f32_16x16x32_bf16 v[220:223], a[188:191], v[60:63], v[220:223]\n"
"ds_read_b64_tr_b16 a[158:159], v9 offset:54784\n"
"v_add_f32 v31, v31, v70\n"
"v_mfma_f32_16x16x32_bf16 v[64:67], a[64:67], v[80:83], v[36:39]\n"
"v_exp_f32 v72, v72\n"
"v_mfma_f32_16x16x32_bf16 v[64:67], a[68:71], v[84:87], v[64:67]\n"
"v_cvt_pk_bf16_f32 v59, v70, v71\n"
"v_add_f32 v31, v31, v71\n"
"v_mfma_f32_16x16x32_bf16 v[64:67], a[72:75], v[88:91], v[64:67]\n"
"v_exp_f32 v73, v73\n"
"v_mfma_f32_16x16x32_bf16 v[64:67], a[76:79], v[92:95], v[64:67]\n"
"ds_read_b128 a[96:99], v4 offset:57344\n"
"v_add_f32 v34, v34, v72\n"
"v_mfma_f32_16x16x32_bf16 v[224:227], a[160:163], v[56:59], v[224:227]\n"
"s_mov_b32 m0, 24576\n"
"v_exp_f32 v74, v74\n"
"v_mfma_f32_16x16x32_bf16 v[228:231], a[164:167], v[56:59], v[228:231]\n"
"ds_read_b128 a[100:103], v5 offset:57344\n"
"s_waitcnt vmcnt(52)\n"
"v_cvt_pk_bf16_f32 v60, v72, v73\n"
"v_add_f32 v34, v34, v73\n"
"v_mfma_f32_16x16x32_bf16 v[232:235], a[168:171], v[56:59], v[232:235]\n"
"buffer_load_dwordx4 v10, s[28:31], s62 offen nt lds\n"
"v_exp_f32 v75, v75\n"
"v_mfma_f32_16x16x32_bf16 v[236:239], a[172:175], v[56:59], v[236:239]\n"
"ds_read_b128 a[104:107], v6 offset:57344\n"
"buffer_load_dwordx4 v11, s[28:31], s62 offen offset:1024 nt lds\n"
"v_add_f32 v34, v34, v74\n"
"v_mfma_f32_16x16x32_bf16 v[68:71], a[80:83], v[80:83], v[36:39]\n"
"buffer_load_dwordx4 v12, s[28:31], s62 offen offset:2048 nt lds\n"
"v_exp_f32 v76, v76\n"
"v_mfma_f32_16x16x32_bf16 v[68:71], a[84:87], v[84:87], v[68:71]\n"
"ds_read_b128 a[108:111], v7 offset:57344\n"
"buffer_load_dwordx4 v13, s[28:31], s62 offen offset:3072 nt lds\n"
"v_cvt_pk_bf16_f32 v61, v74, v75\n"
"v_add_f32 v34, v34, v75\n"
"v_mfma_f32_16x16x32_bf16 v[68:71], a[88:91], v[88:91], v[68:71]\n"
"s_mov_b32 m0, 159744\n"
"s_add_u32 s52, s51, 1605632\n"
"v_exp_f32 v77, v77\n"
"v_mfma_f32_16x16x32_bf16 v[68:71], a[92:95], v[92:95], v[68:71]\n"
"ds_read_b128 a[112:115], v4 offset:61440\n"
"buffer_load_dwordx4 v22, s[44:47], s52 offen lds\n"
"v_add_f32 v34, v34, v76\n"
"v_mfma_f32_16x16x32_bf16 v[240:243], a[176:179], v[56:59], v[240:243]\n"
"buffer_load_dwordx4 v23, s[44:47], s52 offen offset:1024 lds\n"
"v_exp_f32 v78, v78\n"
"v_mfma_f32_16x16x32_bf16 v[244:247], a[180:183], v[56:59], v[244:247]\n"
"ds_read_b128 a[116:119], v5 offset:61440\n"
"buffer_load_dwordx4 v24, s[44:47], s52 offen offset:2048 lds\n"
"v_cvt_pk_bf16_f32 v62, v76, v77\n"
"v_add_f32 v34, v34, v77\n"
"v_mfma_f32_16x16x32_bf16 v[248:251], a[184:187], v[56:59], v[248:251]\n"
"buffer_load_dwordx4 v25, s[44:47], s52 offen offset:3072 lds\n"
"v_exp_f32 v79, v79\n"
"v_mfma_f32_16x16x32_bf16 v[252:255], a[188:191], v[56:59], v[252:255]\n"
"s_add_u32 s51, s51, 65536\n"
"v_add_f32 v34, v34, v78\n"
"v_mfma_f32_16x16x32_bf16 v[72:75], a[64:67], v[96:99], v[40:43]\n"
"v_exp_f32 v64, v64\n"
"v_mfma_f32_16x16x32_bf16 v[72:75], a[68:71], v[100:103], v[72:75]\n"
"v_cvt_pk_bf16_f32 v63, v78, v79\n"
"v_add_f32 v34, v34, v79\n"
"v_mfma_f32_16x16x32_bf16 v[72:75], a[72:75], v[104:107], v[72:75]\n"
"v_exp_f32 v65, v65\n"
"v_mfma_f32_16x16x32_bf16 v[72:75], a[76:79], v[108:111], v[72:75]\n"
"ds_read_b128 a[120:123], v6 offset:61440\n"
"v_add_f32 v29, v29, v64\n"
"v_mfma_f32_16x16x32_bf16 a[192:195], a[160:163], v[60:63], a[192:195]\n"
"s_mov_b32 m0, 28672\n"
"v_exp_f32 v66, v66\n"
"v_mfma_f32_16x16x32_bf16 a[196:199], a[164:167], v[60:63], a[196:199]\n"
"s_waitcnt vmcnt(52)\n"
"v_cvt_pk_bf16_f32 v56, v64, v65\n"
"v_add_f32 v29, v29, v65\n"
"v_mfma_f32_16x16x32_bf16 a[200:203], a[168:171], v[60:63], a[200:203]\n"
"buffer_load_dwordx4 v10, s[28:31], s61 offen nt lds\n"
"v_exp_f32 v67, v67\n"
"v_mfma_f32_16x16x32_bf16 a[204:207], a[172:175], v[60:63], a[204:207]\n"
"ds_read_b128 a[124:127], v7 offset:61440\n"
"v_add_f32 v29, v29, v66\n"
"v_mfma_f32_16x16x32_bf16 v[76:79], a[80:83], v[96:99], v[40:43]\n"
"buffer_load_dwordx4 v11, s[28:31], s61 offen offset:1024 nt lds\n"
"v_exp_f32 v68, v68\n"
"v_mfma_f32_16x16x32_bf16 v[76:79], a[84:87], v[100:103], v[76:79]\n"
"v_cvt_pk_bf16_f32 v57, v66, v67\n"
"v_add_f32 v29, v29, v67\n"
"v_mfma_f32_16x16x32_bf16 v[76:79], a[88:91], v[104:107], v[76:79]\n"
"buffer_load_dwordx4 v12, s[28:31], s61 offen offset:2048 nt lds\n"
"v_exp_f32 v69, v69\n"
"v_mfma_f32_16x16x32_bf16 v[76:79], a[92:95], v[108:111], v[76:79]\n"
"v_add_f32 v29, v29, v68\n"
"v_mfma_f32_16x16x32_bf16 a[208:211], a[176:179], v[60:63], a[208:211]\n"
"buffer_load_dwordx4 v13, s[28:31], s61 offen offset:3072 nt lds\n"
"v_exp_f32 v70, v70\n"
"v_mfma_f32_16x16x32_bf16 a[212:215], a[180:183], v[60:63], a[212:215]\n"
"v_cvt_pk_bf16_f32 v58, v68, v69\n"
"v_add_f32 v29, v29, v69\n"
"v_mfma_f32_16x16x32_bf16 a[216:219], a[184:187], v[60:63], a[216:219]\n"
"v_exp_f32 v71, v71\n"
"v_mfma_f32_16x16x32_bf16 a[220:223], a[188:191], v[60:63], a[220:223]\n"
"v_add_f32 v29, v29, v70\n"
"v_mfma_f32_16x16x32_bf16 v[64:67], a[64:67], v[112:115], v[44:47]\n"
"v_exp_f32 v72, v72\n"
"v_mfma_f32_16x16x32_bf16 v[64:67], a[68:71], v[116:119], v[64:67]\n"
"v_cvt_pk_bf16_f32 v59, v70, v71\n"
"v_add_f32 v29, v29, v71\n"
"v_mfma_f32_16x16x32_bf16 v[64:67], a[72:75], v[120:123], v[64:67]\n"
"v_exp_f32 v73, v73\n"
"v_mfma_f32_16x16x32_bf16 v[64:67], a[76:79], v[124:127], v[64:67]\n"
"s_waitcnt vmcnt(63) lgkmcnt(0)\n"
"s_barrier\n"
"v_mfma_f32_16x16x32_bf16 v[160:163], a[128:131], v[56:59], v[160:163]\n"
"s_mov_b32 m0, 90112\n"
"v_exp_f32 v74, v74\n"
"v_mfma_f32_16x16x32_bf16 v[164:167], a[132:135], v[56:59], v[164:167]\n"
"ds_read_b64_tr_b16 a[160:161], v8 offset:57344\n"
"v_cvt_pk_bf16_f32 v60, v72, v73\n"
"v_mfma_f32_16x16x32_bf16 v[168:171], a[136:139], v[56:59], v[168:171]\n"
"buffer_load_dwordx4 v14, s[36:39], s62 offen nt lds\n"
"v_exp_f32 v75, v75\n"
"v_mfma_f32_16x16x32_bf16 v[172:175], a[140:143], v[56:59], v[172:175]\n"
"ds_read_b64_tr_b16 a[162:163], v8 offset:61440\n"
"ds_read_b64_tr_b16 a[164:165], v9 offset:57344\n"
"v_mfma_f32_16x16x32_bf16 v[68:71], a[80:83], v[112:115], v[44:47]\n"
"buffer_load_dwordx4 v15, s[36:39], s62 offen offset:1024 nt lds\n"
"v_exp_f32 v76, v76\n"
"v_mfma_f32_16x16x32_bf16 v[68:71], a[84:87], v[116:119], v[68:71]\n"
"ds_read_b64_tr_b16 a[166:167], v9 offset:61440\n"
"v_cvt_pk_bf16_f32 v61, v74, v75\n"
"v_mfma_f32_16x16x32_bf16 v[68:71], a[88:91], v[120:123], v[68:71]\n"
"buffer_load_dwordx4 v14, s[36:39], s62 offen offset:2048 nt lds\n"
"v_exp_f32 v77, v77\n"
"v_mfma_f32_16x16x32_bf16 v[68:71], a[92:95], v[124:127], v[68:71]\n"
"ds_read_b64_tr_b16 a[168:169], v8 offset:57856\n"
"ds_read_b64_tr_b16 a[170:171], v8 offset:61952\n"
"v_mfma_f32_16x16x32_bf16 v[176:179], a[144:147], v[56:59], v[176:179]\n"
"buffer_load_dwordx4 v15, s[36:39], s62 offen offset:3072 nt lds\n"
"v_exp_f32 v78, v78\n"
"v_mfma_f32_16x16x32_bf16 v[180:183], a[148:151], v[56:59], v[180:183]\n"
"ds_read_b64_tr_b16 a[172:173], v9 offset:57856\n"
"v_cvt_pk_bf16_f32 v62, v76, v77\n"
"v_mfma_f32_16x16x32_bf16 v[184:187], a[152:155], v[56:59], v[184:187]\n"
"v_exp_f32 v79, v79\n"
"v_mfma_f32_16x16x32_bf16 v[188:191], a[156:159], v[56:59], v[188:191]\n"
"ds_read_b64_tr_b16 a[174:175], v9 offset:61952\n"
"v_mfma_f32_16x16x32_bf16 v[72:75], a[64:67], v[128:131], v[48:51]\n"
"v_exp_f32 v64, v64\n"
"v_mfma_f32_16x16x32_bf16 v[72:75], a[68:71], v[132:135], v[72:75]\n"
"ds_read_b64_tr_b16 a[176:177], v8 offset:58368\n"
"v_cvt_pk_bf16_f32 v63, v78, v79\n"
"v_mfma_f32_16x16x32_bf16 v[72:75], a[72:75], v[136:139], v[72:75]\n"
"v_exp_f32 v65, v65\n"
"v_mfma_f32_16x16x32_bf16 v[72:75], a[76:79], v[140:143], v[72:75]\n"
"ds_read_b64_tr_b16 a[178:179], v8 offset:62464\n"
"v_add_f32 v31, v31, v64\n"
"v_mfma_f32_16x16x32_bf16 v[192:195], a[128:131], v[60:63], v[192:195]\n"
"s_mov_b32 m0, 94208\n"
"v_exp_f32 v66, v66\n"
"v_mfma_f32_16x16x32_bf16 v[196:199], a[132:135], v[60:63], v[196:199]\n"
"ds_read_b64_tr_b16 a[180:181], v9 offset:58368\n"
"s_waitcnt vmcnt(52)\n"
"v_cvt_pk_bf16_f32 v56, v64, v65\n"
"v_add_f32 v31, v31, v65\n"
"v_mfma_f32_16x16x32_bf16 v[200:203], a[136:139], v[60:63], v[200:203]\n"
"buffer_load_dwordx4 v14, s[36:39], s61 offen nt lds\n"
"v_exp_f32 v67, v67\n"
"v_mfma_f32_16x16x32_bf16 v[204:207], a[140:143], v[60:63], v[204:207]\n"
"ds_read_b64_tr_b16 a[182:183], v9 offset:62464\n"
"v_add_f32 v31, v31, v66\n"
"v_mfma_f32_16x16x32_bf16 v[18:21], a[224:227], v[60:63], v[18:21]\n"
"v_mfma_f32_16x16x32_bf16 v[76:79], a[80:83], v[128:131], v[48:51]\n"
"buffer_load_dwordx4 v15, s[36:39], s61 offen offset:1024 nt lds\n"
"v_exp_f32 v68, v68\n"
"v_mfma_f32_16x16x32_bf16 v[76:79], a[84:87], v[132:135], v[76:79]\n"
"ds_read_b64_tr_b16 a[184:185], v8 offset:58880\n"
"v_cvt_pk_bf16_f32 v57, v66, v67\n"
"v_add_f32 v31, v31, v67\n"
"v_mfma_f32_16x16x32_bf16 v[76:79], a[88:91], v[136:139], v[76:79]\n"
"buffer_load_dwordx4 v14, s[36:39], s61 offen offset:2048 nt lds\n"
"v_exp_f32 v69, v69\n"
"v_mfma_f32_16x16x32_bf16 v[76:79], a[92:95], v[140:143], v[76:79]\n"
"ds_read_b64_tr_b16 a[186:187], v8 offset:62976\n"
"v_add_f32 v31, v31, v68\n"
"v_mfma_f32_16x16x32_bf16 v[208:211], a[144:147], v[60:63], v[208:211]\n"
"buffer_load_dwordx4 v15, s[36:39], s61 offen offset:3072 nt lds\n"
"v_exp_f32 v70, v70\n"
"v_mfma_f32_16x16x32_bf16 v[212:215], a[148:151], v[60:63], v[212:215]\n"
"ds_read_b64_tr_b16 a[188:189], v9 offset:58880\n"
"v_cvt_pk_bf16_f32 v58, v68, v69\n"
"v_add_f32 v31, v31, v69\n"
"v_mfma_f32_16x16x32_bf16 v[216:219], a[152:155], v[60:63], v[216:219]\n"
"s_add_u32 s62, s62, 8192\n"
"s_and_b32 s62, s62, 0x3ffff\n"
"s_add_u32 s61, s62, 4096\n"
"v_exp_f32 v71, v71\n"
"v_mfma_f32_16x16x32_bf16 v[220:223], a[156:159], v[60:63], v[220:223]\n"
"ds_read_b64_tr_b16 a[190:191], v9 offset:62976\n"
"v_add_f32 v31, v31, v70\n"
"v_mfma_f32_16x16x32_bf16 v[64:67], a[96:99], v[80:83], v[36:39]\n"
"v_exp_f32 v72, v72\n"
"v_mfma_f32_16x16x32_bf16 v[64:67], a[100:103], v[84:87], v[64:67]\n"
"v_cvt_pk_bf16_f32 v59, v70, v71\n"
"v_add_f32 v31, v31, v71\n"
"v_mfma_f32_16x16x32_bf16 v[64:67], a[104:107], v[88:91], v[64:67]\n"
"v_exp_f32 v73, v73\n"
"v_mfma_f32_16x16x32_bf16 v[64:67], a[108:111], v[92:95], v[64:67]\n"
"ds_read_b128 a[64:67], v4\n"
"v_add_f32 v34, v34, v72\n"
"v_mfma_f32_16x16x32_bf16 v[224:227], a[128:131], v[56:59], v[224:227]\n"
"s_mov_b32 m0, 32768\n"
"v_exp_f32 v74, v74\n"
"v_mfma_f32_16x16x32_bf16 v[228:231], a[132:135], v[56:59], v[228:231]\n"
"ds_read_b128 a[68:71], v5\n"
"v_cvt_pk_bf16_f32 v60, v72, v73\n"
"v_add_f32 v34, v34, v73\n"
"v_mfma_f32_16x16x32_bf16 v[232:235], a[136:139], v[56:59], v[232:235]\n"
"buffer_load_dwordx4 v10, s[28:31], s62 offen nt lds\n"
"v_exp_f32 v75, v75\n"
"v_mfma_f32_16x16x32_bf16 v[236:239], a[140:143], v[56:59], v[236:239]\n"
"ds_read_b128 a[72:75], v6\n"
"v_add_f32 v34, v34, v74\n"
"v_mfma_f32_16x16x32_bf16 v[68:71], a[112:115], v[80:83], v[36:39]\n"
"buffer_load_dwordx4 v11, s[28:31], s62 offen offset:1024 nt lds\n"
"v_exp_f32 v76, v76\n"
"v_mfma_f32_16x16x32_bf16 v[68:71], a[116:119], v[84:87], v[68:71]\n"
"ds_read_b128 a[76:79], v7\n"
"v_cvt_pk_bf16_f32 v61, v74, v75\n"
"v_add_f32 v34, v34, v75\n"
"v_mfma_f32_16x16x32_bf16 v[68:71], a[120:123], v[88:91], v[68:71]\n"
"buffer_load_dwordx4 v12, s[28:31], s62 offen offset:2048 nt lds\n"
"v_exp_f32 v77, v77\n"
"v_mfma_f32_16x16x32_bf16 v[68:71], a[124:127], v[92:95], v[68:71]\n"
"ds_read_b128 a[80:83], v4 offset:4096\n"
"v_add_f32 v34, v34, v76\n"
"v_mfma_f32_16x16x32_bf16 v[240:243], a[144:147], v[56:59], v[240:243]\n"
"buffer_load_dwordx4 v13, s[28:31], s62 offen offset:3072 nt lds\n"
"v_exp_f32 v78, v78\n"
"v_mfma_f32_16x16x32_bf16 v[244:247], a[148:151], v[56:59], v[244:247]\n"
"ds_read_b128 a[84:87], v5 offset:4096\n"
"v_cvt_pk_bf16_f32 v62, v76, v77\n"
"v_add_f32 v34, v34, v77\n"
"v_mfma_f32_16x16x32_bf16 v[248:251], a[152:155], v[56:59], v[248:251]\n"
"v_exp_f32 v79, v79\n"
"v_mfma_f32_16x16x32_bf16 v[252:255], a[156:159], v[56:59], v[252:255]\n"
"v_add_f32 v34, v34, v78\n"
"v_mfma_f32_16x16x32_bf16 v[72:75], a[96:99], v[96:99], v[40:43]\n"
"v_exp_f32 v64, v64\n"
"v_mfma_f32_16x16x32_bf16 v[72:75], a[100:103], v[100:103], v[72:75]\n"
"v_cvt_pk_bf16_f32 v63, v78, v79\n"
"v_add_f32 v34, v34, v79\n"
"v_mfma_f32_16x16x32_bf16 v[72:75], a[104:107], v[104:107], v[72:75]\n"
"v_exp_f32 v65, v65\n"
"v_mfma_f32_16x16x32_bf16 v[72:75], a[108:111], v[108:111], v[72:75]\n"
"ds_read_b128 a[88:91], v6 offset:4096\n"
"v_add_f32 v29, v29, v64\n"
"v_mfma_f32_16x16x32_bf16 a[192:195], a[128:131], v[60:63], a[192:195]\n"
"s_mov_b32 m0, 36864\n"
"v_exp_f32 v66, v66\n"
"v_mfma_f32_16x16x32_bf16 a[196:199], a[132:135], v[60:63], a[196:199]\n"
"s_waitcnt vmcnt(52)\n"
"v_cvt_pk_bf16_f32 v56, v64, v65\n"
"v_add_f32 v29, v29, v65\n"
"v_mfma_f32_16x16x32_bf16 a[200:203], a[136:139], v[60:63], a[200:203]\n"
"buffer_load_dwordx4 v10, s[28:31], s61 offen nt lds\n"
"v_exp_f32 v67, v67\n"
"v_mfma_f32_16x16x32_bf16 a[204:207], a[140:143], v[60:63], a[204:207]\n"
"ds_read_b128 a[92:95], v7 offset:4096\n"
"v_add_f32 v29, v29, v66\n"
"v_mfma_f32_16x16x32_bf16 v[76:79], a[112:115], v[96:99], v[40:43]\n"
"buffer_load_dwordx4 v11, s[28:31], s61 offen offset:1024 nt lds\n"
"v_exp_f32 v68, v68\n"
"v_mfma_f32_16x16x32_bf16 v[76:79], a[116:119], v[100:103], v[76:79]\n"
"v_cvt_pk_bf16_f32 v57, v66, v67\n"
"v_add_f32 v29, v29, v67\n"
"v_mfma_f32_16x16x32_bf16 v[76:79], a[120:123], v[104:107], v[76:79]\n"
"buffer_load_dwordx4 v12, s[28:31], s61 offen offset:2048 nt lds\n"
"v_exp_f32 v69, v69\n"
"v_mfma_f32_16x16x32_bf16 v[76:79], a[124:127], v[108:111], v[76:79]\n"
"v_add_f32 v29, v29, v68\n"
"v_mfma_f32_16x16x32_bf16 a[208:211], a[144:147], v[60:63], a[208:211]\n"
"buffer_load_dwordx4 v13, s[28:31], s61 offen offset:3072 nt lds\n"
"v_exp_f32 v70, v70\n"
"v_mfma_f32_16x16x32_bf16 a[212:215], a[148:151], v[60:63], a[212:215]\n"
"v_cvt_pk_bf16_f32 v58, v68, v69\n"
"v_add_f32 v29, v29, v69\n"
"v_mfma_f32_16x16x32_bf16 a[216:219], a[152:155], v[60:63], a[216:219]\n"
"v_exp_f32 v71, v71\n"
"v_mfma_f32_16x16x32_bf16 a[220:223], a[156:159], v[60:63], a[220:223]\n"
"v_add_f32 v29, v29, v70\n"
"v_mfma_f32_16x16x32_bf16 v[64:67], a[96:99], v[112:115], v[44:47]\n"
"v_exp_f32 v72, v72\n"
"v_mfma_f32_16x16x32_bf16 v[64:67], a[100:103], v[116:119], v[64:67]\n"
"v_cvt_pk_bf16_f32 v59, v70, v71\n"
"v_add_f32 v29, v29, v71\n"
"v_mfma_f32_16x16x32_bf16 v[64:67], a[104:107], v[120:123], v[64:67]\n"
"v_exp_f32 v73, v73\n"
"v_mfma_f32_16x16x32_bf16 v[64:67], a[108:111], v[124:127], v[64:67]\n"
"s_waitcnt vmcnt(20) lgkmcnt(0)\n"
"s_barrier\n"
"v_mfma_f32_16x16x32_bf16 v[160:163], a[160:163], v[56:59], v[160:163]\n"
"s_mov_b32 m0, 98304\n"
"v_exp_f32 v74, v74\n"
"v_mfma_f32_16x16x32_bf16 v[164:167], a[164:167], v[56:59], v[164:167]\n"
"ds_read_b64_tr_b16 a[128:129], v8\n"
"v_cvt_pk_bf16_f32 v60, v72, v73\n"
"v_mfma_f32_16x16x32_bf16 v[168:171], a[168:171], v[56:59], v[168:171]\n"
"buffer_load_dwordx4 v14, s[36:39], s62 offen nt lds\n"
"v_exp_f32 v75, v75\n"
"v_mfma_f32_16x16x32_bf16 v[172:175], a[172:175], v[56:59], v[172:175]\n"
"ds_read_b64_tr_b16 a[130:131], v8 offset:4096\n"
"ds_read_b64_tr_b16 a[132:133], v9\n"
"v_mfma_f32_16x16x32_bf16 v[68:71], a[112:115], v[112:115], v[44:47]\n"
"buffer_load_dwordx4 v15, s[36:39], s62 offen offset:1024 nt lds\n"
"v_exp_f32 v76, v76\n"
"v_mfma_f32_16x16x32_bf16 v[68:71], a[116:119], v[116:119], v[68:71]\n"
"ds_read_b64_tr_b16 a[134:135], v9 offset:4096\n"
"v_cvt_pk_bf16_f32 v61, v74, v75\n"
"v_mfma_f32_16x16x32_bf16 v[68:71], a[120:123], v[120:123], v[68:71]\n"
"buffer_load_dwordx4 v14, s[36:39], s62 offen offset:2048 nt lds\n"
"v_exp_f32 v77, v77\n"
"v_mfma_f32_16x16x32_bf16 v[68:71], a[124:127], v[124:127], v[68:71]\n"
"ds_read_b64_tr_b16 a[136:137], v8 offset:512\n"
"ds_read_b64_tr_b16 a[138:139], v8 offset:4608\n"
"v_mfma_f32_16x16x32_bf16 v[176:179], a[176:179], v[56:59], v[176:179]\n"
"buffer_load_dwordx4 v15, s[36:39], s62 offen offset:3072 nt lds\n"
"v_exp_f32 v78, v78\n"
"v_mfma_f32_16x16x32_bf16 v[180:183], a[180:183], v[56:59], v[180:183]\n"
"ds_read_b64_tr_b16 a[140:141], v9 offset:512\n"
"v_cvt_pk_bf16_f32 v62, v76, v77\n"
"v_mfma_f32_16x16x32_bf16 v[184:187], a[184:187], v[56:59], v[184:187]\n"
"v_exp_f32 v79, v79\n"
"v_mfma_f32_16x16x32_bf16 v[188:191], a[188:191], v[56:59], v[188:191]\n"
"ds_read_b64_tr_b16 a[142:143], v9 offset:4608\n"
"v_mfma_f32_16x16x32_bf16 v[72:75], a[96:99], v[128:131], v[48:51]\n"
"v_exp_f32 v64, v64\n"
"v_mfma_f32_16x16x32_bf16 v[72:75], a[100:103], v[132:135], v[72:75]\n"
"ds_read_b64_tr_b16 a[144:145], v8 offset:1024\n"
"v_cvt_pk_bf16_f32 v63, v78, v79\n"
"v_mfma_f32_16x16x32_bf16 v[72:75], a[104:107], v[136:139], v[72:75]\n"
"v_exp_f32 v65, v65\n"
"v_mfma_f32_16x16x32_bf16 v[72:75], a[108:111], v[140:143], v[72:75]\n"
"ds_read_b64_tr_b16 a[146:147], v8 offset:5120\n"
"v_add_f32 v31, v31, v64\n"
"v_mfma_f32_16x16x32_bf16 v[192:195], a[160:163], v[60:63], v[192:195]\n"
"s_mov_b32 m0, 102400\n"
"v_exp_f32 v66, v66\n"
"v_mfma_f32_16x16x32_bf16 v[196:199], a[164:167], v[60:63], v[196:199]\n"
"ds_read_b64_tr_b16 a[148:149], v9 offset:1024\n"
"v_cvt_pk_bf16_f32 v56, v64, v65\n"
"v_add_f32 v31, v31, v65\n"
"v_mfma_f32_16x16x32_bf16 v[200:203], a[168:171], v[60:63], v[200:203]\n"
"buffer_load_dwordx4 v14, s[36:39], s61 offen nt lds\n"
"v_exp_f32 v67, v67\n"
"v_mfma_f32_16x16x32_bf16 v[204:207], a[172:175], v[60:63], v[204:207]\n"
"ds_read_b64_tr_b16 a[150:151], v9 offset:5120\n"
"v_add_f32 v31, v31, v66\n"
"v_mfma_f32_16x16x32_bf16 v[18:21], a[224:227], v[60:63], v[18:21]\n"
"v_mfma_f32_16x16x32_bf16 v[76:79], a[112:115], v[128:131], v[48:51]\n"
"buffer_load_dwordx4 v15, s[36:39], s61 offen offset:1024 nt lds\n"
"v_exp_f32 v68, v68\n"
"v_mfma_f32_16x16x32_bf16 v[76:79], a[116:119], v[132:135], v[76:79]\n"
"ds_read_b64_tr_b16 a[152:153], v8 offset:1536\n"
"v_cvt_pk_bf16_f32 v57, v66, v67\n"
"v_add_f32 v31, v31, v67\n"
"v_mfma_f32_16x16x32_bf16 v[76:79], a[120:123], v[136:139], v[76:79]\n"
"buffer_load_dwordx4 v14, s[36:39], s61 offen offset:2048 nt lds\n"
"v_exp_f32 v69, v69\n"
"v_mfma_f32_16x16x32_bf16 v[76:79], a[124:127], v[140:143], v[76:79]\n"
"ds_read_b64_tr_b16 a[154:155], v8 offset:5632\n"
"v_add_f32 v31, v31, v68\n"
"v_mfma_f32_16x16x32_bf16 v[208:211], a[176:179], v[60:63], v[208:211]\n"
"buffer_load_dwordx4 v15, s[36:39], s61 offen offset:3072 nt lds\n"
"v_exp_f32 v70, v70\n"
"v_mfma_f32_16x16x32_bf16 v[212:215], a[180:183], v[60:63], v[212:215]\n"
"ds_read_b64_tr_b16 a[156:157], v9 offset:1536\n"
"v_cvt_pk_bf16_f32 v58, v68, v69\n"
"v_add_f32 v31, v31, v69\n"
"v_mfma_f32_16x16x32_bf16 v[216:219], a[184:187], v[60:63], v[216:219]\n"
"s_add_u32 s62, s62, 8192\n"
"s_and_b32 s62, s62, 0x3ffff\n"
"s_add_u32 s61, s62, 4096\n"
"v_exp_f32 v71, v71\n"
"v_mfma_f32_16x16x32_bf16 v[220:223], a[188:191], v[60:63], v[220:223]\n"
"ds_read_b64_tr_b16 a[158:159], v9 offset:5632\n"
"v_add_f32 v31, v31, v70\n"
"s_sub_u32 s63, s63, 1\n"
"s_cbranch_scc0 .Lmha_back4\n"
"v_mfma_f32_16x16x32_bf16 v[64:67], a[64:67], v[80:83], v[36:39]\n"
"v_exp_f32 v72, v72\n"
"v_mfma_f32_16x16x32_bf16 v[64:67], a[68:71], v[84:87], v[64:67]\n"
"v_cvt_pk_bf16_f32 v59, v70, v71\n"
"v_add_f32 v31, v31, v71\n"
"v_mfma_f32_16x16x32_bf16 v[64:67], a[72:75], v[88:91], v[64:67]\n"
"v_exp_f32 v73, v73\n"
"v_mfma_f32_16x16x32_bf16 v[64:67], a[76:79], v[92:95], v[64:67]\n"
"ds_read_b128 a[96:99], v4 offset:8192\n"
"v_add_f32 v34, v34, v72\n"
"v_mfma_f32_16x16x32_bf16 v[224:227], a[160:163], v[56:59], v[224:227]\n"
"s_mov_b32 m0, 40960\n"
"v_exp_f32 v74, v74\n"
"v_mfma_f32_16x16x32_bf16 v[228:231], a[164:167], v[56:59], v[228:231]\n"
"ds_read_b128 a[100:103], v5 offset:8192\n"
"v_cvt_pk_bf16_f32 v60, v72, v73\n"
"v_add_f32 v34, v34, v73\n"
"v_mfma_f32_16x16x32_bf16 v[232:235], a[168:171], v[56:59], v[232:235]\n"
"buffer_load_dwordx4 v10, s[28:31], s62 offen nt lds\n"
"v_exp_f32 v75, v75\n"
"v_mfma_f32_16x16x32_bf16 v[236:239], a[172:175], v[56:59], v[236:239]\n"
"ds_read_b128 a[104:107], v6 offset:8192\n"
"buffer_load_dwordx4 v11, s[28:31], s62 offen offset:1024 nt lds\n"
"v_add_f32 v34, v34, v74\n"
"v_mfma_f32_16x16x32_bf16 v[68:71], a[80:83], v[80:83], v[36:39]\n"
"buffer_load_dwordx4 v12, s[28:31], s62 offen offset:2048 nt lds\n"
"v_exp_f32 v76, v76\n"
"v_mfma_f32_16x16x32_bf16 v[68:71], a[84:87], v[84:87], v[68:71]\n"
"ds_read_b128 a[108:111], v7 offset:8192\n"
"buffer_load_dwordx4 v13, s[28:31], s62 offen offset:3072 nt lds\n"
"v_cvt_pk_bf16_f32 v61, v74, v75\n"
"v_add_f32 v34, v34, v75\n"
"v_mfma_f32_16x16x32_bf16 v[68:71], a[88:91], v[88:91], v[68:71]\n"
"s_mov_b32 m0, 0\n"
"s_mov_b32 s52, 196608\n"
"v_exp_f32 v77, v77\n"
"v_mfma_f32_16x16x32_bf16 v[68:71], a[92:95], v[92:95], v[68:71]\n"
"ds_read_b128 a[112:115], v4 offset:12288\n"
"buffer_load_dwordx4 v22, s[44:47], s52 offen lds\n"
"v_add_f32 v34, v34, v76\n"
"v_mfma_f32_16x16x32_bf16 v[240:243], a[176:179], v[56:59], v[240:243]\n"
"buffer_load_dwordx4 v23, s[44:47], s52 offen offset:1024 lds\n"
"v_exp_f32 v78, v78\n"
"v_mfma_f32_16x16x32_bf16 v[244:247], a[180:183], v[56:59], v[244:247]\n"
"ds_read_b128 a[116:119], v5 offset:12288\n"
"buffer_load_dwordx4 v24, s[44:47], s52 offen offset:2048 lds\n"
"v_cvt_pk_bf16_f32 v62, v76, v77\n"
"v_add_f32 v34, v34, v77\n"
"v_mfma_f32_16x16x32_bf16 v[248:251], a[184:187], v[56:59], v[248:251]\n"
"buffer_load_dwordx4 v25, s[44:47], s52 offen offset:3072 lds\n"
"v_exp_f32 v79, v79\n"
"v_mfma_f32_16x16x32_bf16 v[252:255], a[188:191], v[56:59], v[252:255]\n"
"v_add_f32 v34, v34, v78\n"
"v_mfma_f32_16x16x32_bf16 v[72:75], a[64:67], v[96:99], v[40:43]\n"
"v_exp_f32 v64, v64\n"
"v_mfma_f32_16x16x32_bf16 v[72:75], a[68:71], v[100:103], v[72:75]\n"
"v_cvt_pk_bf16_f32 v63, v78, v79\n"
"v_add_f32 v34, v34, v79\n"
"v_mfma_f32_16x16x32_bf16 v[72:75], a[72:75], v[104:107], v[72:75]\n"
"v_exp_f32 v65, v65\n"
"v_mfma_f32_16x16x32_bf16 v[72:75], a[76:79], v[108:111], v[72:75]\n"
"ds_read_b128 a[120:123], v6 offset:12288\n"
"v_add_f32 v29, v29, v64\n"
"v_mfma_f32_16x16x32_bf16 a[192:195], a[160:163], v[60:63], a[192:195]\n"
"s_mov_b32 m0, 45056\n"
"v_exp_f32 v66, v66\n"
"v_mfma_f32_16x16x32_bf16 a[196:199], a[164:167], v[60:63], a[196:199]\n"
"v_cvt_pk_bf16_f32 v56, v64, v65\n"
"v_add_f32 v29, v29, v65\n"
"v_mfma_f32_16x16x32_bf16 a[200:203], a[168:171], v[60:63], a[200:203]\n"
"buffer_load_dwordx4 v10, s[28:31], s61 offen nt lds\n"
"v_exp_f32 v67, v67\n"
"v_mfma_f32_16x16x32_bf16 a[204:207], a[172:175], v[60:63], a[204:207]\n"
"ds_read_b128 a[124:127], v7 offset:12288\n"
"v_add_f32 v29, v29, v66\n"
"v_mfma_f32_16x16x32_bf16 v[76:79], a[80:83], v[96:99], v[40:43]\n"
"buffer_load_dwordx4 v11, s[28:31], s61 offen offset:1024 nt lds\n"
"v_exp_f32 v68, v68\n"
"v_mfma_f32_16x16x32_bf16 v[76:79], a[84:87], v[100:103], v[76:79]\n"
"v_cvt_pk_bf16_f32 v57, v66, v67\n"
"v_add_f32 v29, v29, v67\n"
"v_mfma_f32_16x16x32_bf16 v[76:79], a[88:91], v[104:107], v[76:79]\n"
"buffer_load_dwordx4 v12, s[28:31], s61 offen offset:2048 nt lds\n"
"v_exp_f32 v69, v69\n"
"v_mfma_f32_16x16x32_bf16 v[76:79], a[92:95], v[108:111], v[76:79]\n"
"v_add_f32 v29, v29, v68\n"
"v_mfma_f32_16x16x32_bf16 a[208:211], a[176:179], v[60:63], a[208:211]\n"
"buffer_load_dwordx4 v13, s[28:31], s61 offen offset:3072 nt lds\n"
"v_exp_f32 v70, v70\n"
"v_mfma_f32_16x16x32_bf16 a[212:215], a[180:183], v[60:63], a[212:215]\n"
"v_cvt_pk_bf16_f32 v58, v68, v69\n"
"v_add_f32 v29, v29, v69\n"
"v_mfma_f32_16x16x32_bf16 a[216:219], a[184:187], v[60:63], a[216:219]\n"
"v_exp_f32 v71, v71\n"
"v_mfma_f32_16x16x32_bf16 a[220:223], a[188:191], v[60:63], a[220:223]\n"
"v_add_f32 v29, v29, v70\n"
"v_mfma_f32_16x16x32_bf16 v[64:67], a[64:67], v[112:115], v[44:47]\n"
"v_exp_f32 v72, v72\n"
"v_mfma_f32_16x16x32_bf16 v[64:67], a[68:71], v[116:119], v[64:67]\n"
"v_cvt_pk_bf16_f32 v59, v70, v71\n"
"v_add_f32 v29, v29, v71\n"
"v_mfma_f32_16x16x32_bf16 v[64:67], a[72:75], v[120:123], v[64:67]\n"
"v_exp_f32 v73, v73\n"
"v_mfma_f32_16x16x32_bf16 v[64:67], a[76:79], v[124:127], v[64:67]\n"
"s_waitcnt lgkmcnt(0)\n"
"s_barrier\n"
"v_mfma_f32_16x16x32_bf16 v[160:163], a[128:131], v[56:59], v[160:163]\n"
"s_mov_b32 m0, 106496\n"
"v_exp_f32 v74, v74\n"
"v_mfma_f32_16x16x32_bf16 v[164:167], a[132:135], v[56:59], v[164:167]\n"
"ds_read_b64_tr_b16 a[160:161], v8 offset:8192\n"
"v_cvt_pk_bf16_f32 v60, v72, v73\n"
"v_mfma_f32_16x16x32_bf16 v[168:171], a[136:139], v[56:59], v[168:171]\n"
"buffer_load_dwordx4 v14, s[36:39], s62 offen nt lds\n"
"v_exp_f32 v75, v75\n"
"v_mfma_f32_16x16x32_bf16 v[172:175], a[140:143], v[56:59], v[172:175]\n"
"ds_read_b64_tr_b16 a[162:163], v8 offset:12288\n"
"ds_read_b64_tr_b16 a[164:165], v9 offset:8192\n"
"buffer_load_dwordx4 v15, s[36:39], s62 offen offset:1024 nt lds\n"
"v_mfma_f32_16x16x32_bf16 v[68:71], a[80:83], v[112:115], v[44:47]\n"
"buffer_load_dwordx4 v14, s[36:39], s62 offen offset:2048 nt lds\n"
"v_exp_f32 v76, v76\n"
"v_mfma_f32_16x16x32_bf16 v[68:71], a[84:87], v[116:119], v[68:71]\n"
"ds_read_b64_tr_b16 a[166:167], v9 offset:12288\n"
"buffer_load_dwordx4 v15, s[36:39], s62 offen offset:3072 nt lds\n"
"v_cvt_pk_bf16_f32 v61, v74, v75\n"
"v_mfma_f32_16x16x32_bf16 v[68:71], a[88:91], v[120:123], v[68:71]\n"
"s_mov_b32 m0, 4096\n"
"s_mov_b32 s52, 720896\n"
"v_exp_f32 v77, v77\n"
"v_mfma_f32_16x16x32_bf16 v[68:71], a[92:95], v[124:127], v[68:71]\n"
"ds_read_b64_tr_b16 a[168:169], v8 offset:8704\n"
"ds_read_b64_tr_b16 a[170:171], v8 offset:12800\n"
"buffer_load_dwordx4 v22, s[44:47], s52 offen lds\n"
"v_mfma_f32_16x16x32_bf16 v[176:179], a[144:147], v[56:59], v[176:179]\n"
"buffer_load_dwordx4 v23, s[44:47], s52 offen offset:1024 lds\n"
"v_exp_f32 v78, v78\n"
"v_mfma_f32_16x16x32_bf16 v[180:183], a[148:151], v[56:59], v[180:183]\n"
"ds_read_b64_tr_b16 a[172:173], v9 offset:8704\n"
"buffer_load_dwordx4 v24, s[44:47], s52 offen offset:2048 lds\n"
"v_cvt_pk_bf16_f32 v62, v76, v77\n"
"v_mfma_f32_16x16x32_bf16 v[184:187], a[152:155], v[56:59], v[184:187]\n"
"buffer_load_dwordx4 v25, s[44:47], s52 offen offset:3072 lds\n"
"v_exp_f32 v79, v79\n"
"v_mfma_f32_16x16x32_bf16 v[188:191], a[156:159], v[56:59], v[188:191]\n"
"ds_read_b64_tr_b16 a[174:175], v9 offset:12800\n"
"v_mfma_f32_16x16x32_bf16 v[72:75], a[64:67], v[128:131], v[48:51]\n"
"v_exp_f32 v64, v64\n"
"v_mfma_f32_16x16x32_bf16 v[72:75], a[68:71], v[132:135], v[72:75]\n"
"ds_read_b64_tr_b16 a[176:177], v8 offset:9216\n"
"v_cvt_pk_bf16_f32 v63, v78, v79\n"
"v_mfma_f32_16x16x32_bf16 v[72:75], a[72:75], v[136:139], v[72:75]\n"
"v_exp_f32 v65, v65\n"
"v_mfma_f32_16x16x32_bf16 v[72:75], a[76:79], v[140:143], v[72:75]\n"
"ds_read_b64_tr_b16 a[178:179], v8 offset:13312\n"
"v_add_f32 v31, v31, v64\n"
"v_mfma_f32_16x16x32_bf16 v[192:195], a[128:131], v[60:63], v[192:195]\n"
"s_mov_b32 m0, 110592\n"
"v_exp_f32 v66, v66\n"
"v_mfma_f32_16x16x32_bf16 v[196:199], a[132:135], v[60:63], v[196:199]\n"
"ds_read_b64_tr_b16 a[180:181], v9 offset:9216\n"
"v_cvt_pk_bf16_f32 v56, v64, v65\n"
"v_add_f32 v31, v31, v65\n"
"v_mfma_f32_16x16x32_bf16 v[200:203], a[136:139], v[60:63], v[200:203]\n"
"buffer_load_dwordx4 v14, s[36:39], s61 offen nt lds\n"
"v_exp_f32 v67, v67\n"
"v_mfma_f32_16x16x32_bf16 v[204:207], a[140:143], v[60:63], v[204:207]\n"
"ds_read_b64_tr_b16 a[182:183], v9 offset:13312\n"
"v_add_f32 v31, v31, v66\n"
"v_mfma_f32_16x16x32_bf16 v[18:21], a[224:227], v[60:63], v[18:21]\n"
"v_mfma_f32_16x16x32_bf16 v[76:79], a[80:83], v[128:131], v[48:51]\n"
"buffer_load_dwordx4 v15, s[36:39], s61 offen offset:1024 nt lds\n"
"v_exp_f32 v68, v68\n"
"v_mfma_f32_16x16x32_bf16 v[76:79], a[84:87], v[132:135], v[76:79]\n"
"ds_read_b64_tr_b16 a[184:185], v8 offset:9728\n"
"v_cvt_pk_bf16_f32 v57, v66, v67\n"
"v_add_f32 v31, v31, v67\n"
"v_mfma_f32_16x16x32_bf16 v[76:79], a[88:91], v[136:139], v[76:79]\n"
"buffer_load_dwordx4 v14, s[36:39], s61 offen offset:2048 nt lds\n"
"v_exp_f32 v69, v69\n"
"v_mfma_f32_16x16x32_bf16 v[76:79], a[92:95], v[140:143], v[76:79]\n"
"ds_read_b64_tr_b16 a[186:187], v8 offset:13824\n"
"v_add_f32 v31, v31, v68\n"
"v_mfma_f32_16x16x32_bf16 v[208:211], a[144:147], v[60:63], v[208:211]\n"
"buffer_load_dwordx4 v15, s[36:39], s61 offen offset:3072 nt lds\n"
"v_exp_f32 v70, v70\n"
"v_mfma_f32_16x16x32_bf16 v[212:215], a[148:151], v[60:63], v[212:215]\n"
"ds_read_b64_tr_b16 a[188:189], v9 offset:9728\n"
"v_cvt_pk_bf16_f32 v58, v68, v69\n"
"v_add_f32 v31, v31, v69\n"
"v_mfma_f32_16x16x32_bf16 v[216:219], a[152:155], v[60:63], v[216:219]\n"
"s_add_u32 s62, s62, 8192\n"
"s_and_b32 s62, s62, 0x3ffff\n"
"s_add_u32 s61, s62, 4096\n"
"v_exp_f32 v71, v71\n"
"v_mfma_f32_16x16x32_bf16 v[220:223], a[156:159], v[60:63], v[220:223]\n"
"ds_read_b64_tr_b16 a[190:191], v9 offset:13824\n"
"v_add_f32 v31, v31, v70\n"
"v_mfma_f32_16x16x32_bf16 v[64:67], a[96:99], v[80:83], v[36:39]\n"
"v_exp_f32 v72, v72\n"
"v_mfma_f32_16x16x32_bf16 v[64:67], a[100:103], v[84:87], v[64:67]\n"
"v_cvt_pk_bf16_f32 v59, v70, v71\n"
"v_add_f32 v31, v31, v71\n"
"v_mfma_f32_16x16x32_bf16 v[64:67], a[104:107], v[88:91], v[64:67]\n"
"v_exp_f32 v73, v73\n"
"v_mfma_f32_16x16x32_bf16 v[64:67], a[108:111], v[92:95], v[64:67]\n"
"ds_read_b128 a[64:67], v4 offset:16384\n"
"v_add_f32 v34, v34, v72\n"
"v_mfma_f32_16x16x32_bf16 v[224:227], a[128:131], v[56:59], v[224:227]\n"
"s_mov_b32 m0, 49152\n"
"v_exp_f32 v74, v74\n"
"v_mfma_f32_16x16x32_bf16 v[228:231], a[132:135], v[56:59], v[228:231]\n"
"ds_read_b128 a[68:71], v5 offset:16384\n"
"v_cvt_pk_bf16_f32 v60, v72, v73\n"
"v_add_f32 v34, v34, v73\n"
"v_mfma_f32_16x16x32_bf16 v[232:235], a[136:139], v[56:59], v[232:235]\n"
"buffer_load_dwordx4 v10, s[28:31], s62 offen nt lds\n"
"v_exp_f32 v75, v75\n"
"v_mfma_f32_16x16x32_bf16 v[236:239], a[140:143], v[56:59], v[236:239]\n"
"ds_read_b128 a[72:75], v6 offset:16384\n"
"buffer_load_dwordx4 v11, s[28:31], s62 offen offset:1024 nt lds\n"
"v_add_f32 v34, v34, v74\n"
"v_mfma_f32_16x16x32_bf16 v[68:71], a[112:115], v[80:83], v[36:39]\n"
"buffer_load_dwordx4 v12, s[28:31], s62 offen offset:2048 nt lds\n"
"v_exp_f32 v76, v76\n"
"v_mfma_f32_16x16x32_bf16 v[68:71], a[116:119], v[84:87], v[68:71]\n"
"ds_read_b128 a[76:79], v7 offset:16384\n"
"buffer_load_dwordx4 v13, s[28:31], s62 offen offset:3072 nt lds\n"
"v_cvt_pk_bf16_f32 v61, v74, v75\n"
"v_add_f32 v34, v34, v75\n"
"v_mfma_f32_16x16x32_bf16 v[68:71], a[120:123], v[88:91], v[68:71]\n"
"s_mov_b32 m0, 65536\n"
"s_mov_b32 s52, 229376\n"
"v_exp_f32 v77, v77\n"
"v_mfma_f32_16x16x32_bf16 v[68:71], a[124:127], v[92:95], v[68:71]\n"
"ds_read_b128 a[80:83], v4 offset:20480\n"
"buffer_load_dwordx4 v22, s[44:47], s52 offen lds\n"
"v_add_f32 v34, v34, v76\n"
"v_mfma_f32_16x16x32_bf16 v[240:243], a[144:147], v[56:59], v[240:243]\n"
"buffer_load_dwordx4 v23, s[44:47], s52 offen offset:1024 lds\n"
"v_exp_f32 v78, v78\n"
"v_mfma_f32_16x16x32_bf16 v[244:247], a[148:151], v[56:59], v[244:247]\n"
"ds_read_b128 a[84:87], v5 offset:20480\n"
"buffer_load_dwordx4 v24, s[44:47], s52 offen offset:2048 lds\n"
"v_cvt_pk_bf16_f32 v62, v76, v77\n"
"v_add_f32 v34, v34, v77\n"
"v_mfma_f32_16x16x32_bf16 v[248:251], a[152:155], v[56:59], v[248:251]\n"
"buffer_load_dwordx4 v25, s[44:47], s52 offen offset:3072 lds\n"
"v_exp_f32 v79, v79\n"
"v_mfma_f32_16x16x32_bf16 v[252:255], a[156:159], v[56:59], v[252:255]\n"
"v_add_f32 v34, v34, v78\n"
"v_mfma_f32_16x16x32_bf16 v[72:75], a[96:99], v[96:99], v[40:43]\n"
"v_exp_f32 v64, v64\n"
"v_mfma_f32_16x16x32_bf16 v[72:75], a[100:103], v[100:103], v[72:75]\n"
"v_cvt_pk_bf16_f32 v63, v78, v79\n"
"v_add_f32 v34, v34, v79\n"
"v_mfma_f32_16x16x32_bf16 v[72:75], a[104:107], v[104:107], v[72:75]\n"
"v_exp_f32 v65, v65\n"
"v_mfma_f32_16x16x32_bf16 v[72:75], a[108:111], v[108:111], v[72:75]\n"
"ds_read_b128 a[88:91], v6 offset:20480\n"
"v_add_f32 v29, v29, v64\n"
"v_mfma_f32_16x16x32_bf16 a[192:195], a[128:131], v[60:63], a[192:195]\n"
"s_mov_b32 m0, 53248\n"
"v_exp_f32 v66, v66\n"
"v_mfma_f32_16x16x32_bf16 a[196:199], a[132:135], v[60:63], a[196:199]\n"
"s_waitcnt vmcnt(52)\n"
"v_cvt_pk_bf16_f32 v56, v64, v65\n"
"v_add_f32 v29, v29, v65\n"
"v_mfma_f32_16x16x32_bf16 a[200:203], a[136:139], v[60:63], a[200:203]\n"
"buffer_load_dwordx4 v10, s[28:31], s61 offen nt lds\n"
"v_exp_f32 v67, v67\n"
"v_mfma_f32_16x16x32_bf16 a[204:207], a[140:143], v[60:63], a[204:207]\n"
"ds_read_b128 a[92:95], v7 offset:20480\n"
"v_add_f32 v29, v29, v66\n"
"v_mfma_f32_16x16x32_bf16 v[76:79], a[112:115], v[96:99], v[40:43]\n"
"buffer_load_dwordx4 v11, s[28:31], s61 offen offset:1024 nt lds\n"
"v_exp_f32 v68, v68\n"
"v_mfma_f32_16x16x32_bf16 v[76:79], a[116:119], v[100:103], v[76:79]\n"
"v_cvt_pk_bf16_f32 v57, v66, v67\n"
"v_add_f32 v29, v29, v67\n"
"v_mfma_f32_16x16x32_bf16 v[76:79], a[120:123], v[104:107], v[76:79]\n"
"buffer_load_dwordx4 v12, s[28:31], s61 offen offset:2048 nt lds\n"
"v_exp_f32 v69, v69\n"
"v_mfma_f32_16x16x32_bf16 v[76:79], a[124:127], v[108:111], v[76:79]\n"
"v_add_f32 v29, v29, v68\n"
"v_mfma_f32_16x16x32_bf16 a[208:211], a[144:147], v[60:63], a[208:211]\n"
"buffer_load_dwordx4 v13, s[28:31], s61 offen offset:3072 nt lds\n"
"v_exp_f32 v70, v70\n"
"v_mfma_f32_16x16x32_bf16 a[212:215], a[148:151], v[60:63], a[212:215]\n"
"v_cvt_pk_bf16_f32 v58, v68, v69\n"
"v_add_f32 v29, v29, v69\n"
"v_mfma_f32_16x16x32_bf16 a[216:219], a[152:155], v[60:63], a[216:219]\n"
"v_exp_f32 v71, v71\n"
"v_mfma_f32_16x16x32_bf16 a[220:223], a[156:159], v[60:63], a[220:223]\n"
"v_add_f32 v29, v29, v70\n"
"v_mfma_f32_16x16x32_bf16 v[64:67], a[96:99], v[112:115], v[44:47]\n"
"v_exp_f32 v72, v72\n"
"v_mfma_f32_16x16x32_bf16 v[64:67], a[100:103], v[116:119], v[64:67]\n"
"v_cvt_pk_bf16_f32 v59, v70, v71\n"
"v_add_f32 v29, v29, v71\n"
"v_mfma_f32_16x16x32_bf16 v[64:67], a[104:107], v[120:123], v[64:67]\n"
"global_load_dword v33, v32, s[18:19]\n"
"v_exp_f32 v73, v73\n"
"v_mfma_f32_16x16x32_bf16 v[64:67], a[108:111], v[124:127], v[64:67]\n"
"s_waitcnt lgkmcnt(0)\n"
"s_barrier\n"
"v_mfma_f32_16x16x32_bf16 v[160:163], a[160:163], v[56:59], v[160:163]\n"
"s_mov_b32 m0, 114688\n"
"v_exp_f32 v74, v74\n"
"v_mfma_f32_16x16x32_bf16 v[164:167], a[164:167], v[56:59], v[164:167]\n"
"ds_read_b64_tr_b16 a[128:129], v8 offset:16384\n"
"s_waitcnt vmcnt(52)\n"
"v_cvt_pk_bf16_f32 v60, v72, v73\n"
"v_mfma_f32_16x16x32_bf16 v[168:171], a[168:171], v[56:59], v[168:171]\n"
"buffer_load_dwordx4 v14, s[36:39], s62 offen nt lds\n"
"v_exp_f32 v75, v75\n"
"v_mfma_f32_16x16x32_bf16 v[172:175], a[172:175], v[56:59], v[172:175]\n"
"ds_read_b64_tr_b16 a[130:131], v8 offset:20480\n"
"ds_read_b64_tr_b16 a[132:133], v9 offset:16384\n"
"buffer_load_dwordx4 v15, s[36:39], s62 offen offset:1024 nt lds\n"
"v_mfma_f32_16x16x32_bf16 v[68:71], a[112:115], v[112:115], v[44:47]\n"
"buffer_load_dwordx4 v14, s[36:39], s62 offen offset:2048 nt lds\n"
"v_exp_f32 v76, v76\n"
"v_mfma_f32_16x16x32_bf16 v[68:71], a[116:119], v[116:119], v[68:71]\n"
"ds_read_b64_tr_b16 a[134:135], v9 offset:20480\n"
"buffer_load_dwordx4 v15, s[36:39], s62 offen offset:3072 nt lds\n"
"v_cvt_pk_bf16_f32 v61, v74, v75\n"
"v_mfma_f32_16x16x32_bf16 v[68:71], a[120:123], v[120:123], v[68:71]\n"
"s_mov_b32 m0, 69632\n"
"s_mov_b32 s52, 753664\n"
"v_exp_f32 v77, v77\n"
"v_mfma_f32_16x16x32_bf16 v[68:71], a[124:127], v[124:127], v[68:71]\n"
"ds_read_b64_tr_b16 a[136:137], v8 offset:16896\n"
"ds_read_b64_tr_b16 a[138:139], v8 offset:20992\n"
"buffer_load_dwordx4 v22, s[44:47], s52 offen lds\n"
"v_mfma_f32_16x16x32_bf16 v[176:179], a[176:179], v[56:59], v[176:179]\n"
"buffer_load_dwordx4 v23, s[44:47], s52 offen offset:1024 lds\n"
"v_exp_f32 v78, v78\n"
"v_mfma_f32_16x16x32_bf16 v[180:183], a[180:183], v[56:59], v[180:183]\n"
"ds_read_b64_tr_b16 a[140:141], v9 offset:16896\n"
"buffer_load_dwordx4 v24, s[44:47], s52 offen offset:2048 lds\n"
"v_cvt_pk_bf16_f32 v62, v76, v77\n"
"v_mfma_f32_16x16x32_bf16 v[184:187], a[184:187], v[56:59], v[184:187]\n"
"buffer_load_dwordx4 v25, s[44:47], s52 offen offset:3072 lds\n"
"v_exp_f32 v79, v79\n"
"v_mfma_f32_16x16x32_bf16 v[188:191], a[188:191], v[56:59], v[188:191]\n"
"ds_read_b64_tr_b16 a[142:143], v9 offset:20992\n"
"v_mfma_f32_16x16x32_bf16 v[72:75], a[96:99], v[128:131], v[48:51]\n"
"v_exp_f32 v64, v64\n"
"v_mfma_f32_16x16x32_bf16 v[72:75], a[100:103], v[132:135], v[72:75]\n"
"ds_read_b64_tr_b16 a[144:145], v8 offset:17408\n"
"v_cvt_pk_bf16_f32 v63, v78, v79\n"
"v_mfma_f32_16x16x32_bf16 v[72:75], a[104:107], v[136:139], v[72:75]\n"
"v_exp_f32 v65, v65\n"
"v_mfma_f32_16x16x32_bf16 v[72:75], a[108:111], v[140:143], v[72:75]\n"
"ds_read_b64_tr_b16 a[146:147], v8 offset:21504\n"
"v_add_f32 v31, v31, v64\n"
"v_mfma_f32_16x16x32_bf16 v[192:195], a[160:163], v[60:63], v[192:195]\n"
"s_mov_b32 m0, 118784\n"
"v_exp_f32 v66, v66\n"
"v_mfma_f32_16x16x32_bf16 v[196:199], a[164:167], v[60:63], v[196:199]\n"
"ds_read_b64_tr_b16 a[148:149], v9 offset:17408\n"
"s_waitcnt vmcnt(52)\n"
"v_cvt_pk_bf16_f32 v56, v64, v65\n"
"v_add_f32 v31, v31, v65\n"
"v_mfma_f32_16x16x32_bf16 v[200:203], a[168:171], v[60:63], v[200:203]\n"
"buffer_load_dwordx4 v14, s[36:39], s61 offen nt lds\n"
"v_exp_f32 v67, v67\n"
"v_mfma_f32_16x16x32_bf16 v[204:207], a[172:175], v[60:63], v[204:207]\n"
"ds_read_b64_tr_b16 a[150:151], v9 offset:21504\n"
"v_add_f32 v31, v31, v66\n"
"v_mfma_f32_16x16x32_bf16 v[18:21], a[224:227], v[60:63], v[18:21]\n"
"v_mfma_f32_16x16x32_bf16 v[76:79], a[112:115], v[128:131], v[48:51]\n"
"buffer_load_dwordx4 v15, s[36:39], s61 offen offset:1024 nt lds\n"
"v_exp_f32 v68, v68\n"
"v_mfma_f32_16x16x32_bf16 v[76:79], a[116:119], v[132:135], v[76:79]\n"
"ds_read_b64_tr_b16 a[152:153], v8 offset:17920\n"
"v_cvt_pk_bf16_f32 v57, v66, v67\n"
"v_add_f32 v31, v31, v67\n"
"v_mfma_f32_16x16x32_bf16 v[76:79], a[120:123], v[136:139], v[76:79]\n"
"buffer_load_dwordx4 v14, s[36:39], s61 offen offset:2048 nt lds\n"
"v_exp_f32 v69, v69\n"
"v_mfma_f32_16x16x32_bf16 v[76:79], a[124:127], v[140:143], v[76:79]\n"
"ds_read_b64_tr_b16 a[154:155], v8 offset:22016\n"
"v_add_f32 v31, v31, v68\n"
"v_mfma_f32_16x16x32_bf16 v[208:211], a[176:179], v[60:63], v[208:211]\n"
"buffer_load_dwordx4 v15, s[36:39], s61 offen offset:3072 nt lds\n"
"v_exp_f32 v70, v70\n"
"v_mfma_f32_16x16x32_bf16 v[212:215], a[180:183], v[60:63], v[212:215]\n"
"ds_read_b64_tr_b16 a[156:157], v9 offset:17920\n"
"v_cvt_pk_bf16_f32 v58, v68, v69\n"
"v_add_f32 v31, v31, v69\n"
"v_mfma_f32_16x16x32_bf16 v[216:219], a[184:187], v[60:63], v[216:219]\n"
"s_add_u32 s62, s62, 8192\n"
"s_and_b32 s62, s62, 0x3ffff\n"
"s_add_u32 s61, s62, 4096\n"
"v_exp_f32 v71, v71\n"
"v_mfma_f32_16x16x32_bf16 v[220:223], a[188:191], v[60:63], v[220:223]\n"
"ds_read_b64_tr_b16 a[158:159], v9 offset:22016\n"
"v_add_f32 v31, v31, v70\n"
"v_mfma_f32_16x16x32_bf16 v[64:67], a[64:67], v[80:83], v[36:39]\n"
"v_exp_f32 v72, v72\n"
"v_mfma_f32_16x16x32_bf16 v[64:67], a[68:71], v[84:87], v[64:67]\n"
"v_cvt_pk_bf16_f32 v59, v70, v71\n"
"v_add_f32 v31, v31, v71\n"
"v_mfma_f32_16x16x32_bf16 v[64:67], a[72:75], v[88:91], v[64:67]\n"
"v_exp_f32 v73, v73\n"
"v_mfma_f32_16x16x32_bf16 v[64:67], a[76:79], v[92:95], v[64:67]\n"
"ds_read_b128 a[96:99], v4 offset:24576\n"
"v_add_f32 v34, v34, v72\n"
"v_mfma_f32_16x16x32_bf16 v[224:227], a[160:163], v[56:59], v[224:227]\n"
"s_mov_b32 m0, 57344\n"
"v_exp_f32 v74, v74\n"
"v_mfma_f32_16x16x32_bf16 v[228:231], a[164:167], v[56:59], v[228:231]\n"
"ds_read_b128 a[100:103], v5 offset:24576\n"
"s_waitcnt vmcnt(52)\n"
"v_cvt_pk_bf16_f32 v60, v72, v73\n"
"v_add_f32 v34, v34, v73\n"
"v_mfma_f32_16x16x32_bf16 v[232:235], a[168:171], v[56:59], v[232:235]\n"
"buffer_load_dwordx4 v10, s[28:31], s62 offen nt lds\n"
"v_exp_f32 v75, v75\n"
"v_mfma_f32_16x16x32_bf16 v[236:239], a[172:175], v[56:59], v[236:239]\n"
"ds_read_b128 a[104:107], v6 offset:24576\n"
"buffer_load_dwordx4 v11, s[28:31], s62 offen offset:1024 nt lds\n"
"v_add_f32 v34, v34, v74\n"
"v_mfma_f32_16x16x32_bf16 v[68:71], a[80:83], v[80:83], v[36:39]\n"
"buffer_load_dwordx4 v12, s[28:31], s62 offen offset:2048 nt lds\n"
"v_exp_f32 v76, v76\n"
"v_mfma_f32_16x16x32_bf16 v[68:71], a[84:87], v[84:87], v[68:71]\n"
"ds_read_b128 a[108:111], v7 offset:24576\n"
"buffer_load_dwordx4 v13, s[28:31], s62 offen offset:3072 nt lds\n"
"v_cvt_pk_bf16_f32 v61, v74, v75\n"
"v_add_f32 v34, v34, v75\n"
"v_mfma_f32_16x16x32_bf16 v[68:71], a[88:91], v[88:91], v[68:71]\n"
"s_mov_b32 m0, 8192\n"
"s_mov_b32 s52, 1245184\n"
"v_exp_f32 v77, v77\n"
"v_mfma_f32_16x16x32_bf16 v[68:71], a[92:95], v[92:95], v[68:71]\n"
"ds_read_b128 a[112:115], v4 offset:28672\n"
"buffer_load_dwordx4 v22, s[44:47], s52 offen lds\n"
"v_add_f32 v34, v34, v76\n"
"v_mfma_f32_16x16x32_bf16 v[240:243], a[176:179], v[56:59], v[240:243]\n"
"buffer_load_dwordx4 v23, s[44:47], s52 offen offset:1024 lds\n"
"v_exp_f32 v78, v78\n"
"v_mfma_f32_16x16x32_bf16 v[244:247], a[180:183], v[56:59], v[244:247]\n"
"ds_read_b128 a[116:119], v5 offset:28672\n"
"buffer_load_dwordx4 v24, s[44:47], s52 offen offset:2048 lds\n"
"v_cvt_pk_bf16_f32 v62, v76, v77\n"
"v_add_f32 v34, v34, v77\n"
"v_mfma_f32_16x16x32_bf16 v[248:251], a[184:187], v[56:59], v[248:251]\n"
"buffer_load_dwordx4 v25, s[44:47], s52 offen offset:3072 lds\n"
"v_exp_f32 v79, v79\n"
"v_mfma_f32_16x16x32_bf16 v[252:255], a[188:191], v[56:59], v[252:255]\n"
"v_add_f32 v34, v34, v78\n"
"v_mfma_f32_16x16x32_bf16 v[72:75], a[64:67], v[96:99], v[40:43]\n"
"v_exp_f32 v64, v64\n"
"v_mfma_f32_16x16x32_bf16 v[72:75], a[68:71], v[100:103], v[72:75]\n"
"v_cvt_pk_bf16_f32 v63, v78, v79\n"
"v_add_f32 v34, v34, v79\n"
"v_mfma_f32_16x16x32_bf16 v[72:75], a[72:75], v[104:107], v[72:75]\n"
"v_exp_f32 v65, v65\n"
"v_mfma_f32_16x16x32_bf16 v[72:75], a[76:79], v[108:111], v[72:75]\n"
"ds_read_b128 a[120:123], v6 offset:28672\n"
"v_add_f32 v29, v29, v64\n"
"v_mfma_f32_16x16x32_bf16 a[192:195], a[160:163], v[60:63], a[192:195]\n"
"s_mov_b32 m0, 61440\n"
"v_exp_f32 v66, v66\n"
"v_mfma_f32_16x16x32_bf16 a[196:199], a[164:167], v[60:63], a[196:199]\n"
"s_waitcnt vmcnt(52)\n"
"v_cvt_pk_bf16_f32 v56, v64, v65\n"
"v_add_f32 v29, v29, v65\n"
"v_mfma_f32_16x16x32_bf16 a[200:203], a[168:171], v[60:63], a[200:203]\n"
"buffer_load_dwordx4 v10, s[28:31], s61 offen nt lds\n"
"v_exp_f32 v67, v67\n"
"v_mfma_f32_16x16x32_bf16 a[204:207], a[172:175], v[60:63], a[204:207]\n"
"ds_read_b128 a[124:127], v7 offset:28672\n"
"v_add_f32 v29, v29, v66\n"
"v_mfma_f32_16x16x32_bf16 v[76:79], a[80:83], v[96:99], v[40:43]\n"
"buffer_load_dwordx4 v11, s[28:31], s61 offen offset:1024 nt lds\n"
"v_exp_f32 v68, v68\n"
"v_mfma_f32_16x16x32_bf16 v[76:79], a[84:87], v[100:103], v[76:79]\n"
"v_cvt_pk_bf16_f32 v57, v66, v67\n"
"v_add_f32 v29, v29, v67\n"
"v_mfma_f32_16x16x32_bf16 v[76:79], a[88:91], v[104:107], v[76:79]\n"
"buffer_load_dwordx4 v12, s[28:31], s61 offen offset:2048 nt lds\n"
"v_exp_f32 v69, v69\n"
"v_mfma_f32_16x16x32_bf16 v[76:79], a[92:95], v[108:111], v[76:79]\n"
"v_add_f32 v29, v29, v68\n"
"v_mfma_f32_16x16x32_bf16 a[208:211], a[176:179], v[60:63], a[208:211]\n"
"buffer_load_dwordx4 v13, s[28:31], s61 offen offset:3072 nt lds\n"
"v_exp_f32 v70, v70\n"
"v_mfma_f32_16x16x32_bf16 a[212:215], a[180:183], v[60:63], a[212:215]\n"
"v_cvt_pk_bf16_f32 v58, v68, v69\n"
"v_add_f32 v29, v29, v69\n"
"v_mfma_f32_16x16x32_bf16 a[216:219], a[184:187], v[60:63], a[216:219]\n"
"v_exp_f32 v71, v71\n"
"v_mfma_f32_16x16x32_bf16 a[220:223], a[188:191], v[60:63], a[220:223]\n"
"v_add_f32 v29, v29, v70\n"
"v_mfma_f32_16x16x32_bf16 v[64:67], a[64:67], v[112:115], v[44:47]\n"
"v_exp_f32 v72, v72\n"
"v_mfma_f32_16x16x32_bf16 v[64:67], a[68:71], v[116:119], v[64:67]\n"
"v_cvt_pk_bf16_f32 v59, v70, v71\n"
"v_add_f32 v29, v29, v71\n"
"v_mfma_f32_16x16x32_bf16 v[64:67], a[72:75], v[120:123], v[64:67]\n"
"v_exp_f32 v73, v73\n"
"v_mfma_f32_16x16x32_bf16 v[64:67], a[76:79], v[124:127], v[64:67]\n"
"s_waitcnt lgkmcnt(0)\n"
"s_barrier\n"
"v_mfma_f32_16x16x32_bf16 v[160:163], a[128:131], v[56:59], v[160:163]\n"
"s_mov_b32 m0, 122880\n"
"v_exp_f32 v74, v74\n"
"v_mfma_f32_16x16x32_bf16 v[164:167], a[132:135], v[56:59], v[164:167]\n"
"ds_read_b64_tr_b16 a[160:161], v8 offset:24576\n"
"s_waitcnt vmcnt(52)\n"
"v_cvt_pk_bf16_f32 v60, v72, v73\n"
"v_mfma_f32_16x16x32_bf16 v[168:171], a[136:139], v[56:59], v[168:171]\n"
"buffer_load_dwordx4 v14, s[36:39], s62 offen nt lds\n"
"v_exp_f32 v75, v75\n"
"v_mfma_f32_16x16x32_bf16 v[172:175], a[140:143], v[56:59], v[172:175]\n"
"ds_read_b64_tr_b16 a[162:163], v8 offset:28672\n"
"ds_read_b64_tr_b16 a[164:165], v9 offset:24576\n"
"buffer_load_dwordx4 v15, s[36:39], s62 offen offset:1024 nt lds\n"
"v_mfma_f32_16x16x32_bf16 v[68:71], a[80:83], v[112:115], v[44:47]\n"
"buffer_load_dwordx4 v14, s[36:39], s62 offen offset:2048 nt lds\n"
"v_exp_f32 v76, v76\n"
"v_mfma_f32_16x16x32_bf16 v[68:71], a[84:87], v[116:119], v[68:71]\n"
"ds_read_b64_tr_b16 a[166:167], v9 offset:28672\n"
"buffer_load_dwordx4 v15, s[36:39], s62 offen offset:3072 nt lds\n"
"v_cvt_pk_bf16_f32 v61, v74, v75\n"
"v_mfma_f32_16x16x32_bf16 v[68:71], a[88:91], v[120:123], v[68:71]\n"
"s_mov_b32 m0, 12288\n"
"s_mov_b32 s52, 1769472\n"
"v_exp_f32 v77, v77\n"
"v_mfma_f32_16x16x32_bf16 v[68:71], a[92:95], v[124:127], v[68:71]\n"
"ds_read_b64_tr_b16 a[168:169], v8 offset:25088\n"
"ds_read_b64_tr_b16 a[170:171], v8 offset:29184\n"
"buffer_load_dwordx4 v22, s[44:47], s52 offen lds\n"
"v_mfma_f32_16x16x32_bf16 v[176:179], a[144:147], v[56:59], v[176:179]\n"
"buffer_load_dwordx4 v23, s[44:47], s52 offen offset:1024 lds\n"
"v_exp_f32 v78, v78\n"
"v_mfma_f32_16x16x32_bf16 v[180:183], a[148:151], v[56:59], v[180:183]\n"
"ds_read_b64_tr_b16 a[172:173], v9 offset:25088\n"
"buffer_load_dwordx4 v24, s[44:47], s52 offen offset:2048 lds\n"
"v_cvt_pk_bf16_f32 v62, v76, v77\n"
"v_mfma_f32_16x16x32_bf16 v[184:187], a[152:155], v[56:59], v[184:187]\n"
"buffer_load_dwordx4 v25, s[44:47], s52 offen offset:3072 lds\n"
"v_exp_f32 v79, v79\n"
"v_mfma_f32_16x16x32_bf16 v[188:191], a[156:159], v[56:59], v[188:191]\n"
"ds_read_b64_tr_b16 a[174:175], v9 offset:29184\n"
"v_mfma_f32_16x16x32_bf16 v[72:75], a[64:67], v[128:131], v[48:51]\n"
"v_exp_f32 v64, v64\n"
"v_mfma_f32_16x16x32_bf16 v[72:75], a[68:71], v[132:135], v[72:75]\n"
"ds_read_b64_tr_b16 a[176:177], v8 offset:25600\n"
"v_cvt_pk_bf16_f32 v63, v78, v79\n"
"v_mfma_f32_16x16x32_bf16 v[72:75], a[72:75], v[136:139], v[72:75]\n"
"v_exp_f32 v65, v65\n"
"v_mfma_f32_16x16x32_bf16 v[72:75], a[76:79], v[140:143], v[72:75]\n"
"ds_read_b64_tr_b16 a[178:179], v8 offset:29696\n"
"v_add_f32 v31, v31, v64\n"
"v_mfma_f32_16x16x32_bf16 v[192:195], a[128:131], v[60:63], v[192:195]\n"
"s_mov_b32 m0, 126976\n"
"v_exp_f32 v66, v66\n"
"v_mfma_f32_16x16x32_bf16 v[196:199], a[132:135], v[60:63], v[196:199]\n"
"ds_read_b64_tr_b16 a[180:181], v9 offset:25600\n"
"s_waitcnt vmcnt(52)\n"
"v_cvt_pk_bf16_f32 v56, v64, v65\n"
"v_add_f32 v31, v31, v65\n"
"v_mfma_f32_16x16x32_bf16 v[200:203], a[136:139], v[60:63], v[200:203]\n"
"buffer_load_dwordx4 v14, s[36:39], s61 offen nt lds\n"
"v_exp_f32 v67, v67\n"
"v_mfma_f32_16x16x32_bf16 v[204:207], a[140:143], v[60:63], v[204:207]\n"
"ds_read_b64_tr_b16 a[182:183], v9 offset:29696\n"
"v_add_f32 v31, v31, v66\n"
"v_mfma_f32_16x16x32_bf16 v[18:21], a[224:227], v[60:63], v[18:21]\n"
"v_mfma_f32_16x16x32_bf16 v[76:79], a[80:83], v[128:131], v[48:51]\n"
"buffer_load_dwordx4 v15, s[36:39], s61 offen offset:1024 nt lds\n"
"v_exp_f32 v68, v68\n"
"v_mfma_f32_16x16x32_bf16 v[76:79], a[84:87], v[132:135], v[76:79]\n"
"ds_read_b64_tr_b16 a[184:185], v8 offset:26112\n"
"v_cvt_pk_bf16_f32 v57, v66, v67\n"
"v_add_f32 v31, v31, v67\n"
"v_mfma_f32_16x16x32_bf16 v[76:79], a[88:91], v[136:139], v[76:79]\n"
"buffer_load_dwordx4 v14, s[36:39], s61 offen offset:2048 nt lds\n"
"v_exp_f32 v69, v69\n"
"v_mfma_f32_16x16x32_bf16 v[76:79], a[92:95], v[140:143], v[76:79]\n"
"ds_read_b64_tr_b16 a[186:187], v8 offset:30208\n"
"v_add_f32 v31, v31, v68\n"
"v_mfma_f32_16x16x32_bf16 v[208:211], a[144:147], v[60:63], v[208:211]\n"
"buffer_load_dwordx4 v15, s[36:39], s61 offen offset:3072 nt lds\n"
"v_exp_f32 v70, v70\n"
"v_mfma_f32_16x16x32_bf16 v[212:215], a[148:151], v[60:63], v[212:215]\n"
"ds_read_b64_tr_b16 a[188:189], v9 offset:26112\n"
"v_cvt_pk_bf16_f32 v58, v68, v69\n"
"v_add_f32 v31, v31, v69\n"
"v_mfma_f32_16x16x32_bf16 v[216:219], a[152:155], v[60:63], v[216:219]\n"
"s_add_u32 s62, s62, 8192\n"
"s_and_b32 s62, s62, 0x3ffff\n"
"s_add_u32 s61, s62, 4096\n"
"v_exp_f32 v71, v71\n"
"v_mfma_f32_16x16x32_bf16 v[220:223], a[156:159], v[60:63], v[220:223]\n"
"ds_read_b64_tr_b16 a[190:191], v9 offset:30208\n"
"v_add_f32 v31, v31, v70\n"
"v_mfma_f32_16x16x32_bf16 v[64:67], a[96:99], v[80:83], v[36:39]\n"
"s_mov_b32 s53, 393216\n"
"v_exp_f32 v72, v72\n"
"v_mfma_f32_16x16x32_bf16 v[64:67], a[100:103], v[84:87], v[64:67]\n"
"v_cvt_pk_bf16_f32 v59, v70, v71\n"
"v_add_f32 v31, v31, v71\n"
"v_mfma_f32_16x16x32_bf16 v[64:67], a[104:107], v[88:91], v[64:67]\n"
"v_exp_f32 v73, v73\n"
"v_mfma_f32_16x16x32_bf16 v[64:67], a[108:111], v[92:95], v[64:67]\n"
"ds_read_b128 a[64:67], v4 offset:32768\n"
"v_add_f32 v34, v34, v72\n"
"v_mfma_f32_16x16x32_bf16 v[224:227], a[128:131], v[56:59], v[224:227]\n"
"s_mov_b32 m0, 73728\n"
"s_mov_b32 s52, 1277952\n"
"v_exp_f32 v74, v74\n"
"v_mfma_f32_16x16x32_bf16 v[228:231], a[132:135], v[56:59], v[228:231]\n"
"ds_read_b128 a[68:71], v5 offset:32768\n"
"s_waitcnt vmcnt(52)\n"
"v_cvt_pk_bf16_f32 v60, v72, v73\n"
"v_add_f32 v34, v34, v73\n"
"v_mfma_f32_16x16x32_bf16 v[232:235], a[136:139], v[56:59], v[232:235]\n"
"buffer_load_dwordx4 v22, s[44:47], s52 offen lds\n"
"v_exp_f32 v75, v75\n"
"v_mfma_f32_16x16x32_bf16 v[236:239], a[140:143], v[56:59], v[236:239]\n"
"ds_read_b128 a[72:75], v6 offset:32768\n"
"buffer_load_dwordx4 v23, s[44:47], s52 offen offset:1024 lds\n"
"v_add_f32 v34, v34, v74\n"
"v_mfma_f32_16x16x32_bf16 v[68:71], a[112:115], v[80:83], v[36:39]\n"
"buffer_load_dwordx4 v24, s[44:47], s52 offen offset:2048 lds\n"
"v_exp_f32 v76, v76\n"
"v_mfma_f32_16x16x32_bf16 v[68:71], a[116:119], v[84:87], v[68:71]\n"
"ds_read_b128 a[76:79], v7 offset:32768\n"
"buffer_load_dwordx4 v25, s[44:47], s52 offen offset:3072 lds\n"
"v_cvt_pk_bf16_f32 v61, v74, v75\n"
"v_add_f32 v34, v34, v75\n"
"v_mfma_f32_16x16x32_bf16 v[68:71], a[120:123], v[88:91], v[68:71]\n"
"s_mov_b32 m0, 77824\n"
"s_mov_b32 s52, 1802240\n"
"v_exp_f32 v77, v77\n"
"v_mfma_f32_16x16x32_bf16 v[68:71], a[124:127], v[92:95], v[68:71]\n"
"ds_read_b128 a[80:83], v4 offset:36864\n"
"buffer_load_dwordx4 v22, s[44:47], s52 offen lds\n"
"v_add_f32 v34, v34, v76\n"
"v_mfma_f32_16x16x32_bf16 v[240:243], a[144:147], v[56:59], v[240:243]\n"
"buffer_load_dwordx4 v23, s[44:47], s52 offen offset:1024 lds\n"
"v_exp_f32 v78, v78\n"
"v_mfma_f32_16x16x32_bf16 v[244:247], a[148:151], v[56:59], v[244:247]\n"
"ds_read_b128 a[84:87], v5 offset:36864\n"
"buffer_load_dwordx4 v24, s[44:47], s52 offen offset:2048 lds\n"
"v_cvt_pk_bf16_f32 v62, v76, v77\n"
"v_add_f32 v34, v34, v77\n"
"v_mfma_f32_16x16x32_bf16 v[248:251], a[152:155], v[56:59], v[248:251]\n"
"buffer_load_dwordx4 v25, s[44:47], s52 offen offset:3072 lds\n"
"v_exp_f32 v79, v79\n"
"v_mfma_f32_16x16x32_bf16 v[252:255], a[156:159], v[56:59], v[252:255]\n"
"buffer_load_dword v33, v26, s[44:47], s53 offen\n"
"v_add_f32 v34, v34, v78\n"
"v_mfma_f32_16x16x32_bf16 v[72:75], a[96:99], v[96:99], v[40:43]\n"
"v_exp_f32 v64, v64\n"
"v_mfma_f32_16x16x32_bf16 v[72:75], a[100:103], v[100:103], v[72:75]\n"
"v_cvt_pk_bf16_f32 v63, v78, v79\n"
"v_add_f32 v34, v34, v79\n"
"v_mfma_f32_16x16x32_bf16 v[72:75], a[104:107], v[104:107], v[72:75]\n"
"v_exp_f32 v65, v65\n"
"v_mfma_f32_16x16x32_bf16 v[72:75], a[108:111], v[108:111], v[72:75]\n"
"ds_read_b128 a[88:91], v6 offset:36864\n"
"v_add_f32 v29, v29, v64\n"
"v_mfma_f32_16x16x32_bf16 a[192:195], a[128:131], v[60:63], a[192:195]\n"
"s_mov_b32 m0, 16384\n"
"s_mov_b32 s52, 262144\n"
"v_exp_f32 v66, v66\n"
"v_mfma_f32_16x16x32_bf16 a[196:199], a[132:135], v[60:63], a[196:199]\n"
"s_waitcnt vmcnt(52)\n"
"v_cvt_pk_bf16_f32 v56, v64, v65\n"
"v_add_f32 v29, v29, v65\n"
"v_mfma_f32_16x16x32_bf16 a[200:203], a[136:139], v[60:63], a[200:203]\n"
"buffer_load_dwordx4 v22, s[44:47], s52 offen lds\n"
"v_exp_f32 v67, v67\n"
"v_mfma_f32_16x16x32_bf16 a[204:207], a[140:143], v[60:63], a[204:207]\n"
"ds_read_b128 a[92:95], v7 offset:36864\n"
"v_add_f32 v29, v29, v66\n"
"v_mfma_f32_16x16x32_bf16 v[76:79], a[112:115], v[96:99], v[40:43]\n"
"buffer_load_dwordx4 v23, s[44:47], s52 offen offset:1024 lds\n"
"v_exp_f32 v68, v68\n"
"v_mfma_f32_16x16x32_bf16 v[76:79], a[116:119], v[100:103], v[76:79]\n"
"v_cvt_pk_bf16_f32 v57, v66, v67\n"
"v_add_f32 v29, v29, v67\n"
"v_mfma_f32_16x16x32_bf16 v[76:79], a[120:123], v[104:107], v[76:79]\n"
"buffer_load_dwordx4 v24, s[44:47], s52 offen offset:2048 lds\n"
"v_exp_f32 v69, v69\n"
"v_mfma_f32_16x16x32_bf16 v[76:79], a[124:127], v[108:111], v[76:79]\n"
"v_add_f32 v29, v29, v68\n"
"v_mfma_f32_16x16x32_bf16 a[208:211], a[144:147], v[60:63], a[208:211]\n"
"buffer_load_dwordx4 v25, s[44:47], s52 offen offset:3072 lds\n"
"v_exp_f32 v70, v70\n"
"v_mfma_f32_16x16x32_bf16 a[212:215], a[148:151], v[60:63], a[212:215]\n"
"v_cvt_pk_bf16_f32 v58, v68, v69\n"
"v_add_f32 v29, v29, v69\n"
"v_mfma_f32_16x16x32_bf16 a[216:219], a[152:155], v[60:63], a[216:219]\n"
"v_exp_f32 v71, v71\n"
"v_mfma_f32_16x16x32_bf16 a[220:223], a[156:159], v[60:63], a[220:223]\n"
"v_add_f32 v29, v29, v70\n"
"v_mfma_f32_16x16x32_bf16 v[64:67], a[96:99], v[112:115], v[44:47]\n"
"s_mov_b32 s53, 1441792\n"
"v_exp_f32 v72, v72\n"
"v_mfma_f32_16x16x32_bf16 v[64:67], a[100:103], v[116:119], v[64:67]\n"
"v_cvt_pk_bf16_f32 v59, v70, v71\n"
"v_add_f32 v29, v29, v71\n"
"v_mfma_f32_16x16x32_bf16 v[64:67], a[104:107], v[120:123], v[64:67]\n"
"v_exp_f32 v73, v73\n"
"v_mfma_f32_16x16x32_bf16 v[64:67], a[108:111], v[124:127], v[64:67]\n"
"s_waitcnt lgkmcnt(0)\n"
"s_barrier\n"
"v_mfma_f32_16x16x32_bf16 v[160:163], a[160:163], v[56:59], v[160:163]\n"
"s_mov_b32 m0, 20480\n"
"s_mov_b32 s52, 786432\n"
"v_exp_f32 v74, v74\n"
"v_mfma_f32_16x16x32_bf16 v[164:167], a[164:167], v[56:59], v[164:167]\n"
"ds_read_b64_tr_b16 a[128:129], v8 offset:32768\n"
"s_waitcnt vmcnt(52)\n"
"v_cvt_pk_bf16_f32 v60, v72, v73\n"
"v_mfma_f32_16x16x32_bf16 v[168:171], a[168:171], v[56:59], v[168:171]\n"
"buffer_load_dwordx4 v22, s[44:47], s52 offen lds\n"
"v_exp_f32 v75, v75\n"
"v_mfma_f32_16x16x32_bf16 v[172:175], a[172:175], v[56:59], v[172:175]\n"
"ds_read_b64_tr_b16 a[130:131], v8 offset:36864\n"
"ds_read_b64_tr_b16 a[132:133], v9 offset:32768\n"
"buffer_load_dwordx4 v23, s[44:47], s52 offen offset:1024 lds\n"
"v_mfma_f32_16x16x32_bf16 v[68:71], a[112:115], v[112:115], v[44:47]\n"
"buffer_load_dwordx4 v24, s[44:47], s52 offen offset:2048 lds\n"
"v_exp_f32 v76, v76\n"
"v_mfma_f32_16x16x32_bf16 v[68:71], a[116:119], v[116:119], v[68:71]\n"
"ds_read_b64_tr_b16 a[134:135], v9 offset:36864\n"
"buffer_load_dwordx4 v25, s[44:47], s52 offen offset:3072 lds\n"
"v_cvt_pk_bf16_f32 v61, v74, v75\n"
"v_mfma_f32_16x16x32_bf16 v[68:71], a[120:123], v[120:123], v[68:71]\n"
"s_mov_b32 m0, 81920\n"
"s_mov_b32 s52, 294912\n"
"v_exp_f32 v77, v77\n"
"v_mfma_f32_16x16x32_bf16 v[68:71], a[124:127], v[124:127], v[68:71]\n"
"ds_read_b64_tr_b16 a[136:137], v8 offset:33280\n"
"ds_read_b64_tr_b16 a[138:139], v8 offset:37376\n"
"buffer_load_dwordx4 v22, s[44:47], s52 offen lds\n"
"v_mfma_f32_16x16x32_bf16 v[176:179], a[176:179], v[56:59], v[176:179]\n"
"buffer_load_dwordx4 v23, s[44:47], s52 offen offset:1024 lds\n"
"v_exp_f32 v78, v78\n"
"v_mfma_f32_16x16x32_bf16 v[180:183], a[180:183], v[56:59], v[180:183]\n"
"ds_read_b64_tr_b16 a[140:141], v9 offset:33280\n"
"buffer_load_dwordx4 v24, s[44:47], s52 offen offset:2048 lds\n"
"v_cvt_pk_bf16_f32 v62, v76, v77\n"
"v_mfma_f32_16x16x32_bf16 v[184:187], a[184:187], v[56:59], v[184:187]\n"
"buffer_load_dwordx4 v25, s[44:47], s52 offen offset:3072 lds\n"
"v_exp_f32 v79, v79\n"
"v_mfma_f32_16x16x32_bf16 v[188:191], a[188:191], v[56:59], v[188:191]\n"
"ds_read_b64_tr_b16 a[142:143], v9 offset:37376\n"
"buffer_load_dword v33, v26, s[44:47], s53 offen\n"
"v_mfma_f32_16x16x32_bf16 v[72:75], a[96:99], v[128:131], v[48:51]\n"
"v_exp_f32 v64, v64\n"
"v_mfma_f32_16x16x32_bf16 v[72:75], a[100:103], v[132:135], v[72:75]\n"
"ds_read_b64_tr_b16 a[144:145], v8 offset:33792\n"
"v_cvt_pk_bf16_f32 v63, v78, v79\n"
"v_mfma_f32_16x16x32_bf16 v[72:75], a[104:107], v[136:139], v[72:75]\n"
"v_exp_f32 v65, v65\n"
"v_mfma_f32_16x16x32_bf16 v[72:75], a[108:111], v[140:143], v[72:75]\n"
"ds_read_b64_tr_b16 a[146:147], v8 offset:37888\n"
"v_add_f32 v31, v31, v64\n"
"v_mfma_f32_16x16x32_bf16 v[192:195], a[160:163], v[60:63], v[192:195]\n"
"s_mov_b32 m0, 86016\n"
"s_mov_b32 s52, 819200\n"
"v_exp_f32 v66, v66\n"
"v_mfma_f32_16x16x32_bf16 v[196:199], a[164:167], v[60:63], v[196:199]\n"
"ds_read_b64_tr_b16 a[148:149], v9 offset:33792\n"
"s_waitcnt vmcnt(52)\n"
"v_cvt_pk_bf16_f32 v56, v64, v65\n"
"v_add_f32 v31, v31, v65\n"
"v_mfma_f32_16x16x32_bf16 v[200:203], a[168:171], v[60:63], v[200:203]\n"
"buffer_load_dwordx4 v22, s[44:47], s52 offen lds\n"
"v_exp_f32 v67, v67\n"
"v_mfma_f32_16x16x32_bf16 v[204:207], a[172:175], v[60:63], v[204:207]\n"
"ds_read_b64_tr_b16 a[150:151], v9 offset:37888\n"
"v_add_f32 v31, v31, v66\n"
"v_mfma_f32_16x16x32_bf16 v[18:21], a[224:227], v[60:63], v[18:21]\n"
"v_mfma_f32_16x16x32_bf16 v[76:79], a[112:115], v[128:131], v[48:51]\n"
"buffer_load_dwordx4 v23, s[44:47], s52 offen offset:1024 lds\n"
"v_exp_f32 v68, v68\n"
"v_mfma_f32_16x16x32_bf16 v[76:79], a[116:119], v[132:135], v[76:79]\n"
"ds_read_b64_tr_b16 a[152:153], v8 offset:34304\n"
"v_cvt_pk_bf16_f32 v57, v66, v67\n"
"v_add_f32 v31, v31, v67\n"
"v_mfma_f32_16x16x32_bf16 v[76:79], a[120:123], v[136:139], v[76:79]\n"
"buffer_load_dwordx4 v24, s[44:47], s52 offen offset:2048 lds\n"
"v_exp_f32 v69, v69\n"
"v_mfma_f32_16x16x32_bf16 v[76:79], a[124:127], v[140:143], v[76:79]\n"
"ds_read_b64_tr_b16 a[154:155], v8 offset:38400\n"
"v_add_f32 v31, v31, v68\n"
"v_mfma_f32_16x16x32_bf16 v[208:211], a[176:179], v[60:63], v[208:211]\n"
"buffer_load_dwordx4 v25, s[44:47], s52 offen offset:3072 lds\n"
"v_exp_f32 v70, v70\n"
"v_mfma_f32_16x16x32_bf16 v[212:215], a[180:183], v[60:63], v[212:215]\n"
"ds_read_b64_tr_b16 a[156:157], v9 offset:34304\n"
"v_cvt_pk_bf16_f32 v58, v68, v69\n"
"v_add_f32 v31, v31, v69\n"
"v_mfma_f32_16x16x32_bf16 v[216:219], a[184:187], v[60:63], v[216:219]\n"
"v_exp_f32 v71, v71\n"
"v_mfma_f32_16x16x32_bf16 v[220:223], a[188:191], v[60:63], v[220:223]\n"
"ds_read_b64_tr_b16 a[158:159], v9 offset:38400\n"
"v_add_f32 v31, v31, v70\n"
"v_mfma_f32_16x16x32_bf16 v[64:67], a[64:67], v[80:83], v[36:39]\n"
"v_exp_f32 v72, v72\n"
"v_mfma_f32_16x16x32_bf16 v[64:67], a[68:71], v[84:87], v[64:67]\n"
"v_cvt_pk_bf16_f32 v59, v70, v71\n"
"v_add_f32 v31, v31, v71\n"
"v_mfma_f32_16x16x32_bf16 v[64:67], a[72:75], v[88:91], v[64:67]\n"
"v_exp_f32 v73, v73\n"
"v_mfma_f32_16x16x32_bf16 v[64:67], a[76:79], v[92:95], v[64:67]\n"
"ds_read_b128 a[96:99], v4 offset:40960\n"
"v_add_f32 v34, v34, v72\n"
"v_mfma_f32_16x16x32_bf16 v[224:227], a[160:163], v[56:59], v[224:227]\n"
"s_mov_b32 m0, 24576\n"
"s_mov_b32 s52, 1310720\n"
"v_exp_f32 v74, v74\n"
"v_mfma_f32_16x16x32_bf16 v[228:231], a[164:167], v[56:59], v[228:231]\n"
"ds_read_b128 a[100:103], v5 offset:40960\n"
"s_waitcnt vmcnt(52)\n"
"v_cvt_pk_bf16_f32 v60, v72, v73\n"
"v_add_f32 v34, v34, v73\n"
"v_mfma_f32_16x16x32_bf16 v[232:235], a[168:171], v[56:59], v[232:235]\n"
"buffer_load_dwordx4 v22, s[44:47], s52 offen lds\n"
"v_exp_f32 v75, v75\n"
"v_mfma_f32_16x16x32_bf16 v[236:239], a[172:175], v[56:59], v[236:239]\n"
"ds_read_b128 a[104:107], v6 offset:40960\n"
"buffer_load_dwordx4 v23, s[44:47], s52 offen offset:1024 lds\n"
"v_add_f32 v34, v34, v74\n"
"v_mfma_f32_16x16x32_bf16 v[68:71], a[80:83], v[80:83], v[36:39]\n"
"buffer_load_dwordx4 v24, s[44:47], s52 offen offset:2048 lds\n"
"v_exp_f32 v76, v76\n"
"v_mfma_f32_16x16x32_bf16 v[68:71], a[84:87], v[84:87], v[68:71]\n"
"ds_read_b128 a[108:111], v7 offset:40960\n"
"buffer_load_dwordx4 v25, s[44:47], s52 offen offset:3072 lds\n"
"v_cvt_pk_bf16_f32 v61, v74, v75\n"
"v_add_f32 v34, v34, v75\n"
"v_mfma_f32_16x16x32_bf16 v[68:71], a[88:91], v[88:91], v[68:71]\n"
"s_mov_b32 m0, 28672\n"
"s_mov_b32 s52, 1835008\n"
"v_exp_f32 v77, v77\n"
"v_mfma_f32_16x16x32_bf16 v[68:71], a[92:95], v[92:95], v[68:71]\n"
"ds_read_b128 a[112:115], v4 offset:45056\n"
"buffer_load_dwordx4 v22, s[44:47], s52 offen lds\n"
"v_add_f32 v34, v34, v76\n"
"v_mfma_f32_16x16x32_bf16 v[240:243], a[176:179], v[56:59], v[240:243]\n"
"buffer_load_dwordx4 v23, s[44:47], s52 offen offset:1024 lds\n"
"v_exp_f32 v78, v78\n"
"v_mfma_f32_16x16x32_bf16 v[244:247], a[180:183], v[56:59], v[244:247]\n"
"ds_read_b128 a[116:119], v5 offset:45056\n"
"buffer_load_dwordx4 v24, s[44:47], s52 offen offset:2048 lds\n"
"v_cvt_pk_bf16_f32 v62, v76, v77\n"
"v_add_f32 v34, v34, v77\n"
"v_mfma_f32_16x16x32_bf16 v[248:251], a[184:187], v[56:59], v[248:251]\n"
"buffer_load_dwordx4 v25, s[44:47], s52 offen offset:3072 lds\n"
"v_exp_f32 v79, v79\n"
"v_mfma_f32_16x16x32_bf16 v[252:255], a[188:191], v[56:59], v[252:255]\n"
"v_add_f32 v34, v34, v78\n"
"v_mfma_f32_16x16x32_bf16 v[72:75], a[64:67], v[96:99], v[40:43]\n"
"v_exp_f32 v64, v64\n"
"v_mfma_f32_16x16x32_bf16 v[72:75], a[68:71], v[100:103], v[72:75]\n"
"v_cvt_pk_bf16_f32 v63, v78, v79\n"
"v_add_f32 v34, v34, v79\n"
"v_mfma_f32_16x16x32_bf16 v[72:75], a[72:75], v[104:107], v[72:75]\n"
"v_exp_f32 v65, v65\n"
"v_mfma_f32_16x16x32_bf16 v[72:75], a[76:79], v[108:111], v[72:75]\n"
"ds_read_b128 a[120:123], v6 offset:45056\n"
"v_add_f32 v29, v29, v64\n"
"v_mfma_f32_16x16x32_bf16 a[192:195], a[160:163], v[60:63], a[192:195]\n"
"s_mov_b32 m0, 90112\n"
"s_mov_b32 s52, 1343488\n"
"v_exp_f32 v66, v66\n"
"v_mfma_f32_16x16x32_bf16 a[196:199], a[164:167], v[60:63], a[196:199]\n"
"s_waitcnt vmcnt(52)\n"
"v_cvt_pk_bf16_f32 v56, v64, v65\n"
"v_add_f32 v29, v29, v65\n"
"v_mfma_f32_16x16x32_bf16 a[200:203], a[168:171], v[60:63], a[200:203]\n"
"buffer_load_dwordx4 v22, s[44:47], s52 offen lds\n"
"v_exp_f32 v67, v67\n"
"v_mfma_f32_16x16x32_bf16 a[204:207], a[172:175], v[60:63], a[204:207]\n"
"ds_read_b128 a[124:127], v7 offset:45056\n"
"v_add_f32 v29, v29, v66\n"
"v_mfma_f32_16x16x32_bf16 v[76:79], a[80:83], v[96:99], v[40:43]\n"
"buffer_load_dwordx4 v23, s[44:47], s52 offen offset:1024 lds\n"
"v_exp_f32 v68, v68\n"
"v_mfma_f32_16x16x32_bf16 v[76:79], a[84:87], v[100:103], v[76:79]\n"
"v_cvt_pk_bf16_f32 v57, v66, v67\n"
"v_add_f32 v29, v29, v67\n"
"v_mfma_f32_16x16x32_bf16 v[76:79], a[88:91], v[104:107], v[76:79]\n"
"buffer_load_dwordx4 v24, s[44:47], s52 offen offset:2048 lds\n"
"v_exp_f32 v69, v69\n"
"v_mfma_f32_16x16x32_bf16 v[76:79], a[92:95], v[108:111], v[76:79]\n"
"v_add_f32 v29, v29, v68\n"
"v_mfma_f32_16x16x32_bf16 a[208:211], a[176:179], v[60:63], a[208:211]\n"
"buffer_load_dwordx4 v25, s[44:47], s52 offen offset:3072 lds\n"
"v_exp_f32 v70, v70\n"
"v_mfma_f32_16x16x32_bf16 a[212:215], a[180:183], v[60:63], a[212:215]\n"
"v_cvt_pk_bf16_f32 v58, v68, v69\n"
"v_add_f32 v29, v29, v69\n"
"v_mfma_f32_16x16x32_bf16 a[216:219], a[184:187], v[60:63], a[216:219]\n"
"v_exp_f32 v71, v71\n"
"v_mfma_f32_16x16x32_bf16 a[220:223], a[188:191], v[60:63], a[220:223]\n"
"v_add_f32 v29, v29, v70\n"
"v_mfma_f32_16x16x32_bf16 v[64:67], a[64:67], v[112:115], v[44:47]\n"
"v_exp_f32 v72, v72\n"
"v_mfma_f32_16x16x32_bf16 v[64:67], a[68:71], v[116:119], v[64:67]\n"
"v_cvt_pk_bf16_f32 v59, v70, v71\n"
"v_add_f32 v29, v29, v71\n"
"v_mfma_f32_16x16x32_bf16 v[64:67], a[72:75], v[120:123], v[64:67]\n"
"v_exp_f32 v73, v73\n"
"v_mfma_f32_16x16x32_bf16 v[64:67], a[76:79], v[124:127], v[64:67]\n"
"s_waitcnt lgkmcnt(0)\n"
"s_barrier\n"
"v_mfma_f32_16x16x32_bf16 v[160:163], a[128:131], v[56:59], v[160:163]\n"
"s_mov_b32 m0, 94208\n"
"s_mov_b32 s52, 1867776\n"
"v_exp_f32 v74, v74\n"
"v_mfma_f32_16x16x32_bf16 v[164:167], a[132:135], v[56:59], v[164:167]\n"
"ds_read_b64_tr_b16 a[160:161], v8 offset:40960\n"
"s_waitcnt vmcnt(52)\n"
"v_cvt_pk_bf16_f32 v60, v72, v73\n"
"v_mfma_f32_16x16x32_bf16 v[168:171], a[136:139], v[56:59], v[168:171]\n"
"buffer_load_dwordx4 v22, s[44:47], s52 offen lds\n"
"v_exp_f32 v75, v75\n"
"v_mfma_f32_16x16x32_bf16 v[172:175], a[140:143], v[56:59], v[172:175]\n"
"ds_read_b64_tr_b16 a[162:163], v8 offset:45056\n"
"ds_read_b64_tr_b16 a[164:165], v9 offset:40960\n"
"buffer_load_dwordx4 v23, s[44:47], s52 offen offset:1024 lds\n"
"v_mfma_f32_16x16x32_bf16 v[68:71], a[80:83], v[112:115], v[44:47]\n"
"buffer_load_dwordx4 v24, s[44:47], s52 offen offset:2048 lds\n"
"v_exp_f32 v76, v76\n"
"v_mfma_f32_16x16x32_bf16 v[68:71], a[84:87], v[116:119], v[68:71]\n"
"ds_read_b64_tr_b16 a[166:167], v9 offset:45056\n"
"buffer_load_dwordx4 v25, s[44:47], s52 offen offset:3072 lds\n"
"v_cvt_pk_bf16_f32 v61, v74, v75\n"
"v_mfma_f32_16x16x32_bf16 v[68:71], a[88:91], v[120:123], v[68:71]\n"
"s_mov_b32 m0, 32768\n"
"s_mov_b32 s52, 327680\n"
"v_exp_f32 v77, v77\n"
"v_mfma_f32_16x16x32_bf16 v[68:71], a[92:95], v[124:127], v[68:71]\n"
"ds_read_b64_tr_b16 a[168:169], v8 offset:41472\n"
"ds_read_b64_tr_b16 a[170:171], v8 offset:45568\n"
"buffer_load_dwordx4 v22, s[44:47], s52 offen lds\n"
"v_mfma_f32_16x16x32_bf16 v[176:179], a[144:147], v[56:59], v[176:179]\n"
"buffer_load_dwordx4 v23, s[44:47], s52 offen offset:1024 lds\n"
"v_exp_f32 v78, v78\n"
"v_mfma_f32_16x16x32_bf16 v[180:183], a[148:151], v[56:59], v[180:183]\n"
"ds_read_b64_tr_b16 a[172:173], v9 offset:41472\n"
"buffer_load_dwordx4 v24, s[44:47], s52 offen offset:2048 lds\n"
"v_cvt_pk_bf16_f32 v62, v76, v77\n"
"v_mfma_f32_16x16x32_bf16 v[184:187], a[152:155], v[56:59], v[184:187]\n"
"buffer_load_dwordx4 v25, s[44:47], s52 offen offset:3072 lds\n"
"v_exp_f32 v79, v79\n"
"v_mfma_f32_16x16x32_bf16 v[188:191], a[156:159], v[56:59], v[188:191]\n"
"ds_read_b64_tr_b16 a[174:175], v9 offset:45568\n"
"v_mfma_f32_16x16x32_bf16 v[72:75], a[64:67], v[128:131], v[48:51]\n"
"v_exp_f32 v64, v64\n"
"v_mfma_f32_16x16x32_bf16 v[72:75], a[68:71], v[132:135], v[72:75]\n"
"ds_read_b64_tr_b16 a[176:177], v8 offset:41984\n"
"v_cvt_pk_bf16_f32 v63, v78, v79\n"
"v_mfma_f32_16x16x32_bf16 v[72:75], a[72:75], v[136:139], v[72:75]\n"
"v_exp_f32 v65, v65\n"
"v_mfma_f32_16x16x32_bf16 v[72:75], a[76:79], v[140:143], v[72:75]\n"
"ds_read_b64_tr_b16 a[178:179], v8 offset:46080\n"
"v_add_f32 v31, v31, v64\n"
"v_mfma_f32_16x16x32_bf16 v[192:195], a[128:131], v[60:63], v[192:195]\n"
"s_mov_b32 m0, 36864\n"
"s_mov_b32 s52, 851968\n"
"v_exp_f32 v66, v66\n"
"v_mfma_f32_16x16x32_bf16 v[196:199], a[132:135], v[60:63], v[196:199]\n"
"ds_read_b64_tr_b16 a[180:181], v9 offset:41984\n"
"s_waitcnt vmcnt(52)\n"
"v_cvt_pk_bf16_f32 v56, v64, v65\n"
"v_add_f32 v31, v31, v65\n"
"v_mfma_f32_16x16x32_bf16 v[200:203], a[136:139], v[60:63], v[200:203]\n"
"buffer_load_dwordx4 v22, s[44:47], s52 offen lds\n"
"v_exp_f32 v67, v67\n"
"v_mfma_f32_16x16x32_bf16 v[204:207], a[140:143], v[60:63], v[204:207]\n"
"ds_read_b64_tr_b16 a[182:183], v9 offset:46080\n"
"v_add_f32 v31, v31, v66\n"
"v_mfma_f32_16x16x32_bf16 v[18:21], a[224:227], v[60:63], v[18:21]\n"
"v_mfma_f32_16x16x32_bf16 v[76:79], a[80:83], v[128:131], v[48:51]\n"
"buffer_load_dwordx4 v23, s[44:47], s52 offen offset:1024 lds\n"
"v_exp_f32 v68, v68\n"
"v_mfma_f32_16x16x32_bf16 v[76:79], a[84:87], v[132:135], v[76:79]\n"
"ds_read_b64_tr_b16 a[184:185], v8 offset:42496\n"
"v_cvt_pk_bf16_f32 v57, v66, v67\n"
"v_add_f32 v31, v31, v67\n"
"v_mfma_f32_16x16x32_bf16 v[76:79], a[88:91], v[136:139], v[76:79]\n"
"buffer_load_dwordx4 v24, s[44:47], s52 offen offset:2048 lds\n"
"v_exp_f32 v69, v69\n"
"v_mfma_f32_16x16x32_bf16 v[76:79], a[92:95], v[140:143], v[76:79]\n"
"ds_read_b64_tr_b16 a[186:187], v8 offset:46592\n"
"v_add_f32 v31, v31, v68\n"
"v_mfma_f32_16x16x32_bf16 v[208:211], a[144:147], v[60:63], v[208:211]\n"
"buffer_load_dwordx4 v25, s[44:47], s52 offen offset:3072 lds\n"
"v_exp_f32 v70, v70\n"
"v_mfma_f32_16x16x32_bf16 v[212:215], a[148:151], v[60:63], v[212:215]\n"
"ds_read_b64_tr_b16 a[188:189], v9 offset:42496\n"
"v_cvt_pk_bf16_f32 v58, v68, v69\n"
"v_add_f32 v31, v31, v69\n"
"v_mfma_f32_16x16x32_bf16 v[216:219], a[152:155], v[60:63], v[216:219]\n"
"v_exp_f32 v71, v71\n"
"v_mfma_f32_16x16x32_bf16 v[220:223], a[156:159], v[60:63], v[220:223]\n"
"ds_read_b64_tr_b16 a[190:191], v9 offset:46592\n"
"v_add_f32 v31, v31, v70\n"
"v_mfma_f32_16x16x32_bf16 v[64:67], a[96:99], v[80:83], v[36:39]\n"
"v_exp_f32 v72, v72\n"
"v_mfma_f32_16x16x32_bf16 v[64:67], a[100:103], v[84:87], v[64:67]\n"
"v_cvt_pk_bf16_f32 v59, v70, v71\n"
"v_add_f32 v31, v31, v71\n"
"v_mfma_f32_16x16x32_bf16 v[64:67], a[104:107], v[88:91], v[64:67]\n"
"v_exp_f32 v73, v73\n"
"v_mfma_f32_16x16x32_bf16 v[64:67], a[108:111], v[92:95], v[64:67]\n"
"ds_read_b128 a[64:67], v4 offset:49152\n"
"v_add_f32 v34, v34, v72\n"
"v_mfma_f32_16x16x32_bf16 v[224:227], a[128:131], v[56:59], v[224:227]\n"
"s_mov_b32 m0, 98304\n"
"s_mov_b32 s52, 360448\n"
"v_exp_f32 v74, v74\n"
"v_mfma_f32_16x16x32_bf16 v[228:231], a[132:135], v[56:59], v[228:231]\n"
"ds_read_b128 a[68:71], v5 offset:49152\n"
"s_waitcnt vmcnt(52)\n"
"v_cvt_pk_bf16_f32 v60, v72, v73\n"
"v_add_f32 v34, v34, v73\n"
"v_mfma_f32_16x16x32_bf16 v[232:235], a[136:139], v[56:59], v[232:235]\n"
"buffer_load_dwordx4 v22, s[44:47], s52 offen lds\n"
"v_exp_f32 v75, v75\n"
"v_mfma_f32_16x16x32_bf16 v[236:239], a[140:143], v[56:59], v[236:239]\n"
"ds_read_b128 a[72:75], v6 offset:49152\n"
"buffer_load_dwordx4 v23, s[44:47], s52 offen offset:1024 lds\n"
"v_add_f32 v34, v34, v74\n"
"v_mfma_f32_16x16x32_bf16 v[68:71], a[112:115], v[80:83], v[36:39]\n"
"buffer_load_dwordx4 v24, s[44:47], s52 offen offset:2048 lds\n"
"v_exp_f32 v76, v76\n"
"v_mfma_f32_16x16x32_bf16 v[68:71], a[116:119], v[84:87], v[68:71]\n"
"ds_read_b128 a[76:79], v7 offset:49152\n"
"buffer_load_dwordx4 v25, s[44:47], s52 offen offset:3072 lds\n"
"v_cvt_pk_bf16_f32 v61, v74, v75\n"
"v_add_f32 v34, v34, v75\n"
"v_mfma_f32_16x16x32_bf16 v[68:71], a[120:123], v[88:91], v[68:71]\n"
"s_mov_b32 m0, 102400\n"
"s_mov_b32 s52, 884736\n"
"v_exp_f32 v77, v77\n"
"v_mfma_f32_16x16x32_bf16 v[68:71], a[124:127], v[92:95], v[68:71]\n"
"ds_read_b128 a[80:83], v4 offset:53248\n"
"buffer_load_dwordx4 v22, s[44:47], s52 offen lds\n"
"v_add_f32 v34, v34, v76\n"
"v_mfma_f32_16x16x32_bf16 v[240:243], a[144:147], v[56:59], v[240:243]\n"
"buffer_load_dwordx4 v23, s[44:47], s52 offen offset:1024 lds\n"
"v_exp_f32 v78, v78\n"
"v_mfma_f32_16x16x32_bf16 v[244:247], a[148:151], v[56:59], v[244:247]\n"
"ds_read_b128 a[84:87], v5 offset:53248\n"
"buffer_load_dwordx4 v24, s[44:47], s52 offen offset:2048 lds\n"
"v_cvt_pk_bf16_f32 v62, v76, v77\n"
"v_add_f32 v34, v34, v77\n"
"v_mfma_f32_16x16x32_bf16 v[248:251], a[152:155], v[56:59], v[248:251]\n"
"buffer_load_dwordx4 v25, s[44:47], s52 offen offset:3072 lds\n"
"v_exp_f32 v79, v79\n"
"v_mfma_f32_16x16x32_bf16 v[252:255], a[156:159], v[56:59], v[252:255]\n"
"v_add_f32 v34, v34, v78\n"
"v_mfma_f32_16x16x32_bf16 v[72:75], a[96:99], v[96:99], v[40:43]\n"
"v_exp_f32 v64, v64\n"
"v_mfma_f32_16x16x32_bf16 v[72:75], a[100:103], v[100:103], v[72:75]\n"
"v_cvt_pk_bf16_f32 v63, v78, v79\n"
"v_add_f32 v34, v34, v79\n"
"v_mfma_f32_16x16x32_bf16 v[72:75], a[104:107], v[104:107], v[72:75]\n"
"v_exp_f32 v65, v65\n"
"v_mfma_f32_16x16x32_bf16 v[72:75], a[108:111], v[108:111], v[72:75]\n"
"ds_read_b128 a[88:91], v6 offset:53248\n"
"v_add_f32 v29, v29, v64\n"
"v_mfma_f32_16x16x32_bf16 a[192:195], a[128:131], v[60:63], a[192:195]\n"
"s_mov_b32 m0, 40960\n"
"s_mov_b32 s52, 1376256\n"
"v_exp_f32 v66, v66\n"
"v_mfma_f32_16x16x32_bf16 a[196:199], a[132:135], v[60:63], a[196:199]\n"
"s_waitcnt vmcnt(52)\n"
"v_cvt_pk_bf16_f32 v56, v64, v65\n"
"v_add_f32 v29, v29, v65\n"
"v_mfma_f32_16x16x32_bf16 a[200:203], a[136:139], v[60:63], a[200:203]\n"
"buffer_load_dwordx4 v22, s[44:47], s52 offen lds\n"
"v_exp_f32 v67, v67\n"
"v_mfma_f32_16x16x32_bf16 a[204:207], a[140:143], v[60:63], a[204:207]\n"
"ds_read_b128 a[92:95], v7 offset:53248\n"
"v_add_f32 v29, v29, v66\n"
"v_mfma_f32_16x16x32_bf16 v[76:79], a[112:115], v[96:99], v[40:43]\n"
"buffer_load_dwordx4 v23, s[44:47], s52 offen offset:1024 lds\n"
"v_exp_f32 v68, v68\n"
"v_mfma_f32_16x16x32_bf16 v[76:79], a[116:119], v[100:103], v[76:79]\n"
"v_cvt_pk_bf16_f32 v57, v66, v67\n"
"v_add_f32 v29, v29, v67\n"
"v_mfma_f32_16x16x32_bf16 v[76:79], a[120:123], v[104:107], v[76:79]\n"
"buffer_load_dwordx4 v24, s[44:47], s52 offen offset:2048 lds\n"
"v_exp_f32 v69, v69\n"
"v_mfma_f32_16x16x32_bf16 v[76:79], a[124:127], v[108:111], v[76:79]\n"
"v_add_f32 v29, v29, v68\n"
"v_mfma_f32_16x16x32_bf16 a[208:211], a[144:147], v[60:63], a[208:211]\n"
"buffer_load_dwordx4 v25, s[44:47], s52 offen offset:3072 lds\n"
"v_exp_f32 v70, v70\n"
"v_mfma_f32_16x16x32_bf16 a[212:215], a[148:151], v[60:63], a[212:215]\n"
"v_cvt_pk_bf16_f32 v58, v68, v69\n"
"v_add_f32 v29, v29, v69\n"
"v_mfma_f32_16x16x32_bf16 a[216:219], a[152:155], v[60:63], a[216:219]\n"
"v_exp_f32 v71, v71\n"
"v_mfma_f32_16x16x32_bf16 a[220:223], a[156:159], v[60:63], a[220:223]\n"
"v_add_f32 v29, v29, v70\n"
"v_mfma_f32_16x16x32_bf16 v[64:67], a[96:99], v[112:115], v[44:47]\n"
"v_exp_f32 v72, v72\n"
"v_mfma_f32_16x16x32_bf16 v[64:67], a[100:103], v[116:119], v[64:67]\n"
"v_cvt_pk_bf16_f32 v59, v70, v71\n"
"v_add_f32 v29, v29, v71\n"
"v_mfma_f32_16x16x32_bf16 v[64:67], a[104:107], v[120:123], v[64:67]\n"
"v_exp_f32 v73, v73\n"
"v_mfma_f32_16x16x32_bf16 v[64:67], a[108:111], v[124:127], v[64:67]\n"
"s_waitcnt lgkmcnt(0)\n"
"s_barrier\n"
"v_mfma_f32_16x16x32_bf16 v[160:163], a[160:163], v[56:59], v[160:163]\n"
"s_mov_b32 m0, 45056\n"
"s_mov_b32 s52, 1900544\n"
"v_exp_f32 v74, v74\n"
"v_mfma_f32_16x16x32_bf16 v[164:167], a[164:167], v[56:59], v[164:167]\n"
"ds_read_b64_tr_b16 a[128:129], v8 offset:49152\n"
"s_waitcnt vmcnt(52)\n"
"v_cvt_pk_bf16_f32 v60, v72, v73\n"
"v_mfma_f32_16x16x32_bf16 v[168:171], a[168:171], v[56:59], v[168:171]\n"
"buffer_load_dwordx4 v22, s[44:47], s52 offen lds\n"
"v_exp_f32 v75, v75\n"
"v_mfma_f32_16x16x32_bf16 v[172:175], a[172:175], v[56:59], v[172:175]\n"
"ds_read_b64_tr_b16 a[130:131], v8 offset:53248\n"
"ds_read_b64_tr_b16 a[132:133], v9 offset:49152\n"
"buffer_load_dwordx4 v23, s[44:47], s52 offen offset:1024 lds\n"
"v_mfma_f32_16x16x32_bf16 v[68:71], a[112:115], v[112:115], v[44:47]\n"
"buffer_load_dwordx4 v24, s[44:47], s52 offen offset:2048 lds\n"
"v_exp_f32 v76, v76\n"
"v_mfma_f32_16x16x32_bf16 v[68:71], a[116:119], v[116:119], v[68:71]\n"
"ds_read_b64_tr_b16 a[134:135], v9 offset:53248\n"
"buffer_load_dwordx4 v25, s[44:47], s52 offen offset:3072 lds\n"
"v_cvt_pk_bf16_f32 v61, v74, v75\n"
"v_mfma_f32_16x16x32_bf16 v[68:71], a[120:123], v[120:123], v[68:71]\n"
"s_mov_b32 m0, 106496\n"
"s_mov_b32 s52, 1409024\n"
"v_exp_f32 v77, v77\n"
"v_mfma_f32_16x16x32_bf16 v[68:71], a[124:127], v[124:127], v[68:71]\n"
"ds_read_b64_tr_b16 a[136:137], v8 offset:49664\n"
"ds_read_b64_tr_b16 a[138:139], v8 offset:53760\n"
"buffer_load_dwordx4 v22, s[44:47], s52 offen lds\n"
"v_mfma_f32_16x16x32_bf16 v[176:179], a[176:179], v[56:59], v[176:179]\n"
"buffer_load_dwordx4 v23, s[44:47], s52 offen offset:1024 lds\n"
"v_exp_f32 v78, v78\n"
"v_mfma_f32_16x16x32_bf16 v[180:183], a[180:183], v[56:59], v[180:183]\n"
"ds_read_b64_tr_b16 a[140:141], v9 offset:49664\n"
"buffer_load_dwordx4 v24, s[44:47], s52 offen offset:2048 lds\n"
"v_cvt_pk_bf16_f32 v62, v76, v77\n"
"v_mfma_f32_16x16x32_bf16 v[184:187], a[184:187], v[56:59], v[184:187]\n"
"buffer_load_dwordx4 v25, s[44:47], s52 offen offset:3072 lds\n"
"v_exp_f32 v79, v79\n"
"v_mfma_f32_16x16x32_bf16 v[188:191], a[188:191], v[56:59], v[188:191]\n"
"ds_read_b64_tr_b16 a[142:143], v9 offset:53760\n"
"v_mfma_f32_16x16x32_bf16 v[72:75], a[96:99], v[128:131], v[48:51]\n"
"v_exp_f32 v64, v64\n"
"v_mfma_f32_16x16x32_bf16 v[72:75], a[100:103], v[132:135], v[72:75]\n"
"ds_read_b64_tr_b16 a[144:145], v8 offset:50176\n"
"v_cvt_pk_bf16_f32 v63, v78, v79\n"
"v_mfma_f32_16x16x32_bf16 v[72:75], a[104:107], v[136:139], v[72:75]\n"
"v_exp_f32 v65, v65\n"
"v_mfma_f32_16x16x32_bf16 v[72:75], a[108:111], v[140:143], v[72:75]\n"
"ds_read_b64_tr_b16 a[146:147], v8 offset:54272\n"
"v_add_f32 v31, v31, v64\n"
"v_mfma_f32_16x16x32_bf16 v[192:195], a[160:163], v[60:63], v[192:195]\n"
"s_mov_b32 m0, 110592\n"
"s_mov_b32 s52, 1933312\n"
"v_exp_f32 v66, v66\n"
"v_mfma_f32_16x16x32_bf16 v[196:199], a[164:167], v[60:63], v[196:199]\n"
"ds_read_b64_tr_b16 a[148:149], v9 offset:50176\n"
"s_waitcnt vmcnt(52)\n"
"v_cvt_pk_bf16_f32 v56, v64, v65\n"
"v_add_f32 v31, v31, v65\n"
"v_mfma_f32_16x16x32_bf16 v[200:203], a[168:171], v[60:63], v[200:203]\n"
"buffer_load_dwordx4 v22, s[44:47], s52 offen lds\n"
"v_exp_f32 v67, v67\n"
"v_mfma_f32_16x16x32_bf16 v[204:207], a[172:175], v[60:63], v[204:207]\n"
"ds_read_b64_tr_b16 a[150:151], v9 offset:54272\n"
"v_add_f32 v31, v31, v66\n"
"v_mfma_f32_16x16x32_bf16 v[18:21], a[224:227], v[60:63], v[18:21]\n"
"v_mfma_f32_16x16x32_bf16 v[76:79], a[112:115], v[128:131], v[48:51]\n"
"buffer_load_dwordx4 v23, s[44:47], s52 offen offset:1024 lds\n"
"v_exp_f32 v68, v68\n"
"v_mfma_f32_16x16x32_bf16 v[76:79], a[116:119], v[132:135], v[76:79]\n"
"ds_read_b64_tr_b16 a[152:153], v8 offset:50688\n"
"v_cvt_pk_bf16_f32 v57, v66, v67\n"
"v_add_f32 v31, v31, v67\n"
"v_mfma_f32_16x16x32_bf16 v[76:79], a[120:123], v[136:139], v[76:79]\n"
"buffer_load_dwordx4 v24, s[44:47], s52 offen offset:2048 lds\n"
"v_exp_f32 v69, v69\n"
"v_mfma_f32_16x16x32_bf16 v[76:79], a[124:127], v[140:143], v[76:79]\n"
"ds_read_b64_tr_b16 a[154:155], v8 offset:54784\n"
"v_add_f32 v31, v31, v68\n"
"v_mfma_f32_16x16x32_bf16 v[208:211], a[176:179], v[60:63], v[208:211]\n"
"buffer_load_dwordx4 v25, s[44:47], s52 offen offset:3072 lds\n"
"v_exp_f32 v70, v70\n"
"v_mfma_f32_16x16x32_bf16 v[212:215], a[180:183], v[60:63], v[212:215]\n"
"ds_read_b64_tr_b16 a[156:157], v9 offset:50688\n"
"v_cvt_pk_bf16_f32 v58, v68, v69\n"
"v_add_f32 v31, v31, v69\n"
"v_mfma_f32_16x16x32_bf16 v[216:219], a[184:187], v[60:63], v[216:219]\n"
"v_exp_f32 v71, v71\n"
"v_mfma_f32_16x16x32_bf16 v[220:223], a[188:191], v[60:63], v[220:223]\n"
"ds_read_b64_tr_b16 a[158:159], v9 offset:54784\n"
"v_add_f32 v31, v31, v70\n"
"v_mfma_f32_16x16x32_bf16 v[64:67], a[64:67], v[80:83], v[36:39]\n"
"v_exp_f32 v72, v72\n"
"v_mfma_f32_16x16x32_bf16 v[64:67], a[68:71], v[84:87], v[64:67]\n"
"ds_read_b128 a[96:99], v4 offset:57344\n"
"v_cvt_pk_bf16_f32 v59, v70, v71\n"
"v_add_f32 v31, v31, v71\n"
"v_mfma_f32_16x16x32_bf16 v[64:67], a[72:75], v[88:91], v[64:67]\n"
"v_exp_f32 v73, v73\n"
"v_mfma_f32_16x16x32_bf16 v[64:67], a[76:79], v[92:95], v[64:67]\n"
"ds_read_b128 a[100:103], v5 offset:57344\n"
"v_add_f32 v34, v34, v72\n"
"v_mfma_f32_16x16x32_bf16 v[224:227], a[160:163], v[56:59], v[224:227]\n"
"s_mov_b32 m0, 49152\n"
"s_mov_b32 s52, 393216\n"
"v_exp_f32 v74, v74\n"
"v_mfma_f32_16x16x32_bf16 v[228:231], a[164:167], v[56:59], v[228:231]\n"
"ds_read_b128 a[104:107], v6 offset:57344\n"
"v_cvt_pk_bf16_f32 v60, v72, v73\n"
"v_add_f32 v34, v34, v73\n"
"v_mfma_f32_16x16x32_bf16 v[232:235], a[168:171], v[56:59], v[232:235]\n"
"buffer_load_dwordx4 v22, s[44:47], s52 offen lds\n"
"v_exp_f32 v75, v75\n"
"v_mfma_f32_16x16x32_bf16 v[236:239], a[172:175], v[56:59], v[236:239]\n"
"ds_read_b128 a[108:111], v7 offset:57344\n"
"v_add_f32 v34, v34, v74\n"
"v_mfma_f32_16x16x32_bf16 v[68:71], a[80:83], v[80:83], v[36:39]\n"
"buffer_load_dwordx4 v23, s[44:47], s52 offen offset:1024 lds\n"
"v_exp_f32 v76, v76\n"
"v_mfma_f32_16x16x32_bf16 v[68:71], a[84:87], v[84:87], v[68:71]\n"
"ds_read_b128 a[112:115], v4 offset:61440\n"
"v_cvt_pk_bf16_f32 v61, v74, v75\n"
"v_add_f32 v34, v34, v75\n"
"v_mfma_f32_16x16x32_bf16 v[68:71], a[88:91], v[88:91], v[68:71]\n"
"buffer_load_dwordx4 v24, s[44:47], s52 offen offset:2048 lds\n"
"v_exp_f32 v77, v77\n"
"v_mfma_f32_16x16x32_bf16 v[68:71], a[92:95], v[92:95], v[68:71]\n"
"ds_read_b128 a[116:119], v5 offset:61440\n"
"v_add_f32 v34, v34, v76\n"
"v_mfma_f32_16x16x32_bf16 v[240:243], a[176:179], v[56:59], v[240:243]\n"
"buffer_load_dwordx4 v25, s[44:47], s52 offen offset:3072 lds\n"
"v_exp_f32 v78, v78\n"
"v_mfma_f32_16x16x32_bf16 v[244:247], a[180:183], v[56:59], v[244:247]\n"
"ds_read_b128 a[120:123], v6 offset:61440\n"
"v_cvt_pk_bf16_f32 v62, v76, v77\n"
"v_add_f32 v34, v34, v77\n"
"v_mfma_f32_16x16x32_bf16 v[248:251], a[184:187], v[56:59], v[248:251]\n"
"v_exp_f32 v79, v79\n"
"v_mfma_f32_16x16x32_bf16 v[252:255], a[188:191], v[56:59], v[252:255]\n"
"ds_read_b128 a[124:127], v7 offset:61440\n"
"v_add_f32 v34, v34, v78\n"
"s_waitcnt lgkmcnt(4)\n"
"v_mfma_f32_16x16x32_bf16 v[72:75], a[96:99], v[80:83], v[36:39]\n"
"v_exp_f32 v64, v64\n"
"v_mfma_f32_16x16x32_bf16 v[72:75], a[100:103], v[84:87], v[72:75]\n"
"v_cvt_pk_bf16_f32 v63, v78, v79\n"
"v_add_f32 v34, v34, v79\n"
"v_mfma_f32_16x16x32_bf16 v[72:75], a[104:107], v[88:91], v[72:75]\n"
"v_exp_f32 v65, v65\n"
"v_mfma_f32_16x16x32_bf16 v[72:75], a[108:111], v[92:95], v[72:75]\n"
"v_add_f32 v29, v29, v64\n"
"v_mfma_f32_16x16x32_bf16 a[192:195], a[160:163], v[60:63], a[192:195]\n"
"s_mov_b32 m0, 53248\n"
"s_mov_b32 s52, 917504\n"
"v_exp_f32 v66, v66\n"
"v_mfma_f32_16x16x32_bf16 a[196:199], a[164:167], v[60:63], a[196:199]\n"
"s_waitcnt vmcnt(52)\n"
"v_cvt_pk_bf16_f32 v56, v64, v65\n"
"v_add_f32 v29, v29, v65\n"
"v_mfma_f32_16x16x32_bf16 a[200:203], a[168:171], v[60:63], a[200:203]\n"
"buffer_load_dwordx4 v22, s[44:47], s52 offen lds\n"
"v_exp_f32 v67, v67\n"
"v_mfma_f32_16x16x32_bf16 a[204:207], a[172:175], v[60:63], a[204:207]\n"
"s_waitcnt lgkmcnt(0)\n"
"s_barrier\n"
"v_add_f32 v29, v29, v66\n"
"v_mfma_f32_16x16x32_bf16 v[76:79], a[112:115], v[80:83], v[36:39]\n"
"buffer_load_dwordx4 v23, s[44:47], s52 offen offset:1024 lds\n"
"v_exp_f32 v68, v68\n"
"v_mfma_f32_16x16x32_bf16 v[76:79], a[116:119], v[84:87], v[76:79]\n"
"v_cvt_pk_bf16_f32 v57, v66, v67\n"
"v_add_f32 v29, v29, v67\n"
"v_mfma_f32_16x16x32_bf16 v[76:79], a[120:123], v[88:91], v[76:79]\n"
"buffer_load_dwordx4 v24, s[44:47], s52 offen offset:2048 lds\n"
"v_exp_f32 v69, v69\n"
"v_mfma_f32_16x16x32_bf16 v[76:79], a[124:127], v[92:95], v[76:79]\n"
"ds_read_b64_tr_b16 a[160:161], v8 offset:57344\n"
"ds_read_b64_tr_b16 a[162:163], v8 offset:61440\n"
"v_add_f32 v29, v29, v68\n"
"v_mfma_f32_16x16x32_bf16 a[208:211], a[176:179], v[60:63], a[208:211]\n"
"buffer_load_dwordx4 v25, s[44:47], s52 offen offset:3072 lds\n"
"v_exp_f32 v70, v70\n"
"v_mfma_f32_16x16x32_bf16 a[212:215], a[180:183], v[60:63], a[212:215]\n"
"v_cvt_pk_bf16_f32 v58, v68, v69\n"
"v_add_f32 v29, v29, v69\n"
"v_mfma_f32_16x16x32_bf16 a[216:219], a[184:187], v[60:63], a[216:219]\n"
"v_exp_f32 v71, v71\n"
"v_mfma_f32_16x16x32_bf16 a[220:223], a[188:191], v[60:63], a[220:223]\n"
"ds_read_b64_tr_b16 a[164:165], v9 offset:57344\n"
"ds_read_b64_tr_b16 a[166:167], v9 offset:61440\n"
"v_add_f32 v29, v29, v70\n"
"v_mfma_f32_16x16x32_bf16 v[64:67], a[64:67], v[96:99], v[40:43]\n"
"v_exp_f32 v72, v72\n"
"v_mfma_f32_16x16x32_bf16 v[64:67], a[68:71], v[100:103], v[64:67]\n"
"v_cvt_pk_bf16_f32 v59, v70, v71\n"
"v_add_f32 v29, v29, v71\n"
"v_mfma_f32_16x16x32_bf16 v[64:67], a[72:75], v[104:107], v[64:67]\n"
"v_exp_f32 v73, v73\n"
"v_mfma_f32_16x16x32_bf16 v[64:67], a[76:79], v[108:111], v[64:67]\n"
"ds_read_b64_tr_b16 a[168:169], v8 offset:57856\n"
"ds_read_b64_tr_b16 a[170:171], v8 offset:61952\n"
"v_add_f32 v29, v29, v72\n"
"v_mfma_f32_16x16x32_bf16 v[160:163], a[128:131], v[56:59], v[160:163]\n"
"s_mov_b32 m0, 57344\n"
"s_mov_b32 s52, 1441792\n"
"v_exp_f32 v74, v74\n"
"v_mfma_f32_16x16x32_bf16 v[164:167], a[132:135], v[56:59], v[164:167]\n"
"s_waitcnt vmcnt(52)\n"
"v_cvt_pk_bf16_f32 v60, v72, v73\n"
"v_add_f32 v29, v29, v73\n"
"v_mfma_f32_16x16x32_bf16 v[168:171], a[136:139], v[56:59], v[168:171]\n"
"buffer_load_dwordx4 v22, s[44:47], s52 offen lds\n"
"v_exp_f32 v75, v75\n"
"v_mfma_f32_16x16x32_bf16 v[172:175], a[140:143], v[56:59], v[172:175]\n"
"ds_read_b64_tr_b16 a[172:173], v9 offset:57856\n"
"ds_read_b64_tr_b16 a[174:175], v9 offset:61952\n"
"buffer_load_dwordx4 v23, s[44:47], s52 offen offset:1024 lds\n"
"v_add_f32 v29, v29, v74\n"
"v_mfma_f32_16x16x32_bf16 v[68:71], a[80:83], v[96:99], v[40:43]\n"
"buffer_load_dwordx4 v24, s[44:47], s52 offen offset:2048 lds\n"
"v_exp_f32 v76, v76\n"
"v_mfma_f32_16x16x32_bf16 v[68:71], a[84:87], v[100:103], v[68:71]\n"
"buffer_load_dwordx4 v25, s[44:47], s52 offen offset:3072 lds\n"
"v_cvt_pk_bf16_f32 v61, v74, v75\n"
"v_add_f32 v29, v29, v75\n"
"v_mfma_f32_16x16x32_bf16 v[68:71], a[88:91], v[104:107], v[68:71]\n"
"s_mov_b32 m0, 61440\n"
"s_mov_b32 s52, 1966080\n"
"v_exp_f32 v77, v77\n"
"v_mfma_f32_16x16x32_bf16 v[68:71], a[92:95], v[108:111], v[68:71]\n"
"ds_read_b64_tr_b16 a[176:177], v8 offset:58368\n"
"ds_read_b64_tr_b16 a[178:179], v8 offset:62464\n"
"buffer_load_dwordx4 v22, s[44:47], s52 offen lds\n"
"v_add_f32 v29, v29, v76\n"
"v_mfma_f32_16x16x32_bf16 v[176:179], a[144:147], v[56:59], v[176:179]\n"
"buffer_load_dwordx4 v23, s[44:47], s52 offen offset:1024 lds\n"
"v_exp_f32 v78, v78\n"
"v_mfma_f32_16x16x32_bf16 v[180:183], a[148:151], v[56:59], v[180:183]\n"
"buffer_load_dwordx4 v24, s[44:47], s52 offen offset:2048 lds\n"
"v_cvt_pk_bf16_f32 v62, v76, v77\n"
"v_add_f32 v29, v29, v77\n"
"v_mfma_f32_16x16x32_bf16 v[184:187], a[152:155], v[56:59], v[184:187]\n"
"buffer_load_dwordx4 v25, s[44:47], s52 offen offset:3072 lds\n"
"v_exp_f32 v79, v79\n"
"v_mfma_f32_16x16x32_bf16 v[188:191], a[156:159], v[56:59], v[188:191]\n"
"ds_read_b64_tr_b16 a[180:181], v9 offset:58368\n"
"ds_read_b64_tr_b16 a[182:183], v9 offset:62464\n"
"v_add_f32 v29, v29, v78\n"
"v_mfma_f32_16x16x32_bf16 v[72:75], a[96:99], v[96:99], v[40:43]\n"
"v_exp_f32 v64, v64\n"
"v_mfma_f32_16x16x32_bf16 v[72:75], a[100:103], v[100:103], v[72:75]\n"
"v_cvt_pk_bf16_f32 v63, v78, v79\n"
"v_add_f32 v29, v29, v79\n"
"v_mfma_f32_16x16x32_bf16 v[72:75], a[104:107], v[104:107], v[72:75]\n"
"v_exp_f32 v65, v65\n"
"v_mfma_f32_16x16x32_bf16 v[72:75], a[108:111], v[108:111], v[72:75]\n"
"ds_read_b64_tr_b16 a[184:185], v8 offset:58880\n"
"s_waitcnt lgkmcnt(5)\n"
"v_mfma_f32_16x16x32_bf16 v[160:163], a[160:163], v[60:63], v[160:163]\n"
"v_exp_f32 v66, v66\n"
"v_mfma_f32_16x16x32_bf16 v[164:167], a[164:167], v[60:63], v[164:167]\n"
"ds_read_b64_tr_b16 a[186:187], v8 offset:62976\n"
"v_cvt_pk_bf16_f32 v56, v64, v65\n"
"v_mfma_f32_16x16x32_bf16 v[168:171], a[168:171], v[60:63], v[168:171]\n"
"v_exp_f32 v67, v67\n"
"v_mfma_f32_16x16x32_bf16 v[172:175], a[172:175], v[60:63], v[172:175]\n"
"ds_read_b64_tr_b16 a[188:189], v9 offset:58880\n"
"ds_read_b64_tr_b16 a[190:191], v9 offset:62976\n"
"v_mfma_f32_16x16x32_bf16 v[76:79], a[112:115], v[96:99], v[40:43]\n"
"v_exp_f32 v68, v68\n"
"v_mfma_f32_16x16x32_bf16 v[76:79], a[116:119], v[100:103], v[76:79]\n"
"v_cvt_pk_bf16_f32 v57, v66, v67\n"
"v_mfma_f32_16x16x32_bf16 v[76:79], a[120:123], v[104:107], v[76:79]\n"
"v_exp_f32 v69, v69\n"
"v_mfma_f32_16x16x32_bf16 v[76:79], a[124:127], v[108:111], v[76:79]\n"
"s_waitcnt lgkmcnt(0)\n"
"v_mfma_f32_16x16x32_bf16 v[176:179], a[176:179], v[60:63], v[176:179]\n"
"v_exp_f32 v70, v70\n"
"v_mfma_f32_16x16x32_bf16 v[180:183], a[180:183], v[60:63], v[180:183]\n"
"v_cvt_pk_bf16_f32 v58, v68, v69\n"
"v_mfma_f32_16x16x32_bf16 v[184:187], a[184:187], v[60:63], v[184:187]\n"
"v_exp_f32 v71, v71\n"
"v_mfma_f32_16x16x32_bf16 v[188:191], a[188:191], v[60:63], v[188:191]\n"
"v_lshlrev_b32 v1, 4, v0\n"
"s_mov_b64 s[64:65], 0\n"
"v_mov_b32 v27, v29\n"
"s_nop 0\n"
"s_nop 0\n"
"v_permlane32_swap_b32 v29, v27\n"
"v_add_f32 v29, v29, v27\n"
"v_mov_b32 v27, v29\n"
"s_nop 0\n"
"s_nop 0\n"
"v_permlane16_swap_b32 v29, v27\n"
"v_add_f32 v29, v29, v27\n"
"v_cmp_nge_f32 vcc, 0x53800000, v29\n"
"v_rcp_f32 v28, v29\n"
"s_or_b64 s[64:65], s[64:65], vcc\n"
"s_waitcnt vmcnt(55)\n"
"v_pk_mul_f32 v[160:161], v[160:161], v[28:29] op_sel_hi:[1,0]\n"
"v_pk_mul_f32 v[162:163], v[162:163], v[28:29] op_sel_hi:[1,0]\n"
"v_pk_mul_f32 v[164:165], v[164:165], v[28:29] op_sel_hi:[1,0]\n"
"v_pk_mul_f32 v[166:167], v[166:167], v[28:29] op_sel_hi:[1,0]\n"
"v_cvt_pk_bf16_f32 v160, v160, v161\n"
"v_cvt_pk_bf16_f32 v161, v162, v163\n"
"v_cvt_pk_bf16_f32 v162, v164, v165\n"
"v_cvt_pk_bf16_f32 v163, v166, v167\n"
"buffer_store_dwordx4 v[160:163], v1, s[40:43], 0 offen sc1\n"
"v_pk_mul_f32 v[168:169], v[168:169], v[28:29] op_sel_hi:[1,0]\n"
"v_pk_mul_f32 v[170:171], v[170:171], v[28:29] op_sel_hi:[1,0]\n"
"v_pk_mul_f32 v[172:173], v[172:173], v[28:29] op_sel_hi:[1,0]\n"
"v_pk_mul_f32 v[174:175], v[174:175], v[28:29] op_sel_hi:[1,0]\n"
"v_cvt_pk_bf16_f32 v168, v168, v169\n"
"v_cvt_pk_bf16_f32 v169, v170, v171\n"
"v_cvt_pk_bf16_f32 v170, v172, v173\n"
"v_cvt_pk_bf16_f32 v171, v174, v175\n"
"buffer_store_dwordx4 v[168:171], v1, s[40:43], 0 offen offset:1024 sc1\n"
"v_pk_mul_f32 v[176:177], v[176:177], v[28:29] op_sel_hi:[1,0]\n"
"v_pk_mul_f32 v[178:179], v[178:179], v[28:29] op_sel_hi:[1,0]\n"
"v_pk_mul_f32 v[180:181], v[180:181], v[28:29] op_sel_hi:[1,0]\n"
"v_pk_mul_f32 v[182:183], v[182:183], v[28:29] op_sel_hi:[1,0]\n"
"v_cvt_pk_bf16_f32 v176, v176, v177\n"
"v_cvt_pk_bf16_f32 v177, v178, v179\n"
"v_cvt_pk_bf16_f32 v178, v180, v181\n"
"v_cvt_pk_bf16_f32 v179, v182, v183\n"
"buffer_store_dwordx4 v[176:179], v1, s[40:43], 0 offen offset:2048 sc1\n"
"v_pk_mul_f32 v[184:185], v[184:185], v[28:29] op_sel_hi:[1,0]\n"
"v_pk_mul_f32 v[186:187], v[186:187], v[28:29] op_sel_hi:[1,0]\n"
"v_pk_mul_f32 v[188:189], v[188:189], v[28:29] op_sel_hi:[1,0]\n"
"v_pk_mul_f32 v[190:191], v[190:191], v[28:29] op_sel_hi:[1,0]\n"
"v_cvt_pk_bf16_f32 v184, v184, v185\n"
"v_cvt_pk_bf16_f32 v185, v186, v187\n"
"v_cvt_pk_bf16_f32 v186, v188, v189\n"
"v_cvt_pk_bf16_f32 v187, v190, v191\n"
"buffer_store_dwordx4 v[184:187], v1, s[40:43], 0 offen offset:3072 sc1\n"
"v_mfma_f32_16x16x32_bf16 v[64:67], a[64:67], v[112:115], v[44:47]\n"
"v_exp_f32 v72, v72\n"
"v_mfma_f32_16x16x32_bf16 v[64:67], a[68:71], v[116:119], v[64:67]\n"
"v_cvt_pk_bf16_f32 v59, v70, v71\n"
"v_mfma_f32_16x16x32_bf16 v[64:67], a[72:75], v[120:123], v[64:67]\n"
"v_exp_f32 v73, v73\n"
"v_mfma_f32_16x16x32_bf16 v[64:67], a[76:79], v[124:127], v[64:67]\n"
"v_mfma_f32_16x16x32_bf16 v[192:195], a[128:131], v[56:59], v[192:195]\n"
"v_exp_f32 v74, v74\n"
"v_mfma_f32_16x16x32_bf16 v[196:199], a[132:135], v[56:59], v[196:199]\n"
"v_cvt_pk_bf16_f32 v60, v72, v73\n"
"v_mfma_f32_16x16x32_bf16 v[200:203], a[136:139], v[56:59], v[200:203]\n"
"v_exp_f32 v75, v75\n"
"v_mfma_f32_16x16x32_bf16 v[204:207], a[140:143], v[56:59], v[204:207]\n"
"v_mfma_f32_16x16x32_bf16 v[18:21], a[224:227], v[56:59], v[18:21]\n"
"v_mfma_f32_16x16x32_bf16 v[68:71], a[80:83], v[112:115], v[44:47]\n"
"v_exp_f32 v76, v76\n"
"v_mfma_f32_16x16x32_bf16 v[68:71], a[84:87], v[116:119], v[68:71]\n"
"v_cvt_pk_bf16_f32 v61, v74, v75\n"
"v_mfma_f32_16x16x32_bf16 v[68:71], a[88:91], v[120:123], v[68:71]\n"
"v_exp_f32 v77, v77\n"
"v_mfma_f32_16x16x32_bf16 v[68:71], a[92:95], v[124:127], v[68:71]\n"
"v_mfma_f32_16x16x32_bf16 v[208:211], a[144:147], v[56:59], v[208:211]\n"
"v_exp_f32 v78, v78\n"
"v_mfma_f32_16x16x32_bf16 v[212:215], a[148:151], v[56:59], v[212:215]\n"
"v_cvt_pk_bf16_f32 v62, v76, v77\n"
"v_mfma_f32_16x16x32_bf16 v[216:219], a[152:155], v[56:59], v[216:219]\n"
"v_exp_f32 v79, v79\n"
"v_mfma_f32_16x16x32_bf16 v[220:223], a[156:159], v[56:59], v[220:223]\n"
"v_mfma_f32_16x16x32_bf16 v[72:75], a[96:99], v[112:115], v[44:47]\n"
"v_exp_f32 v64, v64\n"
"v_mfma_f32_16x16x32_bf16 v[72:75], a[100:103], v[116:119], v[72:75]\n"
"v_cvt_pk_bf16_f32 v63, v78, v79\n"
"v_mfma_f32_16x16x32_bf16 v[72:75], a[104:107], v[120:123], v[72:75]\n"
"v_exp_f32 v65, v65\n"
"v_mfma_f32_16x16x32_bf16 v[72:75], a[108:111], v[124:127], v[72:75]\n"
"v_add_f32 v31, v31, v64\n"
"v_mfma_f32_16x16x32_bf16 v[192:195], a[160:163], v[60:63], v[192:195]\n"
"v_exp_f32 v66, v66\n"
"v_mfma_f32_16x16x32_bf16 v[196:199], a[164:167], v[60:63], v[196:199]\n"
"v_cvt_pk_bf16_f32 v56, v64, v65\n"
"v_add_f32 v31, v31, v65\n"
"v_mfma_f32_16x16x32_bf16 v[200:203], a[168:171], v[60:63], v[200:203]\n"
"v_exp_f32 v67, v67\n"
"v_mfma_f32_16x16x32_bf16 v[204:207], a[172:175], v[60:63], v[204:207]\n"
"v_add_f32 v31, v31, v66\n"
"v_mfma_f32_16x16x32_bf16 v[18:21], a[224:227], v[60:63], v[18:21]\n"
"v_mfma_f32_16x16x32_bf16 v[76:79], a[112:115], v[112:115], v[44:47]\n"
"v_exp_f32 v68, v68\n"
"v_mfma_f32_16x16x32_bf16 v[76:79], a[116:119], v[116:119], v[76:79]\n"
"v_cvt_pk_bf16_f32 v57, v66, v67\n"
"v_add_f32 v31, v31, v67\n"
"v_mfma_f32_16x16x32_bf16 v[76:79], a[120:123], v[120:123], v[76:79]\n"
"v_exp_f32 v69, v69\n"
"v_mfma_f32_16x16x32_bf16 v[76:79], a[124:127], v[124:127], v[76:79]\n"
"v_add_f32 v31, v31, v68\n"
"v_mfma_f32_16x16x32_bf16 v[208:211], a[176:179], v[60:63], v[208:211]\n"
"v_exp_f32 v70, v70\n"
"v_mfma_f32_16x16x32_bf16 v[212:215], a[180:183], v[60:63], v[212:215]\n"
"v_cvt_pk_bf16_f32 v58, v68, v69\n"
"v_add_f32 v31, v31, v69\n"
"v_mfma_f32_16x16x32_bf16 v[216:219], a[184:187], v[60:63], v[216:219]\n"
"v_exp_f32 v71, v71\n"
"v_mfma_f32_16x16x32_bf16 v[220:223], a[188:191], v[60:63], v[220:223]\n"
"v_add_f32 v31, v31, v70\n"
"v_mov_b32 v30, v18\n"
"v_cmp_nge_f32 vcc, 0x53800000, v30\n"
"v_rcp_f32 v28, v30\n"
"s_or_b64 s[64:65], s[64:65], vcc\n"
"s_mov_b32 s55, 4096\n"
"s_waitcnt vmcnt(55)\n"
"v_pk_mul_f32 v[192:193], v[192:193], v[28:29] op_sel_hi:[1,0]\n"
"v_pk_mul_f32 v[194:195], v[194:195], v[28:29] op_sel_hi:[1,0]\n"
"v_pk_mul_f32 v[196:197], v[196:197], v[28:29] op_sel_hi:[1,0]\n"
"v_pk_mul_f32 v[198:199], v[198:199], v[28:29] op_sel_hi:[1,0]\n"
"v_cvt_pk_bf16_f32 v192, v192, v193\n"
"v_cvt_pk_bf16_f32 v193, v194, v195\n"
"v_cvt_pk_bf16_f32 v194, v196, v197\n"
"v_cvt_pk_bf16_f32 v195, v198, v199\n"
"buffer_store_dwordx4 v[192:195], v1, s[40:43], s55 offen sc1\n"
"v_pk_mul_f32 v[200:201], v[200:201], v[28:29] op_sel_hi:[1,0]\n"
"v_pk_mul_f32 v[202:203], v[202:203], v[28:29] op_sel_hi:[1,0]\n"
"v_pk_mul_f32 v[204:205], v[204:205], v[28:29] op_sel_hi:[1,0]\n"
"v_pk_mul_f32 v[206:207], v[206:207], v[28:29] op_sel_hi:[1,0]\n"
"v_cvt_pk_bf16_f32 v200, v200, v201\n"
"v_cvt_pk_bf16_f32 v201, v202, v203\n"
"v_cvt_pk_bf16_f32 v202, v204, v205\n"
"v_cvt_pk_bf16_f32 v203, v206, v207\n"
"buffer_store_dwordx4 v[200:203], v1, s[40:43], s55 offen offset:1024 sc1\n"
"v_pk_mul_f32 v[208:209], v[208:209], v[28:29] op_sel_hi:[1,0]\n"
"v_pk_mul_f32 v[210:211], v[210:211], v[28:29] op_sel_hi:[1,0]\n"
"v_pk_mul_f32 v[212:213], v[212:213], v[28:29] op_sel_hi:[1,0]\n"
"v_pk_mul_f32 v[214:215], v[214:215], v[28:29] op_sel_hi:[1,0]\n"
"v_cvt_pk_bf16_f32 v208, v208, v209\n"
"v_cvt_pk_bf16_f32 v209, v210, v211\n"
"v_cvt_pk_bf16_f32 v210, v212, v213\n"
"v_cvt_pk_bf16_f32 v211, v214, v215\n"
"buffer_store_dwordx4 v[208:211], v1, s[40:43], s55 offen offset:2048 sc1\n"
"v_pk_mul_f32 v[216:217], v[216:217], v[28:29] op_sel_hi:[1,0]\n"
"v_pk_mul_f32 v[218:219], v[218:219], v[28:29] op_sel_hi:[1,0]\n"
"v_pk_mul_f32 v[220:221], v[220:221], v[28:29] op_sel_hi:[1,0]\n"
"v_pk_mul_f32 v[222:223], v[222:223], v[28:29] op_sel_hi:[1,0]\n"
"v_cvt_pk_bf16_f32 v216, v216, v217\n"
"v_cvt_pk_bf16_f32 v217, v218, v219\n"
"v_cvt_pk_bf16_f32 v218, v220, v221\n"
"v_cvt_pk_bf16_f32 v219, v222, v223\n"
"buffer_store_dwordx4 v[216:219], v1, s[40:43], s55 offen offset:3072 sc1\n"
"v_subrev_u32 v4, 131072, v16\n"
"v_subrev_u32 v5, 131072, v17\n"
"v_subrev_u32 v6, 65536, v16\n"
"v_subrev_u32 v7, 65536, v17\n"
"v_subrev_u32 v8, 0, v16\n"
"v_subrev_u32 v9, 0, v17\n"
"v_bfe_u32 v2, v0, 2, 3\n"
"v_lshlrev_b32 v10, 10, v2\n"
"v_lshrrev_b32 v2, 5, v0\n"
"v_lshl_add_u32 v10, v2, 6, v10\n"
"v_bfe_u32 v2, v0, 4, 1\n"
"v_lshlrev_b32 v2, 1, v2\n"
"v_and_b32 v3, 3, v0\n"
"v_xor_b32 v2, v2, v3\n"
"v_lshl_add_u32 v10, v2, 4, v10\n"
"v_add_u32 v11, 7168, v10\n"
"v_add_u32 v12, 14336, v10\n"
"v_add_u32 v13, 21504, v10\n"
"s_mov_b64 s[60:61], s[16:17]\n"
"s_lshl_b32 s52, s49, 14\n"
"s_add_u32 s52, s52, 25165824\n"
"s_add_u32 s60, s60, s52\n"
"s_addc_u32 s61, s61, 0\n"
"s_lshl_b32 s52, s48, 5\n"
"s_add_u32 s52, s52, s50\n"
"s_lshl_b32 s52, s52, 7\n"
"v_mov_b32 v21, s52\n"
"v_mov_b32 v18, s70\n"
"v_mov_b32 v19, s71\n"
"s_andn2_b32 s63, 16, s50\n"
"s_lshl2_add_u32 s63, s48, s63\n"
"s_xor_b32 s52, s69, 1\n"
"s_lshl_b32 s52, s52, 5\n"
"s_add_u32 s52, s52, s63\n"
"v_and_b32 v15, 3, v0\n"
"v_add_u32 v15, s52, v15\n"
"v_lshlrev_b32 v15, 7, v15\n"
"v_mfma_f32_16x16x32_bf16 v[64:67], a[64:67], v[128:131], v[48:51]\n"
"v_exp_f32 v72, v72\n"
"v_mfma_f32_16x16x32_bf16 v[64:67], a[68:71], v[132:135], v[64:67]\n"
"v_cvt_pk_bf16_f32 v59, v70, v71\n"
"v_add_f32 v31, v31, v71\n"
"v_mfma_f32_16x16x32_bf16 v[64:67], a[72:75], v[136:139], v[64:67]\n"
"v_exp_f32 v73, v73\n"
"v_mfma_f32_16x16x32_bf16 v[64:67], a[76:79], v[140:143], v[64:67]\n"
"v_add_f32 v31, v31, v72\n"
"v_mfma_f32_16x16x32_bf16 v[224:227], a[128:131], v[56:59], v[224:227]\n"
"v_exp_f32 v74, v74\n"
"v_mfma_f32_16x16x32_bf16 v[228:231], a[132:135], v[56:59], v[228:231]\n"
"v_cvt_pk_bf16_f32 v60, v72, v73\n"
"v_add_f32 v31, v31, v73\n"
"v_mfma_f32_16x16x32_bf16 v[232:235], a[136:139], v[56:59], v[232:235]\n"
"v_exp_f32 v75, v75\n"
"v_mfma_f32_16x16x32_bf16 v[236:239], a[140:143], v[56:59], v[236:239]\n"
"v_add_f32 v31, v31, v74\n"
"v_mfma_f32_16x16x32_bf16 v[68:71], a[80:83], v[128:131], v[48:51]\n"
"v_exp_f32 v76, v76\n"
"v_mfma_f32_16x16x32_bf16 v[68:71], a[84:87], v[132:135], v[68:71]\n"
"v_cvt_pk_bf16_f32 v61, v74, v75\n"
"v_add_f32 v31, v31, v75\n"
"v_mfma_f32_16x16x32_bf16 v[68:71], a[88:91], v[136:139], v[68:71]\n"
"v_exp_f32 v77, v77\n"
"v_mfma_f32_16x16x32_bf16 v[68:71], a[92:95], v[140:143], v[68:71]\n"
"v_add_f32 v31, v31, v76\n"
"v_mfma_f32_16x16x32_bf16 v[240:243], a[144:147], v[56:59], v[240:243]\n"
"v_exp_f32 v78, v78\n"
"v_mfma_f32_16x16x32_bf16 v[244:247], a[148:151], v[56:59], v[244:247]\n"
"v_cvt_pk_bf16_f32 v62, v76, v77\n"
"v_add_f32 v31, v31, v77\n"
"v_mfma_f32_16x16x32_bf16 v[248:251], a[152:155], v[56:59], v[248:251]\n"
"v_exp_f32 v79, v79\n"
"v_mfma_f32_16x16x32_bf16 v[252:255], a[156:159], v[56:59], v[252:255]\n"
"v_add_f32 v31, v31, v78\n"
"v_mfma_f32_16x16x32_bf16 v[72:75], a[96:99], v[128:131], v[48:51]\n"
"v_exp_f32 v64, v64\n"
"v_mfma_f32_16x16x32_bf16 v[72:75], a[100:103], v[132:135], v[72:75]\n"
"v_cvt_pk_bf16_f32 v63, v78, v79\n"
"v_add_f32 v31, v31, v79\n"
"v_mfma_f32_16x16x32_bf16 v[72:75], a[104:107], v[136:139], v[72:75]\n"
"v_exp_f32 v65, v65\n"
"v_mfma_f32_16x16x32_bf16 v[72:75], a[108:111], v[140:143], v[72:75]\n"
"v_add_f32 v34, v34, v64\n"
"v_mfma_f32_16x16x32_bf16 v[224:227], a[160:163], v[60:63], v[224:227]\n"
"v_exp_f32 v66, v66\n"
"v_mfma_f32_16x16x32_bf16 v[228:231], a[164:167], v[60:63], v[228:231]\n"
"v_cvt_pk_bf16_f32 v56, v64, v65\n"
"v_add_f32 v34, v34, v65\n"
"v_mfma_f32_16x16x32_bf16 v[232:235], a[168:171], v[60:63], v[232:235]\n"
"v_exp_f32 v67, v67\n"
"v_mfma_f32_16x16x32_bf16 v[236:239], a[172:175], v[60:63], v[236:239]\n"
"v_add_f32 v34, v34, v66\n"
"v_mfma_f32_16x16x32_bf16 v[76:79], a[112:115], v[128:131], v[48:51]\n"
"v_exp_f32 v68, v68\n"
"v_mfma_f32_16x16x32_bf16 v[76:79], a[116:119], v[132:135], v[76:79]\n"
"v_cvt_pk_bf16_f32 v57, v66, v67\n"
"v_add_f32 v34, v34, v67\n"
"v_mfma_f32_16x16x32_bf16 v[76:79], a[120:123], v[136:139], v[76:79]\n"
"v_exp_f32 v69, v69\n"
"v_mfma_f32_16x16x32_bf16 v[76:79], a[124:127], v[140:143], v[76:79]\n"
"v_add_f32 v34, v34, v68\n"
"v_mfma_f32_16x16x32_bf16 v[240:243], a[176:179], v[60:63], v[240:243]\n"
"v_exp_f32 v70, v70\n"
"v_mfma_f32_16x16x32_bf16 v[244:247], a[180:183], v[60:63], v[244:247]\n"
"v_cvt_pk_bf16_f32 v58, v68, v69\n"
"v_add_f32 v34, v34, v69\n"
"v_mfma_f32_16x16x32_bf16 v[248:251], a[184:187], v[60:63], v[248:251]\n"
"v_exp_f32 v71, v71\n"
"v_mfma_f32_16x16x32_bf16 v[252:255], a[188:191], v[60:63], v[252:255]\n"
"v_add_f32 v34, v34, v70\n"
"v_mov_b32 v27, v31\n"
"s_nop 0\n"
"s_nop 0\n"
"v_permlane32_swap_b32 v31, v27\n"
"v_add_f32 v31, v31, v27\n"
"v_mov_b32 v27, v31\n"
"s_nop 0\n"
"s_nop 0\n"
"v_permlane16_swap_b32 v31, v27\n"
"v_add_f32 v31, v31, v27\n"
"v_cmp_nge_f32 vcc, 0x53800000, v31\n"
"v_rcp_f32 v28, v31\n"
"s_or_b64 s[64:65], s[64:65], vcc\n"
"s_mov_b32 s55, 8192\n"
"s_waitcnt vmcnt(55)\n"
"v_pk_mul_f32 v[224:225], v[224:225], v[28:29] op_sel_hi:[1,0]\n"
"v_pk_mul_f32 v[226:227], v[226:227], v[28:29] op_sel_hi:[1,0]\n"
"v_pk_mul_f32 v[228:229], v[228:229], v[28:29] op_sel_hi:[1,0]\n"
"v_pk_mul_f32 v[230:231], v[230:231], v[28:29] op_sel_hi:[1,0]\n"
"v_cvt_pk_bf16_f32 v224, v224, v225\n"
"v_cvt_pk_bf16_f32 v225, v226, v227\n"
"v_cvt_pk_bf16_f32 v226, v228, v229\n"
"v_cvt_pk_bf16_f32 v227, v230, v231\n"
"buffer_store_dwordx4 v[224:227], v1, s[40:43], s55 offen sc1\n"
"v_pk_mul_f32 v[232:233], v[232:233], v[28:29] op_sel_hi:[1,0]\n"
"v_pk_mul_f32 v[234:235], v[234:235], v[28:29] op_sel_hi:[1,0]\n"
"v_pk_mul_f32 v[236:237], v[236:237], v[28:29] op_sel_hi:[1,0]\n"
"v_pk_mul_f32 v[238:239], v[238:239], v[28:29] op_sel_hi:[1,0]\n"
"v_cvt_pk_bf16_f32 v232, v232, v233\n"
"v_cvt_pk_bf16_f32 v233, v234, v235\n"
"v_cvt_pk_bf16_f32 v234, v236, v237\n"
"v_cvt_pk_bf16_f32 v235, v238, v239\n"
"buffer_store_dwordx4 v[232:235], v1, s[40:43], s55 offen offset:1024 sc1\n"
"v_pk_mul_f32 v[240:241], v[240:241], v[28:29] op_sel_hi:[1,0]\n"
"v_pk_mul_f32 v[242:243], v[242:243], v[28:29] op_sel_hi:[1,0]\n"
"v_pk_mul_f32 v[244:245], v[244:245], v[28:29] op_sel_hi:[1,0]\n"
"v_pk_mul_f32 v[246:247], v[246:247], v[28:29] op_sel_hi:[1,0]\n"
"v_cvt_pk_bf16_f32 v240, v240, v241\n"
"v_cvt_pk_bf16_f32 v241, v242, v243\n"
"v_cvt_pk_bf16_f32 v242, v244, v245\n"
"v_cvt_pk_bf16_f32 v243, v246, v247\n"
"buffer_store_dwordx4 v[240:243], v1, s[40:43], s55 offen offset:2048 sc1\n"
"v_pk_mul_f32 v[248:249], v[248:249], v[28:29] op_sel_hi:[1,0]\n"
"v_pk_mul_f32 v[250:251], v[250:251], v[28:29] op_sel_hi:[1,0]\n"
"v_pk_mul_f32 v[252:253], v[252:253], v[28:29] op_sel_hi:[1,0]\n"
"v_pk_mul_f32 v[254:255], v[254:255], v[28:29] op_sel_hi:[1,0]\n"
"v_cvt_pk_bf16_f32 v248, v248, v249\n"
"v_cvt_pk_bf16_f32 v249, v250, v251\n"
"v_cvt_pk_bf16_f32 v250, v252, v253\n"
"v_cvt_pk_bf16_f32 v251, v254, v255\n"
"buffer_store_dwordx4 v[248:251], v1, s[40:43], s55 offen offset:3072 sc1\n"
"s_and_b32 s52, s69, 2\n"
"s_lshl2_add_u32 s52, s49, s52\n"
"s_lshl_b32 s52, s52, 5\n"
"s_add_u32 s52, s52, s63\n"
"s_mul_i32 s52, s52, 20480\n"
"s_mov_b64 s[24:25], s[16:17]\n"
"s_add_u32 s24, s24, s52\n"
"s_addc_u32 s25, s25, 0\n"
"s_and_b32 s25, s25, 0xffff\n"
"s_mov_b32 s26, 737280\n"
"s_mov_b32 s27, 0x20000\n"
"s_and_b32 s52, s69, 1\n"
"s_mul_i32 s57, s52, 655360\n"
"s_sub_u32 s56, 655360, s57\n"
"s_lshl_b32 s67, s48, 19\n"
"v_exp_f32 v72, v72\n"
"v_cvt_pk_bf16_f32 v59, v70, v71\n"
"v_add_f32 v34, v34, v71\n"
"v_exp_f32 v73, v73\n"
"v_add_f32 v34, v34, v72\n"
"v_mfma_f32_16x16x32_bf16 a[192:195], a[128:131], v[56:59], a[192:195]\n"
"v_exp_f32 v74, v74\n"
"v_mfma_f32_16x16x32_bf16 a[196:199], a[132:135], v[56:59], a[196:199]\n"
"v_cvt_pk_bf16_f32 v60, v72, v73\n"
"v_add_f32 v34, v34, v73\n"
"v_mfma_f32_16x16x32_bf16 a[200:203], a[136:139], v[56:59], a[200:203]\n"
"v_exp_f32 v75, v75\n"
"v_mfma_f32_16x16x32_bf16 a[204:207], a[140:143], v[56:59], a[204:207]\n"
"v_add_f32 v34, v34, v74\n"
"v_exp_f32 v76, v76\n"
"v_cvt_pk_bf16_f32 v61, v74, v75\n"
"v_add_f32 v34, v34, v75\n"
"v_exp_f32 v77, v77\n"
"v_add_f32 v34, v34, v76\n"
"v_mfma_f32_16x16x32_bf16 a[208:211], a[144:147], v[56:59], a[208:211]\n"
"v_exp_f32 v78, v78\n"
"v_mfma_f32_16x16x32_bf16 a[212:215], a[148:151], v[56:59], a[212:215]\n"
"v_cvt_pk_bf16_f32 v62, v76, v77\n"
"v_add_f32 v34, v34, v77\n"
"v_mfma_f32_16x16x32_bf16 a[216:219], a[152:155], v[56:59], a[216:219]\n"
"v_exp_f32 v79, v79\n"
"v_mfma_f32_16x16x32_bf16 a[220:223], a[156:159], v[56:59], a[220:223]\n"
"v_add_f32 v34, v34, v78\n"
"v_cvt_pk_bf16_f32 v63, v78, v79\n"
"v_add_f32 v34, v34, v79\n"
"s_nop 0\n"
"v_mfma_f32_16x16x32_bf16 a[192:195], a[160:163], v[60:63], a[192:195]\n"
"v_mfma_f32_16x16x32_bf16 a[196:199], a[164:167], v[60:63], a[196:199]\n"
"v_mfma_f32_16x16x32_bf16 a[200:203], a[168:171], v[60:63], a[200:203]\n"
"v_mfma_f32_16x16x32_bf16 a[204:207], a[172:175], v[60:63], a[204:207]\n"
"v_mfma_f32_16x16x32_bf16 a[208:211], a[176:179], v[60:63], a[208:211]\n"
"v_mfma_f32_16x16x32_bf16 a[212:215], a[180:183], v[60:63], a[212:215]\n"
"v_mfma_f32_16x16x32_bf16 a[216:219], a[184:187], v[60:63], a[216:219]\n"
"v_mfma_f32_16x16x32_bf16 a[220:223], a[188:191], v[60:63], a[220:223]\n"
"v_mov_b32 v27, v34\n"
"s_nop 0\n"
"s_nop 0\n"
"v_permlane32_swap_b32 v34, v27\n"
"v_add_f32 v34, v34, v27\n"
"v_mov_b32 v27, v34\n"
"s_nop 0\n"
"s_nop 0\n"
"v_permlane16_swap_b32 v34, v27\n"
"v_add_f32 v34, v34, v27\n"
"v_cmp_nge_f32 vcc, 0x53800000, v34\n"
"v_rcp_f32 v28, v34\n"
"s_or_b64 s[64:65], s[64:65], vcc\n"
"s_or_b32 s64, s64, s65\n"
"s_lshl_b32 s52, s48, 2\n"
"s_add_u32 s52, s52, 114688\n"
"v_mov_b32 v2, s52\n"
"v_mov_b32 v3, s64\n"
"ds_write_b32 v2, v3\n"
"v_mov_b32 v48, 114688\n"
"s_waitcnt vmcnt(28) lgkmcnt(0)\n"
"s_barrier\n"
"ds_read_b128 v[48:51], v48\n"
"s_mov_b32 s55, 12288\n"
"v_accvgpr_read_b32 v160, a192\n"
"v_accvgpr_read_b32 v161, a193\n"
"v_accvgpr_read_b32 v162, a194\n"
"v_accvgpr_read_b32 v163, a195\n"
"v_accvgpr_read_b32 v164, a196\n"
"v_accvgpr_read_b32 v165, a197\n"
"v_accvgpr_read_b32 v166, a198\n"
"v_accvgpr_read_b32 v167, a199\n"
"v_pk_mul_f32 v[160:161], v[160:161], v[28:29] op_sel_hi:[1,0]\n"
"v_pk_mul_f32 v[162:163], v[162:163], v[28:29] op_sel_hi:[1,0]\n"
"v_pk_mul_f32 v[164:165], v[164:165], v[28:29] op_sel_hi:[1,0]\n"
"v_pk_mul_f32 v[166:167], v[166:167], v[28:29] op_sel_hi:[1,0]\n"
"v_cvt_pk_bf16_f32 v160, v160, v161\n"
"v_cvt_pk_bf16_f32 v161, v162, v163\n"
"v_cvt_pk_bf16_f32 v162, v164, v165\n"
"v_cvt_pk_bf16_f32 v163, v166, v167\n"
"buffer_store_dwordx4 v[160:163], v1, s[40:43], s55 offen sc1\n"
"v_accvgpr_read_b32 v168, a200\n"
"v_accvgpr_read_b32 v169, a201\n"
"v_accvgpr_read_b32 v170, a202\n"
"v_accvgpr_read_b32 v171, a203\n"
"v_accvgpr_read_b32 v172, a204\n"
"v_accvgpr_read_b32 v173, a205\n"
"v_accvgpr_read_b32 v174, a206\n"
"v_accvgpr_read_b32 v175, a207\n"
"v_pk_mul_f32 v[168:169], v[168:169], v[28:29] op_sel_hi:[1,0]\n"
"v_pk_mul_f32 v[170:171], v[170:171], v[28:29] op_sel_hi:[1,0]\n"
"v_pk_mul_f32 v[172:173], v[172:173], v[28:29] op_sel_hi:[1,0]\n"
"v_pk_mul_f32 v[174:175], v[174:175], v[28:29] op_sel_hi:[1,0]\n"
"v_cvt_pk_bf16_f32 v168, v168, v169\n"
"v_cvt_pk_bf16_f32 v169, v170, v171\n"
"v_cvt_pk_bf16_f32 v170, v172, v173\n"
"v_cvt_pk_bf16_f32 v171, v174, v175\n"
"buffer_store_dwordx4 v[168:171], v1, s[40:43], s55 offen offset:1024 sc1\n"
"v_accvgpr_read_b32 v176, a208\n"
"v_accvgpr_read_b32 v177, a209\n"
"v_accvgpr_read_b32 v178, a210\n"
"v_accvgpr_read_b32 v179, a211\n"
"v_accvgpr_read_b32 v180, a212\n"
"v_accvgpr_read_b32 v181, a213\n"
"v_accvgpr_read_b32 v182, a214\n"
"v_accvgpr_read_b32 v183, a215\n"
"v_pk_mul_f32 v[176:177], v[176:177], v[28:29] op_sel_hi:[1,0]\n"
"v_pk_mul_f32 v[178:179], v[178:179], v[28:29] op_sel_hi:[1,0]\n"
"v_pk_mul_f32 v[180:181], v[180:181], v[28:29] op_sel_hi:[1,0]\n"
"v_pk_mul_f32 v[182:183], v[182:183], v[28:29] op_sel_hi:[1,0]\n"
"v_cvt_pk_bf16_f32 v176, v176, v177\n"
"v_cvt_pk_bf16_f32 v177, v178, v179\n"
"v_cvt_pk_bf16_f32 v178, v180, v181\n"
"v_cvt_pk_bf16_f32 v179, v182, v183\n"
"buffer_store_dwordx4 v[176:179], v1, s[40:43], s55 offen offset:2048 sc1\n"
"v_accvgpr_read_b32 v184, a216\n"
"v_accvgpr_read_b32 v185, a217\n"
"v_accvgpr_read_b32 v186, a218\n"
"v_accvgpr_read_b32 v187, a219\n"
"v_accvgpr_read_b32 v188, a220\n"
"v_accvgpr_read_b32 v189, a221\n"
"v_accvgpr_read_b32 v190, a222\n"
"v_accvgpr_read_b32 v191, a223\n"
"v_pk_mul_f32 v[184:185], v[184:185], v[28:29] op_sel_hi:[1,0]\n"
"v_pk_mul_f32 v[186:187], v[186:187], v[28:29] op_sel_hi:[1,0]\n"
"v_pk_mul_f32 v[188:189], v[188:189], v[28:29] op_sel_hi:[1,0]\n"
"v_pk_mul_f32 v[190:191], v[190:191], v[28:29] op_sel_hi:[1,0]\n"
"v_cvt_pk_bf16_f32 v184, v184, v185\n"
"v_cvt_pk_bf16_f32 v185, v186, v187\n"
"v_cvt_pk_bf16_f32 v186, v188, v189\n"
"v_cvt_pk_bf16_f32 v187, v190, v191\n"
"buffer_store_dwordx4 v[184:187], v1, s[40:43], s55 offen offset:3072 sc1\n"
"s_waitcnt lgkmcnt(0)\n"
"v_or_b32 v48, v48, v49\n"
"v_or3_b32 v48, v48, v50, v51\n"
"s_nop 0\n"
"v_readfirstlane_b32 s52, v48\n"
"s_waitcnt vmcnt(4)\n"
"s_andn2_b32 s52, s52, s68\n"
"s_cbranch_scc1 .Lmha_redo4\n"
"s_mov_b64 exec, 1\n"
"global_store_dwordx2 v21, v[18:19], s[60:61] sc1\n"
"s_mov_b64 exec, -1\n"
"global_load_dwordx2 v[2:3], v15, s[60:61] sc1\n"
"ds_read_b64_tr_b16 a[64:65], v8\n"
"ds_read_b64_tr_b16 a[66:67], v8 offset:2048\n"
"ds_read_b64_tr_b16 a[68:69], v9\n"
"ds_read_b64_tr_b16 a[70:71], v9 offset:2048\n"
"ds_read_b64_tr_b16 a[72:73], v8 offset:512\n"
"ds_read_b64_tr_b16 a[74:75], v8 offset:2560\n"
"ds_read_b64_tr_b16 a[76:77], v9 offset:512\n"
"ds_read_b64_tr_b16 a[78:79], v9 offset:2560\n"
"ds_read_b64_tr_b16 a[80:81], v8 offset:16384\n"
"ds_read_b64_tr_b16 a[82:83], v8 offset:18432\n"
"ds_read_b64_tr_b16 a[84:85], v9 offset:16384\n"
"ds_read_b64_tr_b16 a[86:87], v9 offset:18432\n"
"ds_read_b64_tr_b16 a[88:89], v8 offset:16896\n"
"ds_read_b64_tr_b16 a[90:91], v8 offset:18944\n"
"ds_read_b64_tr_b16 a[92:93], v9 offset:16896\n"
"ds_read_b64_tr_b16 a[94:95], v9 offset:18944\n"
"ds_read_b64_tr_b16 a[96:97], v4\n"
"ds_read_b64_tr_b16 a[98:99], v4 offset:2048\n"
"ds_read_b64_tr_b16 a[100:101], v5\n"
"ds_read_b64_tr_b16 a[102:103], v5 offset:2048\n"
"ds_read_b64_tr_b16 a[104:105], v4 offset:512\n"
"ds_read_b64_tr_b16 a[106:107], v4 offset:2560\n"
"ds_read_b64_tr_b16 a[108:109], v5 offset:512\n"
"ds_read_b64_tr_b16 a[110:111], v5 offset:2560\n"
"ds_read_b64_tr_b16 a[112:113], v6\n"
"ds_read_b64_tr_b16 a[114:115], v6 offset:2048\n"
"ds_read_b64_tr_b16 a[116:117], v7\n"
"ds_read_b64_tr_b16 a[118:119], v7 offset:2048\n"
"ds_read_b64_tr_b16 a[120:121], v6 offset:512\n"
"ds_read_b64_tr_b16 a[122:123], v6 offset:2560\n"
"ds_read_b64_tr_b16 a[124:125], v7 offset:512\n"
"ds_read_b64_tr_b16 a[126:127], v7 offset:2560\n"
"ds_read_b64_tr_b16 a[128:129], v4 offset:16384\n"
"ds_read_b64_tr_b16 a[130:131], v4 offset:18432\n"
"ds_read_b64_tr_b16 a[132:133], v5 offset:16384\n"
"ds_read_b64_tr_b16 a[134:135], v5 offset:18432\n"
"ds_read_b64_tr_b16 a[136:137], v4 offset:16896\n"
"ds_read_b64_tr_b16 a[138:139], v4 offset:18944\n"
"ds_read_b64_tr_b16 a[140:141], v5 offset:16896\n"
"ds_read_b64_tr_b16 a[142:143], v5 offset:18944\n"
"ds_read_b64_tr_b16 a[144:145], v6 offset:16384\n"
"ds_read_b64_tr_b16 a[146:147], v6 offset:18432\n"
"ds_read_b64_tr_b16 a[148:149], v7 offset:16384\n"
"ds_read_b64_tr_b16 a[150:151], v7 offset:18432\n"
"ds_read_b64_tr_b16 a[152:153], v6 offset:16896\n"
"ds_read_b64_tr_b16 a[154:155], v6 offset:18944\n"
"ds_read_b64_tr_b16 a[156:157], v7 offset:16896\n"
"ds_read_b64_tr_b16 a[158:159], v7 offset:18944\n"
"ds_read_b64_tr_b16 a[160:161], v4 offset:32768\n"
"ds_read_b64_tr_b16 a[162:163], v4 offset:34816\n"
"ds_read_b64_tr_b16 a[164:165], v5 offset:32768\n"
"ds_read_b64_tr_b16 a[166:167], v5 offset:34816\n"
"ds_read_b64_tr_b16 a[168:169], v4 offset:33280\n"
"ds_read_b64_tr_b16 a[170:171], v4 offset:35328\n"
"ds_read_b64_tr_b16 a[172:173], v5 offset:33280\n"
"ds_read_b64_tr_b16 a[174:175], v5 offset:35328\n"
"ds_read_b64_tr_b16 a[176:177], v6 offset:32768\n"
"ds_read_b64_tr_b16 a[178:179], v6 offset:34816\n"
"ds_read_b64_tr_b16 a[180:181], v7 offset:32768\n"
"ds_read_b64_tr_b16 a[182:183], v7 offset:34816\n"
"ds_read_b64_tr_b16 a[184:185], v6 offset:33280\n"
"ds_read_b64_tr_b16 a[186:187], v6 offset:35328\n"
"ds_read_b64_tr_b16 a[188:189], v7 offset:33280\n"
"ds_read_b64_tr_b16 a[190:191], v7 offset:35328\n"
"s_waitcnt lgkmcnt(15)\n"
"s_branch .Lmha_tail\n"
".Lmha_redo4:\n"
"s_waitcnt vmcnt(0) lgkmcnt(0)\n"
"s_barrier\n"
"s_mov_b32 s68, -1\n"
"v_and_b32 v1, 15, v0\n"
"v_lshrrev_b32 v2, 4, v0\n"
"v_and_b32 v3, 3, v0\n"
"v_xor_b32 v36, v1, v2\n"
"v_lshlrev_b32 v36, 4, v36\n"
"v_lshl_add_u32 v4, v1, 8, v36\n"
"v_xor_b32 v5, 64, v4\n"
"v_xor_b32 v6, 128, v4\n"
"v_xor_b32 v7, 192, v4\n"
"v_lshl_add_u32 v10, v2, 8, v36\n"
"v_xor_b32 v11, 64, v10\n"
"v_xor_b32 v12, 128, v10\n"
"v_xor_b32 v13, 192, v10\n"
"v_lshrrev_b32 v36, 5, v0\n"
"v_lshlrev_b32 v36, 11, v36\n"
"v_bfe_u32 v37, v0, 2, 3\n"
"v_lshl_add_u32 v36, v37, 6, v36\n"
"v_lshl_add_u32 v36, v3, 3, v36\n"
"v_and_b32 v38, 1, v2\n"
"v_lshl_add_u32 v36, v38, 5, v36\n"
"v_add_u32 v8, 65536, v36\n"
"v_xor_b32 v9, 32, v8\n"
"v_lshrrev_b32 v36, 5, v0\n"
"v_lshlrev_b32 v36, 6, v36\n"
"v_lshl_add_u32 v36, v37, 8, v36\n"
"v_lshlrev_b32 v38, 1, v38\n"
"v_xor_b32 v38, v38, v3\n"
"v_lshl_add_u32 v36, v38, 4, v36\n"
"v_add_u32 v14, 1024, v36\n"
"v_add_u32 v15, 128, v36\n"
"s_mul_i32 s61, %1, 2048\n"
"s_and_b32 s61, s61, 0x3e000\n"
"v_mov_b32 v56, 0xff800000\n"
"v_mov_b32 v57, 0xff800000\n"
"v_mov_b32 v58, 0xff800000\n"
"v_mov_b32 v59, 0xff800000\n"
"s_mov_b32 s63, 31\n"
".Lmha_fb4:\n"
"s_mov_b32 m0, 0\n"
"s_nop 0\n"
"buffer_load_dwordx4 v10, s[28:31], s61 offen lds\n"
"buffer_load_dwordx4 v11, s[28:31], s61 offen offset:1024 lds\n"
"buffer_load_dwordx4 v12, s[28:31], s61 offen offset:2048 lds\n"
"buffer_load_dwordx4 v13, s[28:31], s61 offen offset:3072 lds\n"
"s_add_u32 s61, s61, 4096\n"
"s_and_b32 s61, s61, 0x3ffff\n"
"s_mov_b32 m0, 4096\n"
"s_nop 0\n"
"buffer_load_dwordx4 v10, s[28:31], s61 offen lds\n"
"buffer_load_dwordx4 v11, s[28:31], s61 offen offset:1024 lds\n"
"buffer_load_dwordx4 v12, s[28:31], s61 offen offset:2048 lds\n"
"buffer_load_dwordx4 v13, s[28:31], s61 offen offset:3072 lds\n"
"s_add_u32 s61, s61, 4096\n"
"s_and_b32 s61, s61, 0x3ffff\n"
"s_waitcnt vmcnt(0)\n"
"s_barrier\n"
"ds_read_b128 a[64:67], v4\n"
"ds_read_b128 a[68:71], v5\n"
"ds_read_b128 a[72:75], v6\n"
"ds_read_b128 a[76:79], v7\n"
"ds_read_b128 a[80:83], v4 offset:4096\n"
"ds_read_b128 a[84:87], v5 offset:4096\n"
"ds_read_b128 a[88:91], v6 offset:4096\n"
"ds_read_b128 a[92:95], v7 offset:4096\n"
"s_waitcnt lgkmcnt(0)\n"
"s_barrier\n"
"v_mfma_f32_16x16x32_bf16 v[64:67], a[64:67], v[80:83], 0\n"
"v_mfma_f32_16x16x32_bf16 v[64:67], a[68:71], v[84:87], v[64:67]\n"
"v_mfma_f32_16x16x32_bf16 v[64:67], a[72:75], v[88:91], v[64:67]\n"
"v_mfma_f32_16x16x32_bf16 v[64:67], a[76:79], v[92:95], v[64:67]\n"
"v_mfma_f32_16x16x32_bf16 v[68:71], a[80:83], v[80:83], 0\n"
"v_mfma_f32_16x16x32_bf16 v[68:71], a[84:87], v[84:87], v[68:71]\n"
"v_mfma_f32_16x16x32_bf16 v[68:71], a[88:91], v[88:91], v[68:71]\n"
"v_mfma_f32_16x16x32_bf16 v[68:71], a[92:95], v[92:95], v[68:71]\n"
"s_nop 7\n"
"v_max3_f32 v56, v56, v64, v65\n"
"v_max3_f32 v56, v56, v66, v67\n"
"v_max3_f32 v56, v56, v68, v69\n"
"v_max3_f32 v56, v56, v70, v71\n"
"v_mfma_f32_16x16x32_bf16 v[64:67], a[64:67], v[96:99], 0\n"
"v_mfma_f32_16x16x32_bf16 v[64:67], a[68:71], v[100:103], v[64:67]\n"
"v_mfma_f32_16x16x32_bf16 v[64:67], a[72:75], v[104:107], v[64:67]\n"
"v_mfma_f32_16x16x32_bf16 v[64:67], a[76:79], v[108:111], v[64:67]\n"
"v_mfma_f32_16x16x32_bf16 v[68:71], a[80:83], v[96:99], 0\n"
"v_mfma_f32_16x16x32_bf16 v[68:71], a[84:87], v[100:103], v[68:71]\n"
"v_mfma_f32_16x16x32_bf16 v[68:71], a[88:91], v[104:107], v[68:71]\n"
"v_mfma_f32_16x16x32_bf16 v[68:71], a[92:95], v[108:111], v[68:71]\n"
"s_nop 7\n"
"v_max3_f32 v57, v57, v64, v65\n"
"v_max3_f32 v57, v57, v66, v67\n"
"v_max3_f32 v57, v57, v68, v69\n"
"v_max3_f32 v57, v57, v70, v71\n"
"v_mfma_f32_16x16x32_bf16 v[64:67], a[64:67], v[112:115], 0\n"
"v_mfma_f32_16x16x32_bf16 v[64:67], a[68:71], v[116:119], v[64:67]\n"
"v_mfma_f32_16x16x32_bf16 v[64:67], a[72:75], v[120:123], v[64:67]\n"
"v_mfma_f32_16x16x32_bf16 v[64:67], a[76:79], v[124:127], v[64:67]\n"
"v_mfma_f32_16x16x32_bf16 v[68:71], a[80:83], v[112:115], 0\n"
"v_mfma_f32_16x16x32_bf16 v[68:71], a[84:87], v[116:119], v[68:71]\n"
"v_mfma_f32_16x16x32_bf16 v[68:71], a[88:91], v[120:123], v[68:71]\n"
"v_mfma_f32_16x16x32_bf16 v[68:71], a[92:95], v[124:127], v[68:71]\n"
"s_nop 7\n"
"v_max3_f32 v58, v58, v64, v65\n"
"v_max3_f32 v58, v58, v66, v67\n"
"v_max3_f32 v58, v58, v68, v69\n"
"v_max3_f32 v58, v58, v70, v71\n"
"v_mfma_f32_16x16x32_bf16 v[64:67], a[64:67], v[128:131], 0\n"
"v_mfma_f32_16x16x32_bf16 v[64:67], a[68:71], v[132:135], v[64:67]\n"
"v_mfma_f32_16x16x32_bf16 v[64:67], a[72:75], v[136:139], v[64:67]\n"
"v_mfma_f32_16x16x32_bf16 v[64:67], a[76:79], v[140:143], v[64:67]\n"
"v_mfma_f32_16x16x32_bf16 v[68:71], a[80:83], v[128:131], 0\n"
"v_mfma_f32_16x16x32_bf16 v[68:71], a[84:87], v[132:135], v[68:71]\n"
"v_mfma_f32_16x16x32_bf16 v[68:71], a[88:91], v[136:139], v[68:71]\n"
"v_mfma_f32_16x16x32_bf16 v[68:71], a[92:95], v[140:143], v[68:71]\n"
"s_nop 7\n"
"v_max3_f32 v59, v59, v64, v65\n"
"v_max3_f32 v59, v59, v66, v67\n"
"v_max3_f32 v59, v59, v68, v69\n"
"v_max3_f32 v59, v59, v70, v71\n"
"s_sub_u32 s63, s63, 1\n"
"s_cbranch_scc0 .Lmha_fb4\n"
"v_mov_b32 v72, v56\n"
"v_mov_b32 v73, v57\n"
"v_mov_b32 v74, v58\n"
"v_mov_b32 v75, v59\n"
"v_permlane32_swap_b32 v56, v72\n"
"v_permlane32_swap_b32 v57, v73\n"
"v_permlane32_swap_b32 v58, v74\n"
"v_permlane32_swap_b32 v59, v75\n"
"v_max_f32 v56, v56, v72\n"
"v_max_f32 v57, v57, v73\n"
"v_max_f32 v58, v58, v74\n"
"v_max_f32 v59, v59, v75\n"
"v_mov_b32 v72, v56\n"
"v_mov_b32 v73, v57\n"
"v_mov_b32 v74, v58\n"
"v_mov_b32 v75, v59\n"
"v_permlane16_swap_b32 v56, v72\n"
"v_permlane16_swap_b32 v57, v73\n"
"v_permlane16_swap_b32 v58, v74\n"
"v_permlane16_swap_b32 v59, v75\n"
"v_max_f32 v56, v56, v72\n"
"v_max_f32 v57, v57, v73\n"
"v_max_f32 v58, v58, v74\n"
"v_max_f32 v59, v59, v75\n"
"v_sub_f32 v36, 0, v56\n"
"v_sub_f32 v37, 0, v56\n"
"v_sub_f32 v38, 0, v56\n"
"v_sub_f32 v39, 0, v56\n"
"v_sub_f32 v40, 0, v57\n"
"v_sub_f32 v41, 0, v57\n"
"v_sub_f32 v42, 0, v57\n"
"v_sub_f32 v43, 0, v57\n"
"v_sub_f32 v44, 0, v58\n"
"v_sub_f32 v45, 0, v58\n"
"v_sub_f32 v46, 0, v58\n"
"v_sub_f32 v47, 0, v58\n"
"v_sub_f32 v48, 0, v59\n"
"v_sub_f32 v49, 0, v59\n"
"v_sub_f32 v50, 0, v59\n"
"v_sub_f32 v51, 0, v59\n"
"v_mov_b32 v29, 0\n"
"v_mov_b32 v30, 0\n"
"v_mov_b32 v31, 0\n"
"v_mov_b32 v34, 0\n"
"s_mul_i32 s61, %1, 2048\n"
"s_and_b32 s61, s61, 0x3e000\n"
"s_mov_b32 s62, s61\n"
"s_mov_b32 m0, 0\n"
"s_waitcnt vmcnt(59)\n"
"buffer_load_dwordx4 v10, s[28:31], s61 offen lds\n"
"buffer_load_dwordx4 v11, s[28:31], s61 offen offset:1024 lds\n"
"buffer_load_dwordx4 v12, s[28:31], s61 offen offset:2048 lds\n"
"buffer_load_dwordx4 v13, s[28:31], s61 offen offset:3072 lds\n"
"s_add_u32 s61, s61, 4096\n"
"s_and_b32 s61, s61, 0x3ffff\n"
"s_mov_b32 m0, 4096\n"
"s_waitcnt vmcnt(59)\n"
"buffer_load_dwordx4 v10, s[28:31], s61 offen lds\n"
"buffer_load_dwordx4 v11, s[28:31], s61 offen offset:1024 lds\n"
"buffer_load_dwordx4 v12, s[28:31], s61 offen offset:2048 lds\n"
"buffer_load_dwordx4 v13, s[28:31], s61 offen offset:3072 lds\n"
"s_add_u32 s61, s61, 4096\n"
"s_and_b32 s61, s61, 0x3ffff\n"
"s_mov_b32 m0, 8192\n"
"s_waitcnt vmcnt(59)\n"
"buffer_load_dwordx4 v10, s[28:31], s61 offen lds\n"
"buffer_load_dwordx4 v11, s[28:31], s61 offen offset:1024 lds\n"
"buffer_load_dwordx4 v12, s[28:31], s61 offen offset:2048 lds\n"
"buffer_load_dwordx4 v13, s[28:31], s61 offen offset:3072 lds\n"
"s_add_u32 s61, s61, 4096\n"
"s_and_b32 s61, s61, 0x3ffff\n"
"s_mov_b32 m0, 12288\n"
"s_waitcnt vmcnt(59)\n"
"buffer_load_dwordx4 v10, s[28:31], s61 offen lds\n"
"buffer_load_dwordx4 v11, s[28:31], s61 offen offset:1024 lds\n"
"buffer_load_dwordx4 v12, s[28:31], s61 offen offset:2048 lds\n"
"buffer_load_dwordx4 v13, s[28:31], s61 offen offset:3072 lds\n"
"s_add_u32 s61, s61, 4096\n"
"s_and_b32 s61, s61, 0x3ffff\n"
"s_mov_b32 m0, 16384\n"
"s_waitcnt vmcnt(59)\n"
"buffer_load_dwordx4 v10, s[28:31], s61 offen lds\n"
"buffer_load_dwordx4 v11, s[28:31], s61 offen offset:1024 lds\n"
"buffer_load_dwordx4 v12, s[28:31], s61 offen offset:2048 lds\n"
"buffer_load_dwordx4 v13, s[28:31], s61 offen offset:3072 lds\n"
"s_add_u32 s61, s61, 4096\n"
"s_and_b32 s61, s61, 0x3ffff\n"
"s_mov_b32 m0, 20480\n"
"s_waitcnt vmcnt(59)\n"
"buffer_load_dwordx4 v10, s[28:31], s61 offen lds\n"
"buffer_load_dwordx4 v11, s[28:31], s61 offen offset:1024 lds\n"
"buffer_load_dwordx4 v12, s[28:31], s61 offen offset:2048 lds\n"
"buffer_load_dwordx4 v13, s[28:31], s61 offen offset:3072 lds\n"
"s_add_u32 s61, s61, 4096\n"
"s_and_b32 s61, s61, 0x3ffff\n"
"s_mov_b32 m0, 24576\n"
"s_waitcnt vmcnt(59)\n"
"buffer_load_dwordx4 v10, s[28:31], s61 offen lds\n"
"buffer_load_dwordx4 v11, s[28:31], s61 offen offset:1024 lds\n"
"buffer_load_dwordx4 v12, s[28:31], s61 offen offset:2048 lds\n"
"buffer_load_dwordx4 v13, s[28:31], s61 offen offset:3072 lds\n"
"s_add_u32 s61, s61, 4096\n"
"s_and_b32 s61, s61, 0x3ffff\n"
"s_mov_b32 m0, 28672\n"
"s_waitcnt vmcnt(59)\n"
"buffer_load_dwordx4 v10, s[28:31], s61 offen lds\n"
"buffer_load_dwordx4 v11, s[28:31], s61 offen offset:1024 lds\n"
"buffer_load_dwordx4 v12, s[28:31], s61 offen offset:2048 lds\n"
"buffer_load_dwordx4 v13, s[28:31], s61 offen offset:3072 lds\n"
"s_add_u32 s61, s61, 4096\n"
"s_and_b32 s61, s61, 0x3ffff\n"
"s_mov_b32 m0, 32768\n"
"s_waitcnt vmcnt(59)\n"
"buffer_load_dwordx4 v10, s[28:31], s61 offen lds\n"
"buffer_load_dwordx4 v11, s[28:31], s61 offen offset:1024 lds\n"
"buffer_load_dwordx4 v12, s[28:31], s61 offen offset:2048 lds\n"
"buffer_load_dwordx4 v13, s[28:31], s61 offen offset:3072 lds\n"
"s_add_u32 s61, s61, 4096\n"
"s_and_b32 s61, s61, 0x3ffff\n"
"s_mov_b32 m0, 36864\n"
"s_waitcnt vmcnt(59)\n"
"buffer_load_dwordx4 v10, s[28:31], s61 offen lds\n"
"buffer_load_dwordx4 v11, s[28:31], s61 offen offset:1024 lds\n"
"buffer_load_dwordx4 v12, s[28:31], s61 offen offset:2048 lds\n"
"buffer_load_dwordx4 v13, s[28:31], s61 offen offset:3072 lds\n"
"s_add_u32 s61, s61, 4096\n"
"s_and_b32 s61, s61, 0x3ffff\n"
"s_mov_b32 m0, 40960\n"
"s_waitcnt vmcnt(59)\n"
"buffer_load_dwordx4 v10, s[28:31], s61 offen lds\n"
"buffer_load_dwordx4 v11, s[28:31], s61 offen offset:1024 lds\n"
"buffer_load_dwordx4 v12, s[28:31], s61 offen offset:2048 lds\n"
"buffer_load_dwordx4 v13, s[28:31], s61 offen offset:3072 lds\n"
"s_add_u32 s61, s61, 4096\n"
"s_and_b32 s61, s61, 0x3ffff\n"
"s_mov_b32 m0, 45056\n"
"s_waitcnt vmcnt(59)\n"
"buffer_load_dwordx4 v10, s[28:31], s61 offen lds\n"
"buffer_load_dwordx4 v11, s[28:31], s61 offen offset:1024 lds\n"
"buffer_load_dwordx4 v12, s[28:31], s61 offen offset:2048 lds\n"
"buffer_load_dwordx4 v13, s[28:31], s61 offen offset:3072 lds\n"
"s_add_u32 s61, s61, 4096\n"
"s_and_b32 s61, s61, 0x3ffff\n"
"s_mov_b32 m0, 65536\n"
"s_waitcnt vmcnt(59)\n"
"buffer_load_dwordx4 v14, s[36:39], s62 offen lds\n"
"buffer_load_dwordx4 v15, s[36:39], s62 offen offset:1024 lds\n"
"buffer_load_dwordx4 v14, s[36:39], s62 offen offset:2048 lds\n"
"buffer_load_dwordx4 v15, s[36:39], s62 offen offset:3072 lds\n"
"s_add_u32 s62, s62, 4096\n"
"s_and_b32 s62, s62, 0x3ffff\n"
"s_mov_b32 m0, 69632\n"
"s_waitcnt vmcnt(59)\n"
"buffer_load_dwordx4 v14, s[36:39], s62 offen lds\n"
"buffer_load_dwordx4 v15, s[36:39], s62 offen offset:1024 lds\n"
"buffer_load_dwordx4 v14, s[36:39], s62 offen offset:2048 lds\n"
"buffer_load_dwordx4 v15, s[36:39], s62 offen offset:3072 lds\n"
"s_add_u32 s62, s62, 4096\n"
"s_and_b32 s62, s62, 0x3ffff\n"
"s_mov_b32 m0, 73728\n"
"s_waitcnt vmcnt(59)\n"
"buffer_load_dwordx4 v14, s[36:39], s62 offen lds\n"
"buffer_load_dwordx4 v15, s[36:39], s62 offen offset:1024 lds\n"
"buffer_load_dwordx4 v14, s[36:39], s62 offen offset:2048 lds\n"
"buffer_load_dwordx4 v15, s[36:39], s62 offen offset:3072 lds\n"
"s_add_u32 s62, s62, 4096\n"
"s_and_b32 s62, s62, 0x3ffff\n"
"s_mov_b32 m0, 77824\n"
"s_waitcnt vmcnt(59)\n"
"buffer_load_dwordx4 v14, s[36:39], s62 offen lds\n"
"buffer_load_dwordx4 v15, s[36:39], s62 offen offset:1024 lds\n"
"buffer_load_dwordx4 v14, s[36:39], s62 offen offset:2048 lds\n"
"buffer_load_dwordx4 v15, s[36:39], s62 offen offset:3072 lds\n"
"s_add_u32 s62, s62, 4096\n"
"s_and_b32 s62, s62, 0x3ffff\n"
"s_mov_b32 m0, 81920\n"
"s_waitcnt vmcnt(59)\n"
"buffer_load_dwordx4 v14, s[36:39], s62 offen lds\n"
"buffer_load_dwordx4 v15, s[36:39], s62 offen offset:1024 lds\n"
"buffer_load_dwordx4 v14, s[36:39], s62 offen offset:2048 lds\n"
"buffer_load_dwordx4 v15, s[36:39], s62 offen offset:3072 lds\n"
"s_add_u32 s62, s62, 4096\n"
"s_and_b32 s62, s62, 0x3ffff\n"
"s_mov_b32 m0, 86016\n"
"s_waitcnt vmcnt(59)\n"
"buffer_load_dwordx4 v14, s[36:39], s62 offen lds\n"
"buffer_load_dwordx4 v15, s[36:39], s62 offen offset:1024 lds\n"
"buffer_load_dwordx4 v14, s[36:39], s62 offen offset:2048 lds\n"
"buffer_load_dwordx4 v15, s[36:39], s62 offen offset:3072 lds\n"
"s_add_u32 s62, s62, 4096\n"
"s_and_b32 s62, s62, 0x3ffff\n"
"s_mov_b32 m0, 90112\n"
"s_waitcnt vmcnt(59)\n"
"buffer_load_dwordx4 v14, s[36:39], s62 offen lds\n"
"buffer_load_dwordx4 v15, s[36:39], s62 offen offset:1024 lds\n"
"buffer_load_dwordx4 v14, s[36:39], s62 offen offset:2048 lds\n"
"buffer_load_dwordx4 v15, s[36:39], s62 offen offset:3072 lds\n"
"s_add_u32 s62, s62, 4096\n"
"s_and_b32 s62, s62, 0x3ffff\n"
"s_mov_b32 m0, 94208\n"
"s_waitcnt vmcnt(59)\n"
"buffer_load_dwordx4 v14, s[36:39], s62 offen lds\n"
"buffer_load_dwordx4 v15, s[36:39], s62 offen offset:1024 lds\n"
"buffer_load_dwordx4 v14, s[36:39], s62 offen offset:2048 lds\n"
"buffer_load_dwordx4 v15, s[36:39], s62 offen offset:3072 lds\n"
"s_add_u32 s62, s62, 4096\n"
"s_and_b32 s62, s62, 0x3ffff\n"
"s_mov_b32 m0, 98304\n"
"s_waitcnt vmcnt(59)\n"
"buffer_load_dwordx4 v14, s[36:39], s62 offen lds\n"
"buffer_load_dwordx4 v15, s[36:39], s62 offen offset:1024 lds\n"
"buffer_load_dwordx4 v14, s[36:39], s62 offen offset:2048 lds\n"
"buffer_load_dwordx4 v15, s[36:39], s62 offen offset:3072 lds\n"
"s_add_u32 s62, s62, 4096\n"
"s_and_b32 s62, s62, 0x3ffff\n"
"s_mov_b32 m0, 102400\n"
"s_waitcnt vmcnt(59)\n"
"buffer_load_dwordx4 v14, s[36:39], s62 offen lds\n"
"buffer_load_dwordx4 v15, s[36:39], s62 offen offset:1024 lds\n"
"buffer_load_dwordx4 v14, s[36:39], s62 offen offset:2048 lds\n"
"buffer_load_dwordx4 v15, s[36:39], s62 offen offset:3072 lds\n"
"s_add_u32 s62, s62, 4096\n"
"s_and_b32 s62, s62, 0x3ffff\n"
"s_waitcnt vmcnt(0)\n"
"s_barrier\n"
"ds_read_b128 a[64:67], v4\n"
"ds_read_b128 a[68:71], v5\n"
"ds_read_b128 a[72:75], v6\n"
"ds_read_b128 a[76:79], v7\n"
"ds_read_b128 a[80:83], v4 offset:4096\n"
"ds_read_b128 a[84:87], v5 offset:4096\n"
"ds_read_b128 a[88:91], v6 offset:4096\n"
"ds_read_b128 a[92:95], v7 offset:4096\n"
"ds_read_b128 a[96:99], v4 offset:8192\n"
"ds_read_b128 a[100:103], v5 offset:8192\n"
"ds_read_b128 a[104:107], v6 offset:8192\n"
"ds_read_b128 a[108:111], v7 offset:8192\n"
"ds_read_b128 a[112:115], v4 offset:12288\n"
"ds_read_b128 a[116:119], v5 offset:12288\n"
"ds_read_b128 a[120:123], v6 offset:12288\n"
"ds_read_b128 a[124:127], v7 offset:12288\n"
"ds_read_b64_tr_b16 a[128:129], v8\n"
"ds_read_b64_tr_b16 a[130:131], v8 offset:4096\n"
"ds_read_b64_tr_b16 a[132:133], v9\n"
"ds_read_b64_tr_b16 a[134:135], v9 offset:4096\n"
"ds_read_b64_tr_b16 a[136:137], v8 offset:512\n"
"ds_read_b64_tr_b16 a[138:139], v8 offset:4608\n"
"ds_read_b64_tr_b16 a[140:141], v9 offset:512\n"
"ds_read_b64_tr_b16 a[142:143], v9 offset:4608\n"
"ds_read_b64_tr_b16 a[144:145], v8 offset:1024\n"
"ds_read_b64_tr_b16 a[146:147], v8 offset:5120\n"
"ds_read_b64_tr_b16 a[148:149], v9 offset:1024\n"
"ds_read_b64_tr_b16 a[150:151], v9 offset:5120\n"
"ds_read_b64_tr_b16 a[152:153], v8 offset:1536\n"
"ds_read_b64_tr_b16 a[154:155], v8 offset:5632\n"
"ds_read_b64_tr_b16 a[156:157], v9 offset:1536\n"
"ds_read_b64_tr_b16 a[158:159], v9 offset:5632\n"
"s_waitcnt lgkmcnt(0)\n"
"v_mfma_f32_16x16x32_bf16 v[64:67], a[64:67], v[80:83], v[36:39]\n"
"v_mfma_f32_16x16x32_bf16 v[64:67], a[68:71], v[84:87], v[64:67]\n"
"v_mfma_f32_16x16x32_bf16 v[64:67], a[72:75], v[88:91], v[64:67]\n"
"v_mfma_f32_16x16x32_bf16 v[64:67], a[76:79], v[92:95], v[64:67]\n"
"v_mfma_f32_16x16x32_bf16 v[68:71], a[80:83], v[80:83], v[36:39]\n"
"v_mfma_f32_16x16x32_bf16 v[68:71], a[84:87], v[84:87], v[68:71]\n"
"v_mfma_f32_16x16x32_bf16 v[68:71], a[88:91], v[88:91], v[68:71]\n"
"v_mfma_f32_16x16x32_bf16 v[68:71], a[92:95], v[92:95], v[68:71]\n"
"s_nop 7\n"
"s_branch .Lmha_att4\n"
".Lmha_tail:\n"
"s_add_u32 m0, s58, 131072\n"
"s_add_u32 s53, s67, 425984\n"
"buffer_load_dwordx4 v10, s[44:47], s53 offen lds\n"
"buffer_load_dwordx4 v11, s[44:47], s53 offen offset:1024 lds\n"
"buffer_load_dwordx4 v12, s[44:47], s53 offen offset:2048 lds\n"
"buffer_load_dwordx4 v13, s[44:47], s53 offen offset:3072 lds\n"
"s_add_u32 m0, s58, 147456\n"
"s_add_u32 s53, s67, 458752\n"
"buffer_load_dwordx4 v10, s[44:47], s53 offen lds\n"
"buffer_load_dwordx4 v11, s[44:47], s53 offen offset:1024 lds\n"
"buffer_load_dwordx4 v12, s[44:47], s53 offen offset:2048 lds\n"
"buffer_load_dwordx4 v13, s[44:47], s53 offen offset:3072 lds\n"
"s_add_u32 m0, s58, 0\n"
"s_add_u32 s53, s67, 491520\n"
"buffer_load_dwordx4 v10, s[44:47], s53 offen lds\n"
"buffer_load_dwordx4 v11, s[44:47], s53 offen offset:1024 lds\n"
"buffer_load_dwordx4 v12, s[44:47], s53 offen offset:2048 lds\n"
"buffer_load_dwordx4 v13, s[44:47], s53 offen offset:3072 lds\n"
"s_waitcnt vmcnt(12)\n"
"v_cmp_ne_u64 vcc, s[70:71], v[2:3]\n"
"s_nop 3\n"
"s_cmp_eq_u64 vcc, 0\n"
"s_cbranch_scc1 .Lmha_pollok\n"
"s_mov_b64 exec, 1\n"
"global_store_dwordx2 v21, v[18:19], s[60:61] offset:8 sc1\n"
"s_mov_b64 exec, -1\n"
"s_mov_b32 s63, 262144\n"
".Lmha_poll:\n"
"v_cmp_ne_u64 vcc, s[70:71], v[2:3]\n"
"s_nop 3\n"
"s_cmp_eq_u64 vcc, 0\n"
"s_cbranch_scc1 .Lmha_pollok\n"
"s_sleep 1\n"
"global_load_dwordx2 v[2:3], v15, s[60:61] sc1\n"
"s_sub_u32 s63, s63, 1\n"
"s_waitcnt vmcnt(0)\n"
"s_cbranch_scc0 .Lmha_poll\n"
".Lmha_pollok:\n"
"s_add_u32 s66, s56, 0\n"
"buffer_load_dwordx4 v[96:99], v1, s[24:27], s66 offen sc1\n"
"buffer_load_dwordx4 v[100:103], v1, s[24:27], s66 offen offset:1024 sc1\n"
"buffer_load_dwordx4 v[104:107], v1, s[24:27], s66 offen offset:2048 sc1\n"
"buffer_load_dwordx4 v[108:111], v1, s[24:27], s66 offen offset:3072 sc1\n"
"s_add_u32 s66, s56, 20480\n"
"buffer_load_dwordx4 v[112:115], v1, s[24:27], s66 offen sc1\n"
"buffer_load_dwordx4 v[116:119], v1, s[24:27], s66 offen offset:1024 sc1\n"
"buffer_load_dwordx4 v[120:123], v1, s[24:27], s66 offen offset:2048 sc1\n"
"buffer_load_dwordx4 v[124:127], v1, s[24:27], s66 offen offset:3072 sc1\n"
"s_add_u32 s66, s56, 40960\n"
"buffer_load_dwordx4 v[128:131], v1, s[24:27], s66 offen sc1\n"
"buffer_load_dwordx4 v[132:135], v1, s[24:27], s66 offen offset:1024 sc1\n"
"buffer_load_dwordx4 v[136:139], v1, s[24:27], s66 offen offset:2048 sc1\n"
"buffer_load_dwordx4 v[140:143], v1, s[24:27], s66 offen offset:3072 sc1\n"
"s_add_u32 s66, s56, 61440\n"
"buffer_load_dwordx4 v[144:147], v1, s[24:27], s66 offen sc1\n"
"buffer_load_dwordx4 v[148:151], v1, s[24:27], s66 offen offset:1024 sc1\n"
"buffer_load_dwordx4 v[152:155], v1, s[24:27], s66 offen offset:2048 sc1\n"
"buffer_load_dwordx4 v[156:159], v1, s[24:27], s66 offen offset:3072 sc1\n"
"s_mov_b64 exec, 1\n"
"global_store_dwordx2 v21, v[18:19], s[60:61] offset:8 sc1\n"
"s_mov_b64 exec, -1\n"
"s_barrier\n"
"ds_read_b64_tr_b16 a[192:193], v4 offset:49152\n"
"ds_read_b64_tr_b16 a[194:195], v4 offset:51200\n"
"ds_read_b64_tr_b16 a[196:197], v5 offset:49152\n"
"ds_read_b64_tr_b16 a[198:199], v5 offset:51200\n"
"ds_read_b64_tr_b16 a[200:201], v4 offset:49664\n"
"ds_read_b64_tr_b16 a[202:203], v4 offset:51712\n"
"ds_read_b64_tr_b16 a[204:205], v5 offset:49664\n"
"ds_read_b64_tr_b16 a[206:207], v5 offset:51712\n"
"s_waitcnt vmcnt(25)\n"
"ds_read_b64_tr_b16 a[208:209], v8\n"
"ds_read_b64_tr_b16 a[210:211], v8 offset:2048\n"
"ds_read_b64_tr_b16 a[212:213], v9\n"
"ds_read_b64_tr_b16 a[214:215], v9 offset:2048\n"
"ds_read_b64_tr_b16 a[216:217], v8 offset:512\n"
"ds_read_b64_tr_b16 a[218:219], v8 offset:2560\n"
"ds_read_b64_tr_b16 a[220:221], v9 offset:512\n"
"ds_read_b64_tr_b16 a[222:223], v9 offset:2560\n"
"s_waitcnt vmcnt(21)\n"
"ds_read_b64_tr_b16 a[224:225], v8 offset:16384\n"
"ds_read_b64_tr_b16 a[226:227], v8 offset:18432\n"
"ds_read_b64_tr_b16 a[228:229], v9 offset:16384\n"
"ds_read_b64_tr_b16 a[230:231], v9 offset:18432\n"
"ds_read_b64_tr_b16 a[232:233], v8 offset:16896\n"
"ds_read_b64_tr_b16 a[234:235], v8 offset:18944\n"
"ds_read_b64_tr_b16 a[236:237], v9 offset:16896\n"
"ds_read_b64_tr_b16 a[238:239], v9 offset:18944\n"
"s_waitcnt vmcnt(17)\n"
"ds_read_b64_tr_b16 a[240:241], v4\n"
"ds_read_b64_tr_b16 a[242:243], v4 offset:2048\n"
"ds_read_b64_tr_b16 a[244:245], v5\n"
"ds_read_b64_tr_b16 a[246:247], v5 offset:2048\n"
"ds_read_b64_tr_b16 a[248:249], v4 offset:512\n"
"ds_read_b64_tr_b16 a[250:251], v4 offset:2560\n"
"ds_read_b64_tr_b16 a[252:253], v5 offset:512\n"
"ds_read_b64_tr_b16 a[254:255], v5 offset:2560\n"
"s_waitcnt vmcnt(13)\n"
"v_mfma_f32_16x16x32_bf16 v[208:211], a[0:3], v[96:99], 0\n"
"s_add_u32 s66, s56, 4096\n"
"v_mfma_f32_16x16x32_bf16 v[208:211], a[16:19], v[100:103], v[208:211]\n"
"buffer_load_dwordx4 v[160:163], v1, s[24:27], s66 offen sc1\n"
"v_mfma_f32_16x16x32_bf16 v[208:211], a[32:35], v[104:107], v[208:211]\n"
"buffer_load_dwordx4 v[164:167], v1, s[24:27], s66 offen offset:1024 sc1\n"
"v_mfma_f32_16x16x32_bf16 v[208:211], a[48:51], v[108:111], v[208:211]\n"
"buffer_load_dwordx4 v[168:171], v1, s[24:27], s66 offen offset:2048 sc1\n"
"v_mfma_f32_16x16x32_bf16 v[212:215], a[4:7], v[96:99], 0\n"
"buffer_load_dwordx4 v[172:175], v1, s[24:27], s66 offen offset:3072 sc1\n"
"v_mfma_f32_16x16x32_bf16 v[212:215], a[20:23], v[100:103], v[212:215]\n"
"v_and_b32 v22, 4, v0\n"
"v_lshlrev_b32 v22, 10, v22\n"
"v_mfma_f32_16x16x32_bf16 v[212:215], a[36:39], v[104:107], v[212:215]\n"
"v_xor_b32 v22, v22, v15\n"
"v_and_b32 v24, 15, v0\n"
"v_mfma_f32_16x16x32_bf16 v[212:215], a[52:55], v[108:111], v[212:215]\n"
"v_lshrrev_b32 v25, 4, v0\n"
"v_xor_b32 v25, v25, v24\n"
"v_mfma_f32_16x16x32_bf16 v[216:219], a[8:11], v[96:99], 0\n"
"v_lshlrev_b32 v25, 4, v25\n"
"v_lshl_add_u32 v240, v24, 8, v25\n"
"v_mfma_f32_16x16x32_bf16 v[216:219], a[24:27], v[100:103], v[216:219]\n"
"v_add_u32 v240, s58, v240\n"
"v_xor_b32 v241, 64, v240\n"
"v_mfma_f32_16x16x32_bf16 v[216:219], a[40:43], v[104:107], v[216:219]\n"
"v_xor_b32 v242, 128, v240\n"
"v_xor_b32 v243, 192, v240\n"
"v_mfma_f32_16x16x32_bf16 v[216:219], a[56:59], v[108:111], v[216:219]\n"
"s_cmp_eq_u32 s69, 2\n"
"s_cselect_b32 s52, 0x40000000, 0\n"
"v_mfma_f32_16x16x32_bf16 v[220:223], a[12:15], v[96:99], 0\n"
"v_add_u32 v14, s52, v1\n"
"v_mfma_f32_16x16x32_bf16 v[220:223], a[28:31], v[100:103], v[220:223]\n"
"s_cmp_eq_u32 s69, 3\n"
"s_cselect_b32 s52, 0x40000000, 0\n"
"v_mfma_f32_16x16x32_bf16 v[220:223], a[44:47], v[104:107], v[220:223]\n"
"v_add_u32 v16, s52, v1\n"
"s_mov_b64 s[20:21], s[14:15]\n"
"v_mfma_f32_16x16x32_bf16 v[220:223], a[60:63], v[108:111], v[220:223]\n"
"s_waitcnt vmcnt(13)\n"
"v_mfma_f32_16x16x32_bf16 v[208:211], a[64:67], v[112:115], v[208:211]\n"
"s_add_u32 s66, s56, 24576\n"
"v_mfma_f32_16x16x32_bf16 v[208:211], a[80:83], v[116:119], v[208:211]\n"
"buffer_load_dwordx4 v[96:99], v1, s[24:27], s66 offen sc1\n"
"v_mfma_f32_16x16x32_bf16 v[208:211], a[96:99], v[120:123], v[208:211]\n"
"buffer_load_dwordx4 v[100:103], v1, s[24:27], s66 offen offset:1024 sc1\n"
"v_mfma_f32_16x16x32_bf16 v[208:211], a[112:115], v[124:127], v[208:211]\n"
"buffer_load_dwordx4 v[104:107], v1, s[24:27], s66 offen offset:2048 sc1\n"
"v_mfma_f32_16x16x32_bf16 v[212:215], a[68:71], v[112:115], v[212:215]\n"
"buffer_load_dwordx4 v[108:111], v1, s[24:27], s66 offen offset:3072 sc1\n"
"v_mfma_f32_16x16x32_bf16 v[212:215], a[84:87], v[116:119], v[212:215]\n"
"s_mul_i32 s52, s49, 311296\n"
"v_mfma_f32_16x16x32_bf16 v[212:215], a[100:103], v[120:123], v[212:215]\n"
"s_add_u32 s20, s20, s52\n"
"s_addc_u32 s21, s21, 0\n"
"v_mfma_f32_16x16x32_bf16 v[212:215], a[116:119], v[124:127], v[212:215]\n"
"s_and_b32 s21, s21, 0xffff\n"
"s_lshl_b32 s52, s49, 5\n"
"v_mfma_f32_16x16x32_bf16 v[216:219], a[72:75], v[112:115], v[216:219]\n"
"s_xor_b32 s53, s50, 0\n"
"s_add_u32 s52, s52, s53\n"
"v_mfma_f32_16x16x32_bf16 v[216:219], a[88:91], v[116:119], v[216:219]\n"
"s_mul_i32 s52, s52, 20480\n"
"s_add_u32 s52, s52, 27262976\n"
"v_mfma_f32_16x16x32_bf16 v[216:219], a[104:107], v[120:123], v[216:219]\n"
"s_add_u32 s36, s16, s52\n"
"s_addc_u32 s37, s17, 0\n"
"v_mfma_f32_16x16x32_bf16 v[216:219], a[120:123], v[124:127], v[216:219]\n"
"s_and_b32 s37, s37, 0xffff\n"
"s_mov_b32 s38, 20480\n"
"v_mfma_f32_16x16x32_bf16 v[220:223], a[76:79], v[112:115], v[220:223]\n"
"s_mov_b32 s39, 0x20000\n"
"s_lshl_b32 s52, s49, 5\n"
"v_mfma_f32_16x16x32_bf16 v[220:223], a[92:95], v[116:119], v[220:223]\n"
"s_xor_b32 s53, s50, 16\n"
"s_add_u32 s52, s52, s53\n"
"v_mfma_f32_16x16x32_bf16 v[220:223], a[108:111], v[120:123], v[220:223]\n"
"s_mul_i32 s52, s52, 20480\n"
"s_add_u32 s52, s52, 27262976\n"
"v_mfma_f32_16x16x32_bf16 v[220:223], a[124:127], v[124:127], v[220:223]\n"
"s_waitcnt vmcnt(13) lgkmcnt(15)\n"
"v_mfma_f32_16x16x32_bf16 v[208:211], a[128:131], v[128:131], v[208:211]\n"
"s_add_u32 s66, s56, 45056\n"
"v_mfma_f32_16x16x32_bf16 v[208:211], a[144:147], v[132:135], v[208:211]\n"
"buffer_load_dwordx4 v[112:115], v1, s[24:27], s66 offen sc1\n"
"v_mfma_f32_16x16x32_bf16 v[208:211], a[160:163], v[136:139], v[208:211]\n"
"buffer_load_dwordx4 v[116:119], v1, s[24:27], s66 offen offset:1024 sc1\n"
"v_mfma_f32_16x16x32_bf16 v[208:211], a[176:179], v[140:143], v[208:211]\n"
"buffer_load_dwordx4 v[120:123], v1, s[24:27], s66 offen offset:2048 sc1\n"
"v_mfma_f32_16x16x32_bf16 v[212:215], a[132:135], v[128:131], v[212:215]\n"
"buffer_load_dwordx4 v[124:127], v1, s[24:27], s66 offen offset:3072 sc1\n"
"v_mfma_f32_16x16x32_bf16 v[212:215], a[148:151], v[132:135], v[212:215]\n"
"s_add_u32 s28, s16, s52\n"
"s_addc_u32 s29, s17, 0\n"
"v_mfma_f32_16x16x32_bf16 v[212:215], a[164:167], v[136:139], v[212:215]\n"
"s_and_b32 s29, s29, 0xffff\n"
"s_mov_b32 s30, 20480\n"
"v_mfma_f32_16x16x32_bf16 v[212:215], a[180:183], v[140:143], v[212:215]\n"
"s_mov_b32 s31, 0x20000\n"
"s_lshl_b32 s52, s48, 11\n"
"v_mfma_f32_16x16x32_bf16 v[216:219], a[136:139], v[128:131], v[216:219]\n"
"v_add_u32 v17, s52, v1\n"
"v_xor_b32 v20, 2048, v21\n"
"v_mfma_f32_16x16x32_bf16 v[216:219], a[152:155], v[132:135], v[216:219]\n"
"s_and_b32 s52, s48, 1\n"
"s_lshl_b32 s52, s52, 3\n"
"v_mfma_f32_16x16x32_bf16 v[216:219], a[168:171], v[136:139], v[216:219]\n"
"v_lshrrev_b32 v24, 3, v0\n"
"v_add_u32 v25, s52, v24\n"
"v_mfma_f32_16x16x32_bf16 v[216:219], a[184:187], v[140:143], v[216:219]\n"
"v_and_b32 v246, 7, v0\n"
"v_lshlrev_b32 v244, 1, v246\n"
"v_mfma_f32_16x16x32_bf16 v[220:223], a[140:143], v[128:131], v[220:223]\n"
"v_xor_b32 v244, v244, v25\n"
"v_lshlrev_b32 v244, 4, v244\n"
"v_mfma_f32_16x16x32_bf16 v[220:223], a[156:159], v[132:135], v[220:223]\n"
"v_lshl_add_u32 v244, v25, 8, v244\n"
"s_lshr_b32 s52, s48, 1\n"
"v_mfma_f32_16x16x32_bf16 v[220:223], a[172:175], v[136:139], v[220:223]\n"
"s_lshl_b32 s52, s52, 14\n"
"v_add_u32 v244, s52, v244\n"
"v_mfma_f32_16x16x32_bf16 v[220:223], a[188:191], v[140:143], v[220:223]\n"
"s_waitcnt vmcnt(13) lgkmcnt(0)\n"
"v_mfma_f32_16x16x32_bf16 v[208:211], a[192:195], v[144:147], v[208:211]\n"
"s_add_u32 s66, s56, 65536\n"
"v_mfma_f32_16x16x32_bf16 v[208:211], a[208:211], v[148:151], v[208:211]\n"
"buffer_load_dwordx4 v[128:131], v1, s[24:27], s66 offen sc1\n"
"v_mfma_f32_16x16x32_bf16 v[208:211], a[224:227], v[152:155], v[208:211]\n"
"buffer_load_dwordx4 v[132:135], v1, s[24:27], s66 offen offset:1024 sc1\n"
"v_mfma_f32_16x16x32_bf16 v[208:211], a[240:243], v[156:159], v[208:211]\n"
"buffer_load_dwordx4 v[136:139], v1, s[24:27], s66 offen offset:2048 sc1\n"
"v_mfma_f32_16x16x32_bf16 v[212:215], a[196:199], v[144:147], v[212:215]\n"
"buffer_load_dwordx4 v[140:143], v1, s[24:27], s66 offen offset:3072 sc1\n"
"v_mfma_f32_16x16x32_bf16 v[212:215], a[212:215], v[148:151], v[212:215]\n"
"v_xor_b32 v245, 16, v244\n"
"v_lshlrev_b32 v246, 4, v246\n"
"v_mfma_f32_16x16x32_bf16 v[212:215], a[228:231], v[152:155], v[212:215]\n"
"v_lshl_add_u32 v246, v24, 10, v246\n"
"s_mul_i32 s54, s69, 81920\n"
"v_mfma_f32_16x16x32_bf16 v[212:215], a[244:247], v[156:159], v[212:215]\n"
"s_lshl_b32 s52, s48, 13\n"
"s_add_u32 s54, s54, s52\n"
"v_mfma_f32_16x16x32_bf16 v[216:219], a[200:203], v[144:147], v[216:219]\n"
"s_and_b32 s52, s50, 7\n"
"s_lshl_b32 s52, s52, 7\n"
"v_mfma_f32_16x16x32_bf16 v[216:219], a[216:219], v[148:151], v[216:219]\n"
"s_add_u32 s54, s54, s52\n"
"v_mfma_f32_16x16x32_bf16 v[216:219], a[232:235], v[152:155], v[216:219]\n"
"v_mfma_f32_16x16x32_bf16 v[216:219], a[248:251], v[156:159], v[216:219]\n"
"v_mfma_f32_16x16x32_bf16 v[220:223], a[204:207], v[144:147], v[220:223]\n"
"v_mfma_f32_16x16x32_bf16 v[220:223], a[220:223], v[148:151], v[220:223]\n"
"v_mfma_f32_16x16x32_bf16 v[220:223], a[236:239], v[152:155], v[220:223]\n"
"v_mfma_f32_16x16x32_bf16 v[220:223], a[252:255], v[156:159], v[220:223]\n"
"s_waitcnt vmcnt(12)\n"
"v_mfma_f32_16x16x32_bf16 v[224:227], a[0:3], v[160:163], 0\n"
"s_add_u32 s66, s56, 8192\n"
"v_mfma_f32_16x16x32_bf16 v[224:227], a[16:19], v[164:167], v[224:227]\n"
"buffer_load_dwordx4 v[144:147], v1, s[24:27], s66 offen sc1\n"
"v_mfma_f32_16x16x32_bf16 v[224:227], a[32:35], v[168:171], v[224:227]\n"
"buffer_load_dwordx4 v[148:151], v1, s[24:27], s66 offen offset:1024 sc1\n"
"v_mfma_f32_16x16x32_bf16 v[224:227], a[48:51], v[172:175], v[224:227]\n"
"ds_write_b128 v240, v[208:211]\n"
"v_mfma_f32_16x16x32_bf16 v[228:231], a[4:7], v[160:163], 0\n"
"buffer_load_dwordx4 v[152:155], v1, s[24:27], s66 offen offset:2048 sc1\n"
"v_mfma_f32_16x16x32_bf16 v[228:231], a[20:23], v[164:167], v[228:231]\n"
"buffer_load_dwordx4 v[156:159], v1, s[24:27], s66 offen offset:3072 sc1\n"
"v_mfma_f32_16x16x32_bf16 v[228:231], a[36:39], v[168:171], v[228:231]\n"
"global_load_dwordx2 v[2:3], v22, s[60:61] offset:8 sc1\n"
"v_mfma_f32_16x16x32_bf16 v[228:231], a[52:55], v[172:175], v[228:231]\n"
"ds_write_b128 v241, v[212:215]\n"
"v_mfma_f32_16x16x32_bf16 v[232:235], a[8:11], v[160:163], 0\n"
"v_mfma_f32_16x16x32_bf16 v[232:235], a[24:27], v[164:167], v[232:235]\n"
"v_mfma_f32_16x16x32_bf16 v[232:235], a[40:43], v[168:171], v[232:235]\n"
"v_mfma_f32_16x16x32_bf16 v[232:235], a[56:59], v[172:175], v[232:235]\n"
"ds_write_b128 v242, v[216:219]\n"
"v_mfma_f32_16x16x32_bf16 v[236:239], a[12:15], v[160:163], 0\n"
"v_mfma_f32_16x16x32_bf16 v[236:239], a[28:31], v[164:167], v[236:239]\n"
"v_mfma_f32_16x16x32_bf16 v[236:239], a[44:47], v[168:171], v[236:239]\n"
"v_mfma_f32_16x16x32_bf16 v[236:239], a[60:63], v[172:175], v[236:239]\n"
"ds_write_b128 v243, v[220:223]\n"
"s_waitcnt vmcnt(13)\n"
"v_mfma_f32_16x16x32_bf16 v[224:227], a[64:67], v[96:99], v[224:227]\n"
"s_add_u32 s66, s56, 28672\n"
"v_mfma_f32_16x16x32_bf16 v[224:227], a[80:83], v[100:103], v[224:227]\n"
"buffer_load_dwordx4 v[160:163], v1, s[24:27], s66 offen sc1\n"
"v_mfma_f32_16x16x32_bf16 v[224:227], a[96:99], v[104:107], v[224:227]\n"
"buffer_load_dwordx4 v[164:167], v1, s[24:27], s66 offen offset:1024 sc1\n"
"v_mfma_f32_16x16x32_bf16 v[224:227], a[112:115], v[108:111], v[224:227]\n"
"buffer_load_dwordx4 v[168:171], v1, s[24:27], s66 offen offset:2048 sc1\n"
"v_mfma_f32_16x16x32_bf16 v[228:231], a[68:71], v[96:99], v[228:231]\n"
"buffer_load_dwordx4 v[172:175], v1, s[24:27], s66 offen offset:3072 sc1\n"
"v_mfma_f32_16x16x32_bf16 v[228:231], a[84:87], v[100:103], v[228:231]\n"
"v_mfma_f32_16x16x32_bf16 v[228:231], a[100:103], v[104:107], v[228:231]\n"
"v_mfma_f32_16x16x32_bf16 v[228:231], a[116:119], v[108:111], v[228:231]\n"
"v_mfma_f32_16x16x32_bf16 v[232:235], a[72:75], v[96:99], v[232:235]\n"
"v_mfma_f32_16x16x32_bf16 v[232:235], a[88:91], v[100:103], v[232:235]\n"
"v_mfma_f32_16x16x32_bf16 v[232:235], a[104:107], v[104:107], v[232:235]\n"
"v_mfma_f32_16x16x32_bf16 v[232:235], a[120:123], v[108:111], v[232:235]\n"
"v_mfma_f32_16x16x32_bf16 v[236:239], a[76:79], v[96:99], v[236:239]\n"
"v_mfma_f32_16x16x32_bf16 v[236:239], a[92:95], v[100:103], v[236:239]\n"
"v_mfma_f32_16x16x32_bf16 v[236:239], a[108:111], v[104:107], v[236:239]\n"
"v_mfma_f32_16x16x32_bf16 v[236:239], a[124:127], v[108:111], v[236:239]\n"
"s_waitcnt vmcnt(13)\n"
"v_mfma_f32_16x16x32_bf16 v[224:227], a[128:131], v[112:115], v[224:227]\n"
"s_add_u32 s66, s56, 49152\n"
"v_mfma_f32_16x16x32_bf16 v[224:227], a[144:147], v[116:119], v[224:227]\n"
"buffer_load_dwordx4 v[96:99], v1, s[24:27], s66 offen sc1\n"
"v_mfma_f32_16x16x32_bf16 v[224:227], a[160:163], v[120:123], v[224:227]\n"
"buffer_load_dwordx4 v[100:103], v1, s[24:27], s66 offen offset:1024 sc1\n"
"v_mfma_f32_16x16x32_bf16 v[224:227], a[176:179], v[124:127], v[224:227]\n"
"buffer_load_dwordx4 v[104:107], v1, s[24:27], s66 offen offset:2048 sc1\n"
"v_mfma_f32_16x16x32_bf16 v[228:231], a[132:135], v[112:115], v[228:231]\n"
"buffer_load_dwordx4 v[108:111], v1, s[24:27], s66 offen offset:3072 sc1\n"
"v_mfma_f32_16x16x32_bf16 v[228:231], a[148:151], v[116:119], v[228:231]\n"
"v_mfma_f32_16x16x32_bf16 v[228:231], a[164:167], v[120:123], v[228:231]\n"
"v_mfma_f32_16x16x32_bf16 v[228:231], a[180:183], v[124:127], v[228:231]\n"
"v_mfma_f32_16x16x32_bf16 v[232:235], a[136:139], v[112:115], v[232:235]\n"
"v_mfma_f32_16x16x32_bf16 v[232:235], a[152:155], v[116:119], v[232:235]\n"
"v_mfma_f32_16x16x32_bf16 v[232:235], a[168:171], v[120:123], v[232:235]\n"
"v_mfma_f32_16x16x32_bf16 v[232:235], a[184:187], v[124:127], v[232:235]\n"
"v_mfma_f32_16x16x32_bf16 v[236:239], a[140:143], v[112:115], v[236:239]\n"
"v_mfma_f32_16x16x32_bf16 v[236:239], a[156:159], v[116:119], v[236:239]\n"
"v_mfma_f32_16x16x32_bf16 v[236:239], a[172:175], v[120:123], v[236:239]\n"
"v_mfma_f32_16x16x32_bf16 v[236:239], a[188:191], v[124:127], v[236:239]\n"
"s_waitcnt vmcnt(13)\n"
"v_mfma_f32_16x16x32_bf16 v[224:227], a[192:195], v[128:131], v[224:227]\n"
"s_add_u32 s66, s56, 69632\n"
"v_mfma_f32_16x16x32_bf16 v[224:227], a[208:211], v[132:135], v[224:227]\n"
"buffer_load_dwordx4 v[112:115], v1, s[24:27], s66 offen sc1\n"
"v_mfma_f32_16x16x32_bf16 v[224:227], a[224:227], v[136:139], v[224:227]\n"
"buffer_load_dwordx4 v[116:119], v1, s[24:27], s66 offen offset:1024 sc1\n"
"v_mfma_f32_16x16x32_bf16 v[224:227], a[240:243], v[140:143], v[224:227]\n"
"buffer_load_dwordx4 v[120:123], v1, s[24:27], s66 offen offset:2048 sc1\n"
"v_mfma_f32_16x16x32_bf16 v[228:231], a[196:199], v[128:131], v[228:231]\n"
"buffer_load_dwordx4 v[124:127], v1, s[24:27], s66 offen offset:3072 sc1\n"
"v_mfma_f32_16x16x32_bf16 v[228:231], a[212:215], v[132:135], v[228:231]\n"
"v_mfma_f32_16x16x32_bf16 v[228:231], a[228:231], v[136:139], v[228:231]\n"
"v_mfma_f32_16x16x32_bf16 v[228:231], a[244:247], v[140:143], v[228:231]\n"
"v_mfma_f32_16x16x32_bf16 v[232:235], a[200:203], v[128:131], v[232:235]\n"
"v_mfma_f32_16x16x32_bf16 v[232:235], a[216:219], v[132:135], v[232:235]\n"
"v_mfma_f32_16x16x32_bf16 v[232:235], a[232:235], v[136:139], v[232:235]\n"
"v_mfma_f32_16x16x32_bf16 v[232:235], a[248:251], v[140:143], v[232:235]\n"
"v_mfma_f32_16x16x32_bf16 v[236:239], a[204:207], v[128:131], v[236:239]\n"
"v_mfma_f32_16x16x32_bf16 v[236:239], a[220:223], v[132:135], v[236:239]\n"
"v_mfma_f32_16x16x32_bf16 v[236:239], a[236:239], v[136:139], v[236:239]\n"
"v_mfma_f32_16x16x32_bf16 v[236:239], a[252:255], v[140:143], v[236:239]\n"
"s_waitcnt vmcnt(12)\n"
"s_mov_b32 s63, 262144\n"
".Lmha_pollb:\n"
"v_cmp_ne_u64 vcc, s[70:71], v[2:3]\n"
"s_nop 3\n"
"s_cmp_eq_u64 vcc, 0\n"
"s_cbranch_scc1 .Lmha_pollbok\n"
"s_sleep 1\n"
"global_load_dwordx2 v[2:3], v22, s[60:61] offset:8 sc1\n"
"s_sub_u32 s63, s63, 1\n"
"s_waitcnt vmcnt(0)\n"
"s_cbranch_scc0 .Lmha_pollb\n"
".Lmha_pollbok:\n"
"v_mfma_f32_16x16x32_bf16 v[208:211], a[0:3], v[144:147], 0\n"
"s_add_u32 s66, s56, 12288\n"
"v_mfma_f32_16x16x32_bf16 v[208:211], a[16:19], v[148:151], v[208:211]\n"
"buffer_load_dwordx4 v[128:131], v1, s[24:27], s66 offen sc1\n"
"v_mfma_f32_16x16x32_bf16 v[208:211], a[32:35], v[152:155], v[208:211]\n"
"buffer_load_dwordx4 v[132:135], v1, s[24:27], s66 offen offset:1024 sc1\n"
"v_mfma_f32_16x16x32_bf16 v[208:211], a[48:51], v[156:159], v[208:211]\n"
"ds_write_b128 v240, v[224:227] offset:16384\n"
"v_mfma_f32_16x16x32_bf16 v[212:215], a[4:7], v[144:147], 0\n"
"buffer_load_dwordx4 v[136:139], v1, s[24:27], s66 offen offset:2048 sc1\n"
"v_mfma_f32_16x16x32_bf16 v[212:215], a[20:23], v[148:151], v[212:215]\n"
"buffer_load_dwordx4 v[140:143], v1, s[24:27], s66 offen offset:3072 sc1\n"
"v_mfma_f32_16x16x32_bf16 v[212:215], a[36:39], v[152:155], v[212:215]\n"
"v_mfma_f32_16x16x32_bf16 v[212:215], a[52:55], v[156:159], v[212:215]\n"
"ds_write_b128 v241, v[228:231] offset:16384\n"
"v_mfma_f32_16x16x32_bf16 v[216:219], a[8:11], v[144:147], 0\n"
"v_mfma_f32_16x16x32_bf16 v[216:219], a[24:27], v[148:151], v[216:219]\n"
"v_mfma_f32_16x16x32_bf16 v[216:219], a[40:43], v[152:155], v[216:219]\n"
"v_mfma_f32_16x16x32_bf16 v[216:219], a[56:59], v[156:159], v[216:219]\n"
"ds_write_b128 v242, v[232:235] offset:16384\n"
"v_mfma_f32_16x16x32_bf16 v[220:223], a[12:15], v[144:147], 0\n"
"v_mfma_f32_16x16x32_bf16 v[220:223], a[28:31], v[148:151], v[220:223]\n"
"v_mfma_f32_16x16x32_bf16 v[220:223], a[44:47], v[152:155], v[220:223]\n"
"v_mfma_f32_16x16x32_bf16 v[220:223], a[60:63], v[156:159], v[220:223]\n"
"ds_write_b128 v243, v[236:239] offset:16384\n"
"s_waitcnt vmcnt(12)\n"
"v_mfma_f32_16x16x32_bf16 v[208:211], a[64:67], v[160:163], v[208:211]\n"
"s_add_u32 s66, s56, 32768\n"
"v_mfma_f32_16x16x32_bf16 v[208:211], a[80:83], v[164:167], v[208:211]\n"
"buffer_load_dwordx4 v[144:147], v1, s[24:27], s66 offen sc1\n"
"v_mfma_f32_16x16x32_bf16 v[208:211], a[96:99], v[168:171], v[208:211]\n"
"buffer_load_dwordx4 v[148:151], v1, s[24:27], s66 offen offset:1024 sc1\n"
"v_mfma_f32_16x16x32_bf16 v[208:211], a[112:115], v[172:175], v[208:211]\n"
"buffer_load_dwordx4 v[152:155], v1, s[24:27], s66 offen offset:2048 sc1\n"
"v_mfma_f32_16x16x32_bf16 v[212:215], a[68:71], v[160:163], v[212:215]\n"
"buffer_load_dwordx4 v[156:159], v1, s[24:27], s66 offen offset:3072 sc1\n"
"v_mfma_f32_16x16x32_bf16 v[212:215], a[84:87], v[164:167], v[212:215]\n"
"s_waitcnt lgkmcnt(0)\n"
"s_barrier\n"
"v_mfma_f32_16x16x32_bf16 v[212:215], a[100:103], v[168:171], v[212:215]\n"
"ds_read_b128 v[24:27], v244\n"
"ds_read_b128 v[28:31], v245\n"
"v_mfma_f32_16x16x32_bf16 v[212:215], a[116:119], v[172:175], v[212:215]\n"
"ds_read_b128 v[32:35], v244 offset:4096\n"
"ds_read_b128 v[36:39], v245 offset:4096\n"
"v_mfma_f32_16x16x32_bf16 v[216:219], a[72:75], v[160:163], v[216:219]\n"
"ds_read_b128 v[40:43], v244 offset:8192\n"
"ds_read_b128 v[44:47], v245 offset:8192\n"
"v_mfma_f32_16x16x32_bf16 v[216:219], a[88:91], v[164:167], v[216:219]\n"
"ds_read_b128 v[48:51], v244 offset:12288\n"
"ds_read_b128 v[52:55], v245 offset:12288\n"
"v_mfma_f32_16x16x32_bf16 v[216:219], a[104:107], v[168:171], v[216:219]\n"
"v_mfma_f32_16x16x32_bf16 v[216:219], a[120:123], v[172:175], v[216:219]\n"
"v_mfma_f32_16x16x32_bf16 v[220:223], a[76:79], v[160:163], v[220:223]\n"
"v_mfma_f32_16x16x32_bf16 v[220:223], a[92:95], v[164:167], v[220:223]\n"
"v_mfma_f32_16x16x32_bf16 v[220:223], a[108:111], v[168:171], v[220:223]\n"
"v_mfma_f32_16x16x32_bf16 v[220:223], a[124:127], v[172:175], v[220:223]\n"
"s_waitcnt vmcnt(12)\n"
"v_mfma_f32_16x16x32_bf16 v[208:211], a[128:131], v[96:99], v[208:211]\n"
"s_waitcnt lgkmcnt(0)\n"
"v_mfma_f32_16x16x32_bf16 v[208:211], a[144:147], v[100:103], v[208:211]\n"
"s_add_u32 s66, s56, 53248\n"
"v_mfma_f32_16x16x32_bf16 v[208:211], a[160:163], v[104:107], v[208:211]\n"
"buffer_load_dwordx4 v[160:163], v1, s[24:27], s66 offen sc1\n"
"v_mfma_f32_16x16x32_bf16 v[208:211], a[176:179], v[108:111], v[208:211]\n"
"buffer_load_dwordx4 v[164:167], v1, s[24:27], s66 offen offset:1024 sc1\n"
"v_mfma_f32_16x16x32_bf16 v[212:215], a[132:135], v[96:99], v[212:215]\n"
"buffer_load_dwordx4 v[168:171], v1, s[24:27], s66 offen offset:2048 sc1\n"
"v_mfma_f32_16x16x32_bf16 v[212:215], a[148:151], v[100:103], v[212:215]\n"
"buffer_load_dwordx4 v[172:175], v1, s[24:27], s66 offen offset:3072 sc1\n"
"v_mfma_f32_16x16x32_bf16 v[212:215], a[164:167], v[104:107], v[212:215]\n"
"v_add_f32 v24, v24, v32\n"
"v_add_f32 v25, v25, v33\n"
"v_mfma_f32_16x16x32_bf16 v[212:215], a[180:183], v[108:111], v[212:215]\n"
"v_add_f32 v26, v26, v34\n"
"v_add_f32 v27, v27, v35\n"
"v_mfma_f32_16x16x32_bf16 v[216:219], a[136:139], v[96:99], v[216:219]\n"
"v_add_f32 v28, v28, v36\n"
"v_add_f32 v29, v29, v37\n"
"v_mfma_f32_16x16x32_bf16 v[216:219], a[152:155], v[100:103], v[216:219]\n"
"v_add_f32 v30, v30, v38\n"
"v_add_f32 v31, v31, v39\n"
"v_mfma_f32_16x16x32_bf16 v[216:219], a[168:171], v[104:107], v[216:219]\n"
"v_add_f32 v40, v40, v48\n"
"v_add_f32 v41, v41, v49\n"
"v_mfma_f32_16x16x32_bf16 v[216:219], a[184:187], v[108:111], v[216:219]\n"
"v_add_f32 v42, v42, v50\n"
"v_add_f32 v43, v43, v51\n"
"v_mfma_f32_16x16x32_bf16 v[220:223], a[140:143], v[96:99], v[220:223]\n"
"v_add_f32 v44, v44, v52\n"
"v_add_f32 v45, v45, v53\n"
"v_mfma_f32_16x16x32_bf16 v[220:223], a[156:159], v[100:103], v[220:223]\n"
"v_add_f32 v46, v46, v54\n"
"v_add_f32 v47, v47, v55\n"
"v_mfma_f32_16x16x32_bf16 v[220:223], a[172:175], v[104:107], v[220:223]\n"
"v_add_f32 v72, v24, v40\n"
"v_add_f32 v73, v25, v41\n"
"v_mfma_f32_16x16x32_bf16 v[220:223], a[188:191], v[108:111], v[220:223]\n"
"v_add_f32 v74, v26, v42\n"
"v_add_f32 v75, v27, v43\n"
"s_waitcnt vmcnt(12)\n"
"v_mfma_f32_16x16x32_bf16 v[208:211], a[192:195], v[112:115], v[208:211]\n"
"s_add_u32 s66, s56, 73728\n"
"v_mfma_f32_16x16x32_bf16 v[208:211], a[208:211], v[116:119], v[208:211]\n"
"buffer_load_dwordx4 v[96:99], v1, s[24:27], s66 offen sc1\n"
"v_mfma_f32_16x16x32_bf16 v[208:211], a[224:227], v[120:123], v[208:211]\n"
"buffer_load_dwordx4 v[100:103], v1, s[24:27], s66 offen offset:1024 sc1\n"
"v_mfma_f32_16x16x32_bf16 v[208:211], a[240:243], v[124:127], v[208:211]\n"
"buffer_load_dwordx4 v[104:107], v1, s[24:27], s66 offen offset:2048 sc1\n"
"v_mfma_f32_16x16x32_bf16 v[212:215], a[196:199], v[112:115], v[212:215]\n"
"buffer_load_dwordx4 v[108:111], v1, s[24:27], s66 offen offset:3072 sc1\n"
"v_mfma_f32_16x16x32_bf16 v[212:215], a[212:215], v[116:119], v[212:215]\n"
"v_add_f32 v76, v28, v44\n"
"v_add_f32 v77, v29, v45\n"
"v_mfma_f32_16x16x32_bf16 v[212:215], a[228:231], v[120:123], v[212:215]\n"
"v_add_f32 v78, v30, v46\n"
"v_add_f32 v79, v31, v47\n"
"v_mfma_f32_16x16x32_bf16 v[212:215], a[244:247], v[124:127], v[212:215]\n"
"v_add_u32 v244, 32768, v244\n"
"v_add_u32 v245, 32768, v245\n"
"v_mfma_f32_16x16x32_bf16 v[216:219], a[200:203], v[112:115], v[216:219]\n"
"v_mfma_f32_16x16x32_bf16 v[216:219], a[216:219], v[116:119], v[216:219]\n"
"v_mfma_f32_16x16x32_bf16 v[216:219], a[232:235], v[120:123], v[216:219]\n"
"s_mov_b32 s55, 0\n"
"v_mfma_f32_16x16x32_bf16 v[216:219], a[248:251], v[124:127], v[216:219]\n"
"buffer_store_dwordx4 v[72:75], v17, s[28:31], s55 offen sc1\n"
"v_mfma_f32_16x16x32_bf16 v[220:223], a[204:207], v[112:115], v[220:223]\n"
"v_mfma_f32_16x16x32_bf16 v[220:223], a[220:223], v[116:119], v[220:223]\n"
"buffer_store_dwordx4 v[76:79], v17, s[28:31], s55 offen offset:1024 sc1\n"
"v_mfma_f32_16x16x32_bf16 v[220:223], a[236:239], v[120:123], v[220:223]\n"
"v_mfma_f32_16x16x32_bf16 v[220:223], a[252:255], v[124:127], v[220:223]\n"
"s_waitcnt vmcnt(14)\n"
"v_mfma_f32_16x16x32_bf16 v[224:227], a[0:3], v[128:131], 0\n"
"s_add_u32 s66, s56, 16384\n"
"v_mfma_f32_16x16x32_bf16 v[224:227], a[16:19], v[132:135], v[224:227]\n"
"buffer_load_dwordx4 v[112:115], v14, s[24:27], s66 offen sc1\n"
"v_mfma_f32_16x16x32_bf16 v[224:227], a[32:35], v[136:139], v[224:227]\n"
"buffer_load_dwordx4 v[116:119], v14, s[24:27], s66 offen offset:1024 sc1\n"
"v_mfma_f32_16x16x32_bf16 v[224:227], a[48:51], v[140:143], v[224:227]\n"
"ds_write_b128 v240, v[208:211] offset:32768\n"
"v_mfma_f32_16x16x32_bf16 v[228:231], a[4:7], v[128:131], 0\n"
"buffer_load_dwordx4 v[120:123], v14, s[24:27], s66 offen offset:2048 sc1\n"
"v_mfma_f32_16x16x32_bf16 v[228:231], a[20:23], v[132:135], v[228:231]\n"
"buffer_load_dwordx4 v[124:127], v14, s[24:27], s66 offen offset:3072 sc1\n"
"v_mfma_f32_16x16x32_bf16 v[228:231], a[36:39], v[136:139], v[228:231]\n"
"v_mfma_f32_16x16x32_bf16 v[228:231], a[52:55], v[140:143], v[228:231]\n"
"ds_write_b128 v241, v[212:215] offset:32768\n"
"v_mfma_f32_16x16x32_bf16 v[232:235], a[8:11], v[128:131], 0\n"
"v_mfma_f32_16x16x32_bf16 v[232:235], a[24:27], v[132:135], v[232:235]\n"
"v_mfma_f32_16x16x32_bf16 v[232:235], a[40:43], v[136:139], v[232:235]\n"
"v_mfma_f32_16x16x32_bf16 v[232:235], a[56:59], v[140:143], v[232:235]\n"
"ds_write_b128 v242, v[216:219] offset:32768\n"
"v_mfma_f32_16x16x32_bf16 v[236:239], a[12:15], v[128:131], 0\n"
"v_mfma_f32_16x16x32_bf16 v[236:239], a[28:31], v[132:135], v[236:239]\n"
"v_mfma_f32_16x16x32_bf16 v[236:239], a[44:47], v[136:139], v[236:239]\n"
"v_mfma_f32_16x16x32_bf16 v[236:239], a[60:63], v[140:143], v[236:239]\n"
"ds_write_b128 v243, v[220:223] offset:32768\n"
"s_waitcnt vmcnt(14)\n"
"v_mfma_f32_16x16x32_bf16 v[224:227], a[64:67], v[144:147], v[224:227]\n"
"s_add_u32 s66, s56, 36864\n"
"v_mfma_f32_16x16x32_bf16 v[224:227], a[80:83], v[148:151], v[224:227]\n"
"buffer_load_dwordx4 v[128:131], v14, s[24:27], s66 offen sc1\n"
"v_mfma_f32_16x16x32_bf16 v[224:227], a[96:99], v[152:155], v[224:227]\n"
"buffer_load_dwordx4 v[132:135], v14, s[24:27], s66 offen offset:1024 sc1\n"
"v_mfma_f32_16x16x32_bf16 v[224:227], a[112:115], v[156:159], v[224:227]\n"
"buffer_load_dwordx4 v[136:139], v14, s[24:27], s66 offen offset:2048 sc1\n"
"v_mfma_f32_16x16x32_bf16 v[228:231], a[68:71], v[144:147], v[228:231]\n"
"buffer_load_dwordx4 v[140:143], v14, s[24:27], s66 offen offset:3072 sc1\n"
"v_mfma_f32_16x16x32_bf16 v[228:231], a[84:87], v[148:151], v[228:231]\n"
"v_mfma_f32_16x16x32_bf16 v[228:231], a[100:103], v[152:155], v[228:231]\n"
"v_mfma_f32_16x16x32_bf16 v[228:231], a[116:119], v[156:159], v[228:231]\n"
"v_mfma_f32_16x16x32_bf16 v[232:235], a[72:75], v[144:147], v[232:235]\n"
"v_mfma_f32_16x16x32_bf16 v[232:235], a[88:91], v[148:151], v[232:235]\n"
"v_mfma_f32_16x16x32_bf16 v[232:235], a[104:107], v[152:155], v[232:235]\n"
"v_mfma_f32_16x16x32_bf16 v[232:235], a[120:123], v[156:159], v[232:235]\n"
"v_mfma_f32_16x16x32_bf16 v[236:239], a[76:79], v[144:147], v[236:239]\n"
"v_mfma_f32_16x16x32_bf16 v[236:239], a[92:95], v[148:151], v[236:239]\n"
"v_mfma_f32_16x16x32_bf16 v[236:239], a[108:111], v[152:155], v[236:239]\n"
"v_mfma_f32_16x16x32_bf16 v[236:239], a[124:127], v[156:159], v[236:239]\n"
"s_waitcnt vmcnt(14)\n"
"v_mfma_f32_16x16x32_bf16 v[224:227], a[128:131], v[160:163], v[224:227]\n"
"s_add_u32 s66, s56, 57344\n"
"v_mfma_f32_16x16x32_bf16 v[224:227], a[144:147], v[164:167], v[224:227]\n"
"buffer_load_dwordx4 v[144:147], v14, s[24:27], s66 offen sc1\n"
"v_mfma_f32_16x16x32_bf16 v[224:227], a[160:163], v[168:171], v[224:227]\n"
"buffer_load_dwordx4 v[148:151], v14, s[24:27], s66 offen offset:1024 sc1\n"
"v_mfma_f32_16x16x32_bf16 v[224:227], a[176:179], v[172:175], v[224:227]\n"
"buffer_load_dwordx4 v[152:155], v14, s[24:27], s66 offen offset:2048 sc1\n"
"v_mfma_f32_16x16x32_bf16 v[228:231], a[132:135], v[160:163], v[228:231]\n"
"buffer_load_dwordx4 v[156:159], v14, s[24:27], s66 offen offset:3072 sc1\n"
"v_mfma_f32_16x16x32_bf16 v[228:231], a[148:151], v[164:167], v[228:231]\n"
"v_mfma_f32_16x16x32_bf16 v[228:231], a[164:167], v[168:171], v[228:231]\n"
"v_mfma_f32_16x16x32_bf16 v[228:231], a[180:183], v[172:175], v[228:231]\n"
"v_mfma_f32_16x16x32_bf16 v[232:235], a[136:139], v[160:163], v[232:235]\n"
"v_mfma_f32_16x16x32_bf16 v[232:235], a[152:155], v[164:167], v[232:235]\n"
"v_mfma_f32_16x16x32_bf16 v[232:235], a[168:171], v[168:171], v[232:235]\n"
"v_mfma_f32_16x16x32_bf16 v[232:235], a[184:187], v[172:175], v[232:235]\n"
"v_mfma_f32_16x16x32_bf16 v[236:239], a[140:143], v[160:163], v[236:239]\n"
"v_mfma_f32_16x16x32_bf16 v[236:239], a[156:159], v[164:167], v[236:239]\n"
"v_mfma_f32_16x16x32_bf16 v[236:239], a[172:175], v[168:171], v[236:239]\n"
"v_mfma_f32_16x16x32_bf16 v[236:239], a[188:191], v[172:175], v[236:239]\n"
"s_waitcnt vmcnt(14)\n"
"v_mfma_f32_16x16x32_bf16 v[224:227], a[192:195], v[96:99], v[224:227]\n"
"s_add_u32 s66, s56, 77824\n"
"v_mfma_f32_16x16x32_bf16 v[224:227], a[208:211], v[100:103], v[224:227]\n"
"buffer_load_dwordx4 v[160:163], v14, s[24:27], s66 offen sc1\n"
"v_mfma_f32_16x16x32_bf16 v[224:227], a[224:227], v[104:107], v[224:227]\n"
"buffer_load_dwordx4 v[164:167], v14, s[24:27], s66 offen offset:1024 sc1\n"
"v_mfma_f32_16x16x32_bf16 v[224:227], a[240:243], v[108:111], v[224:227]\n"
"buffer_load_dwordx4 v[168:171], v14, s[24:27], s66 offen offset:2048 sc1\n"
"v_mfma_f32_16x16x32_bf16 v[228:231], a[196:199], v[96:99], v[228:231]\n"
"buffer_load_dwordx4 v[172:175], v14, s[24:27], s66 offen offset:3072 sc1\n"
"v_mfma_f32_16x16x32_bf16 v[228:231], a[212:215], v[100:103], v[228:231]\n"
"v_mfma_f32_16x16x32_bf16 v[228:231], a[228:231], v[104:107], v[228:231]\n"
"v_mfma_f32_16x16x32_bf16 v[228:231], a[244:247], v[108:111], v[228:231]\n"
"v_mfma_f32_16x16x32_bf16 v[232:235], a[200:203], v[96:99], v[232:235]\n"
"v_mfma_f32_16x16x32_bf16 v[232:235], a[216:219], v[100:103], v[232:235]\n"
"v_mfma_f32_16x16x32_bf16 v[232:235], a[232:235], v[104:107], v[232:235]\n"
"v_mfma_f32_16x16x32_bf16 v[232:235], a[248:251], v[108:111], v[232:235]\n"
"v_mfma_f32_16x16x32_bf16 v[236:239], a[204:207], v[96:99], v[236:239]\n"
"v_mfma_f32_16x16x32_bf16 v[236:239], a[220:223], v[100:103], v[236:239]\n"
"v_mfma_f32_16x16x32_bf16 v[236:239], a[236:239], v[104:107], v[236:239]\n"
"v_mfma_f32_16x16x32_bf16 v[236:239], a[252:255], v[108:111], v[236:239]\n"
"s_waitcnt vmcnt(12)\n"
"v_mfma_f32_16x16x32_bf16 v[208:211], a[0:3], v[112:115], 0\n"
"s_add_u32 s66, s57, 0\n"
"v_mfma_f32_16x16x32_bf16 v[208:211], a[16:19], v[116:119], v[208:211]\n"
"buffer_load_dwordx4 v[96:99], v1, s[24:27], s66 offen sc1\n"
"v_mfma_f32_16x16x32_bf16 v[208:211], a[32:35], v[120:123], v[208:211]\n"
"buffer_load_dwordx4 v[100:103], v1, s[24:27], s66 offen offset:1024 sc1\n"
"v_mfma_f32_16x16x32_bf16 v[208:211], a[48:51], v[124:127], v[208:211]\n"
"ds_write_b128 v240, v[224:227] offset:49152\n"
"v_mfma_f32_16x16x32_bf16 v[212:215], a[4:7], v[112:115], 0\n"
"buffer_load_dwordx4 v[104:107], v1, s[24:27], s66 offen offset:2048 sc1\n"
"v_mfma_f32_16x16x32_bf16 v[212:215], a[20:23], v[116:119], v[212:215]\n"
"buffer_load_dwordx4 v[108:111], v1, s[24:27], s66 offen offset:3072 sc1\n"
"v_mfma_f32_16x16x32_bf16 v[212:215], a[36:39], v[120:123], v[212:215]\n"
"v_mfma_f32_16x16x32_bf16 v[212:215], a[52:55], v[124:127], v[212:215]\n"
"ds_write_b128 v241, v[228:231] offset:49152\n"
"v_mfma_f32_16x16x32_bf16 v[216:219], a[8:11], v[112:115], 0\n"
"v_mfma_f32_16x16x32_bf16 v[216:219], a[24:27], v[116:119], v[216:219]\n"
"v_mfma_f32_16x16x32_bf16 v[216:219], a[40:43], v[120:123], v[216:219]\n"
"v_mfma_f32_16x16x32_bf16 v[216:219], a[56:59], v[124:127], v[216:219]\n"
"ds_write_b128 v242, v[232:235] offset:49152\n"
"v_mfma_f32_16x16x32_bf16 v[220:223], a[12:15], v[112:115], 0\n"
"v_mfma_f32_16x16x32_bf16 v[220:223], a[28:31], v[116:119], v[220:223]\n"
"v_mfma_f32_16x16x32_bf16 v[220:223], a[44:47], v[120:123], v[220:223]\n"
"v_mfma_f32_16x16x32_bf16 v[220:223], a[60:63], v[124:127], v[220:223]\n"
"ds_write_b128 v243, v[236:239] offset:49152\n"
"s_waitcnt vmcnt(12)\n"
"v_mfma_f32_16x16x32_bf16 v[208:211], a[64:67], v[128:131], v[208:211]\n"
"s_add_u32 s66, s57, 20480\n"
"v_mfma_f32_16x16x32_bf16 v[208:211], a[80:83], v[132:135], v[208:211]\n"
"buffer_load_dwordx4 v[112:115], v1, s[24:27], s66 offen sc1\n"
"v_mfma_f32_16x16x32_bf16 v[208:211], a[96:99], v[136:139], v[208:211]\n"
"buffer_load_dwordx4 v[116:119], v1, s[24:27], s66 offen offset:1024 sc1\n"
"v_mfma_f32_16x16x32_bf16 v[208:211], a[112:115], v[140:143], v[208:211]\n"
"buffer_load_dwordx4 v[120:123], v1, s[24:27], s66 offen offset:2048 sc1\n"
"v_mfma_f32_16x16x32_bf16 v[212:215], a[68:71], v[128:131], v[212:215]\n"
"buffer_load_dwordx4 v[124:127], v1, s[24:27], s66 offen offset:3072 sc1\n"
"v_mfma_f32_16x16x32_bf16 v[212:215], a[84:87], v[132:135], v[212:215]\n"
"s_waitcnt lgkmcnt(0)\n"
"s_barrier\n"
"v_mfma_f32_16x16x32_bf16 v[212:215], a[100:103], v[136:139], v[212:215]\n"
"ds_read_b128 v[24:27], v244\n"
"ds_read_b128 v[28:31], v245\n"
"v_mfma_f32_16x16x32_bf16 v[212:215], a[116:119], v[140:143], v[212:215]\n"
"ds_read_b128 v[32:35], v244 offset:4096\n"
"ds_read_b128 v[36:39], v245 offset:4096\n"
"v_mfma_f32_16x16x32_bf16 v[216:219], a[72:75], v[128:131], v[216:219]\n"
"ds_read_b128 v[40:43], v244 offset:8192\n"
"ds_read_b128 v[44:47], v245 offset:8192\n"
"v_mfma_f32_16x16x32_bf16 v[216:219], a[88:91], v[132:135], v[216:219]\n"
"ds_read_b128 v[48:51], v244 offset:12288\n"
"ds_read_b128 v[52:55], v245 offset:12288\n"
"v_mfma_f32_16x16x32_bf16 v[216:219], a[104:107], v[136:139], v[216:219]\n"
"v_add_u32 v240, 65536, v240\n"
"v_add_u32 v241, 65536, v241\n"
"v_mfma_f32_16x16x32_bf16 v[216:219], a[120:123], v[140:143], v[216:219]\n"
"v_add_u32 v242, 65536, v242\n"
"v_add_u32 v243, 65536, v243\n"
"v_mfma_f32_16x16x32_bf16 v[220:223], a[76:79], v[128:131], v[220:223]\n"
"v_mfma_f32_16x16x32_bf16 v[220:223], a[92:95], v[132:135], v[220:223]\n"
"v_mfma_f32_16x16x32_bf16 v[220:223], a[108:111], v[136:139], v[220:223]\n"
"v_mfma_f32_16x16x32_bf16 v[220:223], a[124:127], v[140:143], v[220:223]\n"
"s_waitcnt vmcnt(12)\n"
"v_mfma_f32_16x16x32_bf16 v[208:211], a[128:131], v[144:147], v[208:211]\n"
"s_waitcnt lgkmcnt(0)\n"
"v_mfma_f32_16x16x32_bf16 v[208:211], a[144:147], v[148:151], v[208:211]\n"
"s_add_u32 s66, s57, 40960\n"
"v_mfma_f32_16x16x32_bf16 v[208:211], a[160:163], v[152:155], v[208:211]\n"
"buffer_load_dwordx4 v[128:131], v1, s[24:27], s66 offen sc1\n"
"v_mfma_f32_16x16x32_bf16 v[208:211], a[176:179], v[156:159], v[208:211]\n"
"buffer_load_dwordx4 v[132:135], v1, s[24:27], s66 offen offset:1024 sc1\n"
"v_mfma_f32_16x16x32_bf16 v[212:215], a[132:135], v[144:147], v[212:215]\n"
"buffer_load_dwordx4 v[136:139], v1, s[24:27], s66 offen offset:2048 sc1\n"
"v_mfma_f32_16x16x32_bf16 v[212:215], a[148:151], v[148:151], v[212:215]\n"
"buffer_load_dwordx4 v[140:143], v1, s[24:27], s66 offen offset:3072 sc1\n"
"v_mfma_f32_16x16x32_bf16 v[212:215], a[164:167], v[152:155], v[212:215]\n"
"v_add_f32 v24, v24, v32\n"
"v_add_f32 v25, v25, v33\n"
"v_mfma_f32_16x16x32_bf16 v[212:215], a[180:183], v[156:159], v[212:215]\n"
"v_add_f32 v26, v26, v34\n"
"v_add_f32 v27, v27, v35\n"
"v_mfma_f32_16x16x32_bf16 v[216:219], a[136:139], v[144:147], v[216:219]\n"
"v_add_f32 v28, v28, v36\n"
"v_add_f32 v29, v29, v37\n"
"v_mfma_f32_16x16x32_bf16 v[216:219], a[152:155], v[148:151], v[216:219]\n"
"v_add_f32 v30, v30, v38\n"
"v_add_f32 v31, v31, v39\n"
"v_mfma_f32_16x16x32_bf16 v[216:219], a[168:171], v[152:155], v[216:219]\n"
"v_add_f32 v40, v40, v48\n"
"v_add_f32 v41, v41, v49\n"
"v_mfma_f32_16x16x32_bf16 v[216:219], a[184:187], v[156:159], v[216:219]\n"
"v_add_f32 v42, v42, v50\n"
"v_add_f32 v43, v43, v51\n"
"v_mfma_f32_16x16x32_bf16 v[220:223], a[140:143], v[144:147], v[220:223]\n"
"v_add_f32 v44, v44, v52\n"
"v_add_f32 v45, v45, v53\n"
"v_mfma_f32_16x16x32_bf16 v[220:223], a[156:159], v[148:151], v[220:223]\n"
"v_add_f32 v46, v46, v54\n"
"v_add_f32 v47, v47, v55\n"
"v_mfma_f32_16x16x32_bf16 v[220:223], a[172:175], v[152:155], v[220:223]\n"
"v_add_f32 v80, v24, v40\n"
"v_add_f32 v81, v25, v41\n"
"v_mfma_f32_16x16x32_bf16 v[220:223], a[188:191], v[156:159], v[220:223]\n"
"v_add_f32 v82, v26, v42\n"
"v_add_f32 v83, v27, v43\n"
"s_waitcnt vmcnt(12)\n"
"v_mfma_f32_16x16x32_bf16 v[208:211], a[192:195], v[160:163], v[208:211]\n"
"s_add_u32 s66, s57, 61440\n"
"v_mfma_f32_16x16x32_bf16 v[208:211], a[208:211], v[164:167], v[208:211]\n"
"buffer_load_dwordx4 v[144:147], v1, s[24:27], s66 offen sc1\n"
"v_mfma_f32_16x16x32_bf16 v[208:211], a[224:227], v[168:171], v[208:211]\n"
"buffer_load_dwordx4 v[148:151], v1, s[24:27], s66 offen offset:1024 sc1\n"
"v_mfma_f32_16x16x32_bf16 v[208:211], a[240:243], v[172:175], v[208:211]\n"
"buffer_load_dwordx4 v[152:155], v1, s[24:27], s66 offen offset:2048 sc1\n"
"v_mfma_f32_16x16x32_bf16 v[212:215], a[196:199], v[160:163], v[212:215]\n"
"buffer_load_dwordx4 v[156:159], v1, s[24:27], s66 offen offset:3072 sc1\n"
"v_mfma_f32_16x16x32_bf16 v[212:215], a[212:215], v[164:167], v[212:215]\n"
"v_add_f32 v84, v28, v44\n"
"v_add_f32 v85, v29, v45\n"
"v_mfma_f32_16x16x32_bf16 v[212:215], a[228:231], v[168:171], v[212:215]\n"
"v_add_f32 v86, v30, v46\n"
"v_add_f32 v87, v31, v47\n"
"v_mfma_f32_16x16x32_bf16 v[212:215], a[244:247], v[172:175], v[212:215]\n"
"v_add_u32 v244, 32768, v244\n"
"v_add_u32 v245, 32768, v245\n"
"v_mfma_f32_16x16x32_bf16 v[216:219], a[200:203], v[160:163], v[216:219]\n"
"v_mfma_f32_16x16x32_bf16 v[216:219], a[216:219], v[164:167], v[216:219]\n"
"v_mfma_f32_16x16x32_bf16 v[216:219], a[232:235], v[168:171], v[216:219]\n"
"s_mov_b32 s55, 8192\n"
"v_mfma_f32_16x16x32_bf16 v[216:219], a[248:251], v[172:175], v[216:219]\n"
"buffer_store_dwordx4 v[80:83], v17, s[28:31], s55 offen sc1\n"
"v_mfma_f32_16x16x32_bf16 v[220:223], a[204:207], v[160:163], v[220:223]\n"
"v_mfma_f32_16x16x32_bf16 v[220:223], a[220:223], v[164:167], v[220:223]\n"
"buffer_store_dwordx4 v[84:87], v17, s[28:31], s55 offen offset:1024 sc1\n"
"v_mfma_f32_16x16x32_bf16 v[220:223], a[236:239], v[168:171], v[220:223]\n"
"v_mfma_f32_16x16x32_bf16 v[220:223], a[252:255], v[172:175], v[220:223]\n"
"s_waitcnt vmcnt(14)\n"
"v_mfma_f32_16x16x32_bf16 v[224:227], a[0:3], v[96:99], 0\n"
"s_add_u32 s66, s57, 4096\n"
"v_mfma_f32_16x16x32_bf16 v[224:227], a[16:19], v[100:103], v[224:227]\n"
"buffer_load_dwordx4 v[160:163], v1, s[24:27], s66 offen sc1\n"
"v_mfma_f32_16x16x32_bf16 v[224:227], a[32:35], v[104:107], v[224:227]\n"
"buffer_load_dwordx4 v[164:167], v1, s[24:27], s66 offen offset:1024 sc1\n"
"v_mfma_f32_16x16x32_bf16 v[224:227], a[48:51], v[108:111], v[224:227]\n"
"ds_write_b128 v240, v[208:211]\n"
"v_mfma_f32_16x16x32_bf16 v[228:231], a[4:7], v[96:99], 0\n"
"buffer_load_dwordx4 v[168:171], v1, s[24:27], s66 offen offset:2048 sc1\n"
"v_mfma_f32_16x16x32_bf16 v[228:231], a[20:23], v[100:103], v[228:231]\n"
"buffer_load_dwordx4 v[172:175], v1, s[24:27], s66 offen offset:3072 sc1\n"
"v_mfma_f32_16x16x32_bf16 v[228:231], a[36:39], v[104:107], v[228:231]\n"
"v_mfma_f32_16x16x32_bf16 v[228:231], a[52:55], v[108:111], v[228:231]\n"
"ds_write_b128 v241, v[212:215]\n"
"v_mfma_f32_16x16x32_bf16 v[232:235], a[8:11], v[96:99], 0\n"
"v_mfma_f32_16x16x32_bf16 v[232:235], a[24:27], v[100:103], v[232:235]\n"
"v_mfma_f32_16x16x32_bf16 v[232:235], a[40:43], v[104:107], v[232:235]\n"
"v_mfma_f32_16x16x32_bf16 v[232:235], a[56:59], v[108:111], v[232:235]\n"
"ds_write_b128 v242, v[216:219]\n"
"v_mfma_f32_16x16x32_bf16 v[236:239], a[12:15], v[96:99], 0\n"
"v_mfma_f32_16x16x32_bf16 v[236:239], a[28:31], v[100:103], v[236:239]\n"
"v_mfma_f32_16x16x32_bf16 v[236:239], a[44:47], v[104:107], v[236:239]\n"
"v_mfma_f32_16x16x32_bf16 v[236:239], a[60:63], v[108:111], v[236:239]\n"
"ds_write_b128 v243, v[220:223]\n"
"s_waitcnt vmcnt(14)\n"
"v_mfma_f32_16x16x32_bf16 v[224:227], a[64:67], v[112:115], v[224:227]\n"
"s_add_u32 s66, s57, 24576\n"
"v_mfma_f32_16x16x32_bf16 v[224:227], a[80:83], v[116:119], v[224:227]\n"
"buffer_load_dwordx4 v[96:99], v1, s[24:27], s66 offen sc1\n"
"v_mfma_f32_16x16x32_bf16 v[224:227], a[96:99], v[120:123], v[224:227]\n"
"buffer_load_dwordx4 v[100:103], v1, s[24:27], s66 offen offset:1024 sc1\n"
"v_mfma_f32_16x16x32_bf16 v[224:227], a[112:115], v[124:127], v[224:227]\n"
"buffer_load_dwordx4 v[104:107], v1, s[24:27], s66 offen offset:2048 sc1\n"
"v_mfma_f32_16x16x32_bf16 v[228:231], a[68:71], v[112:115], v[228:231]\n"
"buffer_load_dwordx4 v[108:111], v1, s[24:27], s66 offen offset:3072 sc1\n"
"v_mfma_f32_16x16x32_bf16 v[228:231], a[84:87], v[116:119], v[228:231]\n"
"s_waitcnt lgkmcnt(0)\n"
"s_barrier\n"
"v_mfma_f32_16x16x32_bf16 v[228:231], a[100:103], v[120:123], v[228:231]\n"
"ds_read_b128 v[24:27], v244\n"
"ds_read_b128 v[28:31], v245\n"
"v_mfma_f32_16x16x32_bf16 v[228:231], a[116:119], v[124:127], v[228:231]\n"
"ds_read_b128 v[32:35], v244 offset:4096\n"
"ds_read_b128 v[36:39], v245 offset:4096\n"
"v_mfma_f32_16x16x32_bf16 v[232:235], a[72:75], v[112:115], v[232:235]\n"
"ds_read_b128 v[40:43], v244 offset:8192\n"
"ds_read_b128 v[44:47], v245 offset:8192\n"
"v_mfma_f32_16x16x32_bf16 v[232:235], a[88:91], v[116:119], v[232:235]\n"
"ds_read_b128 v[48:51], v244 offset:12288\n"
"ds_read_b128 v[52:55], v245 offset:12288\n"
"v_mfma_f32_16x16x32_bf16 v[232:235], a[104:107], v[120:123], v[232:235]\n"
"v_mfma_f32_16x16x32_bf16 v[232:235], a[120:123], v[124:127], v[232:235]\n"
"v_mfma_f32_16x16x32_bf16 v[236:239], a[76:79], v[112:115], v[236:239]\n"
"v_mfma_f32_16x16x32_bf16 v[236:239], a[92:95], v[116:119], v[236:239]\n"
"v_mfma_f32_16x16x32_bf16 v[236:239], a[108:111], v[120:123], v[236:239]\n"
"v_mfma_f32_16x16x32_bf16 v[236:239], a[124:127], v[124:127], v[236:239]\n"
"s_waitcnt vmcnt(14)\n"
"v_mfma_f32_16x16x32_bf16 v[224:227], a[128:131], v[128:131], v[224:227]\n"
"s_waitcnt lgkmcnt(0)\n"
"v_mfma_f32_16x16x32_bf16 v[224:227], a[144:147], v[132:135], v[224:227]\n"
"s_add_u32 s66, s57, 45056\n"
"v_mfma_f32_16x16x32_bf16 v[224:227], a[160:163], v[136:139], v[224:227]\n"
"buffer_load_dwordx4 v[112:115], v1, s[24:27], s66 offen sc1\n"
"v_mfma_f32_16x16x32_bf16 v[224:227], a[176:179], v[140:143], v[224:227]\n"
"buffer_load_dwordx4 v[116:119], v1, s[24:27], s66 offen offset:1024 sc1\n"
"v_mfma_f32_16x16x32_bf16 v[228:231], a[132:135], v[128:131], v[228:231]\n"
"buffer_load_dwordx4 v[120:123], v1, s[24:27], s66 offen offset:2048 sc1\n"
"v_mfma_f32_16x16x32_bf16 v[228:231], a[148:151], v[132:135], v[228:231]\n"
"buffer_load_dwordx4 v[124:127], v1, s[24:27], s66 offen offset:3072 sc1\n"
"v_mfma_f32_16x16x32_bf16 v[228:231], a[164:167], v[136:139], v[228:231]\n"
"v_add_f32 v24, v24, v32\n"
"v_add_f32 v25, v25, v33\n"
"v_mfma_f32_16x16x32_bf16 v[228:231], a[180:183], v[140:143], v[228:231]\n"
"v_add_f32 v26, v26, v34\n"
"v_add_f32 v27, v27, v35\n"
"v_mfma_f32_16x16x32_bf16 v[232:235], a[136:139], v[128:131], v[232:235]\n"
"v_add_f32 v28, v28, v36\n"
"v_add_f32 v29, v29, v37\n"
"v_mfma_f32_16x16x32_bf16 v[232:235], a[152:155], v[132:135], v[232:235]\n"
"v_add_f32 v30, v30, v38\n"
"v_add_f32 v31, v31, v39\n"
"v_mfma_f32_16x16x32_bf16 v[232:235], a[168:171], v[136:139], v[232:235]\n"
"v_add_f32 v40, v40, v48\n"
"v_add_f32 v41, v41, v49\n"
"v_mfma_f32_16x16x32_bf16 v[232:235], a[184:187], v[140:143], v[232:235]\n"
"v_add_f32 v42, v42, v50\n"
"v_add_f32 v43, v43, v51\n"
"v_mfma_f32_16x16x32_bf16 v[236:239], a[140:143], v[128:131], v[236:239]\n"
"v_add_f32 v44, v44, v52\n"
"v_add_f32 v45, v45, v53\n"
"v_mfma_f32_16x16x32_bf16 v[236:239], a[156:159], v[132:135], v[236:239]\n"
"v_add_f32 v46, v46, v54\n"
"v_add_f32 v47, v47, v55\n"
"v_mfma_f32_16x16x32_bf16 v[236:239], a[172:175], v[136:139], v[236:239]\n"
"v_add_f32 v88, v24, v40\n"
"v_add_f32 v89, v25, v41\n"
"v_mfma_f32_16x16x32_bf16 v[236:239], a[188:191], v[140:143], v[236:239]\n"
"v_add_f32 v90, v26, v42\n"
"v_add_f32 v91, v27, v43\n"
"s_waitcnt vmcnt(14)\n"
"v_mfma_f32_16x16x32_bf16 v[224:227], a[192:195], v[144:147], v[224:227]\n"
"s_add_u32 s66, s57, 65536\n"
"v_mfma_f32_16x16x32_bf16 v[224:227], a[208:211], v[148:151], v[224:227]\n"
"buffer_load_dwordx4 v[128:131], v1, s[24:27], s66 offen sc1\n"
"v_mfma_f32_16x16x32_bf16 v[224:227], a[224:227], v[152:155], v[224:227]\n"
"buffer_load_dwordx4 v[132:135], v1, s[24:27], s66 offen offset:1024 sc1\n"
"v_mfma_f32_16x16x32_bf16 v[224:227], a[240:243], v[156:159], v[224:227]\n"
"buffer_load_dwordx4 v[136:139], v1, s[24:27], s66 offen offset:2048 sc1\n"
"v_mfma_f32_16x16x32_bf16 v[228:231], a[196:199], v[144:147], v[228:231]\n"
"buffer_load_dwordx4 v[140:143], v1, s[24:27], s66 offen offset:3072 sc1\n"
"v_mfma_f32_16x16x32_bf16 v[228:231], a[212:215], v[148:151], v[228:231]\n"
"v_add_f32 v92, v28, v44\n"
"v_add_f32 v93, v29, v45\n"
"v_mfma_f32_16x16x32_bf16 v[228:231], a[228:231], v[152:155], v[228:231]\n"
"v_add_f32 v94, v30, v46\n"
"v_add_f32 v95, v31, v47\n"
"v_mfma_f32_16x16x32_bf16 v[228:231], a[244:247], v[156:159], v[228:231]\n"
"v_add_u32 v244, 16384, v244\n"
"v_add_u32 v245, 16384, v245\n"
"v_mfma_f32_16x16x32_bf16 v[232:235], a[200:203], v[144:147], v[232:235]\n"
"v_mfma_f32_16x16x32_bf16 v[232:235], a[216:219], v[148:151], v[232:235]\n"
"v_mfma_f32_16x16x32_bf16 v[232:235], a[232:235], v[152:155], v[232:235]\n"
"s_mov_b32 s55, 16384\n"
"v_mfma_f32_16x16x32_bf16 v[232:235], a[248:251], v[156:159], v[232:235]\n"
"buffer_store_dwordx4 v[88:91], v17, s[28:31], s55 offen sc1\n"
"v_mfma_f32_16x16x32_bf16 v[236:239], a[204:207], v[144:147], v[236:239]\n"
"v_mfma_f32_16x16x32_bf16 v[236:239], a[220:223], v[148:151], v[236:239]\n"
"buffer_store_dwordx4 v[92:95], v17, s[28:31], s55 offen offset:1024 sc1\n"
"v_mfma_f32_16x16x32_bf16 v[236:239], a[236:239], v[152:155], v[236:239]\n"
"v_mfma_f32_16x16x32_bf16 v[236:239], a[252:255], v[156:159], v[236:239]\n"
"s_waitcnt vmcnt(14)\n"
"v_mfma_f32_16x16x32_bf16 v[208:211], a[0:3], v[160:163], 0\n"
"s_add_u32 s66, s57, 8192\n"
"v_mfma_f32_16x16x32_bf16 v[208:211], a[16:19], v[164:167], v[208:211]\n"
"buffer_load_dwordx4 v[144:147], v1, s[24:27], s66 offen sc1\n"
"v_mfma_f32_16x16x32_bf16 v[208:211], a[32:35], v[168:171], v[208:211]\n"
"buffer_load_dwordx4 v[148:151], v1, s[24:27], s66 offen offset:1024 sc1\n"
"v_mfma_f32_16x16x32_bf16 v[208:211], a[48:51], v[172:175], v[208:211]\n"
"ds_write_b128 v240, v[224:227] offset:16384\n"
"v_mfma_f32_16x16x32_bf16 v[212:215], a[4:7], v[160:163], 0\n"
"buffer_load_dwordx4 v[152:155], v1, s[24:27], s66 offen offset:2048 sc1\n"
"v_mfma_f32_16x16x32_bf16 v[212:215], a[20:23], v[164:167], v[212:215]\n"
"buffer_load_dwordx4 v[156:159], v1, s[24:27], s66 offen offset:3072 sc1\n"
"v_mfma_f32_16x16x32_bf16 v[212:215], a[36:39], v[168:171], v[212:215]\n"
"v_mfma_f32_16x16x32_bf16 v[212:215], a[52:55], v[172:175], v[212:215]\n"
"ds_write_b128 v241, v[228:231] offset:16384\n"
"v_mfma_f32_16x16x32_bf16 v[216:219], a[8:11], v[160:163], 0\n"
"v_mfma_f32_16x16x32_bf16 v[216:219], a[24:27], v[164:167], v[216:219]\n"
"v_mfma_f32_16x16x32_bf16 v[216:219], a[40:43], v[168:171], v[216:219]\n"
"v_mfma_f32_16x16x32_bf16 v[216:219], a[56:59], v[172:175], v[216:219]\n"
"ds_write_b128 v242, v[232:235] offset:16384\n"
"v_mfma_f32_16x16x32_bf16 v[220:223], a[12:15], v[160:163], 0\n"
"v_mfma_f32_16x16x32_bf16 v[220:223], a[28:31], v[164:167], v[220:223]\n"
"v_mfma_f32_16x16x32_bf16 v[220:223], a[44:47], v[168:171], v[220:223]\n"
"v_mfma_f32_16x16x32_bf16 v[220:223], a[60:63], v[172:175], v[220:223]\n"
"ds_write_b128 v243, v[236:239] offset:16384\n"
"s_waitcnt vmcnt(14)\n"
"v_mfma_f32_16x16x32_bf16 v[208:211], a[64:67], v[96:99], v[208:211]\n"
"s_add_u32 s66, s57, 28672\n"
"v_mfma_f32_16x16x32_bf16 v[208:211], a[80:83], v[100:103], v[208:211]\n"
"buffer_load_dwordx4 v[160:163], v1, s[24:27], s66 offen sc1\n"
"v_mfma_f32_16x16x32_bf16 v[208:211], a[96:99], v[104:107], v[208:211]\n"
"buffer_load_dwordx4 v[164:167], v1, s[24:27], s66 offen offset:1024 sc1\n"
"v_mfma_f32_16x16x32_bf16 v[208:211], a[112:115], v[108:111], v[208:211]\n"
"buffer_load_dwordx4 v[168:171], v1, s[24:27], s66 offen offset:2048 sc1\n"
"v_mfma_f32_16x16x32_bf16 v[212:215], a[68:71], v[96:99], v[212:215]\n"
"buffer_load_dwordx4 v[172:175], v1, s[24:27], s66 offen offset:3072 sc1\n"
"v_mfma_f32_16x16x32_bf16 v[212:215], a[84:87], v[100:103], v[212:215]\n"
"v_mfma_f32_16x16x32_bf16 v[212:215], a[100:103], v[104:107], v[212:215]\n"
"v_mfma_f32_16x16x32_bf16 v[212:215], a[116:119], v[108:111], v[212:215]\n"
"v_mfma_f32_16x16x32_bf16 v[216:219], a[72:75], v[96:99], v[216:219]\n"
"v_mfma_f32_16x16x32_bf16 v[216:219], a[88:91], v[100:103], v[216:219]\n"
"v_mfma_f32_16x16x32_bf16 v[216:219], a[104:107], v[104:107], v[216:219]\n"
"v_mfma_f32_16x16x32_bf16 v[216:219], a[120:123], v[108:111], v[216:219]\n"
"v_mfma_f32_16x16x32_bf16 v[220:223], a[76:79], v[96:99], v[220:223]\n"
"v_mfma_f32_16x16x32_bf16 v[220:223], a[92:95], v[100:103], v[220:223]\n"
"v_mfma_f32_16x16x32_bf16 v[220:223], a[108:111], v[104:107], v[220:223]\n"
"v_mfma_f32_16x16x32_bf16 v[220:223], a[124:127], v[108:111], v[220:223]\n"
"s_waitcnt vmcnt(14)\n"
"v_mfma_f32_16x16x32_bf16 v[208:211], a[128:131], v[112:115], v[208:211]\n"
"s_add_u32 s66, s57, 49152\n"
"v_mfma_f32_16x16x32_bf16 v[208:211], a[144:147], v[116:119], v[208:211]\n"
"buffer_load_dwordx4 v[96:99], v1, s[24:27], s66 offen sc1\n"
"v_mfma_f32_16x16x32_bf16 v[208:211], a[160:163], v[120:123], v[208:211]\n"
"buffer_load_dwordx4 v[100:103], v1, s[24:27], s66 offen offset:1024 sc1\n"
"v_mfma_f32_16x16x32_bf16 v[208:211], a[176:179], v[124:127], v[208:211]\n"
"buffer_load_dwordx4 v[104:107], v1, s[24:27], s66 offen offset:2048 sc1\n"
"v_mfma_f32_16x16x32_bf16 v[212:215], a[132:135], v[112:115], v[212:215]\n"
"buffer_load_dwordx4 v[108:111], v1, s[24:27], s66 offen offset:3072 sc1\n"
"v_mfma_f32_16x16x32_bf16 v[212:215], a[148:151], v[116:119], v[212:215]\n"
"v_mfma_f32_16x16x32_bf16 v[212:215], a[164:167], v[120:123], v[212:215]\n"
"v_mfma_f32_16x16x32_bf16 v[212:215], a[180:183], v[124:127], v[212:215]\n"
"v_mfma_f32_16x16x32_bf16 v[216:219], a[136:139], v[112:115], v[216:219]\n"
"v_mfma_f32_16x16x32_bf16 v[216:219], a[152:155], v[116:119], v[216:219]\n"
"v_mfma_f32_16x16x32_bf16 v[216:219], a[168:171], v[120:123], v[216:219]\n"
"v_mfma_f32_16x16x32_bf16 v[216:219], a[184:187], v[124:127], v[216:219]\n"
"v_mfma_f32_16x16x32_bf16 v[220:223], a[140:143], v[112:115], v[220:223]\n"
"s_waitcnt vmcnt(12)\n"
"s_mov_b64 exec, 1\n"
"global_store_dwordx2 v20, v[18:19], s[60:61] offset:16 sc1\n"
"s_mov_b64 exec, -1\n"
"v_mfma_f32_16x16x32_bf16 v[220:223], a[156:159], v[116:119], v[220:223]\n"
"v_mfma_f32_16x16x32_bf16 v[220:223], a[172:175], v[120:123], v[220:223]\n"
"v_mfma_f32_16x16x32_bf16 v[220:223], a[188:191], v[124:127], v[220:223]\n"
"v_mfma_f32_16x16x32_bf16 v[208:211], a[192:195], v[128:131], v[208:211]\n"
"s_add_u32 s66, s57, 69632\n"
"v_mfma_f32_16x16x32_bf16 v[208:211], a[208:211], v[132:135], v[208:211]\n"
"buffer_load_dwordx4 v[112:115], v1, s[24:27], s66 offen sc1\n"
"v_mfma_f32_16x16x32_bf16 v[208:211], a[224:227], v[136:139], v[208:211]\n"
"buffer_load_dwordx4 v[116:119], v1, s[24:27], s66 offen offset:1024 sc1\n"
"v_mfma_f32_16x16x32_bf16 v[208:211], a[240:243], v[140:143], v[208:211]\n"
"buffer_load_dwordx4 v[120:123], v1, s[24:27], s66 offen offset:2048 sc1\n"
"v_mfma_f32_16x16x32_bf16 v[212:215], a[196:199], v[128:131], v[212:215]\n"
"buffer_load_dwordx4 v[124:127], v1, s[24:27], s66 offen offset:3072 sc1\n"
"v_mfma_f32_16x16x32_bf16 v[212:215], a[212:215], v[132:135], v[212:215]\n"
"v_mfma_f32_16x16x32_bf16 v[212:215], a[228:231], v[136:139], v[212:215]\n"
"v_mfma_f32_16x16x32_bf16 v[212:215], a[244:247], v[140:143], v[212:215]\n"
"v_mfma_f32_16x16x32_bf16 v[216:219], a[200:203], v[128:131], v[216:219]\n"
"v_mfma_f32_16x16x32_bf16 v[216:219], a[216:219], v[132:135], v[216:219]\n"
"v_mfma_f32_16x16x32_bf16 v[216:219], a[232:235], v[136:139], v[216:219]\n"
"v_mfma_f32_16x16x32_bf16 v[216:219], a[248:251], v[140:143], v[216:219]\n"
"v_mfma_f32_16x16x32_bf16 v[220:223], a[204:207], v[128:131], v[220:223]\n"
"v_mfma_f32_16x16x32_bf16 v[220:223], a[220:223], v[132:135], v[220:223]\n"
"v_mfma_f32_16x16x32_bf16 v[220:223], a[236:239], v[136:139], v[220:223]\n"
"v_mfma_f32_16x16x32_bf16 v[220:223], a[252:255], v[140:143], v[220:223]\n"
"s_waitcnt vmcnt(13)\n"
"v_mfma_f32_16x16x32_bf16 v[224:227], a[0:3], v[144:147], 0\n"
"s_add_u32 s66, s57, 12288\n"
"v_mfma_f32_16x16x32_bf16 v[224:227], a[16:19], v[148:151], v[224:227]\n"
"buffer_load_dwordx4 v[128:131], v1, s[24:27], s66 offen sc1\n"
"v_mfma_f32_16x16x32_bf16 v[224:227], a[32:35], v[152:155], v[224:227]\n"
"buffer_load_dwordx4 v[132:135], v1, s[24:27], s66 offen offset:1024 sc1\n"
"v_mfma_f32_16x16x32_bf16 v[224:227], a[48:51], v[156:159], v[224:227]\n"
"ds_write_b128 v240, v[208:211] offset:32768\n"
"v_mfma_f32_16x16x32_bf16 v[228:231], a[4:7], v[144:147], 0\n"
"buffer_load_dwordx4 v[136:139], v1, s[24:27], s66 offen offset:2048 sc1\n"
"v_mfma_f32_16x16x32_bf16 v[228:231], a[20:23], v[148:151], v[228:231]\n"
"buffer_load_dwordx4 v[140:143], v1, s[24:27], s66 offen offset:3072 sc1\n"
"v_mfma_f32_16x16x32_bf16 v[228:231], a[36:39], v[152:155], v[228:231]\n"
"v_mfma_f32_16x16x32_bf16 v[228:231], a[52:55], v[156:159], v[228:231]\n"
"ds_write_b128 v241, v[212:215] offset:32768\n"
"v_mfma_f32_16x16x32_bf16 v[232:235], a[8:11], v[144:147], 0\n"
"v_mfma_f32_16x16x32_bf16 v[232:235], a[24:27], v[148:151], v[232:235]\n"
"v_mfma_f32_16x16x32_bf16 v[232:235], a[40:43], v[152:155], v[232:235]\n"
"v_mfma_f32_16x16x32_bf16 v[232:235], a[56:59], v[156:159], v[232:235]\n"
"ds_write_b128 v242, v[216:219] offset:32768\n"
"v_mfma_f32_16x16x32_bf16 v[236:239], a[12:15], v[144:147], 0\n"
"v_mfma_f32_16x16x32_bf16 v[236:239], a[28:31], v[148:151], v[236:239]\n"
"v_mfma_f32_16x16x32_bf16 v[236:239], a[44:47], v[152:155], v[236:239]\n"
"v_mfma_f32_16x16x32_bf16 v[236:239], a[60:63], v[156:159], v[236:239]\n"
"ds_write_b128 v243, v[220:223] offset:32768\n"
"s_waitcnt vmcnt(13)\n"
"v_mfma_f32_16x16x32_bf16 v[224:227], a[64:67], v[160:163], v[224:227]\n"
"s_add_u32 s66, s57, 32768\n"
"v_mfma_f32_16x16x32_bf16 v[224:227], a[80:83], v[164:167], v[224:227]\n"
"buffer_load_dwordx4 v[144:147], v1, s[24:27], s66 offen sc1\n"
"v_mfma_f32_16x16x32_bf16 v[224:227], a[96:99], v[168:171], v[224:227]\n"
"buffer_load_dwordx4 v[148:151], v1, s[24:27], s66 offen offset:1024 sc1\n"
"v_mfma_f32_16x16x32_bf16 v[224:227], a[112:115], v[172:175], v[224:227]\n"
"buffer_load_dwordx4 v[152:155], v1, s[24:27], s66 offen offset:2048 sc1\n"
"v_mfma_f32_16x16x32_bf16 v[228:231], a[68:71], v[160:163], v[228:231]\n"
"buffer_load_dwordx4 v[156:159], v1, s[24:27], s66 offen offset:3072 sc1\n"
"v_mfma_f32_16x16x32_bf16 v[228:231], a[84:87], v[164:167], v[228:231]\n"
"s_waitcnt lgkmcnt(0)\n"
"s_barrier\n"
"v_mfma_f32_16x16x32_bf16 v[228:231], a[100:103], v[168:171], v[228:231]\n"
"ds_read_b128 v[24:27], v244\n"
"ds_read_b128 v[28:31], v245\n"
"v_mfma_f32_16x16x32_bf16 v[228:231], a[116:119], v[172:175], v[228:231]\n"
"ds_read_b128 v[32:35], v244 offset:4096\n"
"ds_read_b128 v[36:39], v245 offset:4096\n"
"v_mfma_f32_16x16x32_bf16 v[232:235], a[72:75], v[160:163], v[232:235]\n"
"ds_read_b128 v[40:43], v244 offset:8192\n"
"ds_read_b128 v[44:47], v245 offset:8192\n"
"v_mfma_f32_16x16x32_bf16 v[232:235], a[88:91], v[164:167], v[232:235]\n"
"ds_read_b128 v[48:51], v244 offset:12288\n"
"ds_read_b128 v[52:55], v245 offset:12288\n"
"v_mfma_f32_16x16x32_bf16 v[232:235], a[104:107], v[168:171], v[232:235]\n"
"v_mfma_f32_16x16x32_bf16 v[232:235], a[120:123], v[172:175], v[232:235]\n"
"v_mfma_f32_16x16x32_bf16 v[236:239], a[76:79], v[160:163], v[236:239]\n"
"v_mfma_f32_16x16x32_bf16 v[236:239], a[92:95], v[164:167], v[236:239]\n"
"v_mfma_f32_16x16x32_bf16 v[236:239], a[108:111], v[168:171], v[236:239]\n"
"v_mfma_f32_16x16x32_bf16 v[236:239], a[124:127], v[172:175], v[236:239]\n"
"s_waitcnt vmcnt(13)\n"
"v_mfma_f32_16x16x32_bf16 v[224:227], a[128:131], v[96:99], v[224:227]\n"
"s_waitcnt lgkmcnt(0)\n"
"v_mfma_f32_16x16x32_bf16 v[224:227], a[144:147], v[100:103], v[224:227]\n"
"s_add_u32 s66, s57, 53248\n"
"v_mfma_f32_16x16x32_bf16 v[224:227], a[160:163], v[104:107], v[224:227]\n"
"buffer_load_dwordx4 v[160:163], v1, s[24:27], s66 offen sc1\n"
"v_mfma_f32_16x16x32_bf16 v[224:227], a[176:179], v[108:111], v[224:227]\n"
"buffer_load_dwordx4 v[164:167], v1, s[24:27], s66 offen offset:1024 sc1\n"
"v_mfma_f32_16x16x32_bf16 v[228:231], a[132:135], v[96:99], v[228:231]\n"
"buffer_load_dwordx4 v[168:171], v1, s[24:27], s66 offen offset:2048 sc1\n"
"v_mfma_f32_16x16x32_bf16 v[228:231], a[148:151], v[100:103], v[228:231]\n"
"buffer_load_dwordx4 v[172:175], v1, s[24:27], s66 offen offset:3072 sc1\n"
"v_mfma_f32_16x16x32_bf16 v[228:231], a[164:167], v[104:107], v[228:231]\n"
"v_add_f32 v24, v24, v32\n"
"v_add_f32 v25, v25, v33\n"
"v_mfma_f32_16x16x32_bf16 v[228:231], a[180:183], v[108:111], v[228:231]\n"
"v_add_f32 v26, v26, v34\n"
"v_add_f32 v27, v27, v35\n"
"v_mfma_f32_16x16x32_bf16 v[232:235], a[136:139], v[96:99], v[232:235]\n"
"v_add_f32 v28, v28, v36\n"
"v_add_f32 v29, v29, v37\n"
"v_mfma_f32_16x16x32_bf16 v[232:235], a[152:155], v[100:103], v[232:235]\n"
"v_add_f32 v30, v30, v38\n"
"v_add_f32 v31, v31, v39\n"
"v_mfma_f32_16x16x32_bf16 v[232:235], a[168:171], v[104:107], v[232:235]\n"
"v_add_f32 v40, v40, v48\n"
"v_add_f32 v41, v41, v49\n"
"v_mfma_f32_16x16x32_bf16 v[232:235], a[184:187], v[108:111], v[232:235]\n"
"v_add_f32 v42, v42, v50\n"
"v_add_f32 v43, v43, v51\n"
"v_mfma_f32_16x16x32_bf16 v[236:239], a[140:143], v[96:99], v[236:239]\n"
"v_add_f32 v44, v44, v52\n"
"v_add_f32 v45, v45, v53\n"
"v_mfma_f32_16x16x32_bf16 v[236:239], a[156:159], v[100:103], v[236:239]\n"
"v_add_f32 v46, v46, v54\n"
"v_add_f32 v47, v47, v55\n"
"v_mfma_f32_16x16x32_bf16 v[236:239], a[172:175], v[104:107], v[236:239]\n"
"v_add_f32 v56, v24, v40\n"
"v_add_f32 v57, v25, v41\n"
"v_mfma_f32_16x16x32_bf16 v[236:239], a[188:191], v[108:111], v[236:239]\n"
"v_add_f32 v58, v26, v42\n"
"v_add_f32 v59, v27, v43\n"
"s_waitcnt vmcnt(12)\n"
"v_mfma_f32_16x16x32_bf16 v[224:227], a[192:195], v[112:115], v[224:227]\n"
"s_add_u32 s66, s57, 73728\n"
"v_mfma_f32_16x16x32_bf16 v[224:227], a[208:211], v[116:119], v[224:227]\n"
"buffer_load_dwordx4 v[96:99], v1, s[24:27], s66 offen sc1\n"
"v_mfma_f32_16x16x32_bf16 v[224:227], a[224:227], v[120:123], v[224:227]\n"
"buffer_load_dwordx4 v[100:103], v1, s[24:27], s66 offen offset:1024 sc1\n"
"v_mfma_f32_16x16x32_bf16 v[224:227], a[240:243], v[124:127], v[224:227]\n"
"buffer_load_dwordx4 v[104:107], v1, s[24:27], s66 offen offset:2048 sc1\n"
"v_mfma_f32_16x16x32_bf16 v[228:231], a[196:199], v[112:115], v[228:231]\n"
"buffer_load_dwordx4 v[108:111], v1, s[24:27], s66 offen offset:3072 sc1\n"
"v_mfma_f32_16x16x32_bf16 v[228:231], a[212:215], v[116:119], v[228:231]\n"
"v_add_f32 v60, v28, v44\n"
"v_add_f32 v61, v29, v45\n"
"v_mfma_f32_16x16x32_bf16 v[228:231], a[228:231], v[120:123], v[228:231]\n"
"v_add_f32 v62, v30, v46\n"
"v_add_f32 v63, v31, v47\n"
"v_mfma_f32_16x16x32_bf16 v[228:231], a[244:247], v[124:127], v[228:231]\n"
"v_add_u32 v244, 32768, v244\n"
"v_add_u32 v245, 32768, v245\n"
"v_mfma_f32_16x16x32_bf16 v[232:235], a[200:203], v[112:115], v[232:235]\n"
"v_mfma_f32_16x16x32_bf16 v[232:235], a[216:219], v[116:119], v[232:235]\n"
"v_mfma_f32_16x16x32_bf16 v[232:235], a[232:235], v[120:123], v[232:235]\n"
"v_mfma_f32_16x16x32_bf16 v[232:235], a[248:251], v[124:127], v[232:235]\n"
"v_mfma_f32_16x16x32_bf16 v[236:239], a[204:207], v[112:115], v[236:239]\n"
"v_mfma_f32_16x16x32_bf16 v[236:239], a[220:223], v[116:119], v[236:239]\n"
"v_mfma_f32_16x16x32_bf16 v[236:239], a[236:239], v[120:123], v[236:239]\n"
"v_mfma_f32_16x16x32_bf16 v[236:239], a[252:255], v[124:127], v[236:239]\n"
"s_waitcnt vmcnt(12)\n"
"v_mfma_f32_16x16x32_bf16 v[208:211], a[0:3], v[128:131], 0\n"
"s_add_u32 s66, s57, 16384\n"
"v_mfma_f32_16x16x32_bf16 v[208:211], a[16:19], v[132:135], v[208:211]\n"
"buffer_load_dwordx4 v[112:115], v16, s[24:27], s66 offen sc1\n"
"v_mfma_f32_16x16x32_bf16 v[208:211], a[32:35], v[136:139], v[208:211]\n"
"buffer_load_dwordx4 v[116:119], v16, s[24:27], s66 offen offset:1024 sc1\n"
"v_mfma_f32_16x16x32_bf16 v[208:211], a[48:51], v[140:143], v[208:211]\n"
"ds_write_b128 v240, v[224:227] offset:49152\n"
"v_mfma_f32_16x16x32_bf16 v[212:215], a[4:7], v[128:131], 0\n"
"buffer_load_dwordx4 v[120:123], v16, s[24:27], s66 offen offset:2048 sc1\n"
"v_mfma_f32_16x16x32_bf16 v[212:215], a[20:23], v[132:135], v[212:215]\n"
"buffer_load_dwordx4 v[124:127], v16, s[24:27], s66 offen offset:3072 sc1\n"
"v_mfma_f32_16x16x32_bf16 v[212:215], a[36:39], v[136:139], v[212:215]\n"
"global_load_dwordx2 v[2:3], v21, s[60:61] offset:16 sc1\n"
"v_mfma_f32_16x16x32_bf16 v[212:215], a[52:55], v[140:143], v[212:215]\n"
"ds_write_b128 v241, v[228:231] offset:49152\n"
"v_mfma_f32_16x16x32_bf16 v[216:219], a[8:11], v[128:131], 0\n"
"v_mfma_f32_16x16x32_bf16 v[216:219], a[24:27], v[132:135], v[216:219]\n"
"v_mfma_f32_16x16x32_bf16 v[216:219], a[40:43], v[136:139], v[216:219]\n"
"v_mfma_f32_16x16x32_bf16 v[216:219], a[56:59], v[140:143], v[216:219]\n"
"ds_write_b128 v242, v[232:235] offset:49152\n"
"v_mfma_f32_16x16x32_bf16 v[220:223], a[12:15], v[128:131], 0\n"
"v_mfma_f32_16x16x32_bf16 v[220:223], a[28:31], v[132:135], v[220:223]\n"
"v_mfma_f32_16x16x32_bf16 v[220:223], a[44:47], v[136:139], v[220:223]\n"
"v_mfma_f32_16x16x32_bf16 v[220:223], a[60:63], v[140:143], v[220:223]\n"
"ds_write_b128 v243, v[236:239] offset:49152\n"
"s_waitcnt vmcnt(13)\n"
"v_mfma_f32_16x16x32_bf16 v[208:211], a[64:67], v[144:147], v[208:211]\n"
"s_add_u32 s66, s57, 36864\n"
"v_mfma_f32_16x16x32_bf16 v[208:211], a[80:83], v[148:151], v[208:211]\n"
"buffer_load_dwordx4 v[128:131], v16, s[24:27], s66 offen sc1\n"
"v_mfma_f32_16x16x32_bf16 v[208:211], a[96:99], v[152:155], v[208:211]\n"
"buffer_load_dwordx4 v[132:135], v16, s[24:27], s66 offen offset:1024 sc1\n"
"v_mfma_f32_16x16x32_bf16 v[208:211], a[112:115], v[156:159], v[208:211]\n"
"buffer_load_dwordx4 v[136:139], v16, s[24:27], s66 offen offset:2048 sc1\n"
"v_mfma_f32_16x16x32_bf16 v[212:215], a[68:71], v[144:147], v[212:215]\n"
"buffer_load_dwordx4 v[140:143], v16, s[24:27], s66 offen offset:3072 sc1\n"
"v_mfma_f32_16x16x32_bf16 v[212:215], a[84:87], v[148:151], v[212:215]\n"
"v_add_u32 v240, 65536, v240\n"
"v_add_u32 v241, 65536, v241\n"
"v_mfma_f32_16x16x32_bf16 v[212:215], a[100:103], v[152:155], v[212:215]\n"
"v_add_u32 v242, 65536, v242\n"
"v_add_u32 v243, 65536, v243\n"
"v_mfma_f32_16x16x32_bf16 v[212:215], a[116:119], v[156:159], v[212:215]\n"
"v_mfma_f32_16x16x32_bf16 v[216:219], a[72:75], v[144:147], v[216:219]\n"
"v_mfma_f32_16x16x32_bf16 v[216:219], a[88:91], v[148:151], v[216:219]\n"
"v_mfma_f32_16x16x32_bf16 v[216:219], a[104:107], v[152:155], v[216:219]\n"
"v_mfma_f32_16x16x32_bf16 v[216:219], a[120:123], v[156:159], v[216:219]\n"
"v_mfma_f32_16x16x32_bf16 v[220:223], a[76:79], v[144:147], v[220:223]\n"
"v_mfma_f32_16x16x32_bf16 v[220:223], a[92:95], v[148:151], v[220:223]\n"
"v_mfma_f32_16x16x32_bf16 v[220:223], a[108:111], v[152:155], v[220:223]\n"
"v_mfma_f32_16x16x32_bf16 v[220:223], a[124:127], v[156:159], v[220:223]\n"
"s_waitcnt vmcnt(13)\n"
"v_mfma_f32_16x16x32_bf16 v[208:211], a[128:131], v[160:163], v[208:211]\n"
"s_add_u32 s66, s57, 57344\n"
"v_mfma_f32_16x16x32_bf16 v[208:211], a[144:147], v[164:167], v[208:211]\n"
"buffer_load_dwordx4 v[144:147], v16, s[24:27], s66 offen sc1\n"
"v_mfma_f32_16x16x32_bf16 v[208:211], a[160:163], v[168:171], v[208:211]\n"
"buffer_load_dwordx4 v[148:151], v16, s[24:27], s66 offen offset:1024 sc1\n"
"v_mfma_f32_16x16x32_bf16 v[208:211], a[176:179], v[172:175], v[208:211]\n"
"buffer_load_dwordx4 v[152:155], v16, s[24:27], s66 offen offset:2048 sc1\n"
"v_mfma_f32_16x16x32_bf16 v[212:215], a[132:135], v[160:163], v[212:215]\n"
"buffer_load_dwordx4 v[156:159], v16, s[24:27], s66 offen offset:3072 sc1\n"
"v_mfma_f32_16x16x32_bf16 v[212:215], a[148:151], v[164:167], v[212:215]\n"
"v_mfma_f32_16x16x32_bf16 v[212:215], a[164:167], v[168:171], v[212:215]\n"
"v_mfma_f32_16x16x32_bf16 v[212:215], a[180:183], v[172:175], v[212:215]\n"
"v_mfma_f32_16x16x32_bf16 v[216:219], a[136:139], v[160:163], v[216:219]\n"
"v_mfma_f32_16x16x32_bf16 v[216:219], a[152:155], v[164:167], v[216:219]\n"
"v_mfma_f32_16x16x32_bf16 v[216:219], a[168:171], v[168:171], v[216:219]\n"
"v_mfma_f32_16x16x32_bf16 v[216:219], a[184:187], v[172:175], v[216:219]\n"
"v_mfma_f32_16x16x32_bf16 v[220:223], a[140:143], v[160:163], v[220:223]\n"
"v_mfma_f32_16x16x32_bf16 v[220:223], a[156:159], v[164:167], v[220:223]\n"
"v_mfma_f32_16x16x32_bf16 v[220:223], a[172:175], v[168:171], v[220:223]\n"
"v_mfma_f32_16x16x32_bf16 v[220:223], a[188:191], v[172:175], v[220:223]\n"
"s_waitcnt vmcnt(13)\n"
"v_mfma_f32_16x16x32_bf16 v[208:211], a[192:195], v[96:99], v[208:211]\n"
"s_add_u32 s66, s57, 77824\n"
"v_mfma_f32_16x16x32_bf16 v[208:211], a[208:211], v[100:103], v[208:211]\n"
"buffer_load_dwordx4 v[160:163], v16, s[24:27], s66 offen sc1\n"
"v_mfma_f32_16x16x32_bf16 v[208:211], a[224:227], v[104:107], v[208:211]\n"
"buffer_load_dwordx4 v[164:167], v16, s[24:27], s66 offen offset:1024 sc1\n"
"v_mfma_f32_16x16x32_bf16 v[208:211], a[240:243], v[108:111], v[208:211]\n"
"buffer_load_dwordx4 v[168:171], v16, s[24:27], s66 offen offset:2048 sc1\n"
"v_mfma_f32_16x16x32_bf16 v[212:215], a[196:199], v[96:99], v[212:215]\n"
"buffer_load_dwordx4 v[172:175], v16, s[24:27], s66 offen offset:3072 sc1\n"
"v_mfma_f32_16x16x32_bf16 v[212:215], a[212:215], v[100:103], v[212:215]\n"
"v_mfma_f32_16x16x32_bf16 v[212:215], a[228:231], v[104:107], v[212:215]\n"
"v_mfma_f32_16x16x32_bf16 v[212:215], a[244:247], v[108:111], v[212:215]\n"
"v_mfma_f32_16x16x32_bf16 v[216:219], a[200:203], v[96:99], v[216:219]\n"
"v_mfma_f32_16x16x32_bf16 v[216:219], a[216:219], v[100:103], v[216:219]\n"
"v_mfma_f32_16x16x32_bf16 v[216:219], a[232:235], v[104:107], v[216:219]\n"
"v_mfma_f32_16x16x32_bf16 v[216:219], a[248:251], v[108:111], v[216:219]\n"
"v_mfma_f32_16x16x32_bf16 v[220:223], a[204:207], v[96:99], v[220:223]\n"
"v_mfma_f32_16x16x32_bf16 v[220:223], a[220:223], v[100:103], v[220:223]\n"
"v_mfma_f32_16x16x32_bf16 v[220:223], a[236:239], v[104:107], v[220:223]\n"
"v_mfma_f32_16x16x32_bf16 v[220:223], a[252:255], v[108:111], v[220:223]\n"
"s_waitcnt vmcnt(12)\n"
"s_mov_b32 s63, 262144\n"
".Lmha_poll2:\n"
"v_cmp_ne_u64 vcc, s[70:71], v[2:3]\n"
"s_nop 3\n"
"s_cmp_eq_u64 vcc, 0\n"
"s_cbranch_scc1 .Lmha_poll2ok\n"
"s_sleep 1\n"
"global_load_dwordx2 v[2:3], v21, s[60:61] offset:16 sc1\n"
"s_sub_u32 s63, s63, 1\n"
"s_waitcnt vmcnt(0)\n"
"s_cbranch_scc0 .Lmha_poll2\n"
".Lmha_poll2ok:\n"
"v_mfma_f32_16x16x32_bf16 v[224:227], a[0:3], v[112:115], 0\n"
"s_mov_b32 s55, 0\n"
"v_mfma_f32_16x16x32_bf16 v[224:227], a[16:19], v[116:119], v[224:227]\n"
"buffer_load_dwordx4 v[72:75], v17, s[36:39], s55 offen sc1\n"
"v_mfma_f32_16x16x32_bf16 v[224:227], a[32:35], v[120:123], v[224:227]\n"
"buffer_load_dwordx4 v[76:79], v17, s[36:39], s55 offen offset:1024 sc1\n"
"v_mfma_f32_16x16x32_bf16 v[224:227], a[48:51], v[124:127], v[224:227]\n"
"ds_write_b128 v240, v[208:211]\n"
"v_mfma_f32_16x16x32_bf16 v[228:231], a[4:7], v[112:115], 0\n"
"s_mov_b32 s55, 8192\n"
"v_mfma_f32_16x16x32_bf16 v[228:231], a[20:23], v[116:119], v[228:231]\n"
"buffer_load_dwordx4 v[80:83], v17, s[36:39], s55 offen sc1\n"
"v_mfma_f32_16x16x32_bf16 v[228:231], a[36:39], v[120:123], v[228:231]\n"
"buffer_load_dwordx4 v[84:87], v17, s[36:39], s55 offen offset:1024 sc1\n"
"v_mfma_f32_16x16x32_bf16 v[228:231], a[52:55], v[124:127], v[228:231]\n"
"ds_write_b128 v241, v[212:215]\n"
"v_mfma_f32_16x16x32_bf16 v[232:235], a[8:11], v[112:115], 0\n"
"s_mov_b32 s55, 16384\n"
"v_mfma_f32_16x16x32_bf16 v[232:235], a[24:27], v[116:119], v[232:235]\n"
"buffer_load_dwordx4 v[88:91], v17, s[36:39], s55 offen sc1\n"
"v_mfma_f32_16x16x32_bf16 v[232:235], a[40:43], v[120:123], v[232:235]\n"
"buffer_load_dwordx4 v[92:95], v17, s[36:39], s55 offen offset:1024 sc1\n"
"v_mfma_f32_16x16x32_bf16 v[232:235], a[56:59], v[124:127], v[232:235]\n"
"ds_write_b128 v242, v[216:219]\n"
"v_mfma_f32_16x16x32_bf16 v[236:239], a[12:15], v[112:115], 0\n"
"v_mfma_f32_16x16x32_bf16 v[236:239], a[28:31], v[116:119], v[236:239]\n"
"v_mfma_f32_16x16x32_bf16 v[236:239], a[44:47], v[120:123], v[236:239]\n"
"v_mfma_f32_16x16x32_bf16 v[236:239], a[60:63], v[124:127], v[236:239]\n"
"ds_write_b128 v243, v[220:223]\n"
"s_waitcnt vmcnt(14)\n"
"v_mfma_f32_16x16x32_bf16 v[224:227], a[64:67], v[128:131], v[224:227]\n"
"s_waitcnt lgkmcnt(0)\n"
"s_barrier\n"
"v_mfma_f32_16x16x32_bf16 v[224:227], a[80:83], v[132:135], v[224:227]\n"
"ds_read_b128 v[24:27], v244\n"
"ds_read_b128 v[28:31], v245\n"
"v_mfma_f32_16x16x32_bf16 v[224:227], a[96:99], v[136:139], v[224:227]\n"
"ds_read_b128 v[32:35], v244 offset:4096\n"
"ds_read_b128 v[36:39], v245 offset:4096\n"
"v_mfma_f32_16x16x32_bf16 v[224:227], a[112:115], v[140:143], v[224:227]\n"
"ds_read_b128 v[40:43], v244 offset:8192\n"
"ds_read_b128 v[44:47], v245 offset:8192\n"
"v_mfma_f32_16x16x32_bf16 v[228:231], a[68:71], v[128:131], v[228:231]\n"
"ds_read_b128 v[48:51], v244 offset:12288\n"
"ds_read_b128 v[52:55], v245 offset:12288\n"
"v_mfma_f32_16x16x32_bf16 v[228:231], a[84:87], v[132:135], v[228:231]\n"
"v_mfma_f32_16x16x32_bf16 v[228:231], a[100:103], v[136:139], v[228:231]\n"
"v_mfma_f32_16x16x32_bf16 v[228:231], a[116:119], v[140:143], v[228:231]\n"
"v_mfma_f32_16x16x32_bf16 v[232:235], a[72:75], v[128:131], v[232:235]\n"
"v_mfma_f32_16x16x32_bf16 v[232:235], a[88:91], v[132:135], v[232:235]\n"
"v_mfma_f32_16x16x32_bf16 v[232:235], a[104:107], v[136:139], v[232:235]\n"
"v_mfma_f32_16x16x32_bf16 v[232:235], a[120:123], v[140:143], v[232:235]\n"
"v_mfma_f32_16x16x32_bf16 v[236:239], a[76:79], v[128:131], v[236:239]\n"
"v_mfma_f32_16x16x32_bf16 v[236:239], a[92:95], v[132:135], v[236:239]\n"
"v_mfma_f32_16x16x32_bf16 v[236:239], a[108:111], v[136:139], v[236:239]\n"
"v_mfma_f32_16x16x32_bf16 v[236:239], a[124:127], v[140:143], v[236:239]\n"
"s_waitcnt vmcnt(10)\n"
"v_mfma_f32_16x16x32_bf16 v[224:227], a[128:131], v[144:147], v[224:227]\n"
"s_waitcnt lgkmcnt(0)\n"
"v_mfma_f32_16x16x32_bf16 v[224:227], a[144:147], v[148:151], v[224:227]\n"
"v_add_f32 v24, v24, v32\n"
"v_add_f32 v25, v25, v33\n"
"v_mfma_f32_16x16x32_bf16 v[224:227], a[160:163], v[152:155], v[224:227]\n"
"v_add_f32 v26, v26, v34\n"
"v_add_f32 v27, v27, v35\n"
"v_mfma_f32_16x16x32_bf16 v[224:227], a[176:179], v[156:159], v[224:227]\n"
"v_add_f32 v28, v28, v36\n"
"v_add_f32 v29, v29, v37\n"
"v_mfma_f32_16x16x32_bf16 v[228:231], a[132:135], v[144:147], v[228:231]\n"
"v_add_f32 v30, v30, v38\n"
"v_add_f32 v31, v31, v39\n"
"v_mfma_f32_16x16x32_bf16 v[228:231], a[148:151], v[148:151], v[228:231]\n"
"v_add_f32 v40, v40, v48\n"
"v_add_f32 v41, v41, v49\n"
"v_mfma_f32_16x16x32_bf16 v[228:231], a[164:167], v[152:155], v[228:231]\n"
"v_add_f32 v42, v42, v50\n"
"v_add_f32 v43, v43, v51\n"
"v_mfma_f32_16x16x32_bf16 v[228:231], a[180:183], v[156:159], v[228:231]\n"
"v_add_f32 v44, v44, v52\n"
"v_add_f32 v45, v45, v53\n"
"v_mfma_f32_16x16x32_bf16 v[232:235], a[136:139], v[144:147], v[232:235]\n"
"v_add_f32 v46, v46, v54\n"
"v_add_f32 v47, v47, v55\n"
"v_mfma_f32_16x16x32_bf16 v[232:235], a[152:155], v[148:151], v[232:235]\n"
"v_add_f32 v64, v24, v40\n"
"v_add_f32 v65, v25, v41\n"
"v_mfma_f32_16x16x32_bf16 v[232:235], a[168:171], v[152:155], v[232:235]\n"
"v_add_f32 v66, v26, v42\n"
"v_add_f32 v67, v27, v43\n"
"v_mfma_f32_16x16x32_bf16 v[232:235], a[184:187], v[156:159], v[232:235]\n"
"v_mfma_f32_16x16x32_bf16 v[236:239], a[140:143], v[144:147], v[236:239]\n"
"v_mfma_f32_16x16x32_bf16 v[236:239], a[156:159], v[148:151], v[236:239]\n"
"v_mfma_f32_16x16x32_bf16 v[236:239], a[172:175], v[152:155], v[236:239]\n"
"v_mfma_f32_16x16x32_bf16 v[236:239], a[188:191], v[156:159], v[236:239]\n"
"s_waitcnt vmcnt(6)\n"
"v_mfma_f32_16x16x32_bf16 v[224:227], a[192:195], v[160:163], v[224:227]\n"
"v_add_f32 v68, v28, v44\n"
"v_add_f32 v69, v29, v45\n"
"v_mfma_f32_16x16x32_bf16 v[224:227], a[208:211], v[164:167], v[224:227]\n"
"v_add_f32 v70, v30, v46\n"
"v_add_f32 v71, v31, v47\n"
"v_mfma_f32_16x16x32_bf16 v[224:227], a[224:227], v[168:171], v[224:227]\n"
"v_add_u32 v244, 32768, v244\n"
"v_add_u32 v245, 32768, v245\n"
"v_mfma_f32_16x16x32_bf16 v[224:227], a[240:243], v[172:175], v[224:227]\n"
"s_waitcnt vmcnt(2)\n"
"v_add_f32 v56, v56, v72\n"
"v_mfma_f32_16x16x32_bf16 v[228:231], a[196:199], v[160:163], v[228:231]\n"
"v_add_f32 v57, v57, v73\n"
"v_add_f32 v58, v58, v74\n"
"v_mfma_f32_16x16x32_bf16 v[228:231], a[212:215], v[164:167], v[228:231]\n"
"v_add_f32 v59, v59, v75\n"
"v_add_f32 v60, v60, v76\n"
"v_mfma_f32_16x16x32_bf16 v[228:231], a[228:231], v[168:171], v[228:231]\n"
"v_add_f32 v61, v61, v77\n"
"v_add_f32 v62, v62, v78\n"
"v_mfma_f32_16x16x32_bf16 v[228:231], a[244:247], v[172:175], v[228:231]\n"
"v_add_f32 v63, v63, v79\n"
"v_cvt_pk_bf16_f32 v56, v56, v57\n"
"v_mfma_f32_16x16x32_bf16 v[232:235], a[200:203], v[160:163], v[232:235]\n"
"v_cvt_pk_bf16_f32 v57, v58, v59\n"
"v_cvt_pk_bf16_f32 v58, v60, v61\n"
"v_mfma_f32_16x16x32_bf16 v[232:235], a[216:219], v[164:167], v[232:235]\n"
"v_cvt_pk_bf16_f32 v59, v62, v63\n"
"v_add_f32 v64, v64, v80\n"
"v_mfma_f32_16x16x32_bf16 v[232:235], a[232:235], v[168:171], v[232:235]\n"
"v_add_f32 v65, v65, v81\n"
"v_add_f32 v66, v66, v82\n"
"v_mfma_f32_16x16x32_bf16 v[232:235], a[248:251], v[172:175], v[232:235]\n"
"v_add_f32 v67, v67, v83\n"
"v_add_f32 v68, v68, v84\n"
"v_mfma_f32_16x16x32_bf16 v[236:239], a[204:207], v[160:163], v[236:239]\n"
"v_add_f32 v69, v69, v85\n"
"v_add_f32 v70, v70, v86\n"
"v_mfma_f32_16x16x32_bf16 v[236:239], a[220:223], v[164:167], v[236:239]\n"
"v_add_f32 v71, v71, v87\n"
"v_cvt_pk_bf16_f32 v64, v64, v65\n"
"v_mfma_f32_16x16x32_bf16 v[236:239], a[236:239], v[168:171], v[236:239]\n"
"v_cvt_pk_bf16_f32 v65, v66, v67\n"
"v_cvt_pk_bf16_f32 v66, v68, v69\n"
"v_mfma_f32_16x16x32_bf16 v[236:239], a[252:255], v[172:175], v[236:239]\n"
"v_cvt_pk_bf16_f32 v67, v70, v71\n"
"ds_write_b128 v240, v[224:227] offset:16384\n"
"ds_write_b128 v241, v[228:231] offset:16384\n"
"ds_write_b128 v242, v[232:235] offset:16384\n"
"buffer_store_dwordx4 v[56:59], v246, s[20:23], s54 offen sc1\n"
"s_add_u32 s54, s54, 32768\n"
"buffer_store_dwordx4 v[64:67], v246, s[20:23], s54 offen sc1\n"
"s_add_u32 s54, s54, 32768\n"
"s_nop 0\n"
"ds_write_b128 v243, v[236:239] offset:16384\n"
"s_waitcnt lgkmcnt(0)\n"
"s_barrier\n"
"s_cmp_ge_u32 s48, 2\n"
"s_cbranch_scc1 .Lmha_two\n"
"ds_read_b128 v[24:27], v244\n"
"ds_read_b128 v[28:31], v245\n"
"ds_read_b128 v[32:35], v244 offset:4096\n"
"ds_read_b128 v[36:39], v245 offset:4096\n"
"ds_read_b128 v[40:43], v244 offset:8192\n"
"ds_read_b128 v[44:47], v245 offset:8192\n"
"ds_read_b128 v[48:51], v244 offset:12288\n"
"ds_read_b128 v[52:55], v245 offset:12288\n"
"s_waitcnt vmcnt(2) lgkmcnt(4)\n"
"v_pk_add_f32 v[88:89], v[88:89], v[24:25]\n"
"v_pk_add_f32 v[90:91], v[90:91], v[26:27]\n"
"v_pk_add_f32 v[92:93], v[92:93], v[28:29]\n"
"v_pk_add_f32 v[94:95], v[94:95], v[30:31]\n"
"v_pk_add_f32 v[88:89], v[88:89], v[32:33]\n"
"v_pk_add_f32 v[90:91], v[90:91], v[34:35]\n"
"v_pk_add_f32 v[92:93], v[92:93], v[36:37]\n"
"v_pk_add_f32 v[94:95], v[94:95], v[38:39]\n"
"s_waitcnt lgkmcnt(0)\n"
"v_pk_add_f32 v[40:41], v[40:41], v[48:49]\n"
"v_pk_add_f32 v[42:43], v[42:43], v[50:51]\n"
"v_pk_add_f32 v[44:45], v[44:45], v[52:53]\n"
"v_pk_add_f32 v[46:47], v[46:47], v[54:55]\n"
"v_pk_add_f32 v[40:41], v[40:41], v[88:89]\n"
"v_pk_add_f32 v[42:43], v[42:43], v[90:91]\n"
"v_pk_add_f32 v[44:45], v[44:45], v[92:93]\n"
"v_pk_add_f32 v[46:47], v[46:47], v[94:95]\n"
"v_cvt_pk_bf16_f32 v40, v40, v41\n"
"v_cvt_pk_bf16_f32 v41, v42, v43\n"
"v_cvt_pk_bf16_f32 v42, v44, v45\n"
"v_cvt_pk_bf16_f32 v43, v46, v47\n"
"buffer_store_dwordx4 v[40:43], v246, s[20:23], s54 offen sc1\n"
"s_branch .Lmha_done\n"
".Lmha_two:\n"
"s_lshl2_add_u32 s52, s50, s48\n"
"s_cmp_lg_u32 s52, 3\n"
"s_cbranch_scc1 .Lmha_done\n"
"s_mov_b64 s[60:61], s[16:17]\n"
"s_lshl_b32 s52, s49, 7\n"
"s_add_u32 s52, s52, 25296896\n"
"s_add_u32 s60, s60, s52\n"
"s_addc_u32 s61, s61, 0\n"
"s_add_u32 s34, s34, 1\n"
"s_addc_u32 s35, s35, 0\n"
"v_mov_b32 v2, s34\n"
"v_mov_b32 v3, s35\n"
"v_mov_b32 v15, 0\n"
"s_mov_b64 exec, 1\n"
"global_store_dwordx2 v15, v[2:3], s[60:61] sc1\n"
"s_mov_b64 exec, -1\n"
".Lmha_done:\n"
    :: "s"(__builtin_amdgcn_kernarg_segment_ptr()), "s"(blockIdx.x), "s"(wave), "s"((unsigned)(size_t)lds)
    : "v0","v1","v2","v3","v4","v5","v6","v7","v8","v9","v10","v11","v12","v13","v14","v15","v16","v17","v18","v19","v20","v21","v22","v23","v24","v25","v26","v27","v28","v29","v30","v31","v32","v33","v34","v35","v36","v37","v38","v39","v40","v41","v42","v43","v44","v45","v46","v47","v48","v49","v50","v51","v52","v53","v54","v55","v56","v57","v58","v59","v60","v61","v62","v63","v64","v65","v66","v67","v68","v69","v70","v71","v72","v73","v74","v75","v76","v77","v78","v79","v80","v81","v82","v83","v84","v85","v86","v87","v88","v89","v90","v91","v92","v93","v94","v95","v96","v97","v98","v99","v100","v101","v102","v103","v104","v105","v106","v107","v108","v109","v110","v111","v112","v113","v114","v115","v116","v117","v118","v119","v120","v121","v122","v123","v124","v125","v126","v127","v128","v129","v130","v131","v132","v133","v134","v135","v136","v137","v138","v139","v140","v141","v142","v143","v144","v145","v146","v147","v148","v149","v150","v151","v152","v153","v154","v155","v156","v157","v158","v159","v160","v161","v162","v163","v164","v165","v166","v167","v168","v169","v170","v171","v172","v173","v174","v175","v176","v177","v178","v179","v180","v181","v182","v183","v184","v185","v186","v187","v188","v189","v190","v191","v192","v193","v194","v195","v196","v197","v198","v199","v200","v201","v202","v203","v204","v205","v206","v207","v208","v209","v210","v211","v212","v213","v214","v215","v216","v217","v218","v219","v220","v221","v222","v223","v224","v225","v226","v227","v228","v229","v230","v231","v232","v233","v234","v235","v236","v237","v238","v239","v240","v241","v242","v243","v244","v245","v246","v247","v248","v249","v250","v251","v252","v253","v254","v255","a0","a1","a2","a3","a4","a5","a6","a7","a8","a9","a10","a11","a12","a13","a14","a15","a16","a17","a18","a19","a20","a21","a22","a23","a24","a25","a26","a27","a28","a29","a30","a31","a32","a33","a34","a35","a36","a37","a38","a39","a40","a41","a42","a43","a44","a45","a46","a47","a48","a49","a50","a51","a52","a53","a54","a55","a56","a57","a58","a59","a60","a61","a62","a63","a64","a65","a66","a67","a68","a69","a70","a71","a72","a73","a74","a75","a76","a77","a78","a79","a80","a81","a82","a83","a84","a85","a86","a87","a88","a89","a90","a91","a92","a93","a94","a95","a96","a97","a98","a99","a100","a101","a102","a103","a104","a105","a106","a107","a108","a109","a110","a111","a112","a113","a114","a115","a116","a117","a118","a119","a120","a121","a122","a123","a124","a125","a126","a127","a128","a129","a130","a131","a132","a133","a134","a135","a136","a137","a138","a139","a140","a141","a142","a143","a144","a145","a146","a147","a148","a149","a150","a151","a152","a153","a154","a155","a156","a157","a158","a159","a160","a161","a162","a163","a164","a165","a166","a167","a168","a169","a170","a171","a172","a173","a174","a175","a176","a177","a178","a179","a180","a181","a182","a183","a184","a185","a186","a187","a188","a189","a190","a191","a192","a193","a194","a195","a196","a197","a198","a199","a200","a201","a202","a203","a204","a205","a206","a207","a208","a209","a210","a211","a212","a213","a214","a215","a216","a217","a218","a219","a220","a221","a222","a223","a224","a225","a226","a227","a228","a229","a230","a231","a232","a233","a234","a235","a236","a237","a238","a239","a240","a241","a242","a243","a244","a245","a246","a247","a248","a249","a250","a251","a252","a253","a254","a255","s4","s5","s6","s7","s8","s9","s10","s11","s12","s13","s14","s15","s16","s17","s18","s19","s20","s21","s22","s23","s24","s25","s26","s27","s28","s29","s30","s31","s34","s35","s36","s37","s38","s39","s40","s41","s42","s43","s44","s45","s46","s47","s48","s49","s50","s51","s52","s53","s54","s55","s56","s57","s58","s59","s60","s61","s62","s63","s64","s65","s66","s67","s68","s69","s70","s71","s72","s73","s74","s75","s76","s77","s78","s79","s80","s81","s82","s83","s84","s85","s86","s87","s88","s89","s90","s91","s92","s93","s94","s95","s96","s97","s98","s99","vcc","memory");
}

extern "C" void kernel_launch(void* const* d_in, const int* in_sizes, int n_in, void* d_out, int out_size, void* d_ws, size_t ws_size, hipStream_t stream) {
  mha_fused<<<256, 256, 0, stream>>>(d_in[0], d_in[1], d_in[2], d_in[3], d_in[4], d_out, d_ws);
}
```
